```python
import torch

def _softmax(x, axis=-1):
    return torch.softmax(x, dim=axis)
def _gelu(x):
    return torch.nn.functional.gelu(x, approximate="tanh")
def _silu(x):
    return torch.nn.functional.silu(x)
def _sigmoid(x):
    return torch.sigmoid(x)
def _layernorm(x, axis=-1, eps=1e-5):
    m = x.mean(dim=axis, keepdim=True)
    v = x.var(dim=axis, keepdim=True, unbiased=False)
    return (x - m) / torch.sqrt(v + eps)
def _scatter_add(tgt, idx, src, axis=0):
    return tgt.clone().index_add_(axis, idx, src)
def _segment_sum(src, seg, n):
    out = torch.zeros((n,) + src.shape[1:], dtype=src.dtype)
    return out.index_add_(0, seg, src)
def _pad(x, pw):
    flat = tuple(v for pair in reversed(pw) for v in pair)
    return torch.nn.functional.pad(x, flat)

kernel_name = 'concat_heads__mha_block_cached__gqa_block_cached__near_ridge'

def setup_inputs():

    _w = (lambda a: (lambda t: t.to(torch.bfloat16) if t.is_floating_point() else t)(torch.as_tensor(a)))
    def _A_setup():
        B, Sq, D = 16, 112, 576
        H, Sk, Dh = 32, 512, 128
        return {
            "x": _w((lambda *s: torch.randn(*s, dtype=torch.bfloat16) * 0.8)(B, Sq, D)),
            "k_cache": _w((lambda *s: torch.randn(*s, dtype=torch.bfloat16) * 0.8)(B, H, Sk, Dh)),
            "v_cache": _w((lambda *s: torch.randn(*s, dtype=torch.bfloat16) * 0.05)(B, H, Sk, Dh)),
            "wq": _w((lambda *s: torch.randn(*s, dtype=torch.bfloat16) * 0.8)(D, H * Dh) * (D ** -0.5)),
            "wo": _w((lambda *s: torch.randn(*s, dtype=torch.bfloat16) * 0.05)(H * Dh, D) * ((H * Dh) ** -0.5)),
        }
    _A_in = _A_setup()

    def _B_setup():
        B, Sq, D = 16, 112, 576
        Hq, Hkv, Sk, Dh = 32, 32, 512, 128
        assert Hq % Hkv == 0, (Hq, Hkv)
        return {
            "x": _w((lambda *s: torch.randn(*s, dtype=torch.bfloat16) * 0.8)(B, Sq, D)),
            "k_cache": _w((lambda *s: torch.randn(*s, dtype=torch.bfloat16) * 0.8)(B, Hkv, Sk, Dh)),
            "v_cache": _w((lambda *s: torch.randn(*s, dtype=torch.bfloat16) * 0.05)(B, Hkv, Sk, Dh)),
            "wq": _w((lambda *s: torch.randn(*s, dtype=torch.bfloat16) * 0.8)(D, Hq * Dh) * (D ** -0.5)),
            "wo": _w((lambda *s: torch.randn(*s, dtype=torch.bfloat16) * 0.05)(Hq * Dh, D) * ((Hq * Dh) ** -0.5)),
        }
    _B_in = _B_setup()

    return {
        "A_x": _A_in["x"],
        "A_k_cache": _A_in["k_cache"],
        "A_v_cache": _A_in["v_cache"],
        "A_wq": _A_in["wq"],
        "A_wo": _A_in["wo"],
        "B_x": _B_in["x"],
        "B_k_cache": _B_in["k_cache"],
        "B_v_cache": _B_in["v_cache"],
        "B_wq": _B_in["wq"],
        "B_wo": _B_in["wo"],
    }

def reference(A_x, A_k_cache, A_v_cache, A_wq, A_wo, B_x, B_k_cache, B_v_cache, B_wq, B_wo):
    def _A_ref(x, k_cache, v_cache, wq, wo):
        B, Sq, D = x.shape
        H, Dh = 32, 128
        q = (lambda x, *d: x.permute(*d))((x @ wq).reshape(B, Sq, H, Dh), 0, 2, 1, 3)
        scores = torch.einsum("bhqd,bhkd->bhqk", q, k_cache) * (Dh ** -0.5)
        p = _softmax(scores, axis=-1)
        o = torch.einsum("bhqk,bhkd->bhqd", p, v_cache)
        return (lambda x, *d: x.permute(*d))(o, 0, 2, 1, 3).reshape(B, Sq, H * Dh) @ wo

    def _B_ref(x, k_cache, v_cache, wq, wo):
        B, Sq, D = x.shape
        Hq, Dh = 32, 128
        Hkv = k_cache.shape[1]
        rep = Hq // Hkv
        q = (lambda x, *d: x.permute(*d))((x @ wq).reshape(B, Sq, Hq, Dh), 0, 2, 1, 3)
        k = (lambda x, n, axis: torch.repeat_interleave(x, n, dim=axis))(k_cache, rep, axis=1)
        v = (lambda x, n, axis: torch.repeat_interleave(x, n, dim=axis))(v_cache, rep, axis=1)
        scores = torch.einsum("bhqd,bhkd->bhqk", q, k) * (Dh ** -0.5)
        p = _softmax(scores, axis=-1)
        o = torch.einsum("bhqk,bhkd->bhqd", p, v)
        return (lambda x, *d: x.permute(*d))(o, 0, 2, 1, 3).reshape(B, Sq, Hq * Dh) @ wo

    a = _A_ref(A_x, A_k_cache, A_v_cache, A_wq, A_wo)
    b = _B_ref(B_x, B_k_cache, B_v_cache, B_wq, B_wo)
    return torch.concatenate([a, b], axis=-1)
```

```cpp
#include <hip/hip_runtime.h>
#include <hip/hip_bf16.h>
typedef __hip_bfloat16 bf16;
constexpr int B=16,SQ=112,D=576,H=32,SK=512,DH=128;
__device__ inline float b2f(bf16 x){return __bfloat162float(x);}
__device__ inline bf16 f2b(float x){return __float2bfloat16(x);}
__global__ void gemm_naive(const bf16* A,const bf16* W,bf16* C,int M,int N,int K,int ldc){
  int n=blockIdx.x*blockDim.x+threadIdx.x; int m=blockIdx.y;
  if(n>=N) return;
  float acc=0; for(int k=0;k<K;++k) acc+=b2f(A[(size_t)m*K+k])*b2f(W[(size_t)k*N+n]);
  C[(size_t)m*ldc+n]=f2b(acc);
}
__global__ void attn_naive(const bf16* Q,const bf16* Kc,const bf16* Vc,bf16* O){
  int s=blockIdx.x,h=blockIdx.y,b=blockIdx.z,t=threadIdx.x;
  __shared__ float q[DH]; __shared__ float p[SK]; __shared__ float red[128];
  q[t]=b2f(Q[((size_t)(b*SQ+s))*(H*DH)+h*DH+t]); __syncthreads();
  const bf16* Kp=Kc+((size_t)(b*H+h))*SK*DH; const bf16* Vp=Vc+((size_t)(b*H+h))*SK*DH;
  float mx=-1e30f;
  for(int k=t;k<SK;k+=128){float a=0; for(int d=0;d<DH;++d)a+=q[d]*b2f(Kp[k*DH+d]); a*=0.08838834764831845f; p[k]=a; mx=fmaxf(mx,a);}
  red[t]=mx; __syncthreads();
  for(int o=64;o>0;o>>=1){if(t<o)red[t]=fmaxf(red[t],red[t+o]); __syncthreads();}
  mx=red[0]; __syncthreads();
  float sm=0; for(int k=t;k<SK;k+=128){float e=__expf(p[k]-mx); p[k]=e; sm+=e;}
  red[t]=sm; __syncthreads();
  for(int o=64;o>0;o>>=1){if(t<o)red[t]+=red[t+o]; __syncthreads();}
  sm=red[0];
  float a=0; for(int k=0;k<SK;++k)a+=p[k]*b2f(Vp[k*DH+t]);
  O[((size_t)(b*SQ+s))*(H*DH)+h*DH+t]=f2b(a/sm);
}
extern "C" void kernel_launch(void* const* d_in, const int* in_sizes, int n_in, void* d_out, int out_size, void* d_ws, size_t ws_size, hipStream_t stream){
  bf16* q=(bf16*)d_ws; bf16* o=q+(size_t)B*SQ*H*DH;
  for(int br=0;br<2;++br){
    const bf16* x=(const bf16*)d_in[br*5+0]; const bf16* kc=(const bf16*)d_in[br*5+1]; const bf16* vc=(const bf16*)d_in[br*5+2];
    const bf16* wq=(const bf16*)d_in[br*5+3]; const bf16* wo=(const bf16*)d_in[br*5+4];
    gemm_naive<<<dim3(H*DH/256,B*SQ),256,0,stream>>>(x,wq,q,B*SQ,H*DH,D,H*DH);
    attn_naive<<<dim3(SQ,H,B),128,0,stream>>>(q,kc,vc,o);
    gemm_naive<<<dim3(3,B*SQ),192,0,stream>>>(o,wo,(bf16*)d_out+br*D,B*SQ,D,H*DH,2*D);
  }
}
```

```cpp
#include <hip/hip_runtime.h>
struct Ptrs { const char* in[10]; char* part; char* out; };

#define M_ "v_mfma_f32_16x16x32_bf16 "
#define T_ "ds_read_b64_tr_b16 "
#define R_ "ds_read_b128 "
#define L_ "buffer_load_dwordx4 "
#define C_ "v_cvt_pk_bf16_f32 "
#define G_ "v_accvgpr_read_b32 "
#define W_ "v_accvgpr_write_b32 "
#define S_ "s_waitcnt "
#define P_ "v_pk_mul_f32 "
#define A_ "s_add_u32 "
#define KBODY(KVP) asm volatile( \
"; ---- setup\n" \
"s_and_b32 s26,%[bid],1\n" \
"s_mul_i32 s60,s26,40\n" \
"s_load_dwordx8 s[8:15],%[ka],s60 offset:0x0\n" \
"s_load_dwordx2 s[16:17],%[ka],s60 offset:0x20\n" \
"s_load_dwordx4 s[20:23],%[ka],0x50\n" \
"s_memrealtime s[18:19]\n" \
"s_mov_b32 s56,%[wid]\n" \
"s_lshr_b32 s28,%[bid],3\n" \
"v_mbcnt_lo_u32_b32 v255,-1,0\n" \
"v_mbcnt_hi_u32_b32 v255,-1,v255\n" \
"s_lshl_b32 s61,s28,2\n" \
A_"s61,s61,s56\n" \
"s_getpc_b64 s[92:93]\n" \
"LBEG_%=:\n" \
"s_mov_b32 s94,LEND_%=-LBEG_%=\n" \
"s_mov_b32 s95,131072\n" \
"s_lshl_b32 s62,s61,10\n" \
"v_lshlrev_b32 v0,4,v255\n" \
L_"a[252:255],v0,s[92:95],s62 offen\n" \
"s_and_b32 s62,%[bid],7\n" \
A_"s58,s61,s62\n" \
"s_and_b32 s58,s58,15\n" \
"s_and_b32 s61,s28,1\n" \
"s_and_b32 s89,s62,6\n" \
"s_or_b32 s89,s89,s61\n" \
"s_lshr_b32 s27,s28,1\n" \
"s_lshl_b32 s61,s61,2\n" \
A_"s74,s27,s61\n" \
"s_sub_u32 s61,s74,9\n" \
"s_cmp_lt_u32 s74,9\n" \
"s_cselect_b32 s74,s74,s61\n" \
"s_sub_u32 s61,s74,9\n" \
"s_cmp_lt_u32 s74,9\n" \
"s_cselect_b32 s74,s74,s61\n" \
"s_lshl_b32 s29,s89,1\n" \
"s_cmp_lt_u32 s89,7\n" \
"s_cselect_b32 s29,s29,0\n" \
"s_mov_b32 s57,%[lds]\n" \
"s_and_b32 s75,s56,1\n" \
"s_mov_b32 s78,133120\n" \
"s_mov_b32 s79,131072\n" \
"s_mov_b32 s38,4710528\n" \
"s_mov_b32 s39,131072\n" \
"v_lshrrev_b32 v243,3,v255\n" \
"v_lshlrev_b32 v250,13,v243\n" \
"v_bfe_u32 v244,v255,4,1\n" \
"v_lshlrev_b32 v244,2,v244\n" \
"v_and_b32 v245,7,v255\n" \
"v_xor_b32 v245,v245,v244\n" \
"v_lshl_add_u32 v250,v245,4,v250\n" \
"v_add_u32 v251,64512,v250\n" \
"v_add_u32 v252,129024,v250\n" \
"v_add_u32 v253,193536,v250\n" \
"s_mul_i32 s72,s75,40960\n" \
A_"s72,s72,s57\n" \
"s_cmp_lt_u32 s56,2\n" \
"s_cselect_b32 s60,12288,0\n" \
A_"s72,s72,s60\n" \
A_"s72,s72,81920\n" \
"s_lshl_b32 s64,s74,19\n" \
"s_mul_i32 s60,s56,3\n" \
"s_min_u32 s61,s56,1\n" \
A_"s60,s60,s61\n" \
"s_lshl_b32 s73,s60,10\n" \
A_"s73,s73,s57\n" \
A_"s73,s73,108032\n" \
A_"s61,s60,s29\n" \
"v_lshrrev_b32 v243,3,v255\n" \
"v_mul_u32_u24 v247,1152,v243\n" \
"v_lshrrev_b32 v244,4,v255\n" \
"v_lshlrev_b32 v245,1,v244\n" \
"v_and_b32 v246,7,v255\n" \
"v_xor_b32 v246,v246,v245\n" \
"v_lshl_add_u32 v247,v246,4,v247\n" \
A_"s62,s61,0\n" \
"s_sub_u32 s63,s62,14\n" \
"s_cmp_lt_u32 s62,14\n" \
"s_cselect_b32 s62,s62,s63\n" \
"s_mul_i32 s62,s62,9216\n" \
A_"s62,s62,4096\n" \
"v_add_u32 v240,s62,v247\n" \
A_"s62,s61,1\n" \
"s_sub_u32 s63,s62,14\n" \
"s_cmp_lt_u32 s62,14\n" \
"s_cselect_b32 s62,s62,s63\n" \
"s_mul_i32 s62,s62,9216\n" \
A_"s62,s62,3072\n" \
"v_add_u32 v241,s62,v247\n" \
A_"s62,s61,2\n" \
"s_sub_u32 s63,s62,14\n" \
"s_cmp_lt_u32 s62,14\n" \
"s_cselect_b32 s62,s62,s63\n" \
"s_mul_i32 s62,s62,9216\n" \
A_"s62,s62,2048\n" \
"v_add_u32 v242,s62,v247\n" \
A_"s62,s61,3\n" \
"s_sub_u32 s63,s62,14\n" \
"s_cmp_lt_u32 s62,14\n" \
"s_cselect_b32 s62,s62,s63\n" \
"s_mul_i32 s62,s62,9216\n" \
A_"s62,s62,1024\n" \
"v_add_u32 v249,s62,v247\n" \
"s_lshl_b32 s65,s74,7\n" \
"s_mul_i32 s60,s27,129024\n" \
"s_lshl_b32 s61,s89,2\n" \
A_"s61,s61,s75\n" \
"s_lshl_b32 s61,s61,8\n" \
"s_lshr_b32 s62,s56,1\n" \
"s_lshl_b32 s62,s62,7\n" \
A_"s61,s61,s62\n" \
S_"lgkmcnt(0)\n" \
A_"s76,s8,s60\n" \
"s_addc_u32 s77,s9,0\n" \
"s_sub_u32 s76,s76,4096\n" \
"s_subb_u32 s77,s77,0\n" \
A_"s36,s14,s61\n" \
"s_addc_u32 s37,s15,0\n" \
A_"m0,s73,0\n" \
"s_nop 0\n" \
L_"v240,s[76:79],s65 offen lds\n" \
L_"v241,s[76:79],s65 offen offset:1024 lds\n" \
L_"v242,s[76:79],s65 offen offset:2048 lds\n" \
L_"v249,s[76:79],s65 offen offset:3072 lds\n" \
A_"m0,s72,0\n" \
"s_nop 0\n" \
L_"v250,s[36:39],s64 offen lds\n" \
L_"v251,s[36:39],s64 offen offset:1024 lds\n" \
L_"v252,s[36:39],s64 offen offset:2048 lds\n" \
L_"v253,s[36:39],s64 offen offset:3072 lds\n" \
A_"s64,s64,262144\n" \
"s_cmp_eq_u32 s64,4718592\n" \
"s_cselect_b32 s64,0,s64\n" \
A_"m0,s72,4096\n" \
"s_nop 0\n" \
L_"v250,s[36:39],s64 offen lds\n" \
L_"v251,s[36:39],s64 offen offset:1024 lds\n" \
L_"v252,s[36:39],s64 offen offset:2048 lds\n" \
L_"v253,s[36:39],s64 offen offset:3072 lds\n" \
"s_lshl_b32 s61,s89,2\n" \
A_"s61,s61,s56\n" \
"s_lshl_b32 s62,s61,8\n" \
A_"s68,s14,s62\n" \
"s_addc_u32 s69,s15,0\n" \
"s_lshl_b32 s60,s27,5\n" \
A_"s60,s60,s61\n" \
"s_lshl_b32 s60,s60,17\n" \
A_"s40,s10,s60\n" \
"s_addc_u32 s41,s11,0\n" \
"s_sub_u32 s40,s40,128\n" \
"s_subb_u32 s41,s41,0\n" \
A_"s44,s12,s60\n" \
"s_addc_u32 s45,s13,0\n" \
"s_sub_u32 s44,s44,128\n" \
"s_subb_u32 s45,s45,0\n" \
"s_mul_i32 s60,s89,589824\n" \
A_"s48,s16,s60\n" \
"s_addc_u32 s49,s17,0\n" \
"s_lshl_b32 s61,s26,4\n" \
A_"s61,s61,s27\n" \
"s_mul_i32 s60,s61,1032192\n" \
A_"s52,s20,s60\n" \
"s_addc_u32 s53,s21,0\n" \
"s_lshl_b32 s60,s61,13\n" \
A_"s60,s60,50331648\n" \
A_"s84,s20,s60\n" \
"s_addc_u32 s85,s21,0\n" \
"s_mul_i32 s60,s27,258048\n" \
"s_mul_i32 s61,s26,1152\n" \
A_"s60,s60,s61\n" \
A_"s22,s22,s60\n" \
"s_addc_u32 s23,s23,0\n" \
"s_lshl_b32 s60,s27,1\n" \
A_"s60,s60,s75\n" \
"s_mul_i32 s30,s60,18432\n" \
"s_mov_b32 s42,131200\n" \
"s_mov_b32 s43,131072\n" \
"s_mov_b32 s46,131200\n" \
"s_mov_b32 s47,131072\n" \
"s_mov_b32 s50,589824\n" \
"s_mov_b32 s51,131072\n" \
"s_mov_b32 s54,1032192\n" \
"s_mov_b32 s55,131072\n" \
"s_mov_b32 s86,8192\n" \
"s_mov_b32 s87,131072\n" \
"s_mov_b32 s70,4710656\n" \
"s_mov_b32 s71,131072\n" \
"s_mov_b32 s80,0x3e0293ee\n" \
"s_mov_b32 s81,0x3e0293ee\n" \
"s_mov_b32 s82,0x0\n" \
"s_mul_i32 s83,s56,40960\n" \
A_"s83,s83,s57\n" \
"s_lshl_b32 s66,s58,13\n" \
"s_mov_b32 s20,s18\n" \
A_"s60,s57,108032\n" \
"v_lshlrev_b32 v228,4,v255\n" \
"v_add_u32 v228,128,v228\n" \
"v_subrev_u32 v229,32,v228\n" \
"v_subrev_u32 v230,64,v228\n" \
"v_subrev_u32 v231,96,v228\n" \
"v_and_b32 v236,7,v255\n" \
"v_mul_u32_u24 v232,1056,v236\n" \
"v_bfe_u32 v236,v255,3,1\n" \
"v_lshl_add_u32 v232,v236,8,v232\n" \
"v_lshrrev_b32 v237,4,v255\n" \
"v_lshl_add_u32 v232,v237,4,v232\n" \
"v_add_u32 v232,s83,v232\n" \
"v_bfe_u32 v236,v255,2,3\n" \
"v_mul_u32_u24 v233,1056,v236\n" \
"v_lshrrev_b32 v236,5,v255\n" \
"v_lshl_add_u32 v233,v236,8,v233\n" \
"v_and_b32 v236,3,v255\n" \
"v_lshl_add_u32 v233,v236,3,v233\n" \
"v_add_u32 v233,s83,v233\n" \
"v_mov_b32 v224,0x3f803f80\n" \
"v_mov_b32 v225,0x3f803f80\n" \
"v_mov_b32 v226,0x3f803f80\n" \
"v_mov_b32 v227,0x3f803f80\n" \
"v_and_b32 v243,15,v255\n" \
"v_lshrrev_b32 v244,4,v255\n" \
"v_and_b32 v245,6,v243\n" \
"v_xor_b32 v246,v244,v245\n" \
"v_lshlrev_b32 v247,7,v243\n" \
"v_add_u32 v247,s60,v247\n" \
"v_lshl_add_u32 v236,v246,4,v247\n" \
"v_xor_b32 v246,4,v246\n" \
"v_lshl_add_u32 v237,v246,4,v247\n" \
"v_lshrrev_b32 v243,4,v255\n" \
"v_lshlrev_b32 v238,10,v243\n" \
"v_bfe_u32 v244,v255,2,2\n" \
"v_lshl_add_u32 v238,v244,7,v238\n" \
"v_and_b32 v245,3,v255\n" \
"v_lshl_add_u32 v238,v245,4,v238\n" \
"v_add_u32 v238,s72,v238\n" \
"v_lshrrev_b32 v244,1,v244\n" \
"v_xor_b32 v245,1,v244\n" \
"v_lshl_add_u32 v239,v245,6,v238\n" \
"v_lshl_add_u32 v238,v244,6,v238\n" \
"s_lshl_b32 s60,s56,2\n" \
A_"s60,s60,s57\n" \
A_"s60,s60,163776\n" \
"v_mov_b32 v234,s60\n" \
"s_xor_b32 s60,s60,4\n" \
"v_mov_b32 v235,s60\n" \
"v_mov_b32 v247,0\n" \
"ds_write_b32 v234,v247 offset:0\n" \
W_"a160,0\n" \
W_"a161,0\n" \
W_"a162,0\n" \
W_"a163,0\n" \
W_"a164,0\n" \
W_"a165,0\n" \
W_"a166,0\n" \
W_"a167,0\n" \
W_"a168,0\n" \
W_"a169,0\n" \
W_"a170,0\n" \
W_"a171,0\n" \
W_"a172,0\n" \
W_"a173,0\n" \
W_"a174,0\n" \
W_"a175,0\n" \
W_"a176,0\n" \
W_"a177,0\n" \
W_"a178,0\n" \
W_"a179,0\n" \
W_"a180,0\n" \
W_"a181,0\n" \
W_"a182,0\n" \
W_"a183,0\n" \
W_"a184,0\n" \
W_"a185,0\n" \
W_"a186,0\n" \
W_"a187,0\n" \
W_"a188,0\n" \
W_"a189,0\n" \
W_"a190,0\n" \
W_"a191,0\n" \
W_"a192,0\n" \
W_"a193,0\n" \
W_"a194,0\n" \
W_"a195,0\n" \
W_"a196,0\n" \
W_"a197,0\n" \
W_"a198,0\n" \
W_"a199,0\n" \
W_"a200,0\n" \
W_"a201,0\n" \
W_"a202,0\n" \
W_"a203,0\n" \
W_"a204,0\n" \
W_"a205,0\n" \
W_"a206,0\n" \
W_"a207,0\n" \
W_"a208,0\n" \
W_"a209,0\n" \
W_"a210,0\n" \
W_"a211,0\n" \
W_"a212,0\n" \
W_"a213,0\n" \
W_"a214,0\n" \
W_"a215,0\n" \
W_"a216,0\n" \
W_"a217,0\n" \
W_"a218,0\n" \
W_"a219,0\n" \
W_"a220,0\n" \
W_"a221,0\n" \
W_"a222,0\n" \
W_"a223,0\n" \
"s_mov_b64 s[92:93],s[22:23]\n" \
"s_mov_b32 s94,256896\n" \
"s_mov_b32 s95,131072\n" \
"s_mov_b32 s96,1048576\n" \
A_"s15,s89,1\n" \
"s_and_b32 s15,s15,7\n" \
"s_sub_u32 s8,s89,s15\n" \
A_"s62,s8,7\n" \
"s_cmp_lt_u32 s89,s15\n" \
"s_cselect_b32 s8,s62,s8\n" \
"s_lshl_b32 s8,s8,11\n" \
"s_mul_i32 s15,s15,129024\n" \
A_"s8,s8,s15\n" \
"s_cmp_lt_u32 s89,7\n" \
"s_cselect_b32 s8,s8,0x40000000\n" \
A_"s15,s89,2\n" \
"s_and_b32 s15,s15,7\n" \
"s_sub_u32 s9,s89,s15\n" \
A_"s62,s9,7\n" \
"s_cmp_lt_u32 s89,s15\n" \
"s_cselect_b32 s9,s62,s9\n" \
"s_lshl_b32 s9,s9,11\n" \
"s_mul_i32 s15,s15,129024\n" \
A_"s9,s9,s15\n" \
"s_cmp_lt_u32 s89,7\n" \
"s_cselect_b32 s9,s9,0x40000000\n" \
A_"s15,s89,3\n" \
"s_and_b32 s15,s15,7\n" \
"s_sub_u32 s10,s89,s15\n" \
A_"s62,s10,7\n" \
"s_cmp_lt_u32 s89,s15\n" \
"s_cselect_b32 s10,s62,s10\n" \
"s_lshl_b32 s10,s10,11\n" \
"s_mul_i32 s15,s15,129024\n" \
A_"s10,s10,s15\n" \
"s_cmp_lt_u32 s89,7\n" \
"s_cselect_b32 s10,s10,0x40000000\n" \
A_"s15,s89,4\n" \
"s_and_b32 s15,s15,7\n" \
"s_sub_u32 s11,s89,s15\n" \
A_"s62,s11,7\n" \
"s_cmp_lt_u32 s89,s15\n" \
"s_cselect_b32 s11,s62,s11\n" \
"s_lshl_b32 s11,s11,11\n" \
"s_mul_i32 s15,s15,129024\n" \
A_"s11,s11,s15\n" \
"s_cmp_lt_u32 s89,7\n" \
"s_cselect_b32 s11,s11,0x40000000\n" \
A_"s15,s89,5\n" \
"s_and_b32 s15,s15,7\n" \
"s_sub_u32 s12,s89,s15\n" \
A_"s62,s12,7\n" \
"s_cmp_lt_u32 s89,s15\n" \
"s_cselect_b32 s12,s62,s12\n" \
"s_lshl_b32 s12,s12,11\n" \
"s_mul_i32 s15,s15,129024\n" \
A_"s12,s12,s15\n" \
"s_cmp_lt_u32 s89,7\n" \
"s_cselect_b32 s12,s12,0x40000000\n" \
A_"s15,s89,6\n" \
"s_and_b32 s15,s15,7\n" \
"s_sub_u32 s13,s89,s15\n" \
A_"s62,s13,7\n" \
"s_cmp_lt_u32 s89,s15\n" \
"s_cselect_b32 s13,s62,s13\n" \
"s_lshl_b32 s13,s13,11\n" \
"s_mul_i32 s15,s15,129024\n" \
A_"s13,s13,s15\n" \
"s_cmp_lt_u32 s89,7\n" \
"s_cselect_b32 s13,s13,0x40000000\n" \
A_"s15,s89,7\n" \
"s_and_b32 s15,s15,7\n" \
"s_sub_u32 s14,s89,s15\n" \
A_"s62,s14,7\n" \
"s_cmp_lt_u32 s89,s15\n" \
"s_cselect_b32 s14,s62,s14\n" \
"s_lshl_b32 s14,s14,11\n" \
"s_mul_i32 s15,s15,129024\n" \
A_"s14,s14,s15\n" \
"s_cmp_lt_u32 s89,7\n" \
"s_cselect_b32 s14,s14,0x40000000\n" \
"s_mul_i32 s60,s75,40960\n" \
A_"s60,s60,s57\n" \
A_"s60,s60,90624\n" \
"v_lshlrev_b32 v248,4,v255\n" \
"v_add_u32 v248,s60,v248\n" \
S_"vmcnt(4) lgkmcnt(0)\n" \
"s_barrier\n" \
T_"a[0:1],v238 offset:0\n" \
T_"a[2:3],v238 offset:512\n" \
T_"a[4:5],v238 offset:8\n" \
T_"a[6:7],v238 offset:520\n" \
T_"a[8:9],v239 offset:0\n" \
T_"a[10:11],v239 offset:512\n" \
T_"a[12:13],v239 offset:8\n" \
T_"a[14:15],v239 offset:520\n" \
R_"a[32:35],v236 offset:0\n" \
R_"a[36:39],v236 offset:2048\n" \
R_"a[40:43],v236 offset:4096\n" \
R_"a[44:47],v236 offset:6144\n" \
R_"a[48:51],v236 offset:8192\n" \
R_"a[52:55],v236 offset:10240\n" \
R_"a[56:59],v236 offset:12288\n" \
"s_cmp_lt_u32 s56,2\n" \
"s_cbranch_scc0 LROLEB_%=\n" \
"; ---- q1r0\n" \
A_"s65,s65,128\n" \
"s_cmp_eq_u32 s65,1152\n" \
"s_cselect_b32 s65,0,s65\n" \
A_"m0,s73,40960\n" \
"s_nop 0\n" \
L_"v240,s[76:79],s65 offen lds\n" \
L_"v241,s[76:79],s65 offen offset:1024 lds\n" \
L_"v242,s[76:79],s65 offen offset:2048 lds\n" \
L_"v249,s[76:79],s65 offen offset:3072 lds\n" \
A_"s64,s64,262144\n" \
"s_cmp_eq_u32 s64,4718592\n" \
"s_cselect_b32 s64,0,s64\n" \
A_"m0,s72,8192\n" \
"s_nop 0\n" \
L_"v250,s[36:39],s64 offen lds\n" \
L_"v251,s[36:39],s64 offen offset:1024 lds\n" \
L_"v252,s[36:39],s64 offen offset:2048 lds\n" \
L_"v253,s[36:39],s64 offen offset:3072 lds\n" \
S_"lgkmcnt(6)\n" \
M_"v[112:115],a[0:3],a[32:35],0\n" \
M_"v[116:119],a[4:7],a[32:35],0\n" \
M_"v[120:123],a[8:11],a[32:35],0\n" \
M_"v[124:127],a[12:15],a[32:35],0\n" \
S_"vmcnt(8)\n" \
R_"a[32:35],v237 offset:0\n" \
S_"lgkmcnt(6)\n" \
M_"v[128:131],a[0:3],a[36:39],0\n" \
T_"a[16:17],v238 offset:4096\n" \
A_"s64,s64,262144\n" \
"s_cmp_eq_u32 s64,4718592\n" \
"s_cselect_b32 s64,0,s64\n" \
A_"m0,s72,0\n" \
M_"v[132:135],a[4:7],a[36:39],0\n" \
T_"a[18:19],v238 offset:4608\n" \
M_"v[136:139],a[8:11],a[36:39],0\n" \
T_"a[20:21],v238 offset:4104\n" \
M_"v[140:143],a[12:15],a[36:39],0\n" \
R_"a[36:39],v237 offset:2048\n" \
S_"lgkmcnt(5)\n" \
M_"v[144:147],a[0:3],a[40:43],0\n" \
T_"a[22:23],v238 offset:4616\n" \
L_"v250,s[36:39],s64 offen lds\n" \
M_"v[148:151],a[4:7],a[40:43],0\n" \
T_"a[24:25],v239 offset:4096\n" \
M_"v[152:155],a[8:11],a[40:43],0\n" \
T_"a[26:27],v239 offset:4608\n" \
M_"v[156:159],a[12:15],a[40:43],0\n" \
R_"a[40:43],v237 offset:4096\n" \
M_"v[160:163],a[0:3],a[44:47],0\n" \
T_"a[28:29],v239 offset:4104\n" \
M_"v[164:167],a[4:7],a[44:47],0\n" \
T_"a[30:31],v239 offset:4616\n" \
L_"v251,s[36:39],s64 offen offset:1024 lds\n" \
M_"v[168:171],a[8:11],a[44:47],0\n" \
M_"v[172:175],a[12:15],a[44:47],0\n" \
R_"a[44:47],v237 offset:6144\n" \
M_"v[176:179],a[0:3],a[48:51],0\n" \
M_"v[180:183],a[4:7],a[48:51],0\n" \
L_"v252,s[36:39],s64 offen offset:2048 lds\n" \
M_"v[184:187],a[8:11],a[48:51],0\n" \
M_"v[188:191],a[12:15],a[48:51],0\n" \
R_"a[48:51],v237 offset:8192\n" \
M_"v[192:195],a[0:3],a[52:55],0\n" \
M_"v[196:199],a[4:7],a[52:55],0\n" \
M_"v[200:203],a[8:11],a[52:55],0\n" \
L_"v253,s[36:39],s64 offen offset:3072 lds\n" \
M_"v[204:207],a[12:15],a[52:55],0\n" \
R_"a[52:55],v237 offset:10240\n" \
M_"v[208:211],a[0:3],a[56:59],0\n" \
M_"v[212:215],a[4:7],a[56:59],0\n" \
M_"v[216:219],a[8:11],a[56:59],0\n" \
M_"v[220:223],a[12:15],a[56:59],0\n" \
R_"a[56:59],v237 offset:12288\n" \
S_"lgkmcnt(4)\n" \
M_"v[112:115],a[16:19],a[32:35],v[112:115]\n" \
M_"v[116:119],a[20:23],a[32:35],v[116:119]\n" \
M_"v[120:123],a[24:27],a[32:35],v[120:123]\n" \
M_"v[124:127],a[28:31],a[32:35],v[124:127]\n" \
S_"vmcnt(4) lgkmcnt(0)\n" \
"s_barrier\n" \
R_"a[32:35],v236 offset:40960\n" \
M_"v[128:131],a[16:19],a[36:39],v[128:131]\n" \
T_"a[0:1],v238 offset:8192\n" \
A_"s64,s64,262144\n" \
"s_cmp_eq_u32 s64,4718592\n" \
"s_cselect_b32 s64,0,s64\n" \
A_"m0,s72,4096\n" \
M_"v[132:135],a[20:23],a[36:39],v[132:135]\n" \
T_"a[2:3],v238 offset:8704\n" \
L_"v250,s[36:39],s64 offen lds\n" \
M_"v[136:139],a[24:27],a[36:39],v[136:139]\n" \
T_"a[4:5],v238 offset:8200\n" \
M_"v[140:143],a[28:31],a[36:39],v[140:143]\n" \
R_"a[36:39],v236 offset:43008\n" \
M_"v[144:147],a[16:19],a[40:43],v[144:147]\n" \
T_"a[6:7],v238 offset:8712\n" \
L_"v251,s[36:39],s64 offen offset:1024 lds\n" \
M_"v[148:151],a[20:23],a[40:43],v[148:151]\n" \
T_"a[8:9],v239 offset:8192\n" \
M_"v[152:155],a[24:27],a[40:43],v[152:155]\n" \
T_"a[10:11],v239 offset:8704\n" \
L_"v252,s[36:39],s64 offen offset:2048 lds\n" \
M_"v[156:159],a[28:31],a[40:43],v[156:159]\n" \
R_"a[40:43],v236 offset:45056\n" \
M_"v[160:163],a[16:19],a[44:47],v[160:163]\n" \
T_"a[12:13],v239 offset:8200\n" \
M_"v[164:167],a[20:23],a[44:47],v[164:167]\n" \
T_"a[14:15],v239 offset:8712\n" \
L_"v253,s[36:39],s64 offen offset:3072 lds\n" \
M_"v[168:171],a[24:27],a[44:47],v[168:171]\n" \
M_"v[172:175],a[28:31],a[44:47],v[172:175]\n" \
R_"a[44:47],v236 offset:47104\n" \
M_"v[176:179],a[16:19],a[48:51],v[176:179]\n" \
A_"s65,s65,128\n" \
"s_cmp_eq_u32 s65,1152\n" \
"s_cselect_b32 s65,0,s65\n" \
A_"m0,s73,0\n" \
M_"v[180:183],a[20:23],a[48:51],v[180:183]\n" \
L_"v240,s[76:79],s65 offen lds\n" \
M_"v[184:187],a[24:27],a[48:51],v[184:187]\n" \
M_"v[188:191],a[28:31],a[48:51],v[188:191]\n" \
R_"a[48:51],v236 offset:49152\n" \
M_"v[192:195],a[16:19],a[52:55],v[192:195]\n" \
L_"v241,s[76:79],s65 offen offset:1024 lds\n" \
M_"v[196:199],a[20:23],a[52:55],v[196:199]\n" \
M_"v[200:203],a[24:27],a[52:55],v[200:203]\n" \
L_"v242,s[76:79],s65 offen offset:2048 lds\n" \
M_"v[204:207],a[28:31],a[52:55],v[204:207]\n" \
R_"a[52:55],v236 offset:51200\n" \
M_"v[208:211],a[16:19],a[56:59],v[208:211]\n" \
M_"v[212:215],a[20:23],a[56:59],v[212:215]\n" \
L_"v249,s[76:79],s65 offen offset:3072 lds\n" \
M_"v[216:219],a[24:27],a[56:59],v[216:219]\n" \
M_"v[220:223],a[28:31],a[56:59],v[220:223]\n" \
R_"a[56:59],v236 offset:53248\n" \
S_"lgkmcnt(4)\n" \
M_"v[112:115],a[0:3],a[32:35],v[112:115]\n" \
M_"v[116:119],a[4:7],a[32:35],v[116:119]\n" \
M_"v[120:123],a[8:11],a[32:35],v[120:123]\n" \
M_"v[124:127],a[12:15],a[32:35],v[124:127]\n" \
S_"vmcnt(8)\n" \
R_"a[32:35],v237 offset:40960\n" \
M_"v[128:131],a[0:3],a[36:39],v[128:131]\n" \
T_"a[16:17],v238 offset:0\n" \
A_"s64,s64,262144\n" \
"s_cmp_eq_u32 s64,4718592\n" \
"s_cselect_b32 s64,0,s64\n" \
A_"m0,s72,8192\n" \
M_"v[132:135],a[4:7],a[36:39],v[132:135]\n" \
T_"a[18:19],v238 offset:512\n" \
M_"v[136:139],a[8:11],a[36:39],v[136:139]\n" \
T_"a[20:21],v238 offset:8\n" \
M_"v[140:143],a[12:15],a[36:39],v[140:143]\n" \
R_"a[36:39],v237 offset:43008\n" \
M_"v[144:147],a[0:3],a[40:43],v[144:147]\n" \
T_"a[22:23],v238 offset:520\n" \
L_"v250,s[36:39],s64 offen lds\n" \
M_"v[148:151],a[4:7],a[40:43],v[148:151]\n" \
T_"a[24:25],v239 offset:0\n" \
M_"v[152:155],a[8:11],a[40:43],v[152:155]\n" \
T_"a[26:27],v239 offset:512\n" \
M_"v[156:159],a[12:15],a[40:43],v[156:159]\n" \
R_"a[40:43],v237 offset:45056\n" \
S_"lgkmcnt(6)\n" \
M_"v[160:163],a[0:3],a[44:47],v[160:163]\n" \
T_"a[28:29],v239 offset:8\n" \
M_"v[164:167],a[4:7],a[44:47],v[164:167]\n" \
T_"a[30:31],v239 offset:520\n" \
L_"v251,s[36:39],s64 offen offset:1024 lds\n" \
M_"v[168:171],a[8:11],a[44:47],v[168:171]\n" \
M_"v[172:175],a[12:15],a[44:47],v[172:175]\n" \
R_"a[44:47],v237 offset:47104\n" \
M_"v[176:179],a[0:3],a[48:51],v[176:179]\n" \
M_"v[180:183],a[4:7],a[48:51],v[180:183]\n" \
L_"v252,s[36:39],s64 offen offset:2048 lds\n" \
M_"v[184:187],a[8:11],a[48:51],v[184:187]\n" \
M_"v[188:191],a[12:15],a[48:51],v[188:191]\n" \
R_"a[48:51],v237 offset:49152\n" \
M_"v[192:195],a[0:3],a[52:55],v[192:195]\n" \
M_"v[196:199],a[4:7],a[52:55],v[196:199]\n" \
M_"v[200:203],a[8:11],a[52:55],v[200:203]\n" \
L_"v253,s[36:39],s64 offen offset:3072 lds\n" \
M_"v[204:207],a[12:15],a[52:55],v[204:207]\n" \
R_"a[52:55],v237 offset:51200\n" \
M_"v[208:211],a[0:3],a[56:59],v[208:211]\n" \
M_"v[212:215],a[4:7],a[56:59],v[212:215]\n" \
M_"v[216:219],a[8:11],a[56:59],v[216:219]\n" \
M_"v[220:223],a[12:15],a[56:59],v[220:223]\n" \
R_"a[56:59],v237 offset:53248\n" \
S_"lgkmcnt(4)\n" \
M_"v[112:115],a[16:19],a[32:35],v[112:115]\n" \
M_"v[116:119],a[20:23],a[32:35],v[116:119]\n" \
M_"v[120:123],a[24:27],a[32:35],v[120:123]\n" \
M_"v[124:127],a[28:31],a[32:35],v[124:127]\n" \
S_"vmcnt(4) lgkmcnt(0)\n" \
"s_barrier\n" \
R_"a[32:35],v236 offset:0\n" \
M_"v[128:131],a[16:19],a[36:39],v[128:131]\n" \
T_"a[0:1],v238 offset:4096\n" \
A_"s64,s64,262144\n" \
"s_cmp_eq_u32 s64,4718592\n" \
"s_cselect_b32 s64,0,s64\n" \
A_"m0,s72,0\n" \
M_"v[132:135],a[20:23],a[36:39],v[132:135]\n" \
T_"a[2:3],v238 offset:4608\n" \
L_"v250,s[36:39],s64 offen lds\n" \
M_"v[136:139],a[24:27],a[36:39],v[136:139]\n" \
T_"a[4:5],v238 offset:4104\n" \
M_"v[140:143],a[28:31],a[36:39],v[140:143]\n" \
R_"a[36:39],v236 offset:2048\n" \
M_"v[144:147],a[16:19],a[40:43],v[144:147]\n" \
T_"a[6:7],v238 offset:4616\n" \
L_"v251,s[36:39],s64 offen offset:1024 lds\n" \
M_"v[148:151],a[20:23],a[40:43],v[148:151]\n" \
T_"a[8:9],v239 offset:4096\n" \
M_"v[152:155],a[24:27],a[40:43],v[152:155]\n" \
T_"a[10:11],v239 offset:4608\n" \
L_"v252,s[36:39],s64 offen offset:2048 lds\n" \
M_"v[156:159],a[28:31],a[40:43],v[156:159]\n" \
R_"a[40:43],v236 offset:4096\n" \
M_"v[160:163],a[16:19],a[44:47],v[160:163]\n" \
T_"a[12:13],v239 offset:4104\n" \
M_"v[164:167],a[20:23],a[44:47],v[164:167]\n" \
T_"a[14:15],v239 offset:4616\n" \
L_"v253,s[36:39],s64 offen offset:3072 lds\n" \
M_"v[168:171],a[24:27],a[44:47],v[168:171]\n" \
M_"v[172:175],a[28:31],a[44:47],v[172:175]\n" \
R_"a[44:47],v236 offset:6144\n" \
M_"v[176:179],a[16:19],a[48:51],v[176:179]\n" \
A_"s65,s65,128\n" \
"s_cmp_eq_u32 s65,1152\n" \
"s_cselect_b32 s65,0,s65\n" \
A_"m0,s73,40960\n" \
M_"v[180:183],a[20:23],a[48:51],v[180:183]\n" \
L_"v240,s[76:79],s65 offen lds\n" \
M_"v[184:187],a[24:27],a[48:51],v[184:187]\n" \
M_"v[188:191],a[28:31],a[48:51],v[188:191]\n" \
R_"a[48:51],v236 offset:8192\n" \
M_"v[192:195],a[16:19],a[52:55],v[192:195]\n" \
L_"v241,s[76:79],s65 offen offset:1024 lds\n" \
M_"v[196:199],a[20:23],a[52:55],v[196:199]\n" \
M_"v[200:203],a[24:27],a[52:55],v[200:203]\n" \
L_"v242,s[76:79],s65 offen offset:2048 lds\n" \
M_"v[204:207],a[28:31],a[52:55],v[204:207]\n" \
R_"a[52:55],v236 offset:10240\n" \
M_"v[208:211],a[16:19],a[56:59],v[208:211]\n" \
M_"v[212:215],a[20:23],a[56:59],v[212:215]\n" \
L_"v249,s[76:79],s65 offen offset:3072 lds\n" \
M_"v[216:219],a[24:27],a[56:59],v[216:219]\n" \
M_"v[220:223],a[28:31],a[56:59],v[220:223]\n" \
R_"a[56:59],v236 offset:12288\n" \
S_"lgkmcnt(4)\n" \
M_"v[112:115],a[0:3],a[32:35],v[112:115]\n" \
M_"v[116:119],a[4:7],a[32:35],v[116:119]\n" \
M_"v[120:123],a[8:11],a[32:35],v[120:123]\n" \
M_"v[124:127],a[12:15],a[32:35],v[124:127]\n" \
S_"vmcnt(8)\n" \
R_"a[32:35],v237 offset:0\n" \
M_"v[128:131],a[0:3],a[36:39],v[128:131]\n" \
T_"a[16:17],v238 offset:8192\n" \
A_"s64,s64,262144\n" \
"s_cmp_eq_u32 s64,4718592\n" \
"s_cselect_b32 s64,0,s64\n" \
A_"m0,s72,4096\n" \
M_"v[132:135],a[4:7],a[36:39],v[132:135]\n" \
T_"a[18:19],v238 offset:8704\n" \
M_"v[136:139],a[8:11],a[36:39],v[136:139]\n" \
T_"a[20:21],v238 offset:8200\n" \
M_"v[140:143],a[12:15],a[36:39],v[140:143]\n" \
R_"a[36:39],v237 offset:2048\n" \
M_"v[144:147],a[0:3],a[40:43],v[144:147]\n" \
T_"a[22:23],v238 offset:8712\n" \
L_"v250,s[36:39],s64 offen lds\n" \
M_"v[148:151],a[4:7],a[40:43],v[148:151]\n" \
T_"a[24:25],v239 offset:8192\n" \
M_"v[152:155],a[8:11],a[40:43],v[152:155]\n" \
T_"a[26:27],v239 offset:8704\n" \
M_"v[156:159],a[12:15],a[40:43],v[156:159]\n" \
R_"a[40:43],v237 offset:4096\n" \
S_"lgkmcnt(6)\n" \
M_"v[160:163],a[0:3],a[44:47],v[160:163]\n" \
T_"a[28:29],v239 offset:8200\n" \
M_"v[164:167],a[4:7],a[44:47],v[164:167]\n" \
T_"a[30:31],v239 offset:8712\n" \
L_"v251,s[36:39],s64 offen offset:1024 lds\n" \
M_"v[168:171],a[8:11],a[44:47],v[168:171]\n" \
M_"v[172:175],a[12:15],a[44:47],v[172:175]\n" \
R_"a[44:47],v237 offset:6144\n" \
M_"v[176:179],a[0:3],a[48:51],v[176:179]\n" \
M_"v[180:183],a[4:7],a[48:51],v[180:183]\n" \
L_"v252,s[36:39],s64 offen offset:2048 lds\n" \
M_"v[184:187],a[8:11],a[48:51],v[184:187]\n" \
M_"v[188:191],a[12:15],a[48:51],v[188:191]\n" \
R_"a[48:51],v237 offset:8192\n" \
M_"v[192:195],a[0:3],a[52:55],v[192:195]\n" \
M_"v[196:199],a[4:7],a[52:55],v[196:199]\n" \
M_"v[200:203],a[8:11],a[52:55],v[200:203]\n" \
L_"v253,s[36:39],s64 offen offset:3072 lds\n" \
M_"v[204:207],a[12:15],a[52:55],v[204:207]\n" \
R_"a[52:55],v237 offset:10240\n" \
M_"v[208:211],a[0:3],a[56:59],v[208:211]\n" \
M_"v[212:215],a[4:7],a[56:59],v[212:215]\n" \
M_"v[216:219],a[8:11],a[56:59],v[216:219]\n" \
M_"v[220:223],a[12:15],a[56:59],v[220:223]\n" \
R_"a[56:59],v237 offset:12288\n" \
S_"lgkmcnt(4)\n" \
M_"v[112:115],a[16:19],a[32:35],v[112:115]\n" \
M_"v[116:119],a[20:23],a[32:35],v[116:119]\n" \
M_"v[120:123],a[24:27],a[32:35],v[120:123]\n" \
M_"v[124:127],a[28:31],a[32:35],v[124:127]\n" \
S_"vmcnt(4) lgkmcnt(0)\n" \
"s_barrier\n" \
R_"a[32:35],v236 offset:40960\n" \
M_"v[128:131],a[16:19],a[36:39],v[128:131]\n" \
T_"a[0:1],v238 offset:0\n" \
A_"s64,s64,262144\n" \
"s_cmp_eq_u32 s64,4718592\n" \
"s_cselect_b32 s64,0,s64\n" \
A_"m0,s72,8192\n" \
M_"v[132:135],a[20:23],a[36:39],v[132:135]\n" \
T_"a[2:3],v238 offset:512\n" \
L_"v250,s[36:39],s64 offen lds\n" \
M_"v[136:139],a[24:27],a[36:39],v[136:139]\n" \
T_"a[4:5],v238 offset:8\n" \
M_"v[140:143],a[28:31],a[36:39],v[140:143]\n" \
R_"a[36:39],v236 offset:43008\n" \
M_"v[144:147],a[16:19],a[40:43],v[144:147]\n" \
T_"a[6:7],v238 offset:520\n" \
L_"v251,s[36:39],s64 offen offset:1024 lds\n" \
M_"v[148:151],a[20:23],a[40:43],v[148:151]\n" \
T_"a[8:9],v239 offset:0\n" \
M_"v[152:155],a[24:27],a[40:43],v[152:155]\n" \
T_"a[10:11],v239 offset:512\n" \
L_"v252,s[36:39],s64 offen offset:2048 lds\n" \
M_"v[156:159],a[28:31],a[40:43],v[156:159]\n" \
R_"a[40:43],v236 offset:45056\n" \
M_"v[160:163],a[16:19],a[44:47],v[160:163]\n" \
T_"a[12:13],v239 offset:8\n" \
M_"v[164:167],a[20:23],a[44:47],v[164:167]\n" \
T_"a[14:15],v239 offset:520\n" \
L_"v253,s[36:39],s64 offen offset:3072 lds\n" \
M_"v[168:171],a[24:27],a[44:47],v[168:171]\n" \
M_"v[172:175],a[28:31],a[44:47],v[172:175]\n" \
R_"a[44:47],v236 offset:47104\n" \
M_"v[176:179],a[16:19],a[48:51],v[176:179]\n" \
A_"s65,s65,128\n" \
"s_cmp_eq_u32 s65,1152\n" \
"s_cselect_b32 s65,0,s65\n" \
A_"m0,s73,0\n" \
M_"v[180:183],a[20:23],a[48:51],v[180:183]\n" \
L_"v240,s[76:79],s65 offen lds\n" \
M_"v[184:187],a[24:27],a[48:51],v[184:187]\n" \
M_"v[188:191],a[28:31],a[48:51],v[188:191]\n" \
R_"a[48:51],v236 offset:49152\n" \
M_"v[192:195],a[16:19],a[52:55],v[192:195]\n" \
L_"v241,s[76:79],s65 offen offset:1024 lds\n" \
M_"v[196:199],a[20:23],a[52:55],v[196:199]\n" \
M_"v[200:203],a[24:27],a[52:55],v[200:203]\n" \
L_"v242,s[76:79],s65 offen offset:2048 lds\n" \
M_"v[204:207],a[28:31],a[52:55],v[204:207]\n" \
R_"a[52:55],v236 offset:51200\n" \
M_"v[208:211],a[16:19],a[56:59],v[208:211]\n" \
M_"v[212:215],a[20:23],a[56:59],v[212:215]\n" \
L_"v249,s[76:79],s65 offen offset:3072 lds\n" \
M_"v[216:219],a[24:27],a[56:59],v[216:219]\n" \
M_"v[220:223],a[28:31],a[56:59],v[220:223]\n" \
R_"a[56:59],v236 offset:53248\n" \
S_"lgkmcnt(4)\n" \
M_"v[112:115],a[0:3],a[32:35],v[112:115]\n" \
M_"v[116:119],a[4:7],a[32:35],v[116:119]\n" \
M_"v[120:123],a[8:11],a[32:35],v[120:123]\n" \
M_"v[124:127],a[12:15],a[32:35],v[124:127]\n" \
S_"vmcnt(8)\n" \
R_"a[32:35],v237 offset:40960\n" \
M_"v[128:131],a[0:3],a[36:39],v[128:131]\n" \
T_"a[16:17],v238 offset:4096\n" \
A_"s64,s64,262144\n" \
"s_cmp_eq_u32 s64,4718592\n" \
"s_cselect_b32 s64,0,s64\n" \
A_"m0,s72,0\n" \
M_"v[132:135],a[4:7],a[36:39],v[132:135]\n" \
T_"a[18:19],v238 offset:4608\n" \
L_"v250,s[36:39],s64 offen lds\n" \
M_"v[136:139],a[8:11],a[36:39],v[136:139]\n" \
T_"a[20:21],v238 offset:4104\n" \
L_"v251,s[36:39],s64 offen offset:1024 lds\n" \
M_"v[140:143],a[12:15],a[36:39],v[140:143]\n" \
R_"a[36:39],v237 offset:43008\n" \
M_"v[144:147],a[0:3],a[40:43],v[144:147]\n" \
T_"a[22:23],v238 offset:4616\n" \
L_"v252,s[36:39],s64 offen offset:2048 lds\n" \
M_"v[148:151],a[4:7],a[40:43],v[148:151]\n" \
T_"a[24:25],v239 offset:4096\n" \
L_"v253,s[36:39],s64 offen offset:3072 lds\n" \
M_"v[152:155],a[8:11],a[40:43],v[152:155]\n" \
T_"a[26:27],v239 offset:4608\n" \
M_"v[156:159],a[12:15],a[40:43],v[156:159]\n" \
R_"a[40:43],v237 offset:45056\n" \
S_"lgkmcnt(6)\n" \
M_"v[160:163],a[0:3],a[44:47],v[160:163]\n" \
T_"a[28:29],v239 offset:4104\n" \
A_"s67,s66,4096\n" \
A_"m0,s83,0\n" \
M_"v[164:167],a[4:7],a[44:47],v[164:167]\n" \
T_"a[30:31],v239 offset:4616\n" \
L_"v228,s[40:43],s66 offen " KVP " lds\n" \
M_"v[168:171],a[8:11],a[44:47],v[168:171]\n" \
L_"v229,s[40:43],s66 offen offset:1056 " KVP " lds\n" \
M_"v[172:175],a[12:15],a[44:47],v[172:175]\n" \
R_"a[44:47],v237 offset:47104\n" \
M_"v[176:179],a[0:3],a[48:51],v[176:179]\n" \
L_"v230,s[40:43],s66 offen offset:2112 " KVP " lds\n" \
M_"v[180:183],a[4:7],a[48:51],v[180:183]\n" \
L_"v231,s[40:43],s66 offen offset:3168 " KVP " lds\n" \
M_"v[184:187],a[8:11],a[48:51],v[184:187]\n" \
M_"v[188:191],a[12:15],a[48:51],v[188:191]\n" \
R_"a[48:51],v237 offset:49152\n" \
M_"v[192:195],a[0:3],a[52:55],v[192:195]\n" \
A_"m0,s83,4224\n" \
M_"v[196:199],a[4:7],a[52:55],v[196:199]\n" \
L_"v228,s[40:43],s67 offen " KVP " lds\n" \
M_"v[200:203],a[8:11],a[52:55],v[200:203]\n" \
L_"v229,s[40:43],s67 offen offset:1056 " KVP " lds\n" \
M_"v[204:207],a[12:15],a[52:55],v[204:207]\n" \
R_"a[52:55],v237 offset:51200\n" \
M_"v[208:211],a[0:3],a[56:59],v[208:211]\n" \
L_"v230,s[40:43],s67 offen offset:2112 " KVP " lds\n" \
M_"v[212:215],a[4:7],a[56:59],v[212:215]\n" \
L_"v231,s[40:43],s67 offen offset:3168 " KVP " lds\n" \
M_"v[216:219],a[8:11],a[56:59],v[216:219]\n" \
M_"v[220:223],a[12:15],a[56:59],v[220:223]\n" \
R_"a[56:59],v237 offset:53248\n" \
S_"lgkmcnt(4)\n" \
M_"v[112:115],a[16:19],a[32:35],v[112:115]\n" \
M_"v[116:119],a[20:23],a[32:35],v[116:119]\n" \
M_"v[120:123],a[24:27],a[32:35],v[120:123]\n" \
M_"v[124:127],a[28:31],a[32:35],v[124:127]\n" \
S_"vmcnt(12) lgkmcnt(0)\n" \
"s_barrier\n" \
R_"a[32:35],v236 offset:0\n" \
M_"v[128:131],a[16:19],a[36:39],v[128:131]\n" \
T_"a[0:1],v238 offset:8192\n" \
A_"s64,s64,262144\n" \
"s_cmp_eq_u32 s64,4718592\n" \
"s_cselect_b32 s64,0,s64\n" \
A_"m0,s72,4096\n" \
M_"v[132:135],a[20:23],a[36:39],v[132:135]\n" \
T_"a[2:3],v238 offset:8704\n" \
L_"v250,s[36:39],s64 offen lds\n" \
M_"v[136:139],a[24:27],a[36:39],v[136:139]\n" \
T_"a[4:5],v238 offset:8200\n" \
M_"v[140:143],a[28:31],a[36:39],v[140:143]\n" \
R_"a[36:39],v236 offset:2048\n" \
M_"v[144:147],a[16:19],a[40:43],v[144:147]\n" \
T_"a[6:7],v238 offset:8712\n" \
L_"v251,s[36:39],s64 offen offset:1024 lds\n" \
M_"v[148:151],a[20:23],a[40:43],v[148:151]\n" \
T_"a[8:9],v239 offset:8192\n" \
M_"v[152:155],a[24:27],a[40:43],v[152:155]\n" \
T_"a[10:11],v239 offset:8704\n" \
L_"v252,s[36:39],s64 offen offset:2048 lds\n" \
M_"v[156:159],a[28:31],a[40:43],v[156:159]\n" \
R_"a[40:43],v236 offset:4096\n" \
M_"v[160:163],a[16:19],a[44:47],v[160:163]\n" \
T_"a[12:13],v239 offset:8200\n" \
M_"v[164:167],a[20:23],a[44:47],v[164:167]\n" \
T_"a[14:15],v239 offset:8712\n" \
L_"v253,s[36:39],s64 offen offset:3072 lds\n" \
M_"v[168:171],a[24:27],a[44:47],v[168:171]\n" \
M_"v[172:175],a[28:31],a[44:47],v[172:175]\n" \
R_"a[44:47],v236 offset:6144\n" \
M_"v[176:179],a[16:19],a[48:51],v[176:179]\n" \
A_"s65,s65,128\n" \
"s_cmp_eq_u32 s65,1152\n" \
"s_cselect_b32 s65,0,s65\n" \
A_"m0,s73,40960\n" \
M_"v[180:183],a[20:23],a[48:51],v[180:183]\n" \
L_"v240,s[76:79],s65 offen lds\n" \
M_"v[184:187],a[24:27],a[48:51],v[184:187]\n" \
M_"v[188:191],a[28:31],a[48:51],v[188:191]\n" \
R_"a[48:51],v236 offset:8192\n" \
M_"v[192:195],a[16:19],a[52:55],v[192:195]\n" \
L_"v241,s[76:79],s65 offen offset:1024 lds\n" \
M_"v[196:199],a[20:23],a[52:55],v[196:199]\n" \
M_"v[200:203],a[24:27],a[52:55],v[200:203]\n" \
L_"v242,s[76:79],s65 offen offset:2048 lds\n" \
M_"v[204:207],a[28:31],a[52:55],v[204:207]\n" \
R_"a[52:55],v236 offset:10240\n" \
M_"v[208:211],a[16:19],a[56:59],v[208:211]\n" \
M_"v[212:215],a[20:23],a[56:59],v[212:215]\n" \
L_"v249,s[76:79],s65 offen offset:3072 lds\n" \
M_"v[216:219],a[24:27],a[56:59],v[216:219]\n" \
M_"v[220:223],a[28:31],a[56:59],v[220:223]\n" \
R_"a[56:59],v236 offset:12288\n" \
S_"lgkmcnt(4)\n" \
M_"v[112:115],a[0:3],a[32:35],v[112:115]\n" \
M_"v[116:119],a[4:7],a[32:35],v[116:119]\n" \
M_"v[120:123],a[8:11],a[32:35],v[120:123]\n" \
M_"v[124:127],a[12:15],a[32:35],v[124:127]\n" \
S_"vmcnt(16)\n" \
R_"a[32:35],v237 offset:0\n" \
M_"v[128:131],a[0:3],a[36:39],v[128:131]\n" \
T_"a[16:17],v238 offset:0\n" \
A_"s64,s64,262144\n" \
"s_cmp_eq_u32 s64,4718592\n" \
"s_cselect_b32 s64,0,s64\n" \
A_"m0,s72,8192\n" \
M_"v[132:135],a[4:7],a[36:39],v[132:135]\n" \
T_"a[18:19],v238 offset:512\n" \
M_"v[136:139],a[8:11],a[36:39],v[136:139]\n" \
T_"a[20:21],v238 offset:8\n" \
M_"v[140:143],a[12:15],a[36:39],v[140:143]\n" \
R_"a[36:39],v237 offset:2048\n" \
M_"v[144:147],a[0:3],a[40:43],v[144:147]\n" \
T_"a[22:23],v238 offset:520\n" \
L_"v250,s[36:39],s64 offen lds\n" \
M_"v[148:151],a[4:7],a[40:43],v[148:151]\n" \
T_"a[24:25],v239 offset:0\n" \
M_"v[152:155],a[8:11],a[40:43],v[152:155]\n" \
T_"a[26:27],v239 offset:512\n" \
M_"v[156:159],a[12:15],a[40:43],v[156:159]\n" \
R_"a[40:43],v237 offset:4096\n" \
S_"lgkmcnt(6)\n" \
M_"v[160:163],a[0:3],a[44:47],v[160:163]\n" \
T_"a[28:29],v239 offset:8\n" \
M_"v[164:167],a[4:7],a[44:47],v[164:167]\n" \
T_"a[30:31],v239 offset:520\n" \
L_"v251,s[36:39],s64 offen offset:1024 lds\n" \
M_"v[168:171],a[8:11],a[44:47],v[168:171]\n" \
M_"v[172:175],a[12:15],a[44:47],v[172:175]\n" \
R_"a[44:47],v237 offset:6144\n" \
M_"v[176:179],a[0:3],a[48:51],v[176:179]\n" \
M_"v[180:183],a[4:7],a[48:51],v[180:183]\n" \
L_"v252,s[36:39],s64 offen offset:2048 lds\n" \
M_"v[184:187],a[8:11],a[48:51],v[184:187]\n" \
M_"v[188:191],a[12:15],a[48:51],v[188:191]\n" \
R_"a[48:51],v237 offset:8192\n" \
M_"v[192:195],a[0:3],a[52:55],v[192:195]\n" \
M_"v[196:199],a[4:7],a[52:55],v[196:199]\n" \
M_"v[200:203],a[8:11],a[52:55],v[200:203]\n" \
L_"v253,s[36:39],s64 offen offset:3072 lds\n" \
M_"v[204:207],a[12:15],a[52:55],v[204:207]\n" \
R_"a[52:55],v237 offset:10240\n" \
M_"v[208:211],a[0:3],a[56:59],v[208:211]\n" \
M_"v[212:215],a[4:7],a[56:59],v[212:215]\n" \
M_"v[216:219],a[8:11],a[56:59],v[216:219]\n" \
M_"v[220:223],a[12:15],a[56:59],v[220:223]\n" \
R_"a[56:59],v237 offset:12288\n" \
S_"lgkmcnt(4)\n" \
M_"v[112:115],a[16:19],a[32:35],v[112:115]\n" \
M_"v[116:119],a[20:23],a[32:35],v[116:119]\n" \
M_"v[120:123],a[24:27],a[32:35],v[120:123]\n" \
M_"v[124:127],a[28:31],a[32:35],v[124:127]\n" \
S_"vmcnt(4) lgkmcnt(0)\n" \
"s_barrier\n" \
R_"a[32:35],v236 offset:40960\n" \
M_"v[128:131],a[16:19],a[36:39],v[128:131]\n" \
T_"a[0:1],v238 offset:4096\n" \
A_"s64,s64,262144\n" \
"s_cmp_eq_u32 s64,4718592\n" \
"s_cselect_b32 s64,0,s64\n" \
A_"m0,s72,0\n" \
M_"v[132:135],a[20:23],a[36:39],v[132:135]\n" \
T_"a[2:3],v238 offset:4608\n" \
L_"v250,s[36:39],s64 offen lds\n" \
M_"v[136:139],a[24:27],a[36:39],v[136:139]\n" \
T_"a[4:5],v238 offset:4104\n" \
M_"v[140:143],a[28:31],a[36:39],v[140:143]\n" \
R_"a[36:39],v236 offset:43008\n" \
M_"v[144:147],a[16:19],a[40:43],v[144:147]\n" \
T_"a[6:7],v238 offset:4616\n" \
L_"v251,s[36:39],s64 offen offset:1024 lds\n" \
M_"v[148:151],a[20:23],a[40:43],v[148:151]\n" \
T_"a[8:9],v239 offset:4096\n" \
M_"v[152:155],a[24:27],a[40:43],v[152:155]\n" \
T_"a[10:11],v239 offset:4608\n" \
L_"v252,s[36:39],s64 offen offset:2048 lds\n" \
M_"v[156:159],a[28:31],a[40:43],v[156:159]\n" \
R_"a[40:43],v236 offset:45056\n" \
M_"v[160:163],a[16:19],a[44:47],v[160:163]\n" \
T_"a[12:13],v239 offset:4104\n" \
M_"v[164:167],a[20:23],a[44:47],v[164:167]\n" \
T_"a[14:15],v239 offset:4616\n" \
L_"v253,s[36:39],s64 offen offset:3072 lds\n" \
M_"v[168:171],a[24:27],a[44:47],v[168:171]\n" \
M_"v[172:175],a[28:31],a[44:47],v[172:175]\n" \
R_"a[44:47],v236 offset:47104\n" \
M_"v[176:179],a[16:19],a[48:51],v[176:179]\n" \
A_"s65,s65,128\n" \
"s_cmp_eq_u32 s65,1152\n" \
"s_cselect_b32 s65,0,s65\n" \
A_"m0,s73,0\n" \
M_"v[180:183],a[20:23],a[48:51],v[180:183]\n" \
L_"v240,s[76:79],s65 offen lds\n" \
M_"v[184:187],a[24:27],a[48:51],v[184:187]\n" \
M_"v[188:191],a[28:31],a[48:51],v[188:191]\n" \
R_"a[48:51],v236 offset:49152\n" \
M_"v[192:195],a[16:19],a[52:55],v[192:195]\n" \
L_"v241,s[76:79],s65 offen offset:1024 lds\n" \
M_"v[196:199],a[20:23],a[52:55],v[196:199]\n" \
M_"v[200:203],a[24:27],a[52:55],v[200:203]\n" \
L_"v242,s[76:79],s65 offen offset:2048 lds\n" \
M_"v[204:207],a[28:31],a[52:55],v[204:207]\n" \
R_"a[52:55],v236 offset:51200\n" \
M_"v[208:211],a[16:19],a[56:59],v[208:211]\n" \
M_"v[212:215],a[20:23],a[56:59],v[212:215]\n" \
L_"v249,s[76:79],s65 offen offset:3072 lds\n" \
M_"v[216:219],a[24:27],a[56:59],v[216:219]\n" \
M_"v[220:223],a[28:31],a[56:59],v[220:223]\n" \
R_"a[56:59],v236 offset:53248\n" \
S_"lgkmcnt(4)\n" \
M_"v[112:115],a[0:3],a[32:35],v[112:115]\n" \
M_"v[116:119],a[4:7],a[32:35],v[116:119]\n" \
M_"v[120:123],a[8:11],a[32:35],v[120:123]\n" \
M_"v[124:127],a[12:15],a[32:35],v[124:127]\n" \
S_"vmcnt(8)\n" \
R_"a[32:35],v237 offset:40960\n" \
M_"v[128:131],a[0:3],a[36:39],v[128:131]\n" \
T_"a[16:17],v238 offset:8192\n" \
A_"s64,s64,262144\n" \
"s_cmp_eq_u32 s64,4718592\n" \
"s_cselect_b32 s64,0,s64\n" \
A_"m0,s72,4096\n" \
M_"v[132:135],a[4:7],a[36:39],v[132:135]\n" \
T_"a[18:19],v238 offset:8704\n" \
L_"v250,s[36:39],s64 offen lds\n" \
M_"v[136:139],a[8:11],a[36:39],v[136:139]\n" \
T_"a[20:21],v238 offset:8200\n" \
L_"v251,s[36:39],s64 offen offset:1024 lds\n" \
M_"v[140:143],a[12:15],a[36:39],v[140:143]\n" \
R_"a[36:39],v237 offset:43008\n" \
M_"v[144:147],a[0:3],a[40:43],v[144:147]\n" \
T_"a[22:23],v238 offset:8712\n" \
L_"v252,s[36:39],s64 offen offset:2048 lds\n" \
M_"v[148:151],a[4:7],a[40:43],v[148:151]\n" \
T_"a[24:25],v239 offset:8192\n" \
L_"v253,s[36:39],s64 offen offset:3072 lds\n" \
M_"v[152:155],a[8:11],a[40:43],v[152:155]\n" \
T_"a[26:27],v239 offset:8704\n" \
M_"v[156:159],a[12:15],a[40:43],v[156:159]\n" \
R_"a[40:43],v237 offset:45056\n" \
S_"lgkmcnt(6)\n" \
M_"v[160:163],a[0:3],a[44:47],v[160:163]\n" \
T_"a[28:29],v239 offset:8200\n" \
A_"m0,s83,8704\n" \
M_"v[164:167],a[4:7],a[44:47],v[164:167]\n" \
T_"a[30:31],v239 offset:8712\n" \
L_"v228,s[44:47],s66 offen " KVP " lds\n" \
M_"v[168:171],a[8:11],a[44:47],v[168:171]\n" \
L_"v229,s[44:47],s66 offen offset:1056 " KVP " lds\n" \
M_"v[172:175],a[12:15],a[44:47],v[172:175]\n" \
R_"a[44:47],v237 offset:47104\n" \
M_"v[176:179],a[0:3],a[48:51],v[176:179]\n" \
L_"v230,s[44:47],s66 offen offset:2112 " KVP " lds\n" \
M_"v[180:183],a[4:7],a[48:51],v[180:183]\n" \
L_"v231,s[44:47],s66 offen offset:3168 " KVP " lds\n" \
M_"v[184:187],a[8:11],a[48:51],v[184:187]\n" \
M_"v[188:191],a[12:15],a[48:51],v[188:191]\n" \
R_"a[48:51],v237 offset:49152\n" \
M_"v[192:195],a[0:3],a[52:55],v[192:195]\n" \
A_"m0,s83,12928\n" \
M_"v[196:199],a[4:7],a[52:55],v[196:199]\n" \
L_"v228,s[44:47],s67 offen " KVP " lds\n" \
M_"v[200:203],a[8:11],a[52:55],v[200:203]\n" \
L_"v229,s[44:47],s67 offen offset:1056 " KVP " lds\n" \
M_"v[204:207],a[12:15],a[52:55],v[204:207]\n" \
R_"a[52:55],v237 offset:51200\n" \
M_"v[208:211],a[0:3],a[56:59],v[208:211]\n" \
L_"v230,s[44:47],s67 offen offset:2112 " KVP " lds\n" \
M_"v[212:215],a[4:7],a[56:59],v[212:215]\n" \
L_"v231,s[44:47],s67 offen offset:3168 " KVP " lds\n" \
M_"v[216:219],a[8:11],a[56:59],v[216:219]\n" \
M_"v[220:223],a[12:15],a[56:59],v[220:223]\n" \
R_"a[56:59],v237 offset:53248\n" \
S_"lgkmcnt(4)\n" \
M_"v[112:115],a[16:19],a[32:35],v[112:115]\n" \
M_"v[116:119],a[20:23],a[32:35],v[116:119]\n" \
M_"v[120:123],a[24:27],a[32:35],v[120:123]\n" \
M_"v[124:127],a[28:31],a[32:35],v[124:127]\n" \
S_"vmcnt(12) lgkmcnt(0)\n" \
"s_barrier\n" \
R_"a[32:35],v236 offset:0\n" \
M_"v[128:131],a[16:19],a[36:39],v[128:131]\n" \
T_"a[0:1],v238 offset:0\n" \
A_"s64,s64,262144\n" \
"s_cmp_eq_u32 s64,4718592\n" \
"s_cselect_b32 s64,0,s64\n" \
A_"m0,s72,8192\n" \
M_"v[132:135],a[20:23],a[36:39],v[132:135]\n" \
T_"a[2:3],v238 offset:512\n" \
L_"v250,s[36:39],s64 offen lds\n" \
M_"v[136:139],a[24:27],a[36:39],v[136:139]\n" \
T_"a[4:5],v238 offset:8\n" \
M_"v[140:143],a[28:31],a[36:39],v[140:143]\n" \
R_"a[36:39],v236 offset:2048\n" \
M_"v[144:147],a[16:19],a[40:43],v[144:147]\n" \
T_"a[6:7],v238 offset:520\n" \
L_"v251,s[36:39],s64 offen offset:1024 lds\n" \
M_"v[148:151],a[20:23],a[40:43],v[148:151]\n" \
T_"a[8:9],v239 offset:0\n" \
M_"v[152:155],a[24:27],a[40:43],v[152:155]\n" \
T_"a[10:11],v239 offset:512\n" \
L_"v252,s[36:39],s64 offen offset:2048 lds\n" \
M_"v[156:159],a[28:31],a[40:43],v[156:159]\n" \
R_"a[40:43],v236 offset:4096\n" \
M_"v[160:163],a[16:19],a[44:47],v[160:163]\n" \
T_"a[12:13],v239 offset:8\n" \
M_"v[164:167],a[20:23],a[44:47],v[164:167]\n" \
T_"a[14:15],v239 offset:520\n" \
L_"v253,s[36:39],s64 offen offset:3072 lds\n" \
M_"v[168:171],a[24:27],a[44:47],v[168:171]\n" \
M_"v[172:175],a[28:31],a[44:47],v[172:175]\n" \
R_"a[44:47],v236 offset:6144\n" \
M_"v[176:179],a[16:19],a[48:51],v[176:179]\n" \
A_"s65,s65,128\n" \
"s_cmp_eq_u32 s65,1152\n" \
"s_cselect_b32 s65,0,s65\n" \
A_"m0,s73,40960\n" \
M_"v[180:183],a[20:23],a[48:51],v[180:183]\n" \
L_"v240,s[76:79],s65 offen lds\n" \
M_"v[184:187],a[24:27],a[48:51],v[184:187]\n" \
M_"v[188:191],a[28:31],a[48:51],v[188:191]\n" \
R_"a[48:51],v236 offset:8192\n" \
M_"v[192:195],a[16:19],a[52:55],v[192:195]\n" \
L_"v241,s[76:79],s65 offen offset:1024 lds\n" \
M_"v[196:199],a[20:23],a[52:55],v[196:199]\n" \
M_"v[200:203],a[24:27],a[52:55],v[200:203]\n" \
L_"v242,s[76:79],s65 offen offset:2048 lds\n" \
M_"v[204:207],a[28:31],a[52:55],v[204:207]\n" \
R_"a[52:55],v236 offset:10240\n" \
M_"v[208:211],a[16:19],a[56:59],v[208:211]\n" \
M_"v[212:215],a[20:23],a[56:59],v[212:215]\n" \
L_"v249,s[76:79],s65 offen offset:3072 lds\n" \
M_"v[216:219],a[24:27],a[56:59],v[216:219]\n" \
M_"v[220:223],a[28:31],a[56:59],v[220:223]\n" \
R_"a[56:59],v236 offset:12288\n" \
S_"lgkmcnt(4)\n" \
M_"v[112:115],a[0:3],a[32:35],v[112:115]\n" \
M_"v[116:119],a[4:7],a[32:35],v[116:119]\n" \
M_"v[120:123],a[8:11],a[32:35],v[120:123]\n" \
M_"v[124:127],a[12:15],a[32:35],v[124:127]\n" \
S_"vmcnt(16)\n" \
R_"a[32:35],v237 offset:0\n" \
M_"v[128:131],a[0:3],a[36:39],v[128:131]\n" \
T_"a[16:17],v238 offset:4096\n" \
A_"s64,s64,262144\n" \
"s_cmp_eq_u32 s64,4718592\n" \
"s_cselect_b32 s64,0,s64\n" \
A_"m0,s72,0\n" \
M_"v[132:135],a[4:7],a[36:39],v[132:135]\n" \
T_"a[18:19],v238 offset:4608\n" \
M_"v[136:139],a[8:11],a[36:39],v[136:139]\n" \
T_"a[20:21],v238 offset:4104\n" \
M_"v[140:143],a[12:15],a[36:39],v[140:143]\n" \
R_"a[36:39],v237 offset:2048\n" \
M_"v[144:147],a[0:3],a[40:43],v[144:147]\n" \
T_"a[22:23],v238 offset:4616\n" \
L_"v250,s[36:39],s64 offen lds\n" \
M_"v[148:151],a[4:7],a[40:43],v[148:151]\n" \
T_"a[24:25],v239 offset:4096\n" \
M_"v[152:155],a[8:11],a[40:43],v[152:155]\n" \
T_"a[26:27],v239 offset:4608\n" \
M_"v[156:159],a[12:15],a[40:43],v[156:159]\n" \
R_"a[40:43],v237 offset:4096\n" \
S_"lgkmcnt(6)\n" \
M_"v[160:163],a[0:3],a[44:47],v[160:163]\n" \
T_"a[28:29],v239 offset:4104\n" \
M_"v[164:167],a[4:7],a[44:47],v[164:167]\n" \
T_"a[30:31],v239 offset:4616\n" \
L_"v251,s[36:39],s64 offen offset:1024 lds\n" \
M_"v[168:171],a[8:11],a[44:47],v[168:171]\n" \
M_"v[172:175],a[12:15],a[44:47],v[172:175]\n" \
R_"a[44:47],v237 offset:6144\n" \
M_"v[176:179],a[0:3],a[48:51],v[176:179]\n" \
M_"v[180:183],a[4:7],a[48:51],v[180:183]\n" \
L_"v252,s[36:39],s64 offen offset:2048 lds\n" \
M_"v[184:187],a[8:11],a[48:51],v[184:187]\n" \
M_"v[188:191],a[12:15],a[48:51],v[188:191]\n" \
R_"a[48:51],v237 offset:8192\n" \
M_"v[192:195],a[0:3],a[52:55],v[192:195]\n" \
M_"v[196:199],a[4:7],a[52:55],v[196:199]\n" \
M_"v[200:203],a[8:11],a[52:55],v[200:203]\n" \
L_"v253,s[36:39],s64 offen offset:3072 lds\n" \
M_"v[204:207],a[12:15],a[52:55],v[204:207]\n" \
R_"a[52:55],v237 offset:10240\n" \
M_"v[208:211],a[0:3],a[56:59],v[208:211]\n" \
M_"v[212:215],a[4:7],a[56:59],v[212:215]\n" \
M_"v[216:219],a[8:11],a[56:59],v[216:219]\n" \
M_"v[220:223],a[12:15],a[56:59],v[220:223]\n" \
R_"a[56:59],v237 offset:12288\n" \
S_"lgkmcnt(4)\n" \
M_"v[112:115],a[16:19],a[32:35],v[112:115]\n" \
M_"v[116:119],a[20:23],a[32:35],v[116:119]\n" \
M_"v[120:123],a[24:27],a[32:35],v[120:123]\n" \
M_"v[124:127],a[28:31],a[32:35],v[124:127]\n" \
S_"vmcnt(4) lgkmcnt(0)\n" \
"s_barrier\n" \
R_"a[32:35],v236 offset:40960\n" \
M_"v[128:131],a[16:19],a[36:39],v[128:131]\n" \
T_"a[0:1],v238 offset:8192\n" \
A_"s64,s64,262144\n" \
"s_cmp_eq_u32 s64,4718592\n" \
"s_cselect_b32 s64,0,s64\n" \
A_"m0,s72,4096\n" \
M_"v[132:135],a[20:23],a[36:39],v[132:135]\n" \
T_"a[2:3],v238 offset:8704\n" \
L_"v250,s[36:39],s64 offen lds\n" \
M_"v[136:139],a[24:27],a[36:39],v[136:139]\n" \
T_"a[4:5],v238 offset:8200\n" \
M_"v[140:143],a[28:31],a[36:39],v[140:143]\n" \
R_"a[36:39],v236 offset:43008\n" \
M_"v[144:147],a[16:19],a[40:43],v[144:147]\n" \
T_"a[6:7],v238 offset:8712\n" \
L_"v251,s[36:39],s64 offen offset:1024 lds\n" \
M_"v[148:151],a[20:23],a[40:43],v[148:151]\n" \
T_"a[8:9],v239 offset:8192\n" \
M_"v[152:155],a[24:27],a[40:43],v[152:155]\n" \
T_"a[10:11],v239 offset:8704\n" \
L_"v252,s[36:39],s64 offen offset:2048 lds\n" \
M_"v[156:159],a[28:31],a[40:43],v[156:159]\n" \
R_"a[40:43],v236 offset:45056\n" \
M_"v[160:163],a[16:19],a[44:47],v[160:163]\n" \
T_"a[12:13],v239 offset:8200\n" \
M_"v[164:167],a[20:23],a[44:47],v[164:167]\n" \
T_"a[14:15],v239 offset:8712\n" \
L_"v253,s[36:39],s64 offen offset:3072 lds\n" \
M_"v[168:171],a[24:27],a[44:47],v[168:171]\n" \
M_"v[172:175],a[28:31],a[44:47],v[172:175]\n" \
R_"a[44:47],v236 offset:47104\n" \
M_"v[176:179],a[16:19],a[48:51],v[176:179]\n" \
A_"s65,s65,128\n" \
"s_cmp_eq_u32 s65,1152\n" \
"s_cselect_b32 s65,0,s65\n" \
A_"m0,s73,0\n" \
M_"v[180:183],a[20:23],a[48:51],v[180:183]\n" \
L_"v240,s[76:79],s65 offen lds\n" \
M_"v[184:187],a[24:27],a[48:51],v[184:187]\n" \
M_"v[188:191],a[28:31],a[48:51],v[188:191]\n" \
R_"a[48:51],v236 offset:49152\n" \
M_"v[192:195],a[16:19],a[52:55],v[192:195]\n" \
L_"v241,s[76:79],s65 offen offset:1024 lds\n" \
M_"v[196:199],a[20:23],a[52:55],v[196:199]\n" \
M_"v[200:203],a[24:27],a[52:55],v[200:203]\n" \
L_"v242,s[76:79],s65 offen offset:2048 lds\n" \
M_"v[204:207],a[28:31],a[52:55],v[204:207]\n" \
R_"a[52:55],v236 offset:51200\n" \
M_"v[208:211],a[16:19],a[56:59],v[208:211]\n" \
M_"v[212:215],a[20:23],a[56:59],v[212:215]\n" \
L_"v249,s[76:79],s65 offen offset:3072 lds\n" \
M_"v[216:219],a[24:27],a[56:59],v[216:219]\n" \
M_"v[220:223],a[28:31],a[56:59],v[220:223]\n" \
R_"a[56:59],v236 offset:53248\n" \
S_"lgkmcnt(4)\n" \
M_"v[112:115],a[0:3],a[32:35],v[112:115]\n" \
M_"v[116:119],a[4:7],a[32:35],v[116:119]\n" \
M_"v[120:123],a[8:11],a[32:35],v[120:123]\n" \
M_"v[124:127],a[12:15],a[32:35],v[124:127]\n" \
S_"vmcnt(8)\n" \
R_"a[32:35],v237 offset:40960\n" \
M_"v[128:131],a[0:3],a[36:39],v[128:131]\n" \
T_"a[16:17],v238 offset:0\n" \
A_"s64,s64,262144\n" \
"s_cmp_eq_u32 s64,4718592\n" \
"s_cselect_b32 s64,0,s64\n" \
A_"m0,s72,8192\n" \
M_"v[132:135],a[4:7],a[36:39],v[132:135]\n" \
T_"a[18:19],v238 offset:512\n" \
M_"v[136:139],a[8:11],a[36:39],v[136:139]\n" \
T_"a[20:21],v238 offset:8\n" \
M_"v[140:143],a[12:15],a[36:39],v[140:143]\n" \
R_"a[36:39],v237 offset:43008\n" \
M_"v[144:147],a[0:3],a[40:43],v[144:147]\n" \
T_"a[22:23],v238 offset:520\n" \
L_"v250,s[36:39],s64 offen lds\n" \
M_"v[148:151],a[4:7],a[40:43],v[148:151]\n" \
T_"a[24:25],v239 offset:0\n" \
M_"v[152:155],a[8:11],a[40:43],v[152:155]\n" \
T_"a[26:27],v239 offset:512\n" \
M_"v[156:159],a[12:15],a[40:43],v[156:159]\n" \
R_"a[40:43],v237 offset:45056\n" \
S_"lgkmcnt(6)\n" \
M_"v[160:163],a[0:3],a[44:47],v[160:163]\n" \
T_"a[28:29],v239 offset:8\n" \
M_"v[164:167],a[4:7],a[44:47],v[164:167]\n" \
T_"a[30:31],v239 offset:520\n" \
L_"v251,s[36:39],s64 offen offset:1024 lds\n" \
M_"v[168:171],a[8:11],a[44:47],v[168:171]\n" \
M_"v[172:175],a[12:15],a[44:47],v[172:175]\n" \
R_"a[44:47],v237 offset:47104\n" \
M_"v[176:179],a[0:3],a[48:51],v[176:179]\n" \
M_"v[180:183],a[4:7],a[48:51],v[180:183]\n" \
L_"v252,s[36:39],s64 offen offset:2048 lds\n" \
M_"v[184:187],a[8:11],a[48:51],v[184:187]\n" \
M_"v[188:191],a[12:15],a[48:51],v[188:191]\n" \
R_"a[48:51],v237 offset:49152\n" \
M_"v[192:195],a[0:3],a[52:55],v[192:195]\n" \
M_"v[196:199],a[4:7],a[52:55],v[196:199]\n" \
M_"v[200:203],a[8:11],a[52:55],v[200:203]\n" \
L_"v253,s[36:39],s64 offen offset:3072 lds\n" \
M_"v[204:207],a[12:15],a[52:55],v[204:207]\n" \
R_"a[52:55],v237 offset:51200\n" \
M_"v[208:211],a[0:3],a[56:59],v[208:211]\n" \
M_"v[212:215],a[4:7],a[56:59],v[212:215]\n" \
M_"v[216:219],a[8:11],a[56:59],v[216:219]\n" \
M_"v[220:223],a[12:15],a[56:59],v[220:223]\n" \
R_"a[56:59],v237 offset:53248\n" \
S_"lgkmcnt(4)\n" \
M_"v[112:115],a[16:19],a[32:35],v[112:115]\n" \
M_"v[116:119],a[20:23],a[32:35],v[116:119]\n" \
M_"v[120:123],a[24:27],a[32:35],v[120:123]\n" \
M_"v[124:127],a[28:31],a[32:35],v[124:127]\n" \
S_"vmcnt(4) lgkmcnt(0)\n" \
"s_barrier\n" \
R_"a[32:35],v236 offset:0\n" \
M_"v[128:131],a[16:19],a[36:39],v[128:131]\n" \
T_"a[0:1],v238 offset:4096\n" \
A_"s66,s66,8192\n" \
"s_and_b32 s66,s66,131071\n" \
A_"s67,s66,4096\n" \
A_"m0,s83,17408\n" \
M_"v[132:135],a[20:23],a[36:39],v[132:135]\n" \
T_"a[2:3],v238 offset:4608\n" \
L_"v228,s[40:43],s66 offen " KVP " lds\n" \
M_"v[136:139],a[24:27],a[36:39],v[136:139]\n" \
T_"a[4:5],v238 offset:4104\n" \
M_"v[140:143],a[28:31],a[36:39],v[140:143]\n" \
R_"a[36:39],v236 offset:2048\n" \
M_"v[144:147],a[16:19],a[40:43],v[144:147]\n" \
T_"a[6:7],v238 offset:4616\n" \
L_"v229,s[40:43],s66 offen offset:1056 " KVP " lds\n" \
M_"v[148:151],a[20:23],a[40:43],v[148:151]\n" \
T_"a[8:9],v239 offset:4096\n" \
M_"v[152:155],a[24:27],a[40:43],v[152:155]\n" \
T_"a[10:11],v239 offset:4608\n" \
L_"v230,s[40:43],s66 offen offset:2112 " KVP " lds\n" \
M_"v[156:159],a[28:31],a[40:43],v[156:159]\n" \
R_"a[40:43],v236 offset:4096\n" \
M_"v[160:163],a[16:19],a[44:47],v[160:163]\n" \
T_"a[12:13],v239 offset:4104\n" \
M_"v[164:167],a[20:23],a[44:47],v[164:167]\n" \
T_"a[14:15],v239 offset:4616\n" \
L_"v231,s[40:43],s66 offen offset:3168 " KVP " lds\n" \
M_"v[168:171],a[24:27],a[44:47],v[168:171]\n" \
M_"v[172:175],a[28:31],a[44:47],v[172:175]\n" \
R_"a[44:47],v236 offset:6144\n" \
M_"v[176:179],a[16:19],a[48:51],v[176:179]\n" \
A_"m0,s83,21632\n" \
M_"v[180:183],a[20:23],a[48:51],v[180:183]\n" \
L_"v228,s[40:43],s67 offen " KVP " lds\n" \
M_"v[184:187],a[24:27],a[48:51],v[184:187]\n" \
M_"v[188:191],a[28:31],a[48:51],v[188:191]\n" \
R_"a[48:51],v236 offset:8192\n" \
M_"v[192:195],a[16:19],a[52:55],v[192:195]\n" \
L_"v229,s[40:43],s67 offen offset:1056 " KVP " lds\n" \
M_"v[196:199],a[20:23],a[52:55],v[196:199]\n" \
M_"v[200:203],a[24:27],a[52:55],v[200:203]\n" \
L_"v230,s[40:43],s67 offen offset:2112 " KVP " lds\n" \
M_"v[204:207],a[28:31],a[52:55],v[204:207]\n" \
R_"a[52:55],v236 offset:10240\n" \
M_"v[208:211],a[16:19],a[56:59],v[208:211]\n" \
M_"v[212:215],a[20:23],a[56:59],v[212:215]\n" \
L_"v231,s[40:43],s67 offen offset:3168 " KVP " lds\n" \
M_"v[216:219],a[24:27],a[56:59],v[216:219]\n" \
M_"v[220:223],a[28:31],a[56:59],v[220:223]\n" \
R_"a[56:59],v236 offset:12288\n" \
S_"lgkmcnt(4)\n" \
M_"v[112:115],a[0:3],a[32:35],v[112:115]\n" \
M_"v[116:119],a[4:7],a[32:35],v[116:119]\n" \
M_"v[120:123],a[8:11],a[32:35],v[120:123]\n" \
M_"v[124:127],a[12:15],a[32:35],v[124:127]\n" \
S_"vmcnt(8)\n" \
R_"a[32:35],v237 offset:0\n" \
M_"v[128:131],a[0:3],a[36:39],v[128:131]\n" \
T_"a[16:17],v238 offset:8192\n" \
A_"m0,s83,26112\n" \
M_"v[132:135],a[4:7],a[36:39],v[132:135]\n" \
T_"a[18:19],v238 offset:8704\n" \
L_"v228,s[44:47],s66 offen " KVP " lds\n" \
M_"v[136:139],a[8:11],a[36:39],v[136:139]\n" \
T_"a[20:21],v238 offset:8200\n" \
M_"v[140:143],a[12:15],a[36:39],v[140:143]\n" \
R_"a[36:39],v237 offset:2048\n" \
M_"v[144:147],a[0:3],a[40:43],v[144:147]\n" \
T_"a[22:23],v238 offset:8712\n" \
L_"v229,s[44:47],s66 offen offset:1056 " KVP " lds\n" \
M_"v[148:151],a[4:7],a[40:43],v[148:151]\n" \
T_"a[24:25],v239 offset:8192\n" \
M_"v[152:155],a[8:11],a[40:43],v[152:155]\n" \
T_"a[26:27],v239 offset:8704\n" \
L_"v230,s[44:47],s66 offen offset:2112 " KVP " lds\n" \
M_"v[156:159],a[12:15],a[40:43],v[156:159]\n" \
R_"a[40:43],v237 offset:4096\n" \
S_"lgkmcnt(6)\n" \
M_"v[160:163],a[0:3],a[44:47],v[160:163]\n" \
T_"a[28:29],v239 offset:8200\n" \
M_"v[164:167],a[4:7],a[44:47],v[164:167]\n" \
T_"a[30:31],v239 offset:8712\n" \
L_"v231,s[44:47],s66 offen offset:3168 " KVP " lds\n" \
M_"v[168:171],a[8:11],a[44:47],v[168:171]\n" \
M_"v[172:175],a[12:15],a[44:47],v[172:175]\n" \
R_"a[44:47],v237 offset:6144\n" \
M_"v[176:179],a[0:3],a[48:51],v[176:179]\n" \
A_"m0,s83,30336\n" \
M_"v[180:183],a[4:7],a[48:51],v[180:183]\n" \
L_"v228,s[44:47],s67 offen " KVP " lds\n" \
M_"v[184:187],a[8:11],a[48:51],v[184:187]\n" \
M_"v[188:191],a[12:15],a[48:51],v[188:191]\n" \
R_"a[48:51],v237 offset:8192\n" \
M_"v[192:195],a[0:3],a[52:55],v[192:195]\n" \
L_"v229,s[44:47],s67 offen offset:1056 " KVP " lds\n" \
M_"v[196:199],a[4:7],a[52:55],v[196:199]\n" \
M_"v[200:203],a[8:11],a[52:55],v[200:203]\n" \
L_"v230,s[44:47],s67 offen offset:2112 " KVP " lds\n" \
M_"v[204:207],a[12:15],a[52:55],v[204:207]\n" \
R_"a[52:55],v237 offset:10240\n" \
M_"v[208:211],a[0:3],a[56:59],v[208:211]\n" \
M_"v[212:215],a[4:7],a[56:59],v[212:215]\n" \
L_"v231,s[44:47],s67 offen offset:3168 " KVP " lds\n" \
M_"v[216:219],a[8:11],a[56:59],v[216:219]\n" \
M_"v[220:223],a[12:15],a[56:59],v[220:223]\n" \
R_"a[56:59],v237 offset:12288\n" \
S_"lgkmcnt(4)\n" \
M_"v[112:115],a[16:19],a[32:35],v[112:115]\n" \
M_"v[116:119],a[20:23],a[32:35],v[116:119]\n" \
"s_barrier\n" \
M_"v[120:123],a[24:27],a[32:35],v[120:123]\n" \
M_"v[124:127],a[28:31],a[32:35],v[124:127]\n" \
M_"v[128:131],a[16:19],a[36:39],v[128:131]\n" \
M_"v[132:135],a[20:23],a[36:39],v[132:135]\n" \
M_"v[136:139],a[24:27],a[36:39],v[136:139]\n" \
M_"v[140:143],a[28:31],a[36:39],v[140:143]\n" \
M_"v[144:147],a[16:19],a[40:43],v[144:147]\n" \
M_"v[148:151],a[20:23],a[40:43],v[148:151]\n" \
P_"v[112:113],v[112:113],s[80:81]\n" \
P_"v[114:115],v[114:115],s[80:81]\n" \
P_"v[116:117],v[116:117],s[80:81]\n" \
M_"v[152:155],a[24:27],a[40:43],v[152:155]\n" \
P_"v[118:119],v[118:119],s[80:81]\n" \
C_"v0,v112,v113\n" \
C_"v1,v114,v115\n" \
M_"v[156:159],a[28:31],a[40:43],v[156:159]\n" \
C_"v2,v116,v117\n" \
C_"v3,v118,v119\n" \
P_"v[120:121],v[120:121],s[80:81]\n" \
S_"lgkmcnt(0)\n" \
M_"v[160:163],a[16:19],a[44:47],v[160:163]\n" \
P_"v[122:123],v[122:123],s[80:81]\n" \
P_"v[124:125],v[124:125],s[80:81]\n" \
P_"v[126:127],v[126:127],s[80:81]\n" \
M_"v[164:167],a[20:23],a[44:47],v[164:167]\n" \
C_"v4,v120,v121\n" \
C_"v5,v122,v123\n" \
C_"v6,v124,v125\n" \
M_"v[168:171],a[24:27],a[44:47],v[168:171]\n" \
C_"v7,v126,v127\n" \
P_"v[128:129],v[128:129],s[80:81]\n" \
P_"v[130:131],v[130:131],s[80:81]\n" \
M_"v[172:175],a[28:31],a[44:47],v[172:175]\n" \
P_"v[132:133],v[132:133],s[80:81]\n" \
P_"v[134:135],v[134:135],s[80:81]\n" \
C_"v16,v128,v129\n" \
M_"v[176:179],a[16:19],a[48:51],v[176:179]\n" \
C_"v17,v130,v131\n" \
C_"v18,v132,v133\n" \
C_"v19,v134,v135\n" \
M_"v[180:183],a[20:23],a[48:51],v[180:183]\n" \
P_"v[136:137],v[136:137],s[80:81]\n" \
P_"v[138:139],v[138:139],s[80:81]\n" \
P_"v[140:141],v[140:141],s[80:81]\n" \
M_"v[184:187],a[24:27],a[48:51],v[184:187]\n" \
P_"v[142:143],v[142:143],s[80:81]\n" \
C_"v20,v136,v137\n" \
C_"v21,v138,v139\n" \
M_"v[188:191],a[28:31],a[48:51],v[188:191]\n" \
C_"v22,v140,v141\n" \
C_"v23,v142,v143\n" \
P_"v[144:145],v[144:145],s[80:81]\n" \
M_"v[192:195],a[16:19],a[52:55],v[192:195]\n" \
P_"v[146:147],v[146:147],s[80:81]\n" \
P_"v[148:149],v[148:149],s[80:81]\n" \
P_"v[150:151],v[150:151],s[80:81]\n" \
M_"v[196:199],a[20:23],a[52:55],v[196:199]\n" \
C_"v32,v144,v145\n" \
C_"v33,v146,v147\n" \
C_"v34,v148,v149\n" \
M_"v[200:203],a[24:27],a[52:55],v[200:203]\n" \
C_"v35,v150,v151\n" \
P_"v[152:153],v[152:153],s[80:81]\n" \
P_"v[154:155],v[154:155],s[80:81]\n" \
M_"v[204:207],a[28:31],a[52:55],v[204:207]\n" \
P_"v[156:157],v[156:157],s[80:81]\n" \
P_"v[158:159],v[158:159],s[80:81]\n" \
C_"v36,v152,v153\n" \
M_"v[208:211],a[16:19],a[56:59],v[208:211]\n" \
C_"v37,v154,v155\n" \
C_"v38,v156,v157\n" \
C_"v39,v158,v159\n" \
M_"v[212:215],a[20:23],a[56:59],v[212:215]\n" \
P_"v[160:161],v[160:161],s[80:81]\n" \
P_"v[162:163],v[162:163],s[80:81]\n" \
P_"v[164:165],v[164:165],s[80:81]\n" \
M_"v[216:219],a[24:27],a[56:59],v[216:219]\n" \
P_"v[166:167],v[166:167],s[80:81]\n" \
C_"v48,v160,v161\n" \
C_"v49,v162,v163\n" \
M_"v[220:223],a[28:31],a[56:59],v[220:223]\n" \
C_"v50,v164,v165\n" \
C_"v51,v166,v167\n" \
P_"v[168:169],v[168:169],s[80:81]\n" \
P_"v[170:171],v[170:171],s[80:81]\n" \
P_"v[172:173],v[172:173],s[80:81]\n" \
P_"v[174:175],v[174:175],s[80:81]\n" \
C_"v52,v168,v169\n" \
C_"v53,v170,v171\n" \
C_"v54,v172,v173\n" \
C_"v55,v174,v175\n" \
P_"v[176:177],v[176:177],s[80:81]\n" \
P_"v[178:179],v[178:179],s[80:81]\n" \
P_"v[180:181],v[180:181],s[80:81]\n" \
P_"v[182:183],v[182:183],s[80:81]\n" \
C_"v64,v176,v177\n" \
C_"v65,v178,v179\n" \
C_"v66,v180,v181\n" \
C_"v67,v182,v183\n" \
P_"v[184:185],v[184:185],s[80:81]\n" \
P_"v[186:187],v[186:187],s[80:81]\n" \
P_"v[188:189],v[188:189],s[80:81]\n" \
P_"v[190:191],v[190:191],s[80:81]\n" \
C_"v68,v184,v185\n" \
C_"v69,v186,v187\n" \
C_"v70,v188,v189\n" \
C_"v71,v190,v191\n" \
P_"v[192:193],v[192:193],s[80:81]\n" \
P_"v[194:195],v[194:195],s[80:81]\n" \
P_"v[196:197],v[196:197],s[80:81]\n" \
P_"v[198:199],v[198:199],s[80:81]\n" \
C_"v80,v192,v193\n" \
C_"v81,v194,v195\n" \
C_"v82,v196,v197\n" \
C_"v83,v198,v199\n" \
P_"v[200:201],v[200:201],s[80:81]\n" \
P_"v[202:203],v[202:203],s[80:81]\n" \
P_"v[204:205],v[204:205],s[80:81]\n" \
P_"v[206:207],v[206:207],s[80:81]\n" \
C_"v84,v200,v201\n" \
C_"v85,v202,v203\n" \
C_"v86,v204,v205\n" \
C_"v87,v206,v207\n" \
P_"v[208:209],v[208:209],s[80:81]\n" \
P_"v[210:211],v[210:211],s[80:81]\n" \
P_"v[212:213],v[212:213],s[80:81]\n" \
P_"v[214:215],v[214:215],s[80:81]\n" \
C_"v96,v208,v209\n" \
C_"v97,v210,v211\n" \
C_"v98,v212,v213\n" \
C_"v99,v214,v215\n" \
P_"v[216:217],v[216:217],s[80:81]\n" \
P_"v[218:219],v[218:219],s[80:81]\n" \
P_"v[220:221],v[220:221],s[80:81]\n" \
P_"v[222:223],v[222:223],s[80:81]\n" \
C_"v100,v216,v217\n" \
C_"v101,v218,v219\n" \
C_"v102,v220,v221\n" \
C_"v103,v222,v223\n" \
"; ---- tra\n" \
"s_barrier\n" \
R_"v[8:11],v248 offset:0\n" \
R_"v[12:15],v248 offset:1024\n" \
R_"v[24:27],v248 offset:2048\n" \
R_"v[28:31],v248 offset:3072\n" \
R_"v[40:43],v248 offset:4096\n" \
R_"v[44:47],v248 offset:5120\n" \
R_"v[56:59],v248 offset:6144\n" \
R_"v[60:63],v248 offset:7168\n" \
R_"v[72:75],v248 offset:8192\n" \
R_"v[76:79],v248 offset:9216\n" \
R_"v[88:91],v248 offset:10240\n" \
R_"v[92:95],v248 offset:11264\n" \
R_"v[104:107],v248 offset:12288\n" \
R_"v[108:111],v248 offset:13312\n" \
S_"lgkmcnt(0)\n" \
"s_barrier\n" \
"s_branch LATT_%=\n" \
"; ---- q1r1\n" \
"LROLEB_%=:\n" \
A_"s65,s65,128\n" \
"s_cmp_eq_u32 s65,1152\n" \
"s_cselect_b32 s65,0,s65\n" \
A_"m0,s73,40960\n" \
"s_nop 0\n" \
L_"v241,s[76:79],s65 offen offset:1024 lds\n" \
L_"v242,s[76:79],s65 offen offset:2048 lds\n" \
L_"v249,s[76:79],s65 offen offset:3072 lds\n" \
A_"s64,s64,262144\n" \
"s_cmp_eq_u32 s64,4718592\n" \
"s_cselect_b32 s64,0,s64\n" \
A_"m0,s72,8192\n" \
"s_nop 0\n" \
L_"v250,s[36:39],s64 offen lds\n" \
L_"v251,s[36:39],s64 offen offset:1024 lds\n" \
L_"v252,s[36:39],s64 offen offset:2048 lds\n" \
L_"v253,s[36:39],s64 offen offset:3072 lds\n" \
S_"lgkmcnt(6)\n" \
M_"v[112:115],a[0:3],a[32:35],0\n" \
M_"v[116:119],a[4:7],a[32:35],0\n" \
M_"v[120:123],a[8:11],a[32:35],0\n" \
M_"v[124:127],a[12:15],a[32:35],0\n" \
S_"vmcnt(7)\n" \
R_"a[32:35],v237 offset:0\n" \
S_"lgkmcnt(6)\n" \
M_"v[128:131],a[0:3],a[36:39],0\n" \
T_"a[16:17],v238 offset:4096\n" \
A_"s64,s64,262144\n" \
"s_cmp_eq_u32 s64,4718592\n" \
"s_cselect_b32 s64,0,s64\n" \
A_"m0,s72,0\n" \
M_"v[132:135],a[4:7],a[36:39],0\n" \
T_"a[18:19],v238 offset:4608\n" \
L_"v250,s[36:39],s64 offen lds\n" \
M_"v[136:139],a[8:11],a[36:39],0\n" \
T_"a[20:21],v238 offset:4104\n" \
L_"v251,s[36:39],s64 offen offset:1024 lds\n" \
M_"v[140:143],a[12:15],a[36:39],0\n" \
R_"a[36:39],v237 offset:2048\n" \
S_"lgkmcnt(5)\n" \
M_"v[144:147],a[0:3],a[40:43],0\n" \
T_"a[22:23],v238 offset:4616\n" \
M_"v[148:151],a[4:7],a[40:43],0\n" \
T_"a[24:25],v239 offset:4096\n" \
L_"v252,s[36:39],s64 offen offset:2048 lds\n" \
M_"v[152:155],a[8:11],a[40:43],0\n" \
T_"a[26:27],v239 offset:4608\n" \
L_"v253,s[36:39],s64 offen offset:3072 lds\n" \
M_"v[156:159],a[12:15],a[40:43],0\n" \
R_"a[40:43],v237 offset:4096\n" \
M_"v[160:163],a[0:3],a[44:47],0\n" \
T_"a[28:29],v239 offset:4104\n" \
A_"s67,s66,4096\n" \
L_"a[96:99],v228,s[40:43],s66 offen " KVP "\n" \
M_"v[164:167],a[4:7],a[44:47],0\n" \
T_"a[30:31],v239 offset:4616\n" \
M_"v[168:171],a[8:11],a[44:47],0\n" \
L_"a[100:103],v228,s[40:43],s66 offen offset:1024 " KVP "\n" \
M_"v[172:175],a[12:15],a[44:47],0\n" \
R_"a[44:47],v237 offset:6144\n" \
M_"v[176:179],a[0:3],a[48:51],0\n" \
L_"a[104:107],v228,s[40:43],s66 offen offset:2048 " KVP "\n" \
M_"v[180:183],a[4:7],a[48:51],0\n" \
M_"v[184:187],a[8:11],a[48:51],0\n" \
L_"a[108:111],v228,s[40:43],s66 offen offset:3072 " KVP "\n" \
M_"v[188:191],a[12:15],a[48:51],0\n" \
R_"a[48:51],v237 offset:8192\n" \
M_"v[192:195],a[0:3],a[52:55],0\n" \
L_"a[112:115],v228,s[40:43],s67 offen " KVP "\n" \
M_"v[196:199],a[4:7],a[52:55],0\n" \
L_"a[116:119],v228,s[40:43],s67 offen offset:1024 " KVP "\n" \
M_"v[200:203],a[8:11],a[52:55],0\n" \
M_"v[204:207],a[12:15],a[52:55],0\n" \
R_"a[52:55],v237 offset:10240\n" \
M_"v[208:211],a[0:3],a[56:59],0\n" \
L_"a[120:123],v228,s[40:43],s67 offen offset:2048 " KVP "\n" \
M_"v[212:215],a[4:7],a[56:59],0\n" \
L_"a[124:127],v228,s[40:43],s67 offen offset:3072 " KVP "\n" \
M_"v[216:219],a[8:11],a[56:59],0\n" \
M_"v[220:223],a[12:15],a[56:59],0\n" \
R_"a[56:59],v237 offset:12288\n" \
S_"lgkmcnt(4)\n" \
M_"v[112:115],a[16:19],a[32:35],v[112:115]\n" \
M_"v[116:119],a[20:23],a[32:35],v[116:119]\n" \
M_"v[120:123],a[24:27],a[32:35],v[120:123]\n" \
M_"v[124:127],a[28:31],a[32:35],v[124:127]\n" \
S_"vmcnt(12) lgkmcnt(0)\n" \
"s_barrier\n" \
R_"a[32:35],v236 offset:40960\n" \
M_"v[128:131],a[16:19],a[36:39],v[128:131]\n" \
T_"a[0:1],v238 offset:8192\n" \
A_"s64,s64,262144\n" \
"s_cmp_eq_u32 s64,4718592\n" \
"s_cselect_b32 s64,0,s64\n" \
A_"m0,s72,4096\n" \
M_"v[132:135],a[20:23],a[36:39],v[132:135]\n" \
T_"a[2:3],v238 offset:8704\n" \
M_"v[136:139],a[24:27],a[36:39],v[136:139]\n" \
T_"a[4:5],v238 offset:8200\n" \
L_"v250,s[36:39],s64 offen lds\n" \
M_"v[140:143],a[28:31],a[36:39],v[140:143]\n" \
R_"a[36:39],v236 offset:43008\n" \
M_"v[144:147],a[16:19],a[40:43],v[144:147]\n" \
T_"a[6:7],v238 offset:8712\n" \
M_"v[148:151],a[20:23],a[40:43],v[148:151]\n" \
T_"a[8:9],v239 offset:8192\n" \
L_"v251,s[36:39],s64 offen offset:1024 lds\n" \
M_"v[152:155],a[24:27],a[40:43],v[152:155]\n" \
T_"a[10:11],v239 offset:8704\n" \
M_"v[156:159],a[28:31],a[40:43],v[156:159]\n" \
R_"a[40:43],v236 offset:45056\n" \
M_"v[160:163],a[16:19],a[44:47],v[160:163]\n" \
T_"a[12:13],v239 offset:8200\n" \
L_"v252,s[36:39],s64 offen offset:2048 lds\n" \
M_"v[164:167],a[20:23],a[44:47],v[164:167]\n" \
T_"a[14:15],v239 offset:8712\n" \
M_"v[168:171],a[24:27],a[44:47],v[168:171]\n" \
L_"v253,s[36:39],s64 offen offset:3072 lds\n" \
M_"v[172:175],a[28:31],a[44:47],v[172:175]\n" \
R_"a[44:47],v236 offset:47104\n" \
M_"v[176:179],a[16:19],a[48:51],v[176:179]\n" \
M_"v[180:183],a[20:23],a[48:51],v[180:183]\n" \
A_"s65,s65,128\n" \
"s_cmp_eq_u32 s65,1152\n" \
"s_cselect_b32 s65,0,s65\n" \
A_"m0,s73,0\n" \
M_"v[184:187],a[24:27],a[48:51],v[184:187]\n" \
M_"v[188:191],a[28:31],a[48:51],v[188:191]\n" \
R_"a[48:51],v236 offset:49152\n" \
M_"v[192:195],a[16:19],a[52:55],v[192:195]\n" \
L_"v241,s[76:79],s65 offen offset:1024 lds\n" \
M_"v[196:199],a[20:23],a[52:55],v[196:199]\n" \
M_"v[200:203],a[24:27],a[52:55],v[200:203]\n" \
L_"v242,s[76:79],s65 offen offset:2048 lds\n" \
M_"v[204:207],a[28:31],a[52:55],v[204:207]\n" \
R_"a[52:55],v236 offset:51200\n" \
M_"v[208:211],a[16:19],a[56:59],v[208:211]\n" \
M_"v[212:215],a[20:23],a[56:59],v[212:215]\n" \
L_"v249,s[76:79],s65 offen offset:3072 lds\n" \
M_"v[216:219],a[24:27],a[56:59],v[216:219]\n" \
M_"v[220:223],a[28:31],a[56:59],v[220:223]\n" \
R_"a[56:59],v236 offset:53248\n" \
S_"lgkmcnt(4)\n" \
M_"v[112:115],a[0:3],a[32:35],v[112:115]\n" \
M_"v[116:119],a[4:7],a[32:35],v[116:119]\n" \
M_"v[120:123],a[8:11],a[32:35],v[120:123]\n" \
M_"v[124:127],a[12:15],a[32:35],v[124:127]\n" \
S_"vmcnt(15)\n" \
R_"a[32:35],v237 offset:40960\n" \
M_"v[128:131],a[0:3],a[36:39],v[128:131]\n" \
T_"a[16:17],v238 offset:0\n" \
A_"s64,s64,262144\n" \
"s_cmp_eq_u32 s64,4718592\n" \
"s_cselect_b32 s64,0,s64\n" \
A_"m0,s72,8192\n" \
M_"v[132:135],a[4:7],a[36:39],v[132:135]\n" \
T_"a[18:19],v238 offset:512\n" \
M_"v[136:139],a[8:11],a[36:39],v[136:139]\n" \
T_"a[20:21],v238 offset:8\n" \
M_"v[140:143],a[12:15],a[36:39],v[140:143]\n" \
R_"a[36:39],v237 offset:43008\n" \
M_"v[144:147],a[0:3],a[40:43],v[144:147]\n" \
T_"a[22:23],v238 offset:520\n" \
L_"v250,s[36:39],s64 offen lds\n" \
M_"v[148:151],a[4:7],a[40:43],v[148:151]\n" \
T_"a[24:25],v239 offset:0\n" \
M_"v[152:155],a[8:11],a[40:43],v[152:155]\n" \
T_"a[26:27],v239 offset:512\n" \
M_"v[156:159],a[12:15],a[40:43],v[156:159]\n" \
R_"a[40:43],v237 offset:45056\n" \
S_"lgkmcnt(6)\n" \
M_"v[160:163],a[0:3],a[44:47],v[160:163]\n" \
T_"a[28:29],v239 offset:8\n" \
M_"v[164:167],a[4:7],a[44:47],v[164:167]\n" \
T_"a[30:31],v239 offset:520\n" \
L_"v251,s[36:39],s64 offen offset:1024 lds\n" \
M_"v[168:171],a[8:11],a[44:47],v[168:171]\n" \
M_"v[172:175],a[12:15],a[44:47],v[172:175]\n" \
R_"a[44:47],v237 offset:47104\n" \
M_"v[176:179],a[0:3],a[48:51],v[176:179]\n" \
M_"v[180:183],a[4:7],a[48:51],v[180:183]\n" \
L_"v252,s[36:39],s64 offen offset:2048 lds\n" \
M_"v[184:187],a[8:11],a[48:51],v[184:187]\n" \
M_"v[188:191],a[12:15],a[48:51],v[188:191]\n" \
R_"a[48:51],v237 offset:49152\n" \
M_"v[192:195],a[0:3],a[52:55],v[192:195]\n" \
M_"v[196:199],a[4:7],a[52:55],v[196:199]\n" \
M_"v[200:203],a[8:11],a[52:55],v[200:203]\n" \
L_"v253,s[36:39],s64 offen offset:3072 lds\n" \
M_"v[204:207],a[12:15],a[52:55],v[204:207]\n" \
R_"a[52:55],v237 offset:51200\n" \
M_"v[208:211],a[0:3],a[56:59],v[208:211]\n" \
M_"v[212:215],a[4:7],a[56:59],v[212:215]\n" \
M_"v[216:219],a[8:11],a[56:59],v[216:219]\n" \
M_"v[220:223],a[12:15],a[56:59],v[220:223]\n" \
R_"a[56:59],v237 offset:53248\n" \
S_"lgkmcnt(4)\n" \
M_"v[112:115],a[16:19],a[32:35],v[112:115]\n" \
M_"v[116:119],a[20:23],a[32:35],v[116:119]\n" \
M_"v[120:123],a[24:27],a[32:35],v[120:123]\n" \
M_"v[124:127],a[28:31],a[32:35],v[124:127]\n" \
S_"vmcnt(4) lgkmcnt(0)\n" \
"s_barrier\n" \
R_"a[32:35],v236 offset:0\n" \
M_"v[128:131],a[16:19],a[36:39],v[128:131]\n" \
T_"a[0:1],v238 offset:4096\n" \
A_"s64,s64,262144\n" \
"s_cmp_eq_u32 s64,4718592\n" \
"s_cselect_b32 s64,0,s64\n" \
A_"m0,s72,0\n" \
M_"v[132:135],a[20:23],a[36:39],v[132:135]\n" \
T_"a[2:3],v238 offset:4608\n" \
L_"v250,s[36:39],s64 offen lds\n" \
M_"v[136:139],a[24:27],a[36:39],v[136:139]\n" \
T_"a[4:5],v238 offset:4104\n" \
L_"v251,s[36:39],s64 offen offset:1024 lds\n" \
M_"v[140:143],a[28:31],a[36:39],v[140:143]\n" \
R_"a[36:39],v236 offset:2048\n" \
M_"v[144:147],a[16:19],a[40:43],v[144:147]\n" \
T_"a[6:7],v238 offset:4616\n" \
L_"v252,s[36:39],s64 offen offset:2048 lds\n" \
M_"v[148:151],a[20:23],a[40:43],v[148:151]\n" \
T_"a[8:9],v239 offset:4096\n" \
L_"v253,s[36:39],s64 offen offset:3072 lds\n" \
M_"v[152:155],a[24:27],a[40:43],v[152:155]\n" \
T_"a[10:11],v239 offset:4608\n" \
A_"s65,s65,128\n" \
"s_cmp_eq_u32 s65,1152\n" \
"s_cselect_b32 s65,0,s65\n" \
A_"m0,s73,40960\n" \
M_"v[156:159],a[28:31],a[40:43],v[156:159]\n" \
R_"a[40:43],v236 offset:4096\n" \
M_"v[160:163],a[16:19],a[44:47],v[160:163]\n" \
T_"a[12:13],v239 offset:4104\n" \
L_"v241,s[76:79],s65 offen offset:1024 lds\n" \
M_"v[164:167],a[20:23],a[44:47],v[164:167]\n" \
T_"a[14:15],v239 offset:4616\n" \
L_"v242,s[76:79],s65 offen offset:2048 lds\n" \
M_"v[168:171],a[24:27],a[44:47],v[168:171]\n" \
L_"v249,s[76:79],s65 offen offset:3072 lds\n" \
M_"v[172:175],a[28:31],a[44:47],v[172:175]\n" \
R_"a[44:47],v236 offset:6144\n" \
M_"v[176:179],a[16:19],a[48:51],v[176:179]\n" \
L_"a[128:131],v228,s[44:47],s66 offen " KVP "\n" \
M_"v[180:183],a[20:23],a[48:51],v[180:183]\n" \
L_"a[132:135],v228,s[44:47],s66 offen offset:1024 " KVP "\n" \
M_"v[184:187],a[24:27],a[48:51],v[184:187]\n" \
L_"a[136:139],v228,s[44:47],s66 offen offset:2048 " KVP "\n" \
M_"v[188:191],a[28:31],a[48:51],v[188:191]\n" \
R_"a[48:51],v236 offset:8192\n" \
M_"v[192:195],a[16:19],a[52:55],v[192:195]\n" \
L_"a[140:143],v228,s[44:47],s66 offen offset:3072 " KVP "\n" \
M_"v[196:199],a[20:23],a[52:55],v[196:199]\n" \
L_"a[144:147],v228,s[44:47],s67 offen " KVP "\n" \
M_"v[200:203],a[24:27],a[52:55],v[200:203]\n" \
L_"a[148:151],v228,s[44:47],s67 offen offset:1024 " KVP "\n" \
M_"v[204:207],a[28:31],a[52:55],v[204:207]\n" \
R_"a[52:55],v236 offset:10240\n" \
M_"v[208:211],a[16:19],a[56:59],v[208:211]\n" \
L_"a[152:155],v228,s[44:47],s67 offen offset:2048 " KVP "\n" \
M_"v[212:215],a[20:23],a[56:59],v[212:215]\n" \
L_"a[156:159],v228,s[44:47],s67 offen offset:3072 " KVP "\n" \
M_"v[216:219],a[24:27],a[56:59],v[216:219]\n" \
M_"v[220:223],a[28:31],a[56:59],v[220:223]\n" \
R_"a[56:59],v236 offset:12288\n" \
S_"lgkmcnt(4)\n" \
M_"v[112:115],a[0:3],a[32:35],v[112:115]\n" \
M_"v[116:119],a[4:7],a[32:35],v[116:119]\n" \
M_"v[120:123],a[8:11],a[32:35],v[120:123]\n" \
M_"v[124:127],a[12:15],a[32:35],v[124:127]\n" \
S_"vmcnt(15)\n" \
R_"a[32:35],v237 offset:0\n" \
M_"v[128:131],a[0:3],a[36:39],v[128:131]\n" \
T_"a[16:17],v238 offset:8192\n" \
A_"s64,s64,262144\n" \
"s_cmp_eq_u32 s64,4718592\n" \
"s_cselect_b32 s64,0,s64\n" \
A_"m0,s72,4096\n" \
M_"v[132:135],a[4:7],a[36:39],v[132:135]\n" \
T_"a[18:19],v238 offset:8704\n" \
M_"v[136:139],a[8:11],a[36:39],v[136:139]\n" \
T_"a[20:21],v238 offset:8200\n" \
M_"v[140:143],a[12:15],a[36:39],v[140:143]\n" \
R_"a[36:39],v237 offset:2048\n" \
M_"v[144:147],a[0:3],a[40:43],v[144:147]\n" \
T_"a[22:23],v238 offset:8712\n" \
L_"v250,s[36:39],s64 offen lds\n" \
M_"v[148:151],a[4:7],a[40:43],v[148:151]\n" \
T_"a[24:25],v239 offset:8192\n" \
M_"v[152:155],a[8:11],a[40:43],v[152:155]\n" \
T_"a[26:27],v239 offset:8704\n" \
M_"v[156:159],a[12:15],a[40:43],v[156:159]\n" \
R_"a[40:43],v237 offset:4096\n" \
S_"lgkmcnt(6)\n" \
M_"v[160:163],a[0:3],a[44:47],v[160:163]\n" \
T_"a[28:29],v239 offset:8200\n" \
M_"v[164:167],a[4:7],a[44:47],v[164:167]\n" \
T_"a[30:31],v239 offset:8712\n" \
L_"v251,s[36:39],s64 offen offset:1024 lds\n" \
M_"v[168:171],a[8:11],a[44:47],v[168:171]\n" \
M_"v[172:175],a[12:15],a[44:47],v[172:175]\n" \
R_"a[44:47],v237 offset:6144\n" \
M_"v[176:179],a[0:3],a[48:51],v[176:179]\n" \
M_"v[180:183],a[4:7],a[48:51],v[180:183]\n" \
L_"v252,s[36:39],s64 offen offset:2048 lds\n" \
M_"v[184:187],a[8:11],a[48:51],v[184:187]\n" \
M_"v[188:191],a[12:15],a[48:51],v[188:191]\n" \
R_"a[48:51],v237 offset:8192\n" \
M_"v[192:195],a[0:3],a[52:55],v[192:195]\n" \
M_"v[196:199],a[4:7],a[52:55],v[196:199]\n" \
M_"v[200:203],a[8:11],a[52:55],v[200:203]\n" \
L_"v253,s[36:39],s64 offen offset:3072 lds\n" \
M_"v[204:207],a[12:15],a[52:55],v[204:207]\n" \
R_"a[52:55],v237 offset:10240\n" \
M_"v[208:211],a[0:3],a[56:59],v[208:211]\n" \
M_"v[212:215],a[4:7],a[56:59],v[212:215]\n" \
M_"v[216:219],a[8:11],a[56:59],v[216:219]\n" \
M_"v[220:223],a[12:15],a[56:59],v[220:223]\n" \
R_"a[56:59],v237 offset:12288\n" \
S_"lgkmcnt(4)\n" \
M_"v[112:115],a[16:19],a[32:35],v[112:115]\n" \
M_"v[116:119],a[20:23],a[32:35],v[116:119]\n" \
M_"v[120:123],a[24:27],a[32:35],v[120:123]\n" \
M_"v[124:127],a[28:31],a[32:35],v[124:127]\n" \
S_"vmcnt(12) lgkmcnt(0)\n" \
"s_barrier\n" \
R_"a[32:35],v236 offset:40960\n" \
M_"v[128:131],a[16:19],a[36:39],v[128:131]\n" \
T_"a[0:1],v238 offset:0\n" \
A_"s64,s64,262144\n" \
"s_cmp_eq_u32 s64,4718592\n" \
"s_cselect_b32 s64,0,s64\n" \
A_"m0,s72,8192\n" \
M_"v[132:135],a[20:23],a[36:39],v[132:135]\n" \
T_"a[2:3],v238 offset:512\n" \
M_"v[136:139],a[24:27],a[36:39],v[136:139]\n" \
T_"a[4:5],v238 offset:8\n" \
L_"v250,s[36:39],s64 offen lds\n" \
M_"v[140:143],a[28:31],a[36:39],v[140:143]\n" \
R_"a[36:39],v236 offset:43008\n" \
M_"v[144:147],a[16:19],a[40:43],v[144:147]\n" \
T_"a[6:7],v238 offset:520\n" \
M_"v[148:151],a[20:23],a[40:43],v[148:151]\n" \
T_"a[8:9],v239 offset:0\n" \
L_"v251,s[36:39],s64 offen offset:1024 lds\n" \
M_"v[152:155],a[24:27],a[40:43],v[152:155]\n" \
T_"a[10:11],v239 offset:512\n" \
M_"v[156:159],a[28:31],a[40:43],v[156:159]\n" \
R_"a[40:43],v236 offset:45056\n" \
M_"v[160:163],a[16:19],a[44:47],v[160:163]\n" \
T_"a[12:13],v239 offset:8\n" \
L_"v252,s[36:39],s64 offen offset:2048 lds\n" \
M_"v[164:167],a[20:23],a[44:47],v[164:167]\n" \
T_"a[14:15],v239 offset:520\n" \
M_"v[168:171],a[24:27],a[44:47],v[168:171]\n" \
L_"v253,s[36:39],s64 offen offset:3072 lds\n" \
M_"v[172:175],a[28:31],a[44:47],v[172:175]\n" \
R_"a[44:47],v236 offset:47104\n" \
M_"v[176:179],a[16:19],a[48:51],v[176:179]\n" \
M_"v[180:183],a[20:23],a[48:51],v[180:183]\n" \
A_"s65,s65,128\n" \
"s_cmp_eq_u32 s65,1152\n" \
"s_cselect_b32 s65,0,s65\n" \
A_"m0,s73,0\n" \
M_"v[184:187],a[24:27],a[48:51],v[184:187]\n" \
M_"v[188:191],a[28:31],a[48:51],v[188:191]\n" \
R_"a[48:51],v236 offset:49152\n" \
M_"v[192:195],a[16:19],a[52:55],v[192:195]\n" \
L_"v241,s[76:79],s65 offen offset:1024 lds\n" \
M_"v[196:199],a[20:23],a[52:55],v[196:199]\n" \
M_"v[200:203],a[24:27],a[52:55],v[200:203]\n" \
L_"v242,s[76:79],s65 offen offset:2048 lds\n" \
M_"v[204:207],a[28:31],a[52:55],v[204:207]\n" \
R_"a[52:55],v236 offset:51200\n" \
M_"v[208:211],a[16:19],a[56:59],v[208:211]\n" \
M_"v[212:215],a[20:23],a[56:59],v[212:215]\n" \
L_"v249,s[76:79],s65 offen offset:3072 lds\n" \
M_"v[216:219],a[24:27],a[56:59],v[216:219]\n" \
M_"v[220:223],a[28:31],a[56:59],v[220:223]\n" \
R_"a[56:59],v236 offset:53248\n" \
S_"lgkmcnt(4)\n" \
M_"v[112:115],a[0:3],a[32:35],v[112:115]\n" \
M_"v[116:119],a[4:7],a[32:35],v[116:119]\n" \
M_"v[120:123],a[8:11],a[32:35],v[120:123]\n" \
M_"v[124:127],a[12:15],a[32:35],v[124:127]\n" \
S_"vmcnt(7)\n" \
R_"a[32:35],v237 offset:40960\n" \
M_"v[128:131],a[0:3],a[36:39],v[128:131]\n" \
T_"a[16:17],v238 offset:4096\n" \
A_"s64,s64,262144\n" \
"s_cmp_eq_u32 s64,4718592\n" \
"s_cselect_b32 s64,0,s64\n" \
A_"m0,s72,0\n" \
M_"v[132:135],a[4:7],a[36:39],v[132:135]\n" \
T_"a[18:19],v238 offset:4608\n" \
M_"v[136:139],a[8:11],a[36:39],v[136:139]\n" \
T_"a[20:21],v238 offset:4104\n" \
M_"v[140:143],a[12:15],a[36:39],v[140:143]\n" \
R_"a[36:39],v237 offset:43008\n" \
M_"v[144:147],a[0:3],a[40:43],v[144:147]\n" \
T_"a[22:23],v238 offset:4616\n" \
L_"v250,s[36:39],s64 offen lds\n" \
M_"v[148:151],a[4:7],a[40:43],v[148:151]\n" \
T_"a[24:25],v239 offset:4096\n" \
M_"v[152:155],a[8:11],a[40:43],v[152:155]\n" \
T_"a[26:27],v239 offset:4608\n" \
M_"v[156:159],a[12:15],a[40:43],v[156:159]\n" \
R_"a[40:43],v237 offset:45056\n" \
S_"lgkmcnt(6)\n" \
M_"v[160:163],a[0:3],a[44:47],v[160:163]\n" \
T_"a[28:29],v239 offset:4104\n" \
M_"v[164:167],a[4:7],a[44:47],v[164:167]\n" \
T_"a[30:31],v239 offset:4616\n" \
L_"v251,s[36:39],s64 offen offset:1024 lds\n" \
M_"v[168:171],a[8:11],a[44:47],v[168:171]\n" \
M_"v[172:175],a[12:15],a[44:47],v[172:175]\n" \
R_"a[44:47],v237 offset:47104\n" \
M_"v[176:179],a[0:3],a[48:51],v[176:179]\n" \
M_"v[180:183],a[4:7],a[48:51],v[180:183]\n" \
L_"v252,s[36:39],s64 offen offset:2048 lds\n" \
M_"v[184:187],a[8:11],a[48:51],v[184:187]\n" \
M_"v[188:191],a[12:15],a[48:51],v[188:191]\n" \
R_"a[48:51],v237 offset:49152\n" \
M_"v[192:195],a[0:3],a[52:55],v[192:195]\n" \
M_"v[196:199],a[4:7],a[52:55],v[196:199]\n" \
M_"v[200:203],a[8:11],a[52:55],v[200:203]\n" \
L_"v253,s[36:39],s64 offen offset:3072 lds\n" \
M_"v[204:207],a[12:15],a[52:55],v[204:207]\n" \
R_"a[52:55],v237 offset:51200\n" \
M_"v[208:211],a[0:3],a[56:59],v[208:211]\n" \
M_"v[212:215],a[4:7],a[56:59],v[212:215]\n" \
M_"v[216:219],a[8:11],a[56:59],v[216:219]\n" \
M_"v[220:223],a[12:15],a[56:59],v[220:223]\n" \
R_"a[56:59],v237 offset:53248\n" \
S_"lgkmcnt(4)\n" \
M_"v[112:115],a[16:19],a[32:35],v[112:115]\n" \
M_"v[116:119],a[20:23],a[32:35],v[116:119]\n" \
M_"v[120:123],a[24:27],a[32:35],v[120:123]\n" \
M_"v[124:127],a[28:31],a[32:35],v[124:127]\n" \
S_"vmcnt(4) lgkmcnt(0)\n" \
"s_barrier\n" \
R_"a[32:35],v236 offset:0\n" \
M_"v[128:131],a[16:19],a[36:39],v[128:131]\n" \
T_"a[0:1],v238 offset:8192\n" \
A_"s64,s64,262144\n" \
"s_cmp_eq_u32 s64,4718592\n" \
"s_cselect_b32 s64,0,s64\n" \
A_"m0,s72,4096\n" \
M_"v[132:135],a[20:23],a[36:39],v[132:135]\n" \
T_"a[2:3],v238 offset:8704\n" \
M_"v[136:139],a[24:27],a[36:39],v[136:139]\n" \
T_"a[4:5],v238 offset:8200\n" \
L_"v250,s[36:39],s64 offen lds\n" \
M_"v[140:143],a[28:31],a[36:39],v[140:143]\n" \
R_"a[36:39],v236 offset:2048\n" \
M_"v[144:147],a[16:19],a[40:43],v[144:147]\n" \
T_"a[6:7],v238 offset:8712\n" \
M_"v[148:151],a[20:23],a[40:43],v[148:151]\n" \
T_"a[8:9],v239 offset:8192\n" \
L_"v251,s[36:39],s64 offen offset:1024 lds\n" \
M_"v[152:155],a[24:27],a[40:43],v[152:155]\n" \
T_"a[10:11],v239 offset:8704\n" \
M_"v[156:159],a[28:31],a[40:43],v[156:159]\n" \
R_"a[40:43],v236 offset:4096\n" \
M_"v[160:163],a[16:19],a[44:47],v[160:163]\n" \
T_"a[12:13],v239 offset:8200\n" \
L_"v252,s[36:39],s64 offen offset:2048 lds\n" \
M_"v[164:167],a[20:23],a[44:47],v[164:167]\n" \
T_"a[14:15],v239 offset:8712\n" \
M_"v[168:171],a[24:27],a[44:47],v[168:171]\n" \
L_"v253,s[36:39],s64 offen offset:3072 lds\n" \
M_"v[172:175],a[28:31],a[44:47],v[172:175]\n" \
R_"a[44:47],v236 offset:6144\n" \
M_"v[176:179],a[16:19],a[48:51],v[176:179]\n" \
M_"v[180:183],a[20:23],a[48:51],v[180:183]\n" \
A_"s65,s65,128\n" \
"s_cmp_eq_u32 s65,1152\n" \
"s_cselect_b32 s65,0,s65\n" \
A_"m0,s73,40960\n" \
M_"v[184:187],a[24:27],a[48:51],v[184:187]\n" \
M_"v[188:191],a[28:31],a[48:51],v[188:191]\n" \
R_"a[48:51],v236 offset:8192\n" \
M_"v[192:195],a[16:19],a[52:55],v[192:195]\n" \
L_"v241,s[76:79],s65 offen offset:1024 lds\n" \
M_"v[196:199],a[20:23],a[52:55],v[196:199]\n" \
M_"v[200:203],a[24:27],a[52:55],v[200:203]\n" \
L_"v242,s[76:79],s65 offen offset:2048 lds\n" \
M_"v[204:207],a[28:31],a[52:55],v[204:207]\n" \
R_"a[52:55],v236 offset:10240\n" \
M_"v[208:211],a[16:19],a[56:59],v[208:211]\n" \
M_"v[212:215],a[20:23],a[56:59],v[212:215]\n" \
L_"v249,s[76:79],s65 offen offset:3072 lds\n" \
M_"v[216:219],a[24:27],a[56:59],v[216:219]\n" \
M_"v[220:223],a[28:31],a[56:59],v[220:223]\n" \
R_"a[56:59],v236 offset:12288\n" \
S_"lgkmcnt(4)\n" \
M_"v[112:115],a[0:3],a[32:35],v[112:115]\n" \
M_"v[116:119],a[4:7],a[32:35],v[116:119]\n" \
M_"v[120:123],a[8:11],a[32:35],v[120:123]\n" \
M_"v[124:127],a[12:15],a[32:35],v[124:127]\n" \
S_"vmcnt(7)\n" \
R_"a[32:35],v237 offset:0\n" \
M_"v[128:131],a[0:3],a[36:39],v[128:131]\n" \
T_"a[16:17],v238 offset:0\n" \
A_"s64,s64,262144\n" \
"s_cmp_eq_u32 s64,4718592\n" \
"s_cselect_b32 s64,0,s64\n" \
A_"m0,s72,8192\n" \
M_"v[132:135],a[4:7],a[36:39],v[132:135]\n" \
T_"a[18:19],v238 offset:512\n" \
L_"v250,s[36:39],s64 offen lds\n" \
M_"v[136:139],a[8:11],a[36:39],v[136:139]\n" \
T_"a[20:21],v238 offset:8\n" \
L_"v251,s[36:39],s64 offen offset:1024 lds\n" \
M_"v[140:143],a[12:15],a[36:39],v[140:143]\n" \
R_"a[36:39],v237 offset:2048\n" \
M_"v[144:147],a[0:3],a[40:43],v[144:147]\n" \
T_"a[22:23],v238 offset:520\n" \
M_"v[148:151],a[4:7],a[40:43],v[148:151]\n" \
T_"a[24:25],v239 offset:0\n" \
L_"v252,s[36:39],s64 offen offset:2048 lds\n" \
M_"v[152:155],a[8:11],a[40:43],v[152:155]\n" \
T_"a[26:27],v239 offset:512\n" \
L_"v253,s[36:39],s64 offen offset:3072 lds\n" \
M_"v[156:159],a[12:15],a[40:43],v[156:159]\n" \
R_"a[40:43],v237 offset:4096\n" \
S_"lgkmcnt(6)\n" \
M_"v[160:163],a[0:3],a[44:47],v[160:163]\n" \
T_"a[28:29],v239 offset:8\n" \
A_"s66,s66,8192\n" \
"s_and_b32 s66,s66,131071\n" \
A_"s67,s66,4096\n" \
L_"a[160:163],v228,s[40:43],s66 offen " KVP "\n" \
M_"v[164:167],a[4:7],a[44:47],v[164:167]\n" \
T_"a[30:31],v239 offset:520\n" \
M_"v[168:171],a[8:11],a[44:47],v[168:171]\n" \
L_"a[164:167],v228,s[40:43],s66 offen offset:1024 " KVP "\n" \
M_"v[172:175],a[12:15],a[44:47],v[172:175]\n" \
R_"a[44:47],v237 offset:6144\n" \
M_"v[176:179],a[0:3],a[48:51],v[176:179]\n" \
L_"a[168:171],v228,s[40:43],s66 offen offset:2048 " KVP "\n" \
M_"v[180:183],a[4:7],a[48:51],v[180:183]\n" \
M_"v[184:187],a[8:11],a[48:51],v[184:187]\n" \
L_"a[172:175],v228,s[40:43],s66 offen offset:3072 " KVP "\n" \
M_"v[188:191],a[12:15],a[48:51],v[188:191]\n" \
R_"a[48:51],v237 offset:8192\n" \
M_"v[192:195],a[0:3],a[52:55],v[192:195]\n" \
L_"a[176:179],v228,s[40:43],s67 offen " KVP "\n" \
M_"v[196:199],a[4:7],a[52:55],v[196:199]\n" \
L_"a[180:183],v228,s[40:43],s67 offen offset:1024 " KVP "\n" \
M_"v[200:203],a[8:11],a[52:55],v[200:203]\n" \
M_"v[204:207],a[12:15],a[52:55],v[204:207]\n" \
R_"a[52:55],v237 offset:10240\n" \
M_"v[208:211],a[0:3],a[56:59],v[208:211]\n" \
L_"a[184:187],v228,s[40:43],s67 offen offset:2048 " KVP "\n" \
M_"v[212:215],a[4:7],a[56:59],v[212:215]\n" \
L_"a[188:191],v228,s[40:43],s67 offen offset:3072 " KVP "\n" \
M_"v[216:219],a[8:11],a[56:59],v[216:219]\n" \
M_"v[220:223],a[12:15],a[56:59],v[220:223]\n" \
R_"a[56:59],v237 offset:12288\n" \
S_"lgkmcnt(4)\n" \
M_"v[112:115],a[16:19],a[32:35],v[112:115]\n" \
M_"v[116:119],a[20:23],a[32:35],v[116:119]\n" \
M_"v[120:123],a[24:27],a[32:35],v[120:123]\n" \
M_"v[124:127],a[28:31],a[32:35],v[124:127]\n" \
S_"vmcnt(12) lgkmcnt(0)\n" \
"s_barrier\n" \
R_"a[32:35],v236 offset:40960\n" \
M_"v[128:131],a[16:19],a[36:39],v[128:131]\n" \
T_"a[0:1],v238 offset:4096\n" \
A_"s64,s64,262144\n" \
"s_cmp_eq_u32 s64,4718592\n" \
"s_cselect_b32 s64,0,s64\n" \
A_"m0,s72,0\n" \
M_"v[132:135],a[20:23],a[36:39],v[132:135]\n" \
T_"a[2:3],v238 offset:4608\n" \
M_"v[136:139],a[24:27],a[36:39],v[136:139]\n" \
T_"a[4:5],v238 offset:4104\n" \
L_"v250,s[36:39],s64 offen lds\n" \
M_"v[140:143],a[28:31],a[36:39],v[140:143]\n" \
R_"a[36:39],v236 offset:43008\n" \
M_"v[144:147],a[16:19],a[40:43],v[144:147]\n" \
T_"a[6:7],v238 offset:4616\n" \
M_"v[148:151],a[20:23],a[40:43],v[148:151]\n" \
T_"a[8:9],v239 offset:4096\n" \
L_"v251,s[36:39],s64 offen offset:1024 lds\n" \
M_"v[152:155],a[24:27],a[40:43],v[152:155]\n" \
T_"a[10:11],v239 offset:4608\n" \
M_"v[156:159],a[28:31],a[40:43],v[156:159]\n" \
R_"a[40:43],v236 offset:45056\n" \
M_"v[160:163],a[16:19],a[44:47],v[160:163]\n" \
T_"a[12:13],v239 offset:4104\n" \
L_"v252,s[36:39],s64 offen offset:2048 lds\n" \
M_"v[164:167],a[20:23],a[44:47],v[164:167]\n" \
T_"a[14:15],v239 offset:4616\n" \
M_"v[168:171],a[24:27],a[44:47],v[168:171]\n" \
L_"v253,s[36:39],s64 offen offset:3072 lds\n" \
M_"v[172:175],a[28:31],a[44:47],v[172:175]\n" \
R_"a[44:47],v236 offset:47104\n" \
M_"v[176:179],a[16:19],a[48:51],v[176:179]\n" \
M_"v[180:183],a[20:23],a[48:51],v[180:183]\n" \
A_"s65,s65,128\n" \
"s_cmp_eq_u32 s65,1152\n" \
"s_cselect_b32 s65,0,s65\n" \
A_"m0,s73,0\n" \
M_"v[184:187],a[24:27],a[48:51],v[184:187]\n" \
M_"v[188:191],a[28:31],a[48:51],v[188:191]\n" \
R_"a[48:51],v236 offset:49152\n" \
M_"v[192:195],a[16:19],a[52:55],v[192:195]\n" \
L_"v241,s[76:79],s65 offen offset:1024 lds\n" \
M_"v[196:199],a[20:23],a[52:55],v[196:199]\n" \
M_"v[200:203],a[24:27],a[52:55],v[200:203]\n" \
L_"v242,s[76:79],s65 offen offset:2048 lds\n" \
M_"v[204:207],a[28:31],a[52:55],v[204:207]\n" \
R_"a[52:55],v236 offset:51200\n" \
M_"v[208:211],a[16:19],a[56:59],v[208:211]\n" \
M_"v[212:215],a[20:23],a[56:59],v[212:215]\n" \
L_"v249,s[76:79],s65 offen offset:3072 lds\n" \
M_"v[216:219],a[24:27],a[56:59],v[216:219]\n" \
M_"v[220:223],a[28:31],a[56:59],v[220:223]\n" \
R_"a[56:59],v236 offset:53248\n" \
S_"lgkmcnt(4)\n" \
M_"v[112:115],a[0:3],a[32:35],v[112:115]\n" \
M_"v[116:119],a[4:7],a[32:35],v[116:119]\n" \
M_"v[120:123],a[8:11],a[32:35],v[120:123]\n" \
M_"v[124:127],a[12:15],a[32:35],v[124:127]\n" \
S_"vmcnt(15)\n" \
R_"a[32:35],v237 offset:40960\n" \
M_"v[128:131],a[0:3],a[36:39],v[128:131]\n" \
T_"a[16:17],v238 offset:8192\n" \
A_"s64,s64,262144\n" \
"s_cmp_eq_u32 s64,4718592\n" \
"s_cselect_b32 s64,0,s64\n" \
A_"m0,s72,4096\n" \
M_"v[132:135],a[4:7],a[36:39],v[132:135]\n" \
T_"a[18:19],v238 offset:8704\n" \
M_"v[136:139],a[8:11],a[36:39],v[136:139]\n" \
T_"a[20:21],v238 offset:8200\n" \
M_"v[140:143],a[12:15],a[36:39],v[140:143]\n" \
R_"a[36:39],v237 offset:43008\n" \
M_"v[144:147],a[0:3],a[40:43],v[144:147]\n" \
T_"a[22:23],v238 offset:8712\n" \
L_"v250,s[36:39],s64 offen lds\n" \
M_"v[148:151],a[4:7],a[40:43],v[148:151]\n" \
T_"a[24:25],v239 offset:8192\n" \
M_"v[152:155],a[8:11],a[40:43],v[152:155]\n" \
T_"a[26:27],v239 offset:8704\n" \
M_"v[156:159],a[12:15],a[40:43],v[156:159]\n" \
R_"a[40:43],v237 offset:45056\n" \
S_"lgkmcnt(6)\n" \
M_"v[160:163],a[0:3],a[44:47],v[160:163]\n" \
T_"a[28:29],v239 offset:8200\n" \
M_"v[164:167],a[4:7],a[44:47],v[164:167]\n" \
T_"a[30:31],v239 offset:8712\n" \
L_"v251,s[36:39],s64 offen offset:1024 lds\n" \
M_"v[168:171],a[8:11],a[44:47],v[168:171]\n" \
M_"v[172:175],a[12:15],a[44:47],v[172:175]\n" \
R_"a[44:47],v237 offset:47104\n" \
M_"v[176:179],a[0:3],a[48:51],v[176:179]\n" \
M_"v[180:183],a[4:7],a[48:51],v[180:183]\n" \
L_"v252,s[36:39],s64 offen offset:2048 lds\n" \
M_"v[184:187],a[8:11],a[48:51],v[184:187]\n" \
M_"v[188:191],a[12:15],a[48:51],v[188:191]\n" \
R_"a[48:51],v237 offset:49152\n" \
M_"v[192:195],a[0:3],a[52:55],v[192:195]\n" \
M_"v[196:199],a[4:7],a[52:55],v[196:199]\n" \
M_"v[200:203],a[8:11],a[52:55],v[200:203]\n" \
L_"v253,s[36:39],s64 offen offset:3072 lds\n" \
M_"v[204:207],a[12:15],a[52:55],v[204:207]\n" \
R_"a[52:55],v237 offset:51200\n" \
M_"v[208:211],a[0:3],a[56:59],v[208:211]\n" \
M_"v[212:215],a[4:7],a[56:59],v[212:215]\n" \
M_"v[216:219],a[8:11],a[56:59],v[216:219]\n" \
M_"v[220:223],a[12:15],a[56:59],v[220:223]\n" \
R_"a[56:59],v237 offset:53248\n" \
S_"lgkmcnt(4)\n" \
M_"v[112:115],a[16:19],a[32:35],v[112:115]\n" \
M_"v[116:119],a[20:23],a[32:35],v[116:119]\n" \
M_"v[120:123],a[24:27],a[32:35],v[120:123]\n" \
M_"v[124:127],a[28:31],a[32:35],v[124:127]\n" \
S_"vmcnt(4) lgkmcnt(0)\n" \
"s_barrier\n" \
R_"a[32:35],v236 offset:0\n" \
M_"v[128:131],a[16:19],a[36:39],v[128:131]\n" \
T_"a[0:1],v238 offset:0\n" \
A_"s64,s64,262144\n" \
"s_cmp_eq_u32 s64,4718592\n" \
"s_cselect_b32 s64,0,s64\n" \
A_"m0,s72,8192\n" \
M_"v[132:135],a[20:23],a[36:39],v[132:135]\n" \
T_"a[2:3],v238 offset:512\n" \
M_"v[136:139],a[24:27],a[36:39],v[136:139]\n" \
T_"a[4:5],v238 offset:8\n" \
L_"v250,s[36:39],s64 offen lds\n" \
M_"v[140:143],a[28:31],a[36:39],v[140:143]\n" \
R_"a[36:39],v236 offset:2048\n" \
M_"v[144:147],a[16:19],a[40:43],v[144:147]\n" \
T_"a[6:7],v238 offset:520\n" \
M_"v[148:151],a[20:23],a[40:43],v[148:151]\n" \
T_"a[8:9],v239 offset:0\n" \
L_"v251,s[36:39],s64 offen offset:1024 lds\n" \
M_"v[152:155],a[24:27],a[40:43],v[152:155]\n" \
T_"a[10:11],v239 offset:512\n" \
M_"v[156:159],a[28:31],a[40:43],v[156:159]\n" \
R_"a[40:43],v236 offset:4096\n" \
M_"v[160:163],a[16:19],a[44:47],v[160:163]\n" \
T_"a[12:13],v239 offset:8\n" \
L_"v252,s[36:39],s64 offen offset:2048 lds\n" \
M_"v[164:167],a[20:23],a[44:47],v[164:167]\n" \
T_"a[14:15],v239 offset:520\n" \
M_"v[168:171],a[24:27],a[44:47],v[168:171]\n" \
L_"v253,s[36:39],s64 offen offset:3072 lds\n" \
M_"v[172:175],a[28:31],a[44:47],v[172:175]\n" \
R_"a[44:47],v236 offset:6144\n" \
M_"v[176:179],a[16:19],a[48:51],v[176:179]\n" \
M_"v[180:183],a[20:23],a[48:51],v[180:183]\n" \
A_"s65,s65,128\n" \
"s_cmp_eq_u32 s65,1152\n" \
"s_cselect_b32 s65,0,s65\n" \
A_"m0,s73,40960\n" \
M_"v[184:187],a[24:27],a[48:51],v[184:187]\n" \
M_"v[188:191],a[28:31],a[48:51],v[188:191]\n" \
R_"a[48:51],v236 offset:8192\n" \
M_"v[192:195],a[16:19],a[52:55],v[192:195]\n" \
L_"v241,s[76:79],s65 offen offset:1024 lds\n" \
M_"v[196:199],a[20:23],a[52:55],v[196:199]\n" \
M_"v[200:203],a[24:27],a[52:55],v[200:203]\n" \
L_"v242,s[76:79],s65 offen offset:2048 lds\n" \
M_"v[204:207],a[28:31],a[52:55],v[204:207]\n" \
R_"a[52:55],v236 offset:10240\n" \
M_"v[208:211],a[16:19],a[56:59],v[208:211]\n" \
M_"v[212:215],a[20:23],a[56:59],v[212:215]\n" \
L_"v249,s[76:79],s65 offen offset:3072 lds\n" \
M_"v[216:219],a[24:27],a[56:59],v[216:219]\n" \
M_"v[220:223],a[28:31],a[56:59],v[220:223]\n" \
R_"a[56:59],v236 offset:12288\n" \
S_"lgkmcnt(4)\n" \
M_"v[112:115],a[0:3],a[32:35],v[112:115]\n" \
M_"v[116:119],a[4:7],a[32:35],v[116:119]\n" \
M_"v[120:123],a[8:11],a[32:35],v[120:123]\n" \
M_"v[124:127],a[12:15],a[32:35],v[124:127]\n" \
S_"vmcnt(7)\n" \
R_"a[32:35],v237 offset:0\n" \
M_"v[128:131],a[0:3],a[36:39],v[128:131]\n" \
T_"a[16:17],v238 offset:4096\n" \
A_"s64,s64,262144\n" \
"s_cmp_eq_u32 s64,4718592\n" \
"s_cselect_b32 s64,0,s64\n" \
A_"m0,s72,0\n" \
M_"v[132:135],a[4:7],a[36:39],v[132:135]\n" \
T_"a[18:19],v238 offset:4608\n" \
L_"v250,s[36:39],s64 offen lds\n" \
M_"v[136:139],a[8:11],a[36:39],v[136:139]\n" \
T_"a[20:21],v238 offset:4104\n" \
L_"v251,s[36:39],s64 offen offset:1024 lds\n" \
M_"v[140:143],a[12:15],a[36:39],v[140:143]\n" \
R_"a[36:39],v237 offset:2048\n" \
M_"v[144:147],a[0:3],a[40:43],v[144:147]\n" \
T_"a[22:23],v238 offset:4616\n" \
M_"v[148:151],a[4:7],a[40:43],v[148:151]\n" \
T_"a[24:25],v239 offset:4096\n" \
L_"v252,s[36:39],s64 offen offset:2048 lds\n" \
M_"v[152:155],a[8:11],a[40:43],v[152:155]\n" \
T_"a[26:27],v239 offset:4608\n" \
L_"v253,s[36:39],s64 offen offset:3072 lds\n" \
M_"v[156:159],a[12:15],a[40:43],v[156:159]\n" \
R_"a[40:43],v237 offset:4096\n" \
S_"lgkmcnt(6)\n" \
M_"v[160:163],a[0:3],a[44:47],v[160:163]\n" \
T_"a[28:29],v239 offset:4104\n" \
L_"a[192:195],v228,s[44:47],s66 offen " KVP "\n" \
M_"v[164:167],a[4:7],a[44:47],v[164:167]\n" \
T_"a[30:31],v239 offset:4616\n" \
M_"v[168:171],a[8:11],a[44:47],v[168:171]\n" \
L_"a[196:199],v228,s[44:47],s66 offen offset:1024 " KVP "\n" \
M_"v[172:175],a[12:15],a[44:47],v[172:175]\n" \
R_"a[44:47],v237 offset:6144\n" \
M_"v[176:179],a[0:3],a[48:51],v[176:179]\n" \
L_"a[200:203],v228,s[44:47],s66 offen offset:2048 " KVP "\n" \
M_"v[180:183],a[4:7],a[48:51],v[180:183]\n" \
M_"v[184:187],a[8:11],a[48:51],v[184:187]\n" \
L_"a[204:207],v228,s[44:47],s66 offen offset:3072 " KVP "\n" \
M_"v[188:191],a[12:15],a[48:51],v[188:191]\n" \
R_"a[48:51],v237 offset:8192\n" \
M_"v[192:195],a[0:3],a[52:55],v[192:195]\n" \
L_"a[208:211],v228,s[44:47],s67 offen " KVP "\n" \
M_"v[196:199],a[4:7],a[52:55],v[196:199]\n" \
L_"a[212:215],v228,s[44:47],s67 offen offset:1024 " KVP "\n" \
M_"v[200:203],a[8:11],a[52:55],v[200:203]\n" \
M_"v[204:207],a[12:15],a[52:55],v[204:207]\n" \
R_"a[52:55],v237 offset:10240\n" \
M_"v[208:211],a[0:3],a[56:59],v[208:211]\n" \
L_"a[216:219],v228,s[44:47],s67 offen offset:2048 " KVP "\n" \
M_"v[212:215],a[4:7],a[56:59],v[212:215]\n" \
L_"a[220:223],v228,s[44:47],s67 offen offset:3072 " KVP "\n" \
M_"v[216:219],a[8:11],a[56:59],v[216:219]\n" \
M_"v[220:223],a[12:15],a[56:59],v[220:223]\n" \
R_"a[56:59],v237 offset:12288\n" \
S_"lgkmcnt(4)\n" \
M_"v[112:115],a[16:19],a[32:35],v[112:115]\n" \
M_"v[116:119],a[20:23],a[32:35],v[116:119]\n" \
M_"v[120:123],a[24:27],a[32:35],v[120:123]\n" \
M_"v[124:127],a[28:31],a[32:35],v[124:127]\n" \
S_"vmcnt(12) lgkmcnt(0)\n" \
"s_barrier\n" \
R_"a[32:35],v236 offset:40960\n" \
M_"v[128:131],a[16:19],a[36:39],v[128:131]\n" \
T_"a[0:1],v238 offset:8192\n" \
A_"s64,s64,262144\n" \
"s_cmp_eq_u32 s64,4718592\n" \
"s_cselect_b32 s64,0,s64\n" \
A_"m0,s72,4096\n" \
M_"v[132:135],a[20:23],a[36:39],v[132:135]\n" \
T_"a[2:3],v238 offset:8704\n" \
M_"v[136:139],a[24:27],a[36:39],v[136:139]\n" \
T_"a[4:5],v238 offset:8200\n" \
L_"v250,s[36:39],s64 offen lds\n" \
M_"v[140:143],a[28:31],a[36:39],v[140:143]\n" \
R_"a[36:39],v236 offset:43008\n" \
M_"v[144:147],a[16:19],a[40:43],v[144:147]\n" \
T_"a[6:7],v238 offset:8712\n" \
M_"v[148:151],a[20:23],a[40:43],v[148:151]\n" \
T_"a[8:9],v239 offset:8192\n" \
L_"v251,s[36:39],s64 offen offset:1024 lds\n" \
M_"v[152:155],a[24:27],a[40:43],v[152:155]\n" \
T_"a[10:11],v239 offset:8704\n" \
M_"v[156:159],a[28:31],a[40:43],v[156:159]\n" \
R_"a[40:43],v236 offset:45056\n" \
M_"v[160:163],a[16:19],a[44:47],v[160:163]\n" \
T_"a[12:13],v239 offset:8200\n" \
L_"v252,s[36:39],s64 offen offset:2048 lds\n" \
M_"v[164:167],a[20:23],a[44:47],v[164:167]\n" \
T_"a[14:15],v239 offset:8712\n" \
M_"v[168:171],a[24:27],a[44:47],v[168:171]\n" \
L_"v253,s[36:39],s64 offen offset:3072 lds\n" \
M_"v[172:175],a[28:31],a[44:47],v[172:175]\n" \
R_"a[44:47],v236 offset:47104\n" \
M_"v[176:179],a[16:19],a[48:51],v[176:179]\n" \
M_"v[180:183],a[20:23],a[48:51],v[180:183]\n" \
A_"s65,s65,128\n" \
"s_cmp_eq_u32 s65,1152\n" \
"s_cselect_b32 s65,0,s65\n" \
A_"m0,s73,0\n" \
M_"v[184:187],a[24:27],a[48:51],v[184:187]\n" \
M_"v[188:191],a[28:31],a[48:51],v[188:191]\n" \
R_"a[48:51],v236 offset:49152\n" \
M_"v[192:195],a[16:19],a[52:55],v[192:195]\n" \
L_"v241,s[76:79],s65 offen offset:1024 lds\n" \
M_"v[196:199],a[20:23],a[52:55],v[196:199]\n" \
M_"v[200:203],a[24:27],a[52:55],v[200:203]\n" \
L_"v242,s[76:79],s65 offen offset:2048 lds\n" \
M_"v[204:207],a[28:31],a[52:55],v[204:207]\n" \
R_"a[52:55],v236 offset:51200\n" \
M_"v[208:211],a[16:19],a[56:59],v[208:211]\n" \
M_"v[212:215],a[20:23],a[56:59],v[212:215]\n" \
L_"v249,s[76:79],s65 offen offset:3072 lds\n" \
M_"v[216:219],a[24:27],a[56:59],v[216:219]\n" \
M_"v[220:223],a[28:31],a[56:59],v[220:223]\n" \
R_"a[56:59],v236 offset:53248\n" \
S_"lgkmcnt(4)\n" \
M_"v[112:115],a[0:3],a[32:35],v[112:115]\n" \
M_"v[116:119],a[4:7],a[32:35],v[116:119]\n" \
M_"v[120:123],a[8:11],a[32:35],v[120:123]\n" \
M_"v[124:127],a[12:15],a[32:35],v[124:127]\n" \
S_"vmcnt(15)\n" \
R_"a[32:35],v237 offset:40960\n" \
M_"v[128:131],a[0:3],a[36:39],v[128:131]\n" \
T_"a[16:17],v238 offset:0\n" \
A_"s64,s64,262144\n" \
"s_cmp_eq_u32 s64,4718592\n" \
"s_cselect_b32 s64,0,s64\n" \
A_"m0,s72,8192\n" \
M_"v[132:135],a[4:7],a[36:39],v[132:135]\n" \
T_"a[18:19],v238 offset:512\n" \
M_"v[136:139],a[8:11],a[36:39],v[136:139]\n" \
T_"a[20:21],v238 offset:8\n" \
M_"v[140:143],a[12:15],a[36:39],v[140:143]\n" \
R_"a[36:39],v237 offset:43008\n" \
M_"v[144:147],a[0:3],a[40:43],v[144:147]\n" \
T_"a[22:23],v238 offset:520\n" \
L_"v250,s[36:39],s64 offen lds\n" \
M_"v[148:151],a[4:7],a[40:43],v[148:151]\n" \
T_"a[24:25],v239 offset:0\n" \
M_"v[152:155],a[8:11],a[40:43],v[152:155]\n" \
T_"a[26:27],v239 offset:512\n" \
M_"v[156:159],a[12:15],a[40:43],v[156:159]\n" \
R_"a[40:43],v237 offset:45056\n" \
S_"lgkmcnt(6)\n" \
M_"v[160:163],a[0:3],a[44:47],v[160:163]\n" \
T_"a[28:29],v239 offset:8\n" \
M_"v[164:167],a[4:7],a[44:47],v[164:167]\n" \
T_"a[30:31],v239 offset:520\n" \
L_"v251,s[36:39],s64 offen offset:1024 lds\n" \
M_"v[168:171],a[8:11],a[44:47],v[168:171]\n" \
M_"v[172:175],a[12:15],a[44:47],v[172:175]\n" \
R_"a[44:47],v237 offset:47104\n" \
M_"v[176:179],a[0:3],a[48:51],v[176:179]\n" \
M_"v[180:183],a[4:7],a[48:51],v[180:183]\n" \
L_"v252,s[36:39],s64 offen offset:2048 lds\n" \
M_"v[184:187],a[8:11],a[48:51],v[184:187]\n" \
M_"v[188:191],a[12:15],a[48:51],v[188:191]\n" \
R_"a[48:51],v237 offset:49152\n" \
M_"v[192:195],a[0:3],a[52:55],v[192:195]\n" \
M_"v[196:199],a[4:7],a[52:55],v[196:199]\n" \
M_"v[200:203],a[8:11],a[52:55],v[200:203]\n" \
L_"v253,s[36:39],s64 offen offset:3072 lds\n" \
M_"v[204:207],a[12:15],a[52:55],v[204:207]\n" \
R_"a[52:55],v237 offset:51200\n" \
M_"v[208:211],a[0:3],a[56:59],v[208:211]\n" \
M_"v[212:215],a[4:7],a[56:59],v[212:215]\n" \
M_"v[216:219],a[8:11],a[56:59],v[216:219]\n" \
M_"v[220:223],a[12:15],a[56:59],v[220:223]\n" \
R_"a[56:59],v237 offset:53248\n" \
S_"lgkmcnt(4)\n" \
M_"v[112:115],a[16:19],a[32:35],v[112:115]\n" \
M_"v[116:119],a[20:23],a[32:35],v[116:119]\n" \
M_"v[120:123],a[24:27],a[32:35],v[120:123]\n" \
M_"v[124:127],a[28:31],a[32:35],v[124:127]\n" \
S_"vmcnt(4) lgkmcnt(0)\n" \
"s_barrier\n" \
R_"a[32:35],v236 offset:0\n" \
M_"v[128:131],a[16:19],a[36:39],v[128:131]\n" \
T_"a[0:1],v238 offset:4096\n" \
"v_and_b32 v243,15,v255\n" \
M_"v[132:135],a[20:23],a[36:39],v[132:135]\n" \
T_"a[2:3],v238 offset:4608\n" \
"v_lshrrev_b32 v244,4,v255\n" \
M_"v[136:139],a[24:27],a[36:39],v[136:139]\n" \
T_"a[4:5],v238 offset:4104\n" \
"v_lshrrev_b32 v245,1,v244\n" \
M_"v[140:143],a[28:31],a[36:39],v[140:143]\n" \
R_"a[36:39],v236 offset:2048\n" \
M_"v[144:147],a[16:19],a[40:43],v[144:147]\n" \
T_"a[6:7],v238 offset:4616\n" \
"v_lshlrev_b32 v245,17,v245\n" \
M_"v[148:151],a[20:23],a[40:43],v[148:151]\n" \
T_"a[8:9],v239 offset:4096\n" \
"v_and_b32 v246,1,v244\n" \
M_"v[152:155],a[24:27],a[40:43],v[152:155]\n" \
T_"a[10:11],v239 offset:4608\n" \
"v_lshlrev_b32 v246,15,v246\n" \
M_"v[156:159],a[28:31],a[40:43],v[156:159]\n" \
R_"a[40:43],v236 offset:4096\n" \
M_"v[160:163],a[16:19],a[44:47],v[160:163]\n" \
T_"a[12:13],v239 offset:4104\n" \
"v_add_u32 v245,v245,v246\n" \
M_"v[164:167],a[20:23],a[44:47],v[164:167]\n" \
T_"a[14:15],v239 offset:4616\n" \
"v_lshl_add_u32 v250,v243,4,v245\n" \
M_"v[168:171],a[24:27],a[44:47],v[168:171]\n" \
M_"v[172:175],a[28:31],a[44:47],v[172:175]\n" \
R_"a[44:47],v236 offset:6144\n" \
M_"v[176:179],a[16:19],a[48:51],v[176:179]\n" \
"v_add_u32 v251,7104,v250\n" \
M_"v[180:183],a[20:23],a[48:51],v[180:183]\n" \
"v_add_u32 v252,14208,v250\n" \
M_"v[184:187],a[24:27],a[48:51],v[184:187]\n" \
"v_add_u32 v253,21312,v250\n" \
M_"v[188:191],a[28:31],a[48:51],v[188:191]\n" \
R_"a[48:51],v236 offset:8192\n" \
M_"v[192:195],a[16:19],a[52:55],v[192:195]\n" \
A_"s60,s74,7\n" \
M_"v[196:199],a[20:23],a[52:55],v[196:199]\n" \
"s_sub_u32 s61,s60,9\n" \
M_"v[200:203],a[24:27],a[52:55],v[200:203]\n" \
"s_cmp_lt_u32 s60,9\n" \
M_"v[204:207],a[28:31],a[52:55],v[204:207]\n" \
R_"a[52:55],v236 offset:10240\n" \
M_"v[208:211],a[16:19],a[56:59],v[208:211]\n" \
"s_cselect_b32 s60,s60,s61\n" \
M_"v[212:215],a[20:23],a[56:59],v[212:215]\n" \
"s_lshl_b32 s64,s60,19\n" \
M_"v[216:219],a[24:27],a[56:59],v[216:219]\n" \
M_"v[220:223],a[28:31],a[56:59],v[220:223]\n" \
R_"a[56:59],v236 offset:12288\n" \
S_"lgkmcnt(4)\n" \
M_"v[112:115],a[0:3],a[32:35],v[112:115]\n" \
M_"v[116:119],a[4:7],a[32:35],v[116:119]\n" \
M_"v[120:123],a[8:11],a[32:35],v[120:123]\n" \
M_"v[124:127],a[12:15],a[32:35],v[124:127]\n" \
S_"vmcnt(0)\n" \
R_"a[32:35],v237 offset:0\n" \
M_"v[128:131],a[0:3],a[36:39],v[128:131]\n" \
T_"a[16:17],v238 offset:8192\n" \
M_"v[132:135],a[4:7],a[36:39],v[132:135]\n" \
T_"a[18:19],v238 offset:8704\n" \
M_"v[136:139],a[8:11],a[36:39],v[136:139]\n" \
T_"a[20:21],v238 offset:8200\n" \
M_"v[140:143],a[12:15],a[36:39],v[140:143]\n" \
R_"a[36:39],v237 offset:2048\n" \
M_"v[144:147],a[0:3],a[40:43],v[144:147]\n" \
T_"a[22:23],v238 offset:8712\n" \
M_"v[148:151],a[4:7],a[40:43],v[148:151]\n" \
T_"a[24:25],v239 offset:8192\n" \
M_"v[152:155],a[8:11],a[40:43],v[152:155]\n" \
T_"a[26:27],v239 offset:8704\n" \
M_"v[156:159],a[12:15],a[40:43],v[156:159]\n" \
R_"a[40:43],v237 offset:4096\n" \
S_"lgkmcnt(6)\n" \
M_"v[160:163],a[0:3],a[44:47],v[160:163]\n" \
T_"a[28:29],v239 offset:8200\n" \
M_"v[164:167],a[4:7],a[44:47],v[164:167]\n" \
T_"a[30:31],v239 offset:8712\n" \
M_"v[168:171],a[8:11],a[44:47],v[168:171]\n" \
M_"v[172:175],a[12:15],a[44:47],v[172:175]\n" \
R_"a[44:47],v237 offset:6144\n" \
M_"v[176:179],a[0:3],a[48:51],v[176:179]\n" \
M_"v[180:183],a[4:7],a[48:51],v[180:183]\n" \
M_"v[184:187],a[8:11],a[48:51],v[184:187]\n" \
M_"v[188:191],a[12:15],a[48:51],v[188:191]\n" \
R_"a[48:51],v237 offset:8192\n" \
M_"v[192:195],a[0:3],a[52:55],v[192:195]\n" \
M_"v[196:199],a[4:7],a[52:55],v[196:199]\n" \
M_"v[200:203],a[8:11],a[52:55],v[200:203]\n" \
M_"v[204:207],a[12:15],a[52:55],v[204:207]\n" \
R_"a[52:55],v237 offset:10240\n" \
M_"v[208:211],a[0:3],a[56:59],v[208:211]\n" \
M_"v[212:215],a[4:7],a[56:59],v[212:215]\n" \
M_"v[216:219],a[8:11],a[56:59],v[216:219]\n" \
M_"v[220:223],a[12:15],a[56:59],v[220:223]\n" \
R_"a[56:59],v237 offset:12288\n" \
S_"lgkmcnt(4)\n" \
M_"v[112:115],a[16:19],a[32:35],v[112:115]\n" \
A_"m0,s83,0\n" \
M_"v[116:119],a[20:23],a[32:35],v[116:119]\n" \
L_"v250,s[68:71],s64 offen lds\n" \
"s_barrier\n" \
M_"v[120:123],a[24:27],a[32:35],v[120:123]\n" \
L_"v251,s[68:71],s64 offen offset:1088 lds\n" \
M_"v[124:127],a[28:31],a[32:35],v[124:127]\n" \
M_"v[128:131],a[16:19],a[36:39],v[128:131]\n" \
L_"v252,s[68:71],s64 offen offset:2176 lds\n" \
M_"v[132:135],a[20:23],a[36:39],v[132:135]\n" \
L_"v253,s[68:71],s64 offen offset:3264 lds\n" \
M_"v[136:139],a[24:27],a[36:39],v[136:139]\n" \
M_"v[140:143],a[28:31],a[36:39],v[140:143]\n" \
A_"m0,s83,4360\n" \
A_"s60,s64,65536\n" \
M_"v[144:147],a[16:19],a[40:43],v[144:147]\n" \
L_"v250,s[68:71],s60 offen lds\n" \
M_"v[148:151],a[20:23],a[40:43],v[148:151]\n" \
L_"v251,s[68:71],s60 offen offset:1088 lds\n" \
P_"v[112:113],v[112:113],s[80:81]\n" \
P_"v[114:115],v[114:115],s[80:81]\n" \
M_"v[152:155],a[24:27],a[40:43],v[152:155]\n" \
P_"v[116:117],v[116:117],s[80:81]\n" \
P_"v[118:119],v[118:119],s[80:81]\n" \
C_"v0,v112,v113\n" \
M_"v[156:159],a[28:31],a[40:43],v[156:159]\n" \
L_"v252,s[68:71],s60 offen offset:2176 lds\n" \
C_"v1,v114,v115\n" \
C_"v2,v116,v117\n" \
S_"lgkmcnt(0)\n" \
M_"v[160:163],a[16:19],a[44:47],v[160:163]\n" \
L_"v253,s[68:71],s60 offen offset:3264 lds\n" \
C_"v3,v118,v119\n" \
"ds_write_b128 v248,v[0:3] offset:0\n" \
M_"v[164:167],a[20:23],a[44:47],v[164:167]\n" \
P_"v[120:121],v[120:121],s[80:81]\n" \
P_"v[122:123],v[122:123],s[80:81]\n" \
P_"v[124:125],v[124:125],s[80:81]\n" \
M_"v[168:171],a[24:27],a[44:47],v[168:171]\n" \
P_"v[126:127],v[126:127],s[80:81]\n" \
C_"v4,v120,v121\n" \
C_"v5,v122,v123\n" \
M_"v[172:175],a[28:31],a[44:47],v[172:175]\n" \
C_"v6,v124,v125\n" \
C_"v7,v126,v127\n" \
"ds_write_b128 v248,v[4:7] offset:1024\n" \
M_"v[176:179],a[16:19],a[48:51],v[176:179]\n" \
P_"v[128:129],v[128:129],s[80:81]\n" \
P_"v[130:131],v[130:131],s[80:81]\n" \
P_"v[132:133],v[132:133],s[80:81]\n" \
M_"v[180:183],a[20:23],a[48:51],v[180:183]\n" \
P_"v[134:135],v[134:135],s[80:81]\n" \
C_"v16,v128,v129\n" \
C_"v17,v130,v131\n" \
M_"v[184:187],a[24:27],a[48:51],v[184:187]\n" \
C_"v18,v132,v133\n" \
C_"v19,v134,v135\n" \
"ds_write_b128 v248,v[16:19] offset:2048\n" \
M_"v[188:191],a[28:31],a[48:51],v[188:191]\n" \
P_"v[136:137],v[136:137],s[80:81]\n" \
P_"v[138:139],v[138:139],s[80:81]\n" \
P_"v[140:141],v[140:141],s[80:81]\n" \
M_"v[192:195],a[16:19],a[52:55],v[192:195]\n" \
P_"v[142:143],v[142:143],s[80:81]\n" \
C_"v20,v136,v137\n" \
C_"v21,v138,v139\n" \
M_"v[196:199],a[20:23],a[52:55],v[196:199]\n" \
C_"v22,v140,v141\n" \
C_"v23,v142,v143\n" \
"ds_write_b128 v248,v[20:23] offset:3072\n" \
M_"v[200:203],a[24:27],a[52:55],v[200:203]\n" \
P_"v[144:145],v[144:145],s[80:81]\n" \
P_"v[146:147],v[146:147],s[80:81]\n" \
P_"v[148:149],v[148:149],s[80:81]\n" \
M_"v[204:207],a[28:31],a[52:55],v[204:207]\n" \
P_"v[150:151],v[150:151],s[80:81]\n" \
C_"v32,v144,v145\n" \
C_"v33,v146,v147\n" \
M_"v[208:211],a[16:19],a[56:59],v[208:211]\n" \
C_"v34,v148,v149\n" \
C_"v35,v150,v151\n" \
"ds_write_b128 v248,v[32:35] offset:4096\n" \
M_"v[212:215],a[20:23],a[56:59],v[212:215]\n" \
P_"v[152:153],v[152:153],s[80:81]\n" \
P_"v[154:155],v[154:155],s[80:81]\n" \
P_"v[156:157],v[156:157],s[80:81]\n" \
M_"v[216:219],a[24:27],a[56:59],v[216:219]\n" \
P_"v[158:159],v[158:159],s[80:81]\n" \
C_"v36,v152,v153\n" \
C_"v37,v154,v155\n" \
M_"v[220:223],a[28:31],a[56:59],v[220:223]\n" \
C_"v38,v156,v157\n" \
C_"v39,v158,v159\n" \
"ds_write_b128 v248,v[36:39] offset:5120\n" \
P_"v[160:161],v[160:161],s[80:81]\n" \
P_"v[162:163],v[162:163],s[80:81]\n" \
P_"v[164:165],v[164:165],s[80:81]\n" \
P_"v[166:167],v[166:167],s[80:81]\n" \
C_"v48,v160,v161\n" \
C_"v49,v162,v163\n" \
C_"v50,v164,v165\n" \
C_"v51,v166,v167\n" \
"ds_write_b128 v248,v[48:51] offset:6144\n" \
P_"v[168:169],v[168:169],s[80:81]\n" \
P_"v[170:171],v[170:171],s[80:81]\n" \
P_"v[172:173],v[172:173],s[80:81]\n" \
P_"v[174:175],v[174:175],s[80:81]\n" \
C_"v52,v168,v169\n" \
C_"v53,v170,v171\n" \
C_"v54,v172,v173\n" \
C_"v55,v174,v175\n" \
"ds_write_b128 v248,v[52:55] offset:7168\n" \
P_"v[176:177],v[176:177],s[80:81]\n" \
P_"v[178:179],v[178:179],s[80:81]\n" \
P_"v[180:181],v[180:181],s[80:81]\n" \
P_"v[182:183],v[182:183],s[80:81]\n" \
C_"v64,v176,v177\n" \
C_"v65,v178,v179\n" \
C_"v66,v180,v181\n" \
C_"v67,v182,v183\n" \
"ds_write_b128 v248,v[64:67] offset:8192\n" \
P_"v[184:185],v[184:185],s[80:81]\n" \
P_"v[186:187],v[186:187],s[80:81]\n" \
P_"v[188:189],v[188:189],s[80:81]\n" \
P_"v[190:191],v[190:191],s[80:81]\n" \
C_"v68,v184,v185\n" \
C_"v69,v186,v187\n" \
C_"v70,v188,v189\n" \
C_"v71,v190,v191\n" \
"ds_write_b128 v248,v[68:71] offset:9216\n" \
P_"v[192:193],v[192:193],s[80:81]\n" \
P_"v[194:195],v[194:195],s[80:81]\n" \
P_"v[196:197],v[196:197],s[80:81]\n" \
P_"v[198:199],v[198:199],s[80:81]\n" \
C_"v80,v192,v193\n" \
C_"v81,v194,v195\n" \
C_"v82,v196,v197\n" \
C_"v83,v198,v199\n" \
"ds_write_b128 v248,v[80:83] offset:10240\n" \
P_"v[200:201],v[200:201],s[80:81]\n" \
P_"v[202:203],v[202:203],s[80:81]\n" \
P_"v[204:205],v[204:205],s[80:81]\n" \
P_"v[206:207],v[206:207],s[80:81]\n" \
C_"v84,v200,v201\n" \
C_"v85,v202,v203\n" \
C_"v86,v204,v205\n" \
C_"v87,v206,v207\n" \
"ds_write_b128 v248,v[84:87] offset:11264\n" \
P_"v[208:209],v[208:209],s[80:81]\n" \
P_"v[210:211],v[210:211],s[80:81]\n" \
P_"v[212:213],v[212:213],s[80:81]\n" \
P_"v[214:215],v[214:215],s[80:81]\n" \
C_"v96,v208,v209\n" \
C_"v97,v210,v211\n" \
C_"v98,v212,v213\n" \
C_"v99,v214,v215\n" \
"ds_write_b128 v248,v[96:99] offset:12288\n" \
P_"v[216:217],v[216:217],s[80:81]\n" \
P_"v[218:219],v[218:219],s[80:81]\n" \
P_"v[220:221],v[220:221],s[80:81]\n" \
P_"v[222:223],v[222:223],s[80:81]\n" \
C_"v100,v216,v217\n" \
C_"v101,v218,v219\n" \
C_"v102,v220,v221\n" \
C_"v103,v222,v223\n" \
"ds_write_b128 v248,v[100:103] offset:13312\n" \
"; ---- trb\n" \
S_"lgkmcnt(0)\n" \
"s_barrier\n" \
"s_barrier\n" \
"s_memrealtime s[20:21]\n" \
S_"lgkmcnt(0)\n" \
A_"s64,s64,262144\n" \
"s_cmp_eq_u32 s64,4718592\n" \
"s_cselect_b32 s64,0,s64\n" \
A_"m0,s83,8704\n" \
"s_nop 0\n" \
L_"v250,s[68:71],s64 offen lds\n" \
L_"v251,s[68:71],s64 offen offset:1088 lds\n" \
L_"v252,s[68:71],s64 offen offset:2176 lds\n" \
L_"v253,s[68:71],s64 offen offset:3264 lds\n" \
A_"m0,s83,13064\n" \
A_"s60,s64,65536\n" \
L_"v250,s[68:71],s60 offen lds\n" \
L_"v251,s[68:71],s60 offen offset:1088 lds\n" \
L_"v252,s[68:71],s60 offen offset:2176 lds\n" \
L_"v253,s[68:71],s60 offen offset:3264 lds\n" \
A_"s64,s64,262144\n" \
"s_cmp_eq_u32 s64,4718592\n" \
"s_cselect_b32 s64,0,s64\n" \
A_"m0,s83,17408\n" \
"s_nop 0\n" \
L_"v250,s[68:71],s64 offen lds\n" \
L_"v251,s[68:71],s64 offen offset:1088 lds\n" \
L_"v252,s[68:71],s64 offen offset:2176 lds\n" \
L_"v253,s[68:71],s64 offen offset:3264 lds\n" \
A_"m0,s83,21768\n" \
A_"s60,s64,65536\n" \
L_"v250,s[68:71],s60 offen lds\n" \
L_"v251,s[68:71],s60 offen offset:1088 lds\n" \
L_"v252,s[68:71],s60 offen offset:2176 lds\n" \
L_"v253,s[68:71],s60 offen offset:3264 lds\n" \
"v_and_b32 v243,15,v255\n" \
"v_lshrrev_b32 v244,4,v255\n" \
"v_lshrrev_b32 v245,2,v243\n" \
"v_mul_u32_u24 v238,1088,v245\n" \
"v_and_b32 v246,1,v244\n" \
"v_mul_u32_u24 v246,4360,v246\n" \
"v_add_u32 v238,v238,v246\n" \
"v_lshrrev_b32 v246,1,v244\n" \
"v_lshl_add_u32 v238,v246,9,v238\n" \
"v_and_b32 v246,3,v255\n" \
"v_lshl_add_u32 v238,v246,4,v238\n" \
"v_add_u32 v238,s83,v238\n" \
"s_mul_i32 s60,s75,7\n" \
"s_lshl_b32 s73,s60,10\n" \
A_"s73,s73,s57\n" \
A_"s73,s73,108032\n" \
A_"s61,s60,s29\n" \
"v_lshrrev_b32 v243,3,v255\n" \
"v_mul_u32_u24 v247,1152,v243\n" \
"v_lshrrev_b32 v244,4,v255\n" \
"v_lshlrev_b32 v245,1,v244\n" \
"v_and_b32 v246,7,v255\n" \
"v_xor_b32 v246,v246,v245\n" \
"v_lshl_add_u32 v247,v246,4,v247\n" \
A_"s62,s61,0\n" \
"s_sub_u32 s63,s62,14\n" \
"s_cmp_lt_u32 s62,14\n" \
"s_cselect_b32 s62,s62,s63\n" \
"s_mul_i32 s62,s62,9216\n" \
A_"s62,s62,4096\n" \
"v_add_u32 v240,s62,v247\n" \
A_"s62,s61,1\n" \
"s_sub_u32 s63,s62,14\n" \
"s_cmp_lt_u32 s62,14\n" \
"s_cselect_b32 s62,s62,s63\n" \
"s_mul_i32 s62,s62,9216\n" \
A_"s62,s62,3072\n" \
"v_add_u32 v241,s62,v247\n" \
A_"s62,s61,2\n" \
"s_sub_u32 s63,s62,14\n" \
"s_cmp_lt_u32 s62,14\n" \
"s_cselect_b32 s62,s62,s63\n" \
"s_mul_i32 s62,s62,9216\n" \
A_"s62,s62,2048\n" \
"v_add_u32 v242,s62,v247\n" \
A_"s62,s61,3\n" \
"s_sub_u32 s63,s62,14\n" \
"s_cmp_lt_u32 s62,14\n" \
"s_cselect_b32 s62,s62,s63\n" \
"s_mul_i32 s62,s62,9216\n" \
A_"s62,s62,1024\n" \
"v_add_u32 v249,s62,v247\n" \
A_"s62,s61,4\n" \
"s_sub_u32 s63,s62,14\n" \
"s_cmp_lt_u32 s62,14\n" \
"s_cselect_b32 s62,s62,s63\n" \
"s_mul_i32 s62,s62,9216\n" \
A_"s62,s62,4096\n" \
"v_add_u32 v243,s62,v247\n" \
A_"s62,s61,5\n" \
"s_sub_u32 s63,s62,14\n" \
"s_cmp_lt_u32 s62,14\n" \
"s_cselect_b32 s62,s62,s63\n" \
"s_mul_i32 s62,s62,9216\n" \
A_"s62,s62,3072\n" \
"v_add_u32 v244,s62,v247\n" \
A_"s62,s61,6\n" \
"s_sub_u32 s63,s62,14\n" \
"s_cmp_lt_u32 s62,14\n" \
"s_cselect_b32 s62,s62,s63\n" \
"s_mul_i32 s62,s62,9216\n" \
A_"s62,s62,2048\n" \
"v_add_u32 v245,s62,v247\n" \
"v_xor_b32 v246,8,v234\n" \
R_"a[64:67],v236 offset:40960\n" \
R_"a[68:71],v236 offset:43008\n" \
R_"a[72:75],v236 offset:45056\n" \
R_"a[76:79],v236 offset:47104\n" \
R_"a[80:83],v236 offset:49152\n" \
R_"a[84:87],v236 offset:51200\n" \
R_"a[88:91],v236 offset:53248\n" \
S_"vmcnt(16)\n" \
T_"a[0:1],v238 offset:0\n" \
T_"a[2:3],v238 offset:256\n" \
T_"a[4:5],v238 offset:8\n" \
T_"a[6:7],v238 offset:264\n" \
T_"a[8:9],v238 offset:64\n" \
T_"a[10:11],v238 offset:320\n" \
T_"a[12:13],v238 offset:72\n" \
T_"a[14:15],v238 offset:328\n" \
T_"a[16:17],v238 offset:128\n" \
T_"a[18:19],v238 offset:384\n" \
T_"a[20:21],v238 offset:136\n" \
T_"a[22:23],v238 offset:392\n" \
T_"a[24:25],v238 offset:192\n" \
T_"a[26:27],v238 offset:448\n" \
T_"a[28:29],v238 offset:200\n" \
T_"a[30:31],v238 offset:456\n" \
"; ---- q2\n" \
S_"lgkmcnt(14)\n" \
M_"v[0:3],a[0:3],a[64:67],0\n" \
S_"lgkmcnt(12)\n" \
M_"v[4:7],a[4:7],a[64:67],0\n" \
S_"lgkmcnt(10)\n" \
M_"v[8:11],a[8:11],a[64:67],0\n" \
S_"lgkmcnt(8)\n" \
M_"v[12:15],a[12:15],a[64:67],0\n" \
S_"lgkmcnt(6)\n" \
M_"v[16:19],a[16:19],a[64:67],0\n" \
S_"lgkmcnt(4)\n" \
M_"v[20:23],a[20:23],a[64:67],0\n" \
S_"lgkmcnt(0)\n" \
M_"v[24:27],a[24:27],a[64:67],0\n" \
M_"v[28:31],a[28:31],a[64:67],0\n" \
S_"vmcnt(8)\n" \
R_"a[64:67],v237 offset:40960\n" \
M_"v[32:35],a[0:3],a[68:71],0\n" \
T_"a[32:33],v238 offset:8704\n" \
M_"v[36:39],a[4:7],a[68:71],0\n" \
T_"a[34:35],v238 offset:8960\n" \
A_"s64,s64,262144\n" \
"s_cmp_eq_u32 s64,4718592\n" \
"s_cselect_b32 s64,0,s64\n" \
A_"m0,s83,0\n" \
M_"v[40:43],a[8:11],a[68:71],0\n" \
T_"a[36:37],v238 offset:8712\n" \
L_"v250,s[68:71],s64 offen lds\n" \
M_"v[44:47],a[12:15],a[68:71],0\n" \
T_"a[38:39],v238 offset:8968\n" \
M_"v[48:51],a[16:19],a[68:71],0\n" \
T_"a[40:41],v238 offset:8768\n" \
M_"v[52:55],a[20:23],a[68:71],0\n" \
T_"a[42:43],v238 offset:9024\n" \
M_"v[56:59],a[24:27],a[68:71],0\n" \
T_"a[44:45],v238 offset:8776\n" \
L_"v251,s[68:71],s64 offen offset:1088 lds\n" \
M_"v[60:63],a[28:31],a[68:71],0\n" \
S_"lgkmcnt(0)\n" \
"LT49_%=:\n" \
"s_memrealtime s[18:19]\n" \
S_"lgkmcnt(0)\n" \
"s_sub_u32 s18,s18,s20\n" \
"s_cmp_ge_u32 s18,43\n" \
"s_cbranch_scc1 LT49d_%=\n" \
"s_sleep 1\n" \
"s_branch LT49_%=\n" \
"LT49d_%=:\n" \
R_"a[68:71],v237 offset:43008\n" \
M_"v[64:67],a[0:3],a[72:75],0\n" \
T_"a[46:47],v238 offset:9032\n" \
M_"v[68:71],a[4:7],a[72:75],0\n" \
T_"a[48:49],v238 offset:8832\n" \
M_"v[72:75],a[8:11],a[72:75],0\n" \
T_"a[50:51],v238 offset:9088\n" \
L_"v252,s[68:71],s64 offen offset:2176 lds\n" \
M_"v[76:79],a[12:15],a[72:75],0\n" \
T_"a[52:53],v238 offset:8840\n" \
M_"v[80:83],a[16:19],a[72:75],0\n" \
T_"a[54:55],v238 offset:9096\n" \
M_"v[84:87],a[20:23],a[72:75],0\n" \
T_"a[56:57],v238 offset:8896\n" \
M_"v[88:91],a[24:27],a[72:75],0\n" \
T_"a[58:59],v238 offset:9152\n" \
L_"v253,s[68:71],s64 offen offset:3264 lds\n" \
M_"v[92:95],a[28:31],a[72:75],0\n" \
R_"a[72:75],v237 offset:45056\n" \
M_"v[96:99],a[0:3],a[76:79],0\n" \
T_"a[60:61],v238 offset:8904\n" \
M_"v[100:103],a[4:7],a[76:79],0\n" \
T_"a[62:63],v238 offset:9160\n" \
M_"v[104:107],a[8:11],a[76:79],0\n" \
A_"m0,s83,4360\n" \
A_"s60,s64,65536\n" \
M_"v[108:111],a[12:15],a[76:79],0\n" \
L_"v250,s[68:71],s60 offen lds\n" \
M_"v[112:115],a[16:19],a[76:79],0\n" \
M_"v[116:119],a[20:23],a[76:79],0\n" \
M_"v[120:123],a[24:27],a[76:79],0\n" \
L_"v251,s[68:71],s60 offen offset:1088 lds\n" \
M_"v[124:127],a[28:31],a[76:79],0\n" \
S_"lgkmcnt(0)\n" \
"LT50_%=:\n" \
"s_memrealtime s[18:19]\n" \
S_"lgkmcnt(0)\n" \
"s_sub_u32 s18,s18,s20\n" \
"s_cmp_ge_u32 s18,66\n" \
"s_cbranch_scc1 LT50d_%=\n" \
"s_sleep 1\n" \
"s_branch LT50_%=\n" \
"LT50d_%=:\n" \
R_"a[76:79],v237 offset:47104\n" \
M_"v[128:131],a[0:3],a[80:83],0\n" \
M_"v[132:135],a[4:7],a[80:83],0\n" \
M_"v[136:139],a[8:11],a[80:83],0\n" \
L_"v252,s[68:71],s60 offen offset:2176 lds\n" \
M_"v[140:143],a[12:15],a[80:83],0\n" \
M_"v[144:147],a[16:19],a[80:83],0\n" \
M_"v[148:151],a[20:23],a[80:83],0\n" \
M_"v[152:155],a[24:27],a[80:83],0\n" \
L_"v253,s[68:71],s60 offen offset:3264 lds\n" \
M_"v[156:159],a[28:31],a[80:83],0\n" \
R_"a[80:83],v237 offset:49152\n" \
M_"v[160:163],a[0:3],a[84:87],0\n" \
M_"v[164:167],a[4:7],a[84:87],0\n" \
M_"v[168:171],a[8:11],a[84:87],0\n" \
M_"v[172:175],a[12:15],a[84:87],0\n" \
M_"v[176:179],a[16:19],a[84:87],0\n" \
M_"v[180:183],a[20:23],a[84:87],0\n" \
M_"v[184:187],a[24:27],a[84:87],0\n" \
M_"v[188:191],a[28:31],a[84:87],0\n" \
S_"lgkmcnt(0)\n" \
"LT51_%=:\n" \
"s_memrealtime s[18:19]\n" \
S_"lgkmcnt(0)\n" \
"s_sub_u32 s18,s18,s20\n" \
"s_cmp_ge_u32 s18,89\n" \
"s_cbranch_scc1 LT51d_%=\n" \
"s_sleep 1\n" \
"s_branch LT51_%=\n" \
"LT51d_%=:\n" \
R_"a[84:87],v237 offset:51200\n" \
M_"v[192:195],a[0:3],a[88:91],0\n" \
M_"v[196:199],a[4:7],a[88:91],0\n" \
M_"v[200:203],a[8:11],a[88:91],0\n" \
M_"v[204:207],a[12:15],a[88:91],0\n" \
M_"v[208:211],a[16:19],a[88:91],0\n" \
M_"v[212:215],a[20:23],a[88:91],0\n" \
M_"v[216:219],a[24:27],a[88:91],0\n" \
M_"v[220:223],a[28:31],a[88:91],0\n" \
R_"a[88:91],v237 offset:53248\n" \
S_"lgkmcnt(4)\n" \
M_"v[0:3],a[32:35],a[64:67],v[0:3]\n" \
M_"v[4:7],a[36:39],a[64:67],v[4:7]\n" \
M_"v[8:11],a[40:43],a[64:67],v[8:11]\n" \
M_"v[12:15],a[44:47],a[64:67],v[12:15]\n" \
S_"lgkmcnt(0)\n" \
"LT52_%=:\n" \
"s_memrealtime s[18:19]\n" \
S_"lgkmcnt(0)\n" \
"s_sub_u32 s18,s18,s20\n" \
"s_cmp_ge_u32 s18,113\n" \
"s_cbranch_scc1 LT52d_%=\n" \
"s_sleep 1\n" \
"s_branch LT52_%=\n" \
"LT52d_%=:\n" \
M_"v[16:19],a[48:51],a[64:67],v[16:19]\n" \
M_"v[20:23],a[52:55],a[64:67],v[20:23]\n" \
M_"v[24:27],a[56:59],a[64:67],v[24:27]\n" \
M_"v[28:31],a[60:63],a[64:67],v[28:31]\n" \
"v_mov_b32 v247,1\n" \
S_"vmcnt(8)\n" \
"ds_write_b32 v234,v247 offset:0\n" \
M_"v[32:35],a[32:35],a[68:71],v[32:35]\n" \
T_"a[0:1],v238 offset:17408\n" \
A_"s64,s64,262144\n" \
"s_cmp_eq_u32 s64,4718592\n" \
"s_cselect_b32 s64,0,s64\n" \
A_"m0,s83,8704\n" \
M_"v[36:39],a[36:39],a[68:71],v[36:39]\n" \
T_"a[2:3],v238 offset:17664\n" \
L_"v250,s[68:71],s64 offen lds\n" \
M_"v[40:43],a[40:43],a[68:71],v[40:43]\n" \
T_"a[4:5],v238 offset:17416\n" \
M_"v[44:47],a[44:47],a[68:71],v[44:47]\n" \
T_"a[6:7],v238 offset:17672\n" \
L_"v251,s[68:71],s64 offen offset:1088 lds\n" \
M_"v[48:51],a[48:51],a[68:71],v[48:51]\n" \
T_"a[8:9],v238 offset:17472\n" \
M_"v[52:55],a[52:55],a[68:71],v[52:55]\n" \
T_"a[10:11],v238 offset:17728\n" \
L_"v252,s[68:71],s64 offen offset:2176 lds\n" \
M_"v[56:59],a[56:59],a[68:71],v[56:59]\n" \
T_"a[12:13],v238 offset:17480\n" \
M_"v[60:63],a[60:63],a[68:71],v[60:63]\n" \
S_"lgkmcnt(0)\n" \
"LT53_%=:\n" \
"s_memrealtime s[18:19]\n" \
S_"lgkmcnt(0)\n" \
"s_sub_u32 s18,s18,s20\n" \
"s_cmp_ge_u32 s18,136\n" \
"s_cbranch_scc1 LT53d_%=\n" \
"s_sleep 1\n" \
"s_branch LT53_%=\n" \
"LT53d_%=:\n" \
T_"a[14:15],v238 offset:17736\n" \
L_"v253,s[68:71],s64 offen offset:3264 lds\n" \
M_"v[64:67],a[32:35],a[72:75],v[64:67]\n" \
"ds_read_b32 v248,v235 offset:0\n" \
A_"m0,s83,13064\n" \
A_"s60,s64,65536\n" \
M_"v[68:71],a[36:39],a[72:75],v[68:71]\n" \
T_"a[16:17],v238 offset:17536\n" \
M_"v[72:75],a[40:43],a[72:75],v[72:75]\n" \
T_"a[18:19],v238 offset:17792\n" \
L_"v250,s[68:71],s60 offen lds\n" \
M_"v[76:79],a[44:47],a[72:75],v[76:79]\n" \
T_"a[20:21],v238 offset:17544\n" \
M_"v[80:83],a[48:51],a[72:75],v[80:83]\n" \
T_"a[22:23],v238 offset:17800\n" \
L_"v251,s[68:71],s60 offen offset:1088 lds\n" \
M_"v[84:87],a[52:55],a[72:75],v[84:87]\n" \
T_"a[24:25],v238 offset:17600\n" \
S_"lgkmcnt(5)\n" \
"v_readfirstlane_b32 s63,v248\n" \
M_"v[88:91],a[56:59],a[72:75],v[88:91]\n" \
T_"a[26:27],v238 offset:17856\n" \
"s_cmp_ge_u32 s63,1\n" \
"s_cbranch_scc0 LPS0_%=\n" \
"LPS0d_%=:\n" \
M_"v[92:95],a[60:63],a[72:75],v[92:95]\n" \
T_"a[28:29],v238 offset:17608\n" \
L_"v252,s[68:71],s60 offen offset:2176 lds\n" \
M_"v[96:99],a[32:35],a[76:79],v[96:99]\n" \
T_"a[30:31],v238 offset:17864\n" \
L_"v253,s[68:71],s60 offen offset:3264 lds\n" \
M_"v[100:103],a[36:39],a[76:79],v[100:103]\n" \
R_"a[64:67],v236 offset:0\n" \
M_"v[104:107],a[40:43],a[76:79],v[104:107]\n" \
R_"a[68:71],v236 offset:2048\n" \
M_"v[108:111],a[44:47],a[76:79],v[108:111]\n" \
R_"a[72:75],v236 offset:4096\n" \
M_"v[112:115],a[48:51],a[76:79],v[112:115]\n" \
A_"s65,s65,128\n" \
"s_cmp_eq_u32 s65,1152\n" \
"s_cselect_b32 s65,0,s65\n" \
A_"m0,s73,40960\n" \
M_"v[116:119],a[52:55],a[76:79],v[116:119]\n" \
L_"v240,s[76:79],s65 offen lds\n" \
M_"v[120:123],a[56:59],a[76:79],v[120:123]\n" \
M_"v[124:127],a[60:63],a[76:79],v[124:127]\n" \
S_"lgkmcnt(0)\n" \
"LT54_%=:\n" \
"s_memrealtime s[18:19]\n" \
S_"lgkmcnt(0)\n" \
"s_sub_u32 s18,s18,s20\n" \
"s_cmp_ge_u32 s18,159\n" \
"s_cbranch_scc1 LT54d_%=\n" \
"s_sleep 1\n" \
"s_branch LT54_%=\n" \
"LT54d_%=:\n" \
R_"a[76:79],v236 offset:6144\n" \
M_"v[128:131],a[32:35],a[80:83],v[128:131]\n" \
L_"v241,s[76:79],s65 offen offset:1024 lds\n" \
M_"v[132:135],a[36:39],a[80:83],v[132:135]\n" \
M_"v[136:139],a[40:43],a[80:83],v[136:139]\n" \
M_"v[140:143],a[44:47],a[80:83],v[140:143]\n" \
L_"v242,s[76:79],s65 offen offset:2048 lds\n" \
M_"v[144:147],a[48:51],a[80:83],v[144:147]\n" \
M_"v[148:151],a[52:55],a[80:83],v[148:151]\n" \
M_"v[152:155],a[56:59],a[80:83],v[152:155]\n" \
L_"v249,s[76:79],s65 offen offset:3072 lds\n" \
M_"v[156:159],a[60:63],a[80:83],v[156:159]\n" \
R_"a[80:83],v236 offset:8192\n" \
M_"v[160:163],a[32:35],a[84:87],v[160:163]\n" \
A_"m0,s73,45056\n" \
M_"v[164:167],a[36:39],a[84:87],v[164:167]\n" \
L_"v243,s[76:79],s65 offen lds\n" \
M_"v[168:171],a[40:43],a[84:87],v[168:171]\n" \
M_"v[172:175],a[44:47],a[84:87],v[172:175]\n" \
M_"v[176:179],a[48:51],a[84:87],v[176:179]\n" \
L_"v244,s[76:79],s65 offen offset:1024 lds\n" \
M_"v[180:183],a[52:55],a[84:87],v[180:183]\n" \
M_"v[184:187],a[56:59],a[84:87],v[184:187]\n" \
M_"v[188:191],a[60:63],a[84:87],v[188:191]\n" \
S_"lgkmcnt(0)\n" \
"LT55_%=:\n" \
"s_memrealtime s[18:19]\n" \
S_"lgkmcnt(0)\n" \
"s_sub_u32 s18,s18,s20\n" \
"s_cmp_ge_u32 s18,182\n" \
"s_cbranch_scc1 LT55d_%=\n" \
"s_sleep 1\n" \
"s_branch LT55_%=\n" \
"LT55d_%=:\n" \
R_"a[84:87],v236 offset:10240\n" \
M_"v[192:195],a[32:35],a[88:91],v[192:195]\n" \
L_"v245,s[76:79],s65 offen offset:2048 lds\n" \
M_"v[196:199],a[36:39],a[88:91],v[196:199]\n" \
M_"v[200:203],a[40:43],a[88:91],v[200:203]\n" \
M_"v[204:207],a[44:47],a[88:91],v[204:207]\n" \
M_"v[208:211],a[48:51],a[88:91],v[208:211]\n" \
M_"v[212:215],a[52:55],a[88:91],v[212:215]\n" \
M_"v[216:219],a[56:59],a[88:91],v[216:219]\n" \
M_"v[220:223],a[60:63],a[88:91],v[220:223]\n" \
R_"a[88:91],v236 offset:12288\n" \
S_"lgkmcnt(4)\n" \
M_"v[0:3],a[0:3],a[64:67],v[0:3]\n" \
M_"v[4:7],a[4:7],a[64:67],v[4:7]\n" \
M_"v[8:11],a[8:11],a[64:67],v[8:11]\n" \
M_"v[12:15],a[12:15],a[64:67],v[12:15]\n" \
S_"lgkmcnt(0)\n" \
"LT56_%=:\n" \
"s_memrealtime s[18:19]\n" \
S_"lgkmcnt(0)\n" \
"s_sub_u32 s18,s18,s20\n" \
"s_cmp_ge_u32 s18,206\n" \
"s_cbranch_scc1 LT56d_%=\n" \
"s_sleep 1\n" \
"s_branch LT56_%=\n" \
"LT56d_%=:\n" \
M_"v[16:19],a[16:19],a[64:67],v[16:19]\n" \
M_"v[20:23],a[20:23],a[64:67],v[20:23]\n" \
M_"v[24:27],a[24:27],a[64:67],v[24:27]\n" \
M_"v[28:31],a[28:31],a[64:67],v[28:31]\n" \
S_"vmcnt(15)\n" \
R_"a[64:67],v237 offset:0\n" \
M_"v[32:35],a[0:3],a[68:71],v[32:35]\n" \
T_"a[32:33],v238 offset:0\n" \
M_"v[36:39],a[4:7],a[68:71],v[36:39]\n" \
T_"a[34:35],v238 offset:256\n" \
A_"s64,s64,262144\n" \
"s_cmp_eq_u32 s64,4718592\n" \
"s_cselect_b32 s64,0,s64\n" \
A_"m0,s83,17408\n" \
M_"v[40:43],a[8:11],a[68:71],v[40:43]\n" \
T_"a[36:37],v238 offset:8\n" \
L_"v250,s[68:71],s64 offen lds\n" \
M_"v[44:47],a[12:15],a[68:71],v[44:47]\n" \
T_"a[38:39],v238 offset:264\n" \
M_"v[48:51],a[16:19],a[68:71],v[48:51]\n" \
T_"a[40:41],v238 offset:64\n" \
M_"v[52:55],a[20:23],a[68:71],v[52:55]\n" \
T_"a[42:43],v238 offset:320\n" \
M_"v[56:59],a[24:27],a[68:71],v[56:59]\n" \
T_"a[44:45],v238 offset:72\n" \
L_"v251,s[68:71],s64 offen offset:1088 lds\n" \
M_"v[60:63],a[28:31],a[68:71],v[60:63]\n" \
S_"lgkmcnt(0)\n" \
"LT57_%=:\n" \
"s_memrealtime s[18:19]\n" \
S_"lgkmcnt(0)\n" \
"s_sub_u32 s18,s18,s20\n" \
"s_cmp_ge_u32 s18,229\n" \
"s_cbranch_scc1 LT57d_%=\n" \
"s_sleep 1\n" \
"s_branch LT57_%=\n" \
"LT57d_%=:\n" \
R_"a[68:71],v237 offset:2048\n" \
M_"v[64:67],a[0:3],a[72:75],v[64:67]\n" \
T_"a[46:47],v238 offset:328\n" \
M_"v[68:71],a[4:7],a[72:75],v[68:71]\n" \
T_"a[48:49],v238 offset:128\n" \
M_"v[72:75],a[8:11],a[72:75],v[72:75]\n" \
T_"a[50:51],v238 offset:384\n" \
L_"v252,s[68:71],s64 offen offset:2176 lds\n" \
M_"v[76:79],a[12:15],a[72:75],v[76:79]\n" \
T_"a[52:53],v238 offset:136\n" \
M_"v[80:83],a[16:19],a[72:75],v[80:83]\n" \
T_"a[54:55],v238 offset:392\n" \
M_"v[84:87],a[20:23],a[72:75],v[84:87]\n" \
T_"a[56:57],v238 offset:192\n" \
M_"v[88:91],a[24:27],a[72:75],v[88:91]\n" \
T_"a[58:59],v238 offset:448\n" \
L_"v253,s[68:71],s64 offen offset:3264 lds\n" \
M_"v[92:95],a[28:31],a[72:75],v[92:95]\n" \
R_"a[72:75],v237 offset:4096\n" \
M_"v[96:99],a[0:3],a[76:79],v[96:99]\n" \
T_"a[60:61],v238 offset:200\n" \
M_"v[100:103],a[4:7],a[76:79],v[100:103]\n" \
T_"a[62:63],v238 offset:456\n" \
M_"v[104:107],a[8:11],a[76:79],v[104:107]\n" \
A_"m0,s83,21768\n" \
A_"s60,s64,65536\n" \
M_"v[108:111],a[12:15],a[76:79],v[108:111]\n" \
L_"v250,s[68:71],s60 offen lds\n" \
M_"v[112:115],a[16:19],a[76:79],v[112:115]\n" \
M_"v[116:119],a[20:23],a[76:79],v[116:119]\n" \
M_"v[120:123],a[24:27],a[76:79],v[120:123]\n" \
L_"v251,s[68:71],s60 offen offset:1088 lds\n" \
M_"v[124:127],a[28:31],a[76:79],v[124:127]\n" \
S_"lgkmcnt(0)\n" \
"LT58_%=:\n" \
"s_memrealtime s[18:19]\n" \
S_"lgkmcnt(0)\n" \
"s_sub_u32 s18,s18,s20\n" \
"s_cmp_ge_u32 s18,252\n" \
"s_cbranch_scc1 LT58d_%=\n" \
"s_sleep 1\n" \
"s_branch LT58_%=\n" \
"LT58d_%=:\n" \
R_"a[76:79],v237 offset:6144\n" \
M_"v[128:131],a[0:3],a[80:83],v[128:131]\n" \
M_"v[132:135],a[4:7],a[80:83],v[132:135]\n" \
M_"v[136:139],a[8:11],a[80:83],v[136:139]\n" \
L_"v252,s[68:71],s60 offen offset:2176 lds\n" \
M_"v[140:143],a[12:15],a[80:83],v[140:143]\n" \
M_"v[144:147],a[16:19],a[80:83],v[144:147]\n" \
M_"v[148:151],a[20:23],a[80:83],v[148:151]\n" \
M_"v[152:155],a[24:27],a[80:83],v[152:155]\n" \
L_"v253,s[68:71],s60 offen offset:3264 lds\n" \
M_"v[156:159],a[28:31],a[80:83],v[156:159]\n" \
R_"a[80:83],v237 offset:8192\n" \
M_"v[160:163],a[0:3],a[84:87],v[160:163]\n" \
M_"v[164:167],a[4:7],a[84:87],v[164:167]\n" \
M_"v[168:171],a[8:11],a[84:87],v[168:171]\n" \
M_"v[172:175],a[12:15],a[84:87],v[172:175]\n" \
M_"v[176:179],a[16:19],a[84:87],v[176:179]\n" \
M_"v[180:183],a[20:23],a[84:87],v[180:183]\n" \
M_"v[184:187],a[24:27],a[84:87],v[184:187]\n" \
M_"v[188:191],a[28:31],a[84:87],v[188:191]\n" \
S_"lgkmcnt(0)\n" \
"LT59_%=:\n" \
"s_memrealtime s[18:19]\n" \
S_"lgkmcnt(0)\n" \
"s_sub_u32 s18,s18,s20\n" \
"s_cmp_ge_u32 s18,275\n" \
"s_cbranch_scc1 LT59d_%=\n" \
"s_sleep 1\n" \
"s_branch LT59_%=\n" \
"LT59d_%=:\n" \
R_"a[84:87],v237 offset:10240\n" \
M_"v[192:195],a[0:3],a[88:91],v[192:195]\n" \
M_"v[196:199],a[4:7],a[88:91],v[196:199]\n" \
M_"v[200:203],a[8:11],a[88:91],v[200:203]\n" \
M_"v[204:207],a[12:15],a[88:91],v[204:207]\n" \
M_"v[208:211],a[16:19],a[88:91],v[208:211]\n" \
M_"v[212:215],a[20:23],a[88:91],v[212:215]\n" \
M_"v[216:219],a[24:27],a[88:91],v[216:219]\n" \
M_"v[220:223],a[28:31],a[88:91],v[220:223]\n" \
R_"a[88:91],v237 offset:12288\n" \
S_"lgkmcnt(4)\n" \
M_"v[0:3],a[32:35],a[64:67],v[0:3]\n" \
M_"v[4:7],a[36:39],a[64:67],v[4:7]\n" \
M_"v[8:11],a[40:43],a[64:67],v[8:11]\n" \
M_"v[12:15],a[44:47],a[64:67],v[12:15]\n" \
S_"lgkmcnt(0)\n" \
"LT60_%=:\n" \
"s_memrealtime s[18:19]\n" \
S_"lgkmcnt(0)\n" \
"s_sub_u32 s18,s18,s20\n" \
"s_cmp_ge_u32 s18,299\n" \
"s_cbranch_scc1 LT60d_%=\n" \
"s_sleep 1\n" \
"s_branch LT60_%=\n" \
"LT60d_%=:\n" \
M_"v[16:19],a[48:51],a[64:67],v[16:19]\n" \
M_"v[20:23],a[52:55],a[64:67],v[20:23]\n" \
M_"v[24:27],a[56:59],a[64:67],v[24:27]\n" \
M_"v[28:31],a[60:63],a[64:67],v[28:31]\n" \
"v_mov_b32 v247,2\n" \
S_"vmcnt(8)\n" \
"ds_write_b32 v234,v247 offset:0\n" \
M_"v[32:35],a[32:35],a[68:71],v[32:35]\n" \
T_"a[0:1],v238 offset:8704\n" \
A_"s64,s64,262144\n" \
"s_cmp_eq_u32 s64,4718592\n" \
"s_cselect_b32 s64,0,s64\n" \
A_"m0,s83,0\n" \
M_"v[36:39],a[36:39],a[68:71],v[36:39]\n" \
T_"a[2:3],v238 offset:8960\n" \
L_"v250,s[68:71],s64 offen lds\n" \
M_"v[40:43],a[40:43],a[68:71],v[40:43]\n" \
T_"a[4:5],v238 offset:8712\n" \
M_"v[44:47],a[44:47],a[68:71],v[44:47]\n" \
T_"a[6:7],v238 offset:8968\n" \
L_"v251,s[68:71],s64 offen offset:1088 lds\n" \
M_"v[48:51],a[48:51],a[68:71],v[48:51]\n" \
T_"a[8:9],v238 offset:8768\n" \
M_"v[52:55],a[52:55],a[68:71],v[52:55]\n" \
T_"a[10:11],v238 offset:9024\n" \
L_"v252,s[68:71],s64 offen offset:2176 lds\n" \
M_"v[56:59],a[56:59],a[68:71],v[56:59]\n" \
T_"a[12:13],v238 offset:8776\n" \
M_"v[60:63],a[60:63],a[68:71],v[60:63]\n" \
S_"lgkmcnt(0)\n" \
"LT61_%=:\n" \
"s_memrealtime s[18:19]\n" \
S_"lgkmcnt(0)\n" \
"s_sub_u32 s18,s18,s20\n" \
"s_cmp_ge_u32 s18,322\n" \
"s_cbranch_scc1 LT61d_%=\n" \
"s_sleep 1\n" \
"s_branch LT61_%=\n" \
"LT61d_%=:\n" \
T_"a[14:15],v238 offset:9032\n" \
L_"v253,s[68:71],s64 offen offset:3264 lds\n" \
M_"v[64:67],a[32:35],a[72:75],v[64:67]\n" \
"ds_read_b32 v248,v235 offset:0\n" \
A_"m0,s83,4360\n" \
A_"s60,s64,65536\n" \
M_"v[68:71],a[36:39],a[72:75],v[68:71]\n" \
T_"a[16:17],v238 offset:8832\n" \
M_"v[72:75],a[40:43],a[72:75],v[72:75]\n" \
T_"a[18:19],v238 offset:9088\n" \
L_"v250,s[68:71],s60 offen lds\n" \
M_"v[76:79],a[44:47],a[72:75],v[76:79]\n" \
T_"a[20:21],v238 offset:8840\n" \
M_"v[80:83],a[48:51],a[72:75],v[80:83]\n" \
T_"a[22:23],v238 offset:9096\n" \
L_"v251,s[68:71],s60 offen offset:1088 lds\n" \
M_"v[84:87],a[52:55],a[72:75],v[84:87]\n" \
T_"a[24:25],v238 offset:8896\n" \
S_"lgkmcnt(5)\n" \
"v_readfirstlane_b32 s63,v248\n" \
M_"v[88:91],a[56:59],a[72:75],v[88:91]\n" \
T_"a[26:27],v238 offset:9152\n" \
"s_cmp_ge_u32 s63,2\n" \
"s_cbranch_scc0 LPS1_%=\n" \
"LPS1d_%=:\n" \
M_"v[92:95],a[60:63],a[72:75],v[92:95]\n" \
T_"a[28:29],v238 offset:8904\n" \
L_"v252,s[68:71],s60 offen offset:2176 lds\n" \
M_"v[96:99],a[32:35],a[76:79],v[96:99]\n" \
T_"a[30:31],v238 offset:9160\n" \
L_"v253,s[68:71],s60 offen offset:3264 lds\n" \
M_"v[100:103],a[36:39],a[76:79],v[100:103]\n" \
R_"a[64:67],v236 offset:40960\n" \
M_"v[104:107],a[40:43],a[76:79],v[104:107]\n" \
R_"a[68:71],v236 offset:43008\n" \
M_"v[108:111],a[44:47],a[76:79],v[108:111]\n" \
R_"a[72:75],v236 offset:45056\n" \
M_"v[112:115],a[48:51],a[76:79],v[112:115]\n" \
A_"s65,s65,128\n" \
"s_cmp_eq_u32 s65,1152\n" \
"s_cselect_b32 s65,0,s65\n" \
A_"m0,s73,0\n" \
M_"v[116:119],a[52:55],a[76:79],v[116:119]\n" \
L_"v240,s[76:79],s65 offen lds\n" \
M_"v[120:123],a[56:59],a[76:79],v[120:123]\n" \
M_"v[124:127],a[60:63],a[76:79],v[124:127]\n" \
S_"lgkmcnt(0)\n" \
"LT62_%=:\n" \
"s_memrealtime s[18:19]\n" \
S_"lgkmcnt(0)\n" \
"s_sub_u32 s18,s18,s20\n" \
"s_cmp_ge_u32 s18,345\n" \
"s_cbranch_scc1 LT62d_%=\n" \
"s_sleep 1\n" \
"s_branch LT62_%=\n" \
"LT62d_%=:\n" \
R_"a[76:79],v236 offset:47104\n" \
M_"v[128:131],a[32:35],a[80:83],v[128:131]\n" \
L_"v241,s[76:79],s65 offen offset:1024 lds\n" \
M_"v[132:135],a[36:39],a[80:83],v[132:135]\n" \
M_"v[136:139],a[40:43],a[80:83],v[136:139]\n" \
M_"v[140:143],a[44:47],a[80:83],v[140:143]\n" \
L_"v242,s[76:79],s65 offen offset:2048 lds\n" \
M_"v[144:147],a[48:51],a[80:83],v[144:147]\n" \
M_"v[148:151],a[52:55],a[80:83],v[148:151]\n" \
M_"v[152:155],a[56:59],a[80:83],v[152:155]\n" \
L_"v249,s[76:79],s65 offen offset:3072 lds\n" \
M_"v[156:159],a[60:63],a[80:83],v[156:159]\n" \
R_"a[80:83],v236 offset:49152\n" \
M_"v[160:163],a[32:35],a[84:87],v[160:163]\n" \
A_"m0,s73,4096\n" \
M_"v[164:167],a[36:39],a[84:87],v[164:167]\n" \
L_"v243,s[76:79],s65 offen lds\n" \
M_"v[168:171],a[40:43],a[84:87],v[168:171]\n" \
M_"v[172:175],a[44:47],a[84:87],v[172:175]\n" \
M_"v[176:179],a[48:51],a[84:87],v[176:179]\n" \
L_"v244,s[76:79],s65 offen offset:1024 lds\n" \
M_"v[180:183],a[52:55],a[84:87],v[180:183]\n" \
M_"v[184:187],a[56:59],a[84:87],v[184:187]\n" \
M_"v[188:191],a[60:63],a[84:87],v[188:191]\n" \
S_"lgkmcnt(0)\n" \
"LT63_%=:\n" \
"s_memrealtime s[18:19]\n" \
S_"lgkmcnt(0)\n" \
"s_sub_u32 s18,s18,s20\n" \
"s_cmp_ge_u32 s18,368\n" \
"s_cbranch_scc1 LT63d_%=\n" \
"s_sleep 1\n" \
"s_branch LT63_%=\n" \
"LT63d_%=:\n" \
R_"a[84:87],v236 offset:51200\n" \
M_"v[192:195],a[32:35],a[88:91],v[192:195]\n" \
L_"v245,s[76:79],s65 offen offset:2048 lds\n" \
M_"v[196:199],a[36:39],a[88:91],v[196:199]\n" \
M_"v[200:203],a[40:43],a[88:91],v[200:203]\n" \
M_"v[204:207],a[44:47],a[88:91],v[204:207]\n" \
M_"v[208:211],a[48:51],a[88:91],v[208:211]\n" \
M_"v[212:215],a[52:55],a[88:91],v[212:215]\n" \
M_"v[216:219],a[56:59],a[88:91],v[216:219]\n" \
M_"v[220:223],a[60:63],a[88:91],v[220:223]\n" \
R_"a[88:91],v236 offset:53248\n" \
S_"lgkmcnt(4)\n" \
M_"v[0:3],a[0:3],a[64:67],v[0:3]\n" \
M_"v[4:7],a[4:7],a[64:67],v[4:7]\n" \
M_"v[8:11],a[8:11],a[64:67],v[8:11]\n" \
M_"v[12:15],a[12:15],a[64:67],v[12:15]\n" \
S_"lgkmcnt(0)\n" \
"LT64_%=:\n" \
"s_memrealtime s[18:19]\n" \
S_"lgkmcnt(0)\n" \
"s_sub_u32 s18,s18,s20\n" \
"s_cmp_ge_u32 s18,392\n" \
"s_cbranch_scc1 LT64d_%=\n" \
"s_sleep 1\n" \
"s_branch LT64_%=\n" \
"LT64d_%=:\n" \
M_"v[16:19],a[16:19],a[64:67],v[16:19]\n" \
M_"v[20:23],a[20:23],a[64:67],v[20:23]\n" \
M_"v[24:27],a[24:27],a[64:67],v[24:27]\n" \
M_"v[28:31],a[28:31],a[64:67],v[28:31]\n" \
S_"vmcnt(15)\n" \
R_"a[64:67],v237 offset:40960\n" \
M_"v[32:35],a[0:3],a[68:71],v[32:35]\n" \
T_"a[32:33],v238 offset:17408\n" \
M_"v[36:39],a[4:7],a[68:71],v[36:39]\n" \
T_"a[34:35],v238 offset:17664\n" \
A_"s64,s64,262144\n" \
"s_cmp_eq_u32 s64,4718592\n" \
"s_cselect_b32 s64,0,s64\n" \
A_"m0,s83,8704\n" \
M_"v[40:43],a[8:11],a[68:71],v[40:43]\n" \
T_"a[36:37],v238 offset:17416\n" \
L_"v250,s[68:71],s64 offen lds\n" \
M_"v[44:47],a[12:15],a[68:71],v[44:47]\n" \
T_"a[38:39],v238 offset:17672\n" \
M_"v[48:51],a[16:19],a[68:71],v[48:51]\n" \
T_"a[40:41],v238 offset:17472\n" \
M_"v[52:55],a[20:23],a[68:71],v[52:55]\n" \
T_"a[42:43],v238 offset:17728\n" \
M_"v[56:59],a[24:27],a[68:71],v[56:59]\n" \
T_"a[44:45],v238 offset:17480\n" \
L_"v251,s[68:71],s64 offen offset:1088 lds\n" \
M_"v[60:63],a[28:31],a[68:71],v[60:63]\n" \
S_"lgkmcnt(0)\n" \
"LT65_%=:\n" \
"s_memrealtime s[18:19]\n" \
S_"lgkmcnt(0)\n" \
"s_sub_u32 s18,s18,s20\n" \
"s_cmp_ge_u32 s18,415\n" \
"s_cbranch_scc1 LT65d_%=\n" \
"s_sleep 1\n" \
"s_branch LT65_%=\n" \
"LT65d_%=:\n" \
R_"a[68:71],v237 offset:43008\n" \
M_"v[64:67],a[0:3],a[72:75],v[64:67]\n" \
T_"a[46:47],v238 offset:17736\n" \
M_"v[68:71],a[4:7],a[72:75],v[68:71]\n" \
T_"a[48:49],v238 offset:17536\n" \
M_"v[72:75],a[8:11],a[72:75],v[72:75]\n" \
T_"a[50:51],v238 offset:17792\n" \
L_"v252,s[68:71],s64 offen offset:2176 lds\n" \
M_"v[76:79],a[12:15],a[72:75],v[76:79]\n" \
T_"a[52:53],v238 offset:17544\n" \
M_"v[80:83],a[16:19],a[72:75],v[80:83]\n" \
T_"a[54:55],v238 offset:17800\n" \
M_"v[84:87],a[20:23],a[72:75],v[84:87]\n" \
T_"a[56:57],v238 offset:17600\n" \
M_"v[88:91],a[24:27],a[72:75],v[88:91]\n" \
T_"a[58:59],v238 offset:17856\n" \
L_"v253,s[68:71],s64 offen offset:3264 lds\n" \
M_"v[92:95],a[28:31],a[72:75],v[92:95]\n" \
R_"a[72:75],v237 offset:45056\n" \
M_"v[96:99],a[0:3],a[76:79],v[96:99]\n" \
T_"a[60:61],v238 offset:17608\n" \
M_"v[100:103],a[4:7],a[76:79],v[100:103]\n" \
T_"a[62:63],v238 offset:17864\n" \
M_"v[104:107],a[8:11],a[76:79],v[104:107]\n" \
A_"m0,s83,13064\n" \
A_"s60,s64,65536\n" \
M_"v[108:111],a[12:15],a[76:79],v[108:111]\n" \
L_"v250,s[68:71],s60 offen lds\n" \
M_"v[112:115],a[16:19],a[76:79],v[112:115]\n" \
M_"v[116:119],a[20:23],a[76:79],v[116:119]\n" \
M_"v[120:123],a[24:27],a[76:79],v[120:123]\n" \
L_"v251,s[68:71],s60 offen offset:1088 lds\n" \
M_"v[124:127],a[28:31],a[76:79],v[124:127]\n" \
S_"lgkmcnt(0)\n" \
"LT66_%=:\n" \
"s_memrealtime s[18:19]\n" \
S_"lgkmcnt(0)\n" \
"s_sub_u32 s18,s18,s20\n" \
"s_cmp_ge_u32 s18,438\n" \
"s_cbranch_scc1 LT66d_%=\n" \
"s_sleep 1\n" \
"s_branch LT66_%=\n" \
"LT66d_%=:\n" \
R_"a[76:79],v237 offset:47104\n" \
M_"v[128:131],a[0:3],a[80:83],v[128:131]\n" \
M_"v[132:135],a[4:7],a[80:83],v[132:135]\n" \
M_"v[136:139],a[8:11],a[80:83],v[136:139]\n" \
L_"v252,s[68:71],s60 offen offset:2176 lds\n" \
M_"v[140:143],a[12:15],a[80:83],v[140:143]\n" \
M_"v[144:147],a[16:19],a[80:83],v[144:147]\n" \
M_"v[148:151],a[20:23],a[80:83],v[148:151]\n" \
M_"v[152:155],a[24:27],a[80:83],v[152:155]\n" \
L_"v253,s[68:71],s60 offen offset:3264 lds\n" \
M_"v[156:159],a[28:31],a[80:83],v[156:159]\n" \
R_"a[80:83],v237 offset:49152\n" \
M_"v[160:163],a[0:3],a[84:87],v[160:163]\n" \
M_"v[164:167],a[4:7],a[84:87],v[164:167]\n" \
M_"v[168:171],a[8:11],a[84:87],v[168:171]\n" \
M_"v[172:175],a[12:15],a[84:87],v[172:175]\n" \
M_"v[176:179],a[16:19],a[84:87],v[176:179]\n" \
M_"v[180:183],a[20:23],a[84:87],v[180:183]\n" \
M_"v[184:187],a[24:27],a[84:87],v[184:187]\n" \
M_"v[188:191],a[28:31],a[84:87],v[188:191]\n" \
S_"lgkmcnt(0)\n" \
"LT67_%=:\n" \
"s_memrealtime s[18:19]\n" \
S_"lgkmcnt(0)\n" \
"s_sub_u32 s18,s18,s20\n" \
"s_cmp_ge_u32 s18,461\n" \
"s_cbranch_scc1 LT67d_%=\n" \
"s_sleep 1\n" \
"s_branch LT67_%=\n" \
"LT67d_%=:\n" \
R_"a[84:87],v237 offset:51200\n" \
M_"v[192:195],a[0:3],a[88:91],v[192:195]\n" \
M_"v[196:199],a[4:7],a[88:91],v[196:199]\n" \
M_"v[200:203],a[8:11],a[88:91],v[200:203]\n" \
M_"v[204:207],a[12:15],a[88:91],v[204:207]\n" \
M_"v[208:211],a[16:19],a[88:91],v[208:211]\n" \
M_"v[212:215],a[20:23],a[88:91],v[212:215]\n" \
M_"v[216:219],a[24:27],a[88:91],v[216:219]\n" \
M_"v[220:223],a[28:31],a[88:91],v[220:223]\n" \
R_"a[88:91],v237 offset:53248\n" \
S_"lgkmcnt(4)\n" \
M_"v[0:3],a[32:35],a[64:67],v[0:3]\n" \
M_"v[4:7],a[36:39],a[64:67],v[4:7]\n" \
M_"v[8:11],a[40:43],a[64:67],v[8:11]\n" \
M_"v[12:15],a[44:47],a[64:67],v[12:15]\n" \
S_"lgkmcnt(0)\n" \
"LT68_%=:\n" \
"s_memrealtime s[18:19]\n" \
S_"lgkmcnt(0)\n" \
"s_sub_u32 s18,s18,s20\n" \
"s_cmp_ge_u32 s18,485\n" \
"s_cbranch_scc1 LT68d_%=\n" \
"s_sleep 1\n" \
"s_branch LT68_%=\n" \
"LT68d_%=:\n" \
M_"v[16:19],a[48:51],a[64:67],v[16:19]\n" \
M_"v[20:23],a[52:55],a[64:67],v[20:23]\n" \
M_"v[24:27],a[56:59],a[64:67],v[24:27]\n" \
M_"v[28:31],a[60:63],a[64:67],v[28:31]\n" \
"v_mov_b32 v247,3\n" \
S_"vmcnt(8)\n" \
"ds_write_b32 v234,v247 offset:0\n" \
M_"v[32:35],a[32:35],a[68:71],v[32:35]\n" \
T_"a[0:1],v238 offset:0\n" \
A_"s64,s64,262144\n" \
"s_cmp_eq_u32 s64,4718592\n" \
"s_cselect_b32 s64,0,s64\n" \
A_"m0,s83,17408\n" \
M_"v[36:39],a[36:39],a[68:71],v[36:39]\n" \
T_"a[2:3],v238 offset:256\n" \
L_"v250,s[68:71],s64 offen lds\n" \
M_"v[40:43],a[40:43],a[68:71],v[40:43]\n" \
T_"a[4:5],v238 offset:8\n" \
M_"v[44:47],a[44:47],a[68:71],v[44:47]\n" \
T_"a[6:7],v238 offset:264\n" \
L_"v251,s[68:71],s64 offen offset:1088 lds\n" \
M_"v[48:51],a[48:51],a[68:71],v[48:51]\n" \
T_"a[8:9],v238 offset:64\n" \
M_"v[52:55],a[52:55],a[68:71],v[52:55]\n" \
T_"a[10:11],v238 offset:320\n" \
L_"v252,s[68:71],s64 offen offset:2176 lds\n" \
M_"v[56:59],a[56:59],a[68:71],v[56:59]\n" \
T_"a[12:13],v238 offset:72\n" \
M_"v[60:63],a[60:63],a[68:71],v[60:63]\n" \
S_"lgkmcnt(0)\n" \
"LT69_%=:\n" \
"s_memrealtime s[18:19]\n" \
S_"lgkmcnt(0)\n" \
"s_sub_u32 s18,s18,s20\n" \
"s_cmp_ge_u32 s18,508\n" \
"s_cbranch_scc1 LT69d_%=\n" \
"s_sleep 1\n" \
"s_branch LT69_%=\n" \
"LT69d_%=:\n" \
T_"a[14:15],v238 offset:328\n" \
L_"v253,s[68:71],s64 offen offset:3264 lds\n" \
M_"v[64:67],a[32:35],a[72:75],v[64:67]\n" \
"ds_read_b32 v248,v235 offset:0\n" \
A_"m0,s83,21768\n" \
A_"s60,s64,65536\n" \
M_"v[68:71],a[36:39],a[72:75],v[68:71]\n" \
T_"a[16:17],v238 offset:128\n" \
M_"v[72:75],a[40:43],a[72:75],v[72:75]\n" \
T_"a[18:19],v238 offset:384\n" \
L_"v250,s[68:71],s60 offen lds\n" \
M_"v[76:79],a[44:47],a[72:75],v[76:79]\n" \
T_"a[20:21],v238 offset:136\n" \
M_"v[80:83],a[48:51],a[72:75],v[80:83]\n" \
T_"a[22:23],v238 offset:392\n" \
L_"v251,s[68:71],s60 offen offset:1088 lds\n" \
M_"v[84:87],a[52:55],a[72:75],v[84:87]\n" \
T_"a[24:25],v238 offset:192\n" \
S_"lgkmcnt(5)\n" \
"v_readfirstlane_b32 s63,v248\n" \
M_"v[88:91],a[56:59],a[72:75],v[88:91]\n" \
T_"a[26:27],v238 offset:448\n" \
"s_cmp_ge_u32 s63,3\n" \
"s_cbranch_scc0 LPS2_%=\n" \
"LPS2d_%=:\n" \
M_"v[92:95],a[60:63],a[72:75],v[92:95]\n" \
T_"a[28:29],v238 offset:200\n" \
L_"v252,s[68:71],s60 offen offset:2176 lds\n" \
M_"v[96:99],a[32:35],a[76:79],v[96:99]\n" \
T_"a[30:31],v238 offset:456\n" \
L_"v253,s[68:71],s60 offen offset:3264 lds\n" \
M_"v[100:103],a[36:39],a[76:79],v[100:103]\n" \
R_"a[64:67],v236 offset:0\n" \
M_"v[104:107],a[40:43],a[76:79],v[104:107]\n" \
R_"a[68:71],v236 offset:2048\n" \
M_"v[108:111],a[44:47],a[76:79],v[108:111]\n" \
R_"a[72:75],v236 offset:4096\n" \
M_"v[112:115],a[48:51],a[76:79],v[112:115]\n" \
A_"s65,s65,128\n" \
"s_cmp_eq_u32 s65,1152\n" \
"s_cselect_b32 s65,0,s65\n" \
A_"m0,s73,40960\n" \
M_"v[116:119],a[52:55],a[76:79],v[116:119]\n" \
L_"v240,s[76:79],s65 offen lds\n" \
M_"v[120:123],a[56:59],a[76:79],v[120:123]\n" \
M_"v[124:127],a[60:63],a[76:79],v[124:127]\n" \
S_"lgkmcnt(0)\n" \
"LT70_%=:\n" \
"s_memrealtime s[18:19]\n" \
S_"lgkmcnt(0)\n" \
"s_sub_u32 s18,s18,s20\n" \
"s_cmp_ge_u32 s18,531\n" \
"s_cbranch_scc1 LT70d_%=\n" \
"s_sleep 1\n" \
"s_branch LT70_%=\n" \
"LT70d_%=:\n" \
R_"a[76:79],v236 offset:6144\n" \
M_"v[128:131],a[32:35],a[80:83],v[128:131]\n" \
L_"v241,s[76:79],s65 offen offset:1024 lds\n" \
M_"v[132:135],a[36:39],a[80:83],v[132:135]\n" \
M_"v[136:139],a[40:43],a[80:83],v[136:139]\n" \
M_"v[140:143],a[44:47],a[80:83],v[140:143]\n" \
L_"v242,s[76:79],s65 offen offset:2048 lds\n" \
M_"v[144:147],a[48:51],a[80:83],v[144:147]\n" \
M_"v[148:151],a[52:55],a[80:83],v[148:151]\n" \
M_"v[152:155],a[56:59],a[80:83],v[152:155]\n" \
L_"v249,s[76:79],s65 offen offset:3072 lds\n" \
M_"v[156:159],a[60:63],a[80:83],v[156:159]\n" \
R_"a[80:83],v236 offset:8192\n" \
M_"v[160:163],a[32:35],a[84:87],v[160:163]\n" \
A_"m0,s73,45056\n" \
M_"v[164:167],a[36:39],a[84:87],v[164:167]\n" \
L_"v243,s[76:79],s65 offen lds\n" \
M_"v[168:171],a[40:43],a[84:87],v[168:171]\n" \
M_"v[172:175],a[44:47],a[84:87],v[172:175]\n" \
M_"v[176:179],a[48:51],a[84:87],v[176:179]\n" \
L_"v244,s[76:79],s65 offen offset:1024 lds\n" \
M_"v[180:183],a[52:55],a[84:87],v[180:183]\n" \
M_"v[184:187],a[56:59],a[84:87],v[184:187]\n" \
M_"v[188:191],a[60:63],a[84:87],v[188:191]\n" \
S_"lgkmcnt(0)\n" \
"LT71_%=:\n" \
"s_memrealtime s[18:19]\n" \
S_"lgkmcnt(0)\n" \
"s_sub_u32 s18,s18,s20\n" \
"s_cmp_ge_u32 s18,554\n" \
"s_cbranch_scc1 LT71d_%=\n" \
"s_sleep 1\n" \
"s_branch LT71_%=\n" \
"LT71d_%=:\n" \
R_"a[84:87],v236 offset:10240\n" \
M_"v[192:195],a[32:35],a[88:91],v[192:195]\n" \
L_"v245,s[76:79],s65 offen offset:2048 lds\n" \
M_"v[196:199],a[36:39],a[88:91],v[196:199]\n" \
M_"v[200:203],a[40:43],a[88:91],v[200:203]\n" \
M_"v[204:207],a[44:47],a[88:91],v[204:207]\n" \
M_"v[208:211],a[48:51],a[88:91],v[208:211]\n" \
M_"v[212:215],a[52:55],a[88:91],v[212:215]\n" \
M_"v[216:219],a[56:59],a[88:91],v[216:219]\n" \
M_"v[220:223],a[60:63],a[88:91],v[220:223]\n" \
R_"a[88:91],v236 offset:12288\n" \
S_"lgkmcnt(4)\n" \
M_"v[0:3],a[0:3],a[64:67],v[0:3]\n" \
M_"v[4:7],a[4:7],a[64:67],v[4:7]\n" \
M_"v[8:11],a[8:11],a[64:67],v[8:11]\n" \
M_"v[12:15],a[12:15],a[64:67],v[12:15]\n" \
S_"lgkmcnt(0)\n" \
"LT72_%=:\n" \
"s_memrealtime s[18:19]\n" \
S_"lgkmcnt(0)\n" \
"s_sub_u32 s18,s18,s20\n" \
"s_cmp_ge_u32 s18,578\n" \
"s_cbranch_scc1 LT72d_%=\n" \
"s_sleep 1\n" \
"s_branch LT72_%=\n" \
"LT72d_%=:\n" \
M_"v[16:19],a[16:19],a[64:67],v[16:19]\n" \
M_"v[20:23],a[20:23],a[64:67],v[20:23]\n" \
M_"v[24:27],a[24:27],a[64:67],v[24:27]\n" \
M_"v[28:31],a[28:31],a[64:67],v[28:31]\n" \
S_"vmcnt(15)\n" \
R_"a[64:67],v237 offset:0\n" \
M_"v[32:35],a[0:3],a[68:71],v[32:35]\n" \
T_"a[32:33],v238 offset:8704\n" \
M_"v[36:39],a[4:7],a[68:71],v[36:39]\n" \
T_"a[34:35],v238 offset:8960\n" \
A_"s64,s64,262144\n" \
"s_cmp_eq_u32 s64,4718592\n" \
"s_cselect_b32 s64,0,s64\n" \
A_"m0,s83,0\n" \
M_"v[40:43],a[8:11],a[68:71],v[40:43]\n" \
T_"a[36:37],v238 offset:8712\n" \
L_"v250,s[68:71],s64 offen lds\n" \
M_"v[44:47],a[12:15],a[68:71],v[44:47]\n" \
T_"a[38:39],v238 offset:8968\n" \
M_"v[48:51],a[16:19],a[68:71],v[48:51]\n" \
T_"a[40:41],v238 offset:8768\n" \
M_"v[52:55],a[20:23],a[68:71],v[52:55]\n" \
T_"a[42:43],v238 offset:9024\n" \
M_"v[56:59],a[24:27],a[68:71],v[56:59]\n" \
T_"a[44:45],v238 offset:8776\n" \
L_"v251,s[68:71],s64 offen offset:1088 lds\n" \
M_"v[60:63],a[28:31],a[68:71],v[60:63]\n" \
S_"lgkmcnt(0)\n" \
"LT73_%=:\n" \
"s_memrealtime s[18:19]\n" \
S_"lgkmcnt(0)\n" \
"s_sub_u32 s18,s18,s20\n" \
"s_cmp_ge_u32 s18,601\n" \
"s_cbranch_scc1 LT73d_%=\n" \
"s_sleep 1\n" \
"s_branch LT73_%=\n" \
"LT73d_%=:\n" \
R_"a[68:71],v237 offset:2048\n" \
M_"v[64:67],a[0:3],a[72:75],v[64:67]\n" \
T_"a[46:47],v238 offset:9032\n" \
M_"v[68:71],a[4:7],a[72:75],v[68:71]\n" \
T_"a[48:49],v238 offset:8832\n" \
M_"v[72:75],a[8:11],a[72:75],v[72:75]\n" \
T_"a[50:51],v238 offset:9088\n" \
L_"v252,s[68:71],s64 offen offset:2176 lds\n" \
M_"v[76:79],a[12:15],a[72:75],v[76:79]\n" \
T_"a[52:53],v238 offset:8840\n" \
M_"v[80:83],a[16:19],a[72:75],v[80:83]\n" \
T_"a[54:55],v238 offset:9096\n" \
M_"v[84:87],a[20:23],a[72:75],v[84:87]\n" \
T_"a[56:57],v238 offset:8896\n" \
M_"v[88:91],a[24:27],a[72:75],v[88:91]\n" \
T_"a[58:59],v238 offset:9152\n" \
L_"v253,s[68:71],s64 offen offset:3264 lds\n" \
M_"v[92:95],a[28:31],a[72:75],v[92:95]\n" \
R_"a[72:75],v237 offset:4096\n" \
M_"v[96:99],a[0:3],a[76:79],v[96:99]\n" \
T_"a[60:61],v238 offset:8904\n" \
M_"v[100:103],a[4:7],a[76:79],v[100:103]\n" \
T_"a[62:63],v238 offset:9160\n" \
M_"v[104:107],a[8:11],a[76:79],v[104:107]\n" \
A_"m0,s83,4360\n" \
A_"s60,s64,65536\n" \
M_"v[108:111],a[12:15],a[76:79],v[108:111]\n" \
L_"v250,s[68:71],s60 offen lds\n" \
M_"v[112:115],a[16:19],a[76:79],v[112:115]\n" \
M_"v[116:119],a[20:23],a[76:79],v[116:119]\n" \
M_"v[120:123],a[24:27],a[76:79],v[120:123]\n" \
L_"v251,s[68:71],s60 offen offset:1088 lds\n" \
M_"v[124:127],a[28:31],a[76:79],v[124:127]\n" \
S_"lgkmcnt(0)\n" \
"LT74_%=:\n" \
"s_memrealtime s[18:19]\n" \
S_"lgkmcnt(0)\n" \
"s_sub_u32 s18,s18,s20\n" \
"s_cmp_ge_u32 s18,624\n" \
"s_cbranch_scc1 LT74d_%=\n" \
"s_sleep 1\n" \
"s_branch LT74_%=\n" \
"LT74d_%=:\n" \
R_"a[76:79],v237 offset:6144\n" \
M_"v[128:131],a[0:3],a[80:83],v[128:131]\n" \
M_"v[132:135],a[4:7],a[80:83],v[132:135]\n" \
M_"v[136:139],a[8:11],a[80:83],v[136:139]\n" \
L_"v252,s[68:71],s60 offen offset:2176 lds\n" \
M_"v[140:143],a[12:15],a[80:83],v[140:143]\n" \
M_"v[144:147],a[16:19],a[80:83],v[144:147]\n" \
M_"v[148:151],a[20:23],a[80:83],v[148:151]\n" \
M_"v[152:155],a[24:27],a[80:83],v[152:155]\n" \
L_"v253,s[68:71],s60 offen offset:3264 lds\n" \
M_"v[156:159],a[28:31],a[80:83],v[156:159]\n" \
R_"a[80:83],v237 offset:8192\n" \
M_"v[160:163],a[0:3],a[84:87],v[160:163]\n" \
M_"v[164:167],a[4:7],a[84:87],v[164:167]\n" \
M_"v[168:171],a[8:11],a[84:87],v[168:171]\n" \
M_"v[172:175],a[12:15],a[84:87],v[172:175]\n" \
M_"v[176:179],a[16:19],a[84:87],v[176:179]\n" \
M_"v[180:183],a[20:23],a[84:87],v[180:183]\n" \
M_"v[184:187],a[24:27],a[84:87],v[184:187]\n" \
M_"v[188:191],a[28:31],a[84:87],v[188:191]\n" \
S_"lgkmcnt(0)\n" \
"LT75_%=:\n" \
"s_memrealtime s[18:19]\n" \
S_"lgkmcnt(0)\n" \
"s_sub_u32 s18,s18,s20\n" \
"s_cmp_ge_u32 s18,647\n" \
"s_cbranch_scc1 LT75d_%=\n" \
"s_sleep 1\n" \
"s_branch LT75_%=\n" \
"LT75d_%=:\n" \
R_"a[84:87],v237 offset:10240\n" \
M_"v[192:195],a[0:3],a[88:91],v[192:195]\n" \
M_"v[196:199],a[4:7],a[88:91],v[196:199]\n" \
M_"v[200:203],a[8:11],a[88:91],v[200:203]\n" \
M_"v[204:207],a[12:15],a[88:91],v[204:207]\n" \
M_"v[208:211],a[16:19],a[88:91],v[208:211]\n" \
M_"v[212:215],a[20:23],a[88:91],v[212:215]\n" \
M_"v[216:219],a[24:27],a[88:91],v[216:219]\n" \
M_"v[220:223],a[28:31],a[88:91],v[220:223]\n" \
R_"a[88:91],v237 offset:12288\n" \
S_"lgkmcnt(4)\n" \
M_"v[0:3],a[32:35],a[64:67],v[0:3]\n" \
M_"v[4:7],a[36:39],a[64:67],v[4:7]\n" \
M_"v[8:11],a[40:43],a[64:67],v[8:11]\n" \
M_"v[12:15],a[44:47],a[64:67],v[12:15]\n" \
S_"lgkmcnt(0)\n" \
"LT76_%=:\n" \
"s_memrealtime s[18:19]\n" \
S_"lgkmcnt(0)\n" \
"s_sub_u32 s18,s18,s20\n" \
"s_cmp_ge_u32 s18,671\n" \
"s_cbranch_scc1 LT76d_%=\n" \
"s_sleep 1\n" \
"s_branch LT76_%=\n" \
"LT76d_%=:\n" \
M_"v[16:19],a[48:51],a[64:67],v[16:19]\n" \
M_"v[20:23],a[52:55],a[64:67],v[20:23]\n" \
M_"v[24:27],a[56:59],a[64:67],v[24:27]\n" \
M_"v[28:31],a[60:63],a[64:67],v[28:31]\n" \
"v_mov_b32 v247,4\n" \
S_"vmcnt(8)\n" \
"ds_write_b32 v234,v247 offset:0\n" \
M_"v[32:35],a[32:35],a[68:71],v[32:35]\n" \
T_"a[0:1],v238 offset:17408\n" \
A_"s64,s64,262144\n" \
"s_cmp_eq_u32 s64,4718592\n" \
"s_cselect_b32 s64,0,s64\n" \
A_"m0,s83,8704\n" \
M_"v[36:39],a[36:39],a[68:71],v[36:39]\n" \
T_"a[2:3],v238 offset:17664\n" \
L_"v250,s[68:71],s64 offen lds\n" \
M_"v[40:43],a[40:43],a[68:71],v[40:43]\n" \
T_"a[4:5],v238 offset:17416\n" \
M_"v[44:47],a[44:47],a[68:71],v[44:47]\n" \
T_"a[6:7],v238 offset:17672\n" \
L_"v251,s[68:71],s64 offen offset:1088 lds\n" \
M_"v[48:51],a[48:51],a[68:71],v[48:51]\n" \
T_"a[8:9],v238 offset:17472\n" \
M_"v[52:55],a[52:55],a[68:71],v[52:55]\n" \
T_"a[10:11],v238 offset:17728\n" \
L_"v252,s[68:71],s64 offen offset:2176 lds\n" \
M_"v[56:59],a[56:59],a[68:71],v[56:59]\n" \
T_"a[12:13],v238 offset:17480\n" \
M_"v[60:63],a[60:63],a[68:71],v[60:63]\n" \
S_"lgkmcnt(0)\n" \
"LT77_%=:\n" \
"s_memrealtime s[18:19]\n" \
S_"lgkmcnt(0)\n" \
"s_sub_u32 s18,s18,s20\n" \
"s_cmp_ge_u32 s18,694\n" \
"s_cbranch_scc1 LT77d_%=\n" \
"s_sleep 1\n" \
"s_branch LT77_%=\n" \
"LT77d_%=:\n" \
T_"a[14:15],v238 offset:17736\n" \
L_"v253,s[68:71],s64 offen offset:3264 lds\n" \
M_"v[64:67],a[32:35],a[72:75],v[64:67]\n" \
"ds_read_b32 v248,v235 offset:0\n" \
A_"m0,s83,13064\n" \
A_"s60,s64,65536\n" \
M_"v[68:71],a[36:39],a[72:75],v[68:71]\n" \
T_"a[16:17],v238 offset:17536\n" \
M_"v[72:75],a[40:43],a[72:75],v[72:75]\n" \
T_"a[18:19],v238 offset:17792\n" \
L_"v250,s[68:71],s60 offen lds\n" \
M_"v[76:79],a[44:47],a[72:75],v[76:79]\n" \
T_"a[20:21],v238 offset:17544\n" \
M_"v[80:83],a[48:51],a[72:75],v[80:83]\n" \
T_"a[22:23],v238 offset:17800\n" \
L_"v251,s[68:71],s60 offen offset:1088 lds\n" \
M_"v[84:87],a[52:55],a[72:75],v[84:87]\n" \
T_"a[24:25],v238 offset:17600\n" \
S_"lgkmcnt(5)\n" \
"v_readfirstlane_b32 s63,v248\n" \
M_"v[88:91],a[56:59],a[72:75],v[88:91]\n" \
T_"a[26:27],v238 offset:17856\n" \
"s_cmp_ge_u32 s63,4\n" \
"s_cbranch_scc0 LPS3_%=\n" \
"LPS3d_%=:\n" \
M_"v[92:95],a[60:63],a[72:75],v[92:95]\n" \
T_"a[28:29],v238 offset:17608\n" \
L_"v252,s[68:71],s60 offen offset:2176 lds\n" \
M_"v[96:99],a[32:35],a[76:79],v[96:99]\n" \
T_"a[30:31],v238 offset:17864\n" \
L_"v253,s[68:71],s60 offen offset:3264 lds\n" \
M_"v[100:103],a[36:39],a[76:79],v[100:103]\n" \
R_"a[64:67],v236 offset:40960\n" \
M_"v[104:107],a[40:43],a[76:79],v[104:107]\n" \
R_"a[68:71],v236 offset:43008\n" \
M_"v[108:111],a[44:47],a[76:79],v[108:111]\n" \
R_"a[72:75],v236 offset:45056\n" \
M_"v[112:115],a[48:51],a[76:79],v[112:115]\n" \
A_"s65,s65,128\n" \
"s_cmp_eq_u32 s65,1152\n" \
"s_cselect_b32 s65,0,s65\n" \
A_"m0,s73,0\n" \
M_"v[116:119],a[52:55],a[76:79],v[116:119]\n" \
L_"v240,s[76:79],s65 offen lds\n" \
M_"v[120:123],a[56:59],a[76:79],v[120:123]\n" \
M_"v[124:127],a[60:63],a[76:79],v[124:127]\n" \
S_"lgkmcnt(0)\n" \
"LT78_%=:\n" \
"s_memrealtime s[18:19]\n" \
S_"lgkmcnt(0)\n" \
"s_sub_u32 s18,s18,s20\n" \
"s_cmp_ge_u32 s18,717\n" \
"s_cbranch_scc1 LT78d_%=\n" \
"s_sleep 1\n" \
"s_branch LT78_%=\n" \
"LT78d_%=:\n" \
R_"a[76:79],v236 offset:47104\n" \
M_"v[128:131],a[32:35],a[80:83],v[128:131]\n" \
L_"v241,s[76:79],s65 offen offset:1024 lds\n" \
M_"v[132:135],a[36:39],a[80:83],v[132:135]\n" \
M_"v[136:139],a[40:43],a[80:83],v[136:139]\n" \
M_"v[140:143],a[44:47],a[80:83],v[140:143]\n" \
L_"v242,s[76:79],s65 offen offset:2048 lds\n" \
M_"v[144:147],a[48:51],a[80:83],v[144:147]\n" \
M_"v[148:151],a[52:55],a[80:83],v[148:151]\n" \
M_"v[152:155],a[56:59],a[80:83],v[152:155]\n" \
L_"v249,s[76:79],s65 offen offset:3072 lds\n" \
M_"v[156:159],a[60:63],a[80:83],v[156:159]\n" \
R_"a[80:83],v236 offset:49152\n" \
M_"v[160:163],a[32:35],a[84:87],v[160:163]\n" \
A_"m0,s73,4096\n" \
M_"v[164:167],a[36:39],a[84:87],v[164:167]\n" \
L_"v243,s[76:79],s65 offen lds\n" \
M_"v[168:171],a[40:43],a[84:87],v[168:171]\n" \
M_"v[172:175],a[44:47],a[84:87],v[172:175]\n" \
M_"v[176:179],a[48:51],a[84:87],v[176:179]\n" \
L_"v244,s[76:79],s65 offen offset:1024 lds\n" \
M_"v[180:183],a[52:55],a[84:87],v[180:183]\n" \
M_"v[184:187],a[56:59],a[84:87],v[184:187]\n" \
M_"v[188:191],a[60:63],a[84:87],v[188:191]\n" \
S_"lgkmcnt(0)\n" \
"LT79_%=:\n" \
"s_memrealtime s[18:19]\n" \
S_"lgkmcnt(0)\n" \
"s_sub_u32 s18,s18,s20\n" \
"s_cmp_ge_u32 s18,740\n" \
"s_cbranch_scc1 LT79d_%=\n" \
"s_sleep 1\n" \
"s_branch LT79_%=\n" \
"LT79d_%=:\n" \
R_"a[84:87],v236 offset:51200\n" \
M_"v[192:195],a[32:35],a[88:91],v[192:195]\n" \
L_"v245,s[76:79],s65 offen offset:2048 lds\n" \
M_"v[196:199],a[36:39],a[88:91],v[196:199]\n" \
M_"v[200:203],a[40:43],a[88:91],v[200:203]\n" \
M_"v[204:207],a[44:47],a[88:91],v[204:207]\n" \
M_"v[208:211],a[48:51],a[88:91],v[208:211]\n" \
M_"v[212:215],a[52:55],a[88:91],v[212:215]\n" \
M_"v[216:219],a[56:59],a[88:91],v[216:219]\n" \
M_"v[220:223],a[60:63],a[88:91],v[220:223]\n" \
R_"a[88:91],v236 offset:53248\n" \
S_"lgkmcnt(4)\n" \
M_"v[0:3],a[0:3],a[64:67],v[0:3]\n" \
M_"v[4:7],a[4:7],a[64:67],v[4:7]\n" \
M_"v[8:11],a[8:11],a[64:67],v[8:11]\n" \
M_"v[12:15],a[12:15],a[64:67],v[12:15]\n" \
S_"lgkmcnt(0)\n" \
"LT80_%=:\n" \
"s_memrealtime s[18:19]\n" \
S_"lgkmcnt(0)\n" \
"s_sub_u32 s18,s18,s20\n" \
"s_cmp_ge_u32 s18,764\n" \
"s_cbranch_scc1 LT80d_%=\n" \
"s_sleep 1\n" \
"s_branch LT80_%=\n" \
"LT80d_%=:\n" \
M_"v[16:19],a[16:19],a[64:67],v[16:19]\n" \
M_"v[20:23],a[20:23],a[64:67],v[20:23]\n" \
M_"v[24:27],a[24:27],a[64:67],v[24:27]\n" \
M_"v[28:31],a[28:31],a[64:67],v[28:31]\n" \
S_"vmcnt(15)\n" \
R_"a[64:67],v237 offset:40960\n" \
M_"v[32:35],a[0:3],a[68:71],v[32:35]\n" \
T_"a[32:33],v238 offset:0\n" \
M_"v[36:39],a[4:7],a[68:71],v[36:39]\n" \
T_"a[34:35],v238 offset:256\n" \
A_"s64,s64,262144\n" \
"s_cmp_eq_u32 s64,4718592\n" \
"s_cselect_b32 s64,0,s64\n" \
A_"m0,s83,17408\n" \
M_"v[40:43],a[8:11],a[68:71],v[40:43]\n" \
T_"a[36:37],v238 offset:8\n" \
L_"v250,s[68:71],s64 offen lds\n" \
M_"v[44:47],a[12:15],a[68:71],v[44:47]\n" \
T_"a[38:39],v238 offset:264\n" \
M_"v[48:51],a[16:19],a[68:71],v[48:51]\n" \
T_"a[40:41],v238 offset:64\n" \
M_"v[52:55],a[20:23],a[68:71],v[52:55]\n" \
T_"a[42:43],v238 offset:320\n" \
M_"v[56:59],a[24:27],a[68:71],v[56:59]\n" \
T_"a[44:45],v238 offset:72\n" \
L_"v251,s[68:71],s64 offen offset:1088 lds\n" \
M_"v[60:63],a[28:31],a[68:71],v[60:63]\n" \
S_"lgkmcnt(0)\n" \
"LT81_%=:\n" \
"s_memrealtime s[18:19]\n" \
S_"lgkmcnt(0)\n" \
"s_sub_u32 s18,s18,s20\n" \
"s_cmp_ge_u32 s18,787\n" \
"s_cbranch_scc1 LT81d_%=\n" \
"s_sleep 1\n" \
"s_branch LT81_%=\n" \
"LT81d_%=:\n" \
R_"a[68:71],v237 offset:43008\n" \
M_"v[64:67],a[0:3],a[72:75],v[64:67]\n" \
T_"a[46:47],v238 offset:328\n" \
M_"v[68:71],a[4:7],a[72:75],v[68:71]\n" \
T_"a[48:49],v238 offset:128\n" \
M_"v[72:75],a[8:11],a[72:75],v[72:75]\n" \
T_"a[50:51],v238 offset:384\n" \
L_"v252,s[68:71],s64 offen offset:2176 lds\n" \
M_"v[76:79],a[12:15],a[72:75],v[76:79]\n" \
T_"a[52:53],v238 offset:136\n" \
M_"v[80:83],a[16:19],a[72:75],v[80:83]\n" \
T_"a[54:55],v238 offset:392\n" \
M_"v[84:87],a[20:23],a[72:75],v[84:87]\n" \
T_"a[56:57],v238 offset:192\n" \
M_"v[88:91],a[24:27],a[72:75],v[88:91]\n" \
T_"a[58:59],v238 offset:448\n" \
L_"v253,s[68:71],s64 offen offset:3264 lds\n" \
M_"v[92:95],a[28:31],a[72:75],v[92:95]\n" \
R_"a[72:75],v237 offset:45056\n" \
M_"v[96:99],a[0:3],a[76:79],v[96:99]\n" \
T_"a[60:61],v238 offset:200\n" \
M_"v[100:103],a[4:7],a[76:79],v[100:103]\n" \
T_"a[62:63],v238 offset:456\n" \
M_"v[104:107],a[8:11],a[76:79],v[104:107]\n" \
A_"m0,s83,21768\n" \
A_"s60,s64,65536\n" \
M_"v[108:111],a[12:15],a[76:79],v[108:111]\n" \
L_"v250,s[68:71],s60 offen lds\n" \
M_"v[112:115],a[16:19],a[76:79],v[112:115]\n" \
M_"v[116:119],a[20:23],a[76:79],v[116:119]\n" \
M_"v[120:123],a[24:27],a[76:79],v[120:123]\n" \
L_"v251,s[68:71],s60 offen offset:1088 lds\n" \
M_"v[124:127],a[28:31],a[76:79],v[124:127]\n" \
S_"lgkmcnt(0)\n" \
"LT82_%=:\n" \
"s_memrealtime s[18:19]\n" \
S_"lgkmcnt(0)\n" \
"s_sub_u32 s18,s18,s20\n" \
"s_cmp_ge_u32 s18,810\n" \
"s_cbranch_scc1 LT82d_%=\n" \
"s_sleep 1\n" \
"s_branch LT82_%=\n" \
"LT82d_%=:\n" \
R_"a[76:79],v237 offset:47104\n" \
M_"v[128:131],a[0:3],a[80:83],v[128:131]\n" \
M_"v[132:135],a[4:7],a[80:83],v[132:135]\n" \
M_"v[136:139],a[8:11],a[80:83],v[136:139]\n" \
L_"v252,s[68:71],s60 offen offset:2176 lds\n" \
M_"v[140:143],a[12:15],a[80:83],v[140:143]\n" \
M_"v[144:147],a[16:19],a[80:83],v[144:147]\n" \
M_"v[148:151],a[20:23],a[80:83],v[148:151]\n" \
M_"v[152:155],a[24:27],a[80:83],v[152:155]\n" \
L_"v253,s[68:71],s60 offen offset:3264 lds\n" \
M_"v[156:159],a[28:31],a[80:83],v[156:159]\n" \
R_"a[80:83],v237 offset:49152\n" \
M_"v[160:163],a[0:3],a[84:87],v[160:163]\n" \
M_"v[164:167],a[4:7],a[84:87],v[164:167]\n" \
M_"v[168:171],a[8:11],a[84:87],v[168:171]\n" \
M_"v[172:175],a[12:15],a[84:87],v[172:175]\n" \
M_"v[176:179],a[16:19],a[84:87],v[176:179]\n" \
M_"v[180:183],a[20:23],a[84:87],v[180:183]\n" \
M_"v[184:187],a[24:27],a[84:87],v[184:187]\n" \
M_"v[188:191],a[28:31],a[84:87],v[188:191]\n" \
S_"lgkmcnt(0)\n" \
"LT83_%=:\n" \
"s_memrealtime s[18:19]\n" \
S_"lgkmcnt(0)\n" \
"s_sub_u32 s18,s18,s20\n" \
"s_cmp_ge_u32 s18,833\n" \
"s_cbranch_scc1 LT83d_%=\n" \
"s_sleep 1\n" \
"s_branch LT83_%=\n" \
"LT83d_%=:\n" \
R_"a[84:87],v237 offset:51200\n" \
M_"v[192:195],a[0:3],a[88:91],v[192:195]\n" \
M_"v[196:199],a[4:7],a[88:91],v[196:199]\n" \
M_"v[200:203],a[8:11],a[88:91],v[200:203]\n" \
M_"v[204:207],a[12:15],a[88:91],v[204:207]\n" \
M_"v[208:211],a[16:19],a[88:91],v[208:211]\n" \
M_"v[212:215],a[20:23],a[88:91],v[212:215]\n" \
M_"v[216:219],a[24:27],a[88:91],v[216:219]\n" \
M_"v[220:223],a[28:31],a[88:91],v[220:223]\n" \
R_"a[88:91],v237 offset:53248\n" \
S_"lgkmcnt(4)\n" \
M_"v[0:3],a[32:35],a[64:67],v[0:3]\n" \
M_"v[4:7],a[36:39],a[64:67],v[4:7]\n" \
M_"v[8:11],a[40:43],a[64:67],v[8:11]\n" \
M_"v[12:15],a[44:47],a[64:67],v[12:15]\n" \
S_"lgkmcnt(0)\n" \
"LT84_%=:\n" \
"s_memrealtime s[18:19]\n" \
S_"lgkmcnt(0)\n" \
"s_sub_u32 s18,s18,s20\n" \
"s_cmp_ge_u32 s18,857\n" \
"s_cbranch_scc1 LT84d_%=\n" \
"s_sleep 1\n" \
"s_branch LT84_%=\n" \
"LT84d_%=:\n" \
M_"v[16:19],a[48:51],a[64:67],v[16:19]\n" \
M_"v[20:23],a[52:55],a[64:67],v[20:23]\n" \
M_"v[24:27],a[56:59],a[64:67],v[24:27]\n" \
M_"v[28:31],a[60:63],a[64:67],v[28:31]\n" \
"v_mov_b32 v247,5\n" \
S_"vmcnt(8)\n" \
"ds_write_b32 v234,v247 offset:0\n" \
M_"v[32:35],a[32:35],a[68:71],v[32:35]\n" \
T_"a[0:1],v238 offset:8704\n" \
A_"s64,s64,262144\n" \
"s_cmp_eq_u32 s64,4718592\n" \
"s_cselect_b32 s64,0,s64\n" \
A_"m0,s83,0\n" \
M_"v[36:39],a[36:39],a[68:71],v[36:39]\n" \
T_"a[2:3],v238 offset:8960\n" \
L_"v250,s[68:71],s64 offen lds\n" \
M_"v[40:43],a[40:43],a[68:71],v[40:43]\n" \
T_"a[4:5],v238 offset:8712\n" \
M_"v[44:47],a[44:47],a[68:71],v[44:47]\n" \
T_"a[6:7],v238 offset:8968\n" \
L_"v251,s[68:71],s64 offen offset:1088 lds\n" \
M_"v[48:51],a[48:51],a[68:71],v[48:51]\n" \
T_"a[8:9],v238 offset:8768\n" \
M_"v[52:55],a[52:55],a[68:71],v[52:55]\n" \
T_"a[10:11],v238 offset:9024\n" \
L_"v252,s[68:71],s64 offen offset:2176 lds\n" \
M_"v[56:59],a[56:59],a[68:71],v[56:59]\n" \
T_"a[12:13],v238 offset:8776\n" \
M_"v[60:63],a[60:63],a[68:71],v[60:63]\n" \
S_"lgkmcnt(0)\n" \
"LT85_%=:\n" \
"s_memrealtime s[18:19]\n" \
S_"lgkmcnt(0)\n" \
"s_sub_u32 s18,s18,s20\n" \
"s_cmp_ge_u32 s18,880\n" \
"s_cbranch_scc1 LT85d_%=\n" \
"s_sleep 1\n" \
"s_branch LT85_%=\n" \
"LT85d_%=:\n" \
T_"a[14:15],v238 offset:9032\n" \
L_"v253,s[68:71],s64 offen offset:3264 lds\n" \
M_"v[64:67],a[32:35],a[72:75],v[64:67]\n" \
"ds_read_b32 v248,v235 offset:0\n" \
A_"m0,s83,4360\n" \
A_"s60,s64,65536\n" \
M_"v[68:71],a[36:39],a[72:75],v[68:71]\n" \
T_"a[16:17],v238 offset:8832\n" \
M_"v[72:75],a[40:43],a[72:75],v[72:75]\n" \
T_"a[18:19],v238 offset:9088\n" \
L_"v250,s[68:71],s60 offen lds\n" \
M_"v[76:79],a[44:47],a[72:75],v[76:79]\n" \
T_"a[20:21],v238 offset:8840\n" \
M_"v[80:83],a[48:51],a[72:75],v[80:83]\n" \
T_"a[22:23],v238 offset:9096\n" \
L_"v251,s[68:71],s60 offen offset:1088 lds\n" \
M_"v[84:87],a[52:55],a[72:75],v[84:87]\n" \
T_"a[24:25],v238 offset:8896\n" \
S_"lgkmcnt(5)\n" \
"v_readfirstlane_b32 s63,v248\n" \
M_"v[88:91],a[56:59],a[72:75],v[88:91]\n" \
T_"a[26:27],v238 offset:9152\n" \
"s_cmp_ge_u32 s63,5\n" \
"s_cbranch_scc0 LPS4_%=\n" \
"LPS4d_%=:\n" \
M_"v[92:95],a[60:63],a[72:75],v[92:95]\n" \
T_"a[28:29],v238 offset:8904\n" \
L_"v252,s[68:71],s60 offen offset:2176 lds\n" \
M_"v[96:99],a[32:35],a[76:79],v[96:99]\n" \
T_"a[30:31],v238 offset:9160\n" \
L_"v253,s[68:71],s60 offen offset:3264 lds\n" \
M_"v[100:103],a[36:39],a[76:79],v[100:103]\n" \
R_"a[64:67],v236 offset:0\n" \
M_"v[104:107],a[40:43],a[76:79],v[104:107]\n" \
R_"a[68:71],v236 offset:2048\n" \
M_"v[108:111],a[44:47],a[76:79],v[108:111]\n" \
R_"a[72:75],v236 offset:4096\n" \
M_"v[112:115],a[48:51],a[76:79],v[112:115]\n" \
A_"s65,s65,128\n" \
"s_cmp_eq_u32 s65,1152\n" \
"s_cselect_b32 s65,0,s65\n" \
A_"m0,s73,40960\n" \
M_"v[116:119],a[52:55],a[76:79],v[116:119]\n" \
L_"v240,s[76:79],s65 offen lds\n" \
M_"v[120:123],a[56:59],a[76:79],v[120:123]\n" \
M_"v[124:127],a[60:63],a[76:79],v[124:127]\n" \
S_"lgkmcnt(0)\n" \
"LT86_%=:\n" \
"s_memrealtime s[18:19]\n" \
S_"lgkmcnt(0)\n" \
"s_sub_u32 s18,s18,s20\n" \
"s_cmp_ge_u32 s18,903\n" \
"s_cbranch_scc1 LT86d_%=\n" \
"s_sleep 1\n" \
"s_branch LT86_%=\n" \
"LT86d_%=:\n" \
R_"a[76:79],v236 offset:6144\n" \
M_"v[128:131],a[32:35],a[80:83],v[128:131]\n" \
L_"v241,s[76:79],s65 offen offset:1024 lds\n" \
M_"v[132:135],a[36:39],a[80:83],v[132:135]\n" \
M_"v[136:139],a[40:43],a[80:83],v[136:139]\n" \
M_"v[140:143],a[44:47],a[80:83],v[140:143]\n" \
L_"v242,s[76:79],s65 offen offset:2048 lds\n" \
M_"v[144:147],a[48:51],a[80:83],v[144:147]\n" \
M_"v[148:151],a[52:55],a[80:83],v[148:151]\n" \
M_"v[152:155],a[56:59],a[80:83],v[152:155]\n" \
L_"v249,s[76:79],s65 offen offset:3072 lds\n" \
M_"v[156:159],a[60:63],a[80:83],v[156:159]\n" \
R_"a[80:83],v236 offset:8192\n" \
M_"v[160:163],a[32:35],a[84:87],v[160:163]\n" \
A_"m0,s73,45056\n" \
M_"v[164:167],a[36:39],a[84:87],v[164:167]\n" \
L_"v243,s[76:79],s65 offen lds\n" \
M_"v[168:171],a[40:43],a[84:87],v[168:171]\n" \
M_"v[172:175],a[44:47],a[84:87],v[172:175]\n" \
M_"v[176:179],a[48:51],a[84:87],v[176:179]\n" \
L_"v244,s[76:79],s65 offen offset:1024 lds\n" \
M_"v[180:183],a[52:55],a[84:87],v[180:183]\n" \
M_"v[184:187],a[56:59],a[84:87],v[184:187]\n" \
M_"v[188:191],a[60:63],a[84:87],v[188:191]\n" \
S_"lgkmcnt(0)\n" \
"LT87_%=:\n" \
"s_memrealtime s[18:19]\n" \
S_"lgkmcnt(0)\n" \
"s_sub_u32 s18,s18,s20\n" \
"s_cmp_ge_u32 s18,926\n" \
"s_cbranch_scc1 LT87d_%=\n" \
"s_sleep 1\n" \
"s_branch LT87_%=\n" \
"LT87d_%=:\n" \
R_"a[84:87],v236 offset:10240\n" \
M_"v[192:195],a[32:35],a[88:91],v[192:195]\n" \
L_"v245,s[76:79],s65 offen offset:2048 lds\n" \
M_"v[196:199],a[36:39],a[88:91],v[196:199]\n" \
M_"v[200:203],a[40:43],a[88:91],v[200:203]\n" \
M_"v[204:207],a[44:47],a[88:91],v[204:207]\n" \
M_"v[208:211],a[48:51],a[88:91],v[208:211]\n" \
M_"v[212:215],a[52:55],a[88:91],v[212:215]\n" \
M_"v[216:219],a[56:59],a[88:91],v[216:219]\n" \
M_"v[220:223],a[60:63],a[88:91],v[220:223]\n" \
R_"a[88:91],v236 offset:12288\n" \
S_"lgkmcnt(4)\n" \
M_"v[0:3],a[0:3],a[64:67],v[0:3]\n" \
M_"v[4:7],a[4:7],a[64:67],v[4:7]\n" \
M_"v[8:11],a[8:11],a[64:67],v[8:11]\n" \
M_"v[12:15],a[12:15],a[64:67],v[12:15]\n" \
S_"lgkmcnt(0)\n" \
"LT88_%=:\n" \
"s_memrealtime s[18:19]\n" \
S_"lgkmcnt(0)\n" \
"s_sub_u32 s18,s18,s20\n" \
"s_cmp_ge_u32 s18,950\n" \
"s_cbranch_scc1 LT88d_%=\n" \
"s_sleep 1\n" \
"s_branch LT88_%=\n" \
"LT88d_%=:\n" \
M_"v[16:19],a[16:19],a[64:67],v[16:19]\n" \
M_"v[20:23],a[20:23],a[64:67],v[20:23]\n" \
M_"v[24:27],a[24:27],a[64:67],v[24:27]\n" \
M_"v[28:31],a[28:31],a[64:67],v[28:31]\n" \
S_"vmcnt(15)\n" \
R_"a[64:67],v237 offset:0\n" \
M_"v[32:35],a[0:3],a[68:71],v[32:35]\n" \
T_"a[32:33],v238 offset:17408\n" \
M_"v[36:39],a[4:7],a[68:71],v[36:39]\n" \
T_"a[34:35],v238 offset:17664\n" \
A_"s64,s64,262144\n" \
"s_cmp_eq_u32 s64,4718592\n" \
"s_cselect_b32 s64,0,s64\n" \
A_"m0,s83,8704\n" \
M_"v[40:43],a[8:11],a[68:71],v[40:43]\n" \
T_"a[36:37],v238 offset:17416\n" \
L_"v250,s[68:71],s64 offen lds\n" \
M_"v[44:47],a[12:15],a[68:71],v[44:47]\n" \
T_"a[38:39],v238 offset:17672\n" \
M_"v[48:51],a[16:19],a[68:71],v[48:51]\n" \
T_"a[40:41],v238 offset:17472\n" \
M_"v[52:55],a[20:23],a[68:71],v[52:55]\n" \
T_"a[42:43],v238 offset:17728\n" \
M_"v[56:59],a[24:27],a[68:71],v[56:59]\n" \
T_"a[44:45],v238 offset:17480\n" \
L_"v251,s[68:71],s64 offen offset:1088 lds\n" \
M_"v[60:63],a[28:31],a[68:71],v[60:63]\n" \
S_"lgkmcnt(0)\n" \
"LT89_%=:\n" \
"s_memrealtime s[18:19]\n" \
S_"lgkmcnt(0)\n" \
"s_sub_u32 s18,s18,s20\n" \
"s_cmp_ge_u32 s18,973\n" \
"s_cbranch_scc1 LT89d_%=\n" \
"s_sleep 1\n" \
"s_branch LT89_%=\n" \
"LT89d_%=:\n" \
R_"a[68:71],v237 offset:2048\n" \
M_"v[64:67],a[0:3],a[72:75],v[64:67]\n" \
T_"a[46:47],v238 offset:17736\n" \
M_"v[68:71],a[4:7],a[72:75],v[68:71]\n" \
T_"a[48:49],v238 offset:17536\n" \
M_"v[72:75],a[8:11],a[72:75],v[72:75]\n" \
T_"a[50:51],v238 offset:17792\n" \
L_"v252,s[68:71],s64 offen offset:2176 lds\n" \
M_"v[76:79],a[12:15],a[72:75],v[76:79]\n" \
T_"a[52:53],v238 offset:17544\n" \
M_"v[80:83],a[16:19],a[72:75],v[80:83]\n" \
T_"a[54:55],v238 offset:17800\n" \
M_"v[84:87],a[20:23],a[72:75],v[84:87]\n" \
T_"a[56:57],v238 offset:17600\n" \
M_"v[88:91],a[24:27],a[72:75],v[88:91]\n" \
T_"a[58:59],v238 offset:17856\n" \
L_"v253,s[68:71],s64 offen offset:3264 lds\n" \
M_"v[92:95],a[28:31],a[72:75],v[92:95]\n" \
R_"a[72:75],v237 offset:4096\n" \
M_"v[96:99],a[0:3],a[76:79],v[96:99]\n" \
T_"a[60:61],v238 offset:17608\n" \
M_"v[100:103],a[4:7],a[76:79],v[100:103]\n" \
T_"a[62:63],v238 offset:17864\n" \
M_"v[104:107],a[8:11],a[76:79],v[104:107]\n" \
A_"m0,s83,13064\n" \
A_"s60,s64,65536\n" \
M_"v[108:111],a[12:15],a[76:79],v[108:111]\n" \
L_"v250,s[68:71],s60 offen lds\n" \
M_"v[112:115],a[16:19],a[76:79],v[112:115]\n" \
M_"v[116:119],a[20:23],a[76:79],v[116:119]\n" \
M_"v[120:123],a[24:27],a[76:79],v[120:123]\n" \
L_"v251,s[68:71],s60 offen offset:1088 lds\n" \
M_"v[124:127],a[28:31],a[76:79],v[124:127]\n" \
S_"lgkmcnt(0)\n" \
"LT90_%=:\n" \
"s_memrealtime s[18:19]\n" \
S_"lgkmcnt(0)\n" \
"s_sub_u32 s18,s18,s20\n" \
"s_cmp_ge_u32 s18,996\n" \
"s_cbranch_scc1 LT90d_%=\n" \
"s_sleep 1\n" \
"s_branch LT90_%=\n" \
"LT90d_%=:\n" \
R_"a[76:79],v237 offset:6144\n" \
M_"v[128:131],a[0:3],a[80:83],v[128:131]\n" \
M_"v[132:135],a[4:7],a[80:83],v[132:135]\n" \
M_"v[136:139],a[8:11],a[80:83],v[136:139]\n" \
L_"v252,s[68:71],s60 offen offset:2176 lds\n" \
M_"v[140:143],a[12:15],a[80:83],v[140:143]\n" \
M_"v[144:147],a[16:19],a[80:83],v[144:147]\n" \
M_"v[148:151],a[20:23],a[80:83],v[148:151]\n" \
M_"v[152:155],a[24:27],a[80:83],v[152:155]\n" \
L_"v253,s[68:71],s60 offen offset:3264 lds\n" \
M_"v[156:159],a[28:31],a[80:83],v[156:159]\n" \
R_"a[80:83],v237 offset:8192\n" \
M_"v[160:163],a[0:3],a[84:87],v[160:163]\n" \
M_"v[164:167],a[4:7],a[84:87],v[164:167]\n" \
M_"v[168:171],a[8:11],a[84:87],v[168:171]\n" \
M_"v[172:175],a[12:15],a[84:87],v[172:175]\n" \
M_"v[176:179],a[16:19],a[84:87],v[176:179]\n" \
M_"v[180:183],a[20:23],a[84:87],v[180:183]\n" \
M_"v[184:187],a[24:27],a[84:87],v[184:187]\n" \
M_"v[188:191],a[28:31],a[84:87],v[188:191]\n" \
S_"lgkmcnt(0)\n" \
"LT91_%=:\n" \
"s_memrealtime s[18:19]\n" \
S_"lgkmcnt(0)\n" \
"s_sub_u32 s18,s18,s20\n" \
"s_cmp_ge_u32 s18,1019\n" \
"s_cbranch_scc1 LT91d_%=\n" \
"s_sleep 1\n" \
"s_branch LT91_%=\n" \
"LT91d_%=:\n" \
R_"a[84:87],v237 offset:10240\n" \
M_"v[192:195],a[0:3],a[88:91],v[192:195]\n" \
M_"v[196:199],a[4:7],a[88:91],v[196:199]\n" \
M_"v[200:203],a[8:11],a[88:91],v[200:203]\n" \
M_"v[204:207],a[12:15],a[88:91],v[204:207]\n" \
M_"v[208:211],a[16:19],a[88:91],v[208:211]\n" \
M_"v[212:215],a[20:23],a[88:91],v[212:215]\n" \
M_"v[216:219],a[24:27],a[88:91],v[216:219]\n" \
M_"v[220:223],a[28:31],a[88:91],v[220:223]\n" \
R_"a[88:91],v237 offset:12288\n" \
S_"lgkmcnt(4)\n" \
M_"v[0:3],a[32:35],a[64:67],v[0:3]\n" \
M_"v[4:7],a[36:39],a[64:67],v[4:7]\n" \
M_"v[8:11],a[40:43],a[64:67],v[8:11]\n" \
M_"v[12:15],a[44:47],a[64:67],v[12:15]\n" \
S_"lgkmcnt(0)\n" \
"LT92_%=:\n" \
"s_memrealtime s[18:19]\n" \
S_"lgkmcnt(0)\n" \
"s_sub_u32 s18,s18,s20\n" \
"s_cmp_ge_u32 s18,1043\n" \
"s_cbranch_scc1 LT92d_%=\n" \
"s_sleep 1\n" \
"s_branch LT92_%=\n" \
"LT92d_%=:\n" \
M_"v[16:19],a[48:51],a[64:67],v[16:19]\n" \
M_"v[20:23],a[52:55],a[64:67],v[20:23]\n" \
M_"v[24:27],a[56:59],a[64:67],v[24:27]\n" \
M_"v[28:31],a[60:63],a[64:67],v[28:31]\n" \
"v_mov_b32 v247,6\n" \
S_"vmcnt(8)\n" \
"ds_write_b32 v234,v247 offset:0\n" \
M_"v[32:35],a[32:35],a[68:71],v[32:35]\n" \
T_"a[0:1],v238 offset:0\n" \
A_"s64,s64,262144\n" \
"s_cmp_eq_u32 s64,4718592\n" \
"s_cselect_b32 s64,0,s64\n" \
A_"m0,s83,17408\n" \
M_"v[36:39],a[36:39],a[68:71],v[36:39]\n" \
T_"a[2:3],v238 offset:256\n" \
L_"v250,s[68:71],s64 offen lds\n" \
M_"v[40:43],a[40:43],a[68:71],v[40:43]\n" \
T_"a[4:5],v238 offset:8\n" \
M_"v[44:47],a[44:47],a[68:71],v[44:47]\n" \
T_"a[6:7],v238 offset:264\n" \
L_"v251,s[68:71],s64 offen offset:1088 lds\n" \
M_"v[48:51],a[48:51],a[68:71],v[48:51]\n" \
T_"a[8:9],v238 offset:64\n" \
M_"v[52:55],a[52:55],a[68:71],v[52:55]\n" \
T_"a[10:11],v238 offset:320\n" \
L_"v252,s[68:71],s64 offen offset:2176 lds\n" \
M_"v[56:59],a[56:59],a[68:71],v[56:59]\n" \
T_"a[12:13],v238 offset:72\n" \
M_"v[60:63],a[60:63],a[68:71],v[60:63]\n" \
S_"lgkmcnt(0)\n" \
"LT93_%=:\n" \
"s_memrealtime s[18:19]\n" \
S_"lgkmcnt(0)\n" \
"s_sub_u32 s18,s18,s20\n" \
"s_cmp_ge_u32 s18,1066\n" \
"s_cbranch_scc1 LT93d_%=\n" \
"s_sleep 1\n" \
"s_branch LT93_%=\n" \
"LT93d_%=:\n" \
T_"a[14:15],v238 offset:328\n" \
L_"v253,s[68:71],s64 offen offset:3264 lds\n" \
M_"v[64:67],a[32:35],a[72:75],v[64:67]\n" \
"ds_read_b32 v248,v235 offset:0\n" \
A_"m0,s83,21768\n" \
A_"s60,s64,65536\n" \
M_"v[68:71],a[36:39],a[72:75],v[68:71]\n" \
T_"a[16:17],v238 offset:128\n" \
M_"v[72:75],a[40:43],a[72:75],v[72:75]\n" \
T_"a[18:19],v238 offset:384\n" \
L_"v250,s[68:71],s60 offen lds\n" \
M_"v[76:79],a[44:47],a[72:75],v[76:79]\n" \
T_"a[20:21],v238 offset:136\n" \
M_"v[80:83],a[48:51],a[72:75],v[80:83]\n" \
T_"a[22:23],v238 offset:392\n" \
L_"v251,s[68:71],s60 offen offset:1088 lds\n" \
M_"v[84:87],a[52:55],a[72:75],v[84:87]\n" \
T_"a[24:25],v238 offset:192\n" \
S_"lgkmcnt(5)\n" \
"v_readfirstlane_b32 s63,v248\n" \
M_"v[88:91],a[56:59],a[72:75],v[88:91]\n" \
T_"a[26:27],v238 offset:448\n" \
"s_cmp_ge_u32 s63,6\n" \
"s_cbranch_scc0 LPS5_%=\n" \
"LPS5d_%=:\n" \
M_"v[92:95],a[60:63],a[72:75],v[92:95]\n" \
T_"a[28:29],v238 offset:200\n" \
L_"v252,s[68:71],s60 offen offset:2176 lds\n" \
M_"v[96:99],a[32:35],a[76:79],v[96:99]\n" \
T_"a[30:31],v238 offset:456\n" \
L_"v253,s[68:71],s60 offen offset:3264 lds\n" \
M_"v[100:103],a[36:39],a[76:79],v[100:103]\n" \
R_"a[64:67],v236 offset:40960\n" \
M_"v[104:107],a[40:43],a[76:79],v[104:107]\n" \
R_"a[68:71],v236 offset:43008\n" \
M_"v[108:111],a[44:47],a[76:79],v[108:111]\n" \
R_"a[72:75],v236 offset:45056\n" \
M_"v[112:115],a[48:51],a[76:79],v[112:115]\n" \
A_"s65,s65,128\n" \
"s_cmp_eq_u32 s65,1152\n" \
"s_cselect_b32 s65,0,s65\n" \
A_"m0,s73,0\n" \
M_"v[116:119],a[52:55],a[76:79],v[116:119]\n" \
L_"v240,s[76:79],s65 offen lds\n" \
M_"v[120:123],a[56:59],a[76:79],v[120:123]\n" \
M_"v[124:127],a[60:63],a[76:79],v[124:127]\n" \
S_"lgkmcnt(0)\n" \
"LT94_%=:\n" \
"s_memrealtime s[18:19]\n" \
S_"lgkmcnt(0)\n" \
"s_sub_u32 s18,s18,s20\n" \
"s_cmp_ge_u32 s18,1089\n" \
"s_cbranch_scc1 LT94d_%=\n" \
"s_sleep 1\n" \
"s_branch LT94_%=\n" \
"LT94d_%=:\n" \
R_"a[76:79],v236 offset:47104\n" \
M_"v[128:131],a[32:35],a[80:83],v[128:131]\n" \
L_"v241,s[76:79],s65 offen offset:1024 lds\n" \
M_"v[132:135],a[36:39],a[80:83],v[132:135]\n" \
M_"v[136:139],a[40:43],a[80:83],v[136:139]\n" \
M_"v[140:143],a[44:47],a[80:83],v[140:143]\n" \
L_"v242,s[76:79],s65 offen offset:2048 lds\n" \
M_"v[144:147],a[48:51],a[80:83],v[144:147]\n" \
M_"v[148:151],a[52:55],a[80:83],v[148:151]\n" \
M_"v[152:155],a[56:59],a[80:83],v[152:155]\n" \
L_"v249,s[76:79],s65 offen offset:3072 lds\n" \
M_"v[156:159],a[60:63],a[80:83],v[156:159]\n" \
R_"a[80:83],v236 offset:49152\n" \
M_"v[160:163],a[32:35],a[84:87],v[160:163]\n" \
A_"m0,s73,4096\n" \
M_"v[164:167],a[36:39],a[84:87],v[164:167]\n" \
L_"v243,s[76:79],s65 offen lds\n" \
M_"v[168:171],a[40:43],a[84:87],v[168:171]\n" \
M_"v[172:175],a[44:47],a[84:87],v[172:175]\n" \
M_"v[176:179],a[48:51],a[84:87],v[176:179]\n" \
L_"v244,s[76:79],s65 offen offset:1024 lds\n" \
M_"v[180:183],a[52:55],a[84:87],v[180:183]\n" \
M_"v[184:187],a[56:59],a[84:87],v[184:187]\n" \
M_"v[188:191],a[60:63],a[84:87],v[188:191]\n" \
S_"lgkmcnt(0)\n" \
"LT95_%=:\n" \
"s_memrealtime s[18:19]\n" \
S_"lgkmcnt(0)\n" \
"s_sub_u32 s18,s18,s20\n" \
"s_cmp_ge_u32 s18,1112\n" \
"s_cbranch_scc1 LT95d_%=\n" \
"s_sleep 1\n" \
"s_branch LT95_%=\n" \
"LT95d_%=:\n" \
R_"a[84:87],v236 offset:51200\n" \
M_"v[192:195],a[32:35],a[88:91],v[192:195]\n" \
L_"v245,s[76:79],s65 offen offset:2048 lds\n" \
M_"v[196:199],a[36:39],a[88:91],v[196:199]\n" \
M_"v[200:203],a[40:43],a[88:91],v[200:203]\n" \
M_"v[204:207],a[44:47],a[88:91],v[204:207]\n" \
M_"v[208:211],a[48:51],a[88:91],v[208:211]\n" \
M_"v[212:215],a[52:55],a[88:91],v[212:215]\n" \
M_"v[216:219],a[56:59],a[88:91],v[216:219]\n" \
M_"v[220:223],a[60:63],a[88:91],v[220:223]\n" \
R_"a[88:91],v236 offset:53248\n" \
S_"lgkmcnt(4)\n" \
M_"v[0:3],a[0:3],a[64:67],v[0:3]\n" \
M_"v[4:7],a[4:7],a[64:67],v[4:7]\n" \
M_"v[8:11],a[8:11],a[64:67],v[8:11]\n" \
M_"v[12:15],a[12:15],a[64:67],v[12:15]\n" \
S_"lgkmcnt(0)\n" \
"LT96_%=:\n" \
"s_memrealtime s[18:19]\n" \
S_"lgkmcnt(0)\n" \
"s_sub_u32 s18,s18,s20\n" \
"s_cmp_ge_u32 s18,1136\n" \
"s_cbranch_scc1 LT96d_%=\n" \
"s_sleep 1\n" \
"s_branch LT96_%=\n" \
"LT96d_%=:\n" \
M_"v[16:19],a[16:19],a[64:67],v[16:19]\n" \
M_"v[20:23],a[20:23],a[64:67],v[20:23]\n" \
M_"v[24:27],a[24:27],a[64:67],v[24:27]\n" \
M_"v[28:31],a[28:31],a[64:67],v[28:31]\n" \
S_"vmcnt(15)\n" \
R_"a[64:67],v237 offset:40960\n" \
M_"v[32:35],a[0:3],a[68:71],v[32:35]\n" \
T_"a[32:33],v238 offset:8704\n" \
M_"v[36:39],a[4:7],a[68:71],v[36:39]\n" \
T_"a[34:35],v238 offset:8960\n" \
A_"s64,s64,262144\n" \
"s_cmp_eq_u32 s64,4718592\n" \
"s_cselect_b32 s64,0,s64\n" \
A_"m0,s83,0\n" \
M_"v[40:43],a[8:11],a[68:71],v[40:43]\n" \
T_"a[36:37],v238 offset:8712\n" \
L_"v250,s[68:71],s64 offen lds\n" \
M_"v[44:47],a[12:15],a[68:71],v[44:47]\n" \
T_"a[38:39],v238 offset:8968\n" \
M_"v[48:51],a[16:19],a[68:71],v[48:51]\n" \
T_"a[40:41],v238 offset:8768\n" \
M_"v[52:55],a[20:23],a[68:71],v[52:55]\n" \
T_"a[42:43],v238 offset:9024\n" \
M_"v[56:59],a[24:27],a[68:71],v[56:59]\n" \
T_"a[44:45],v238 offset:8776\n" \
L_"v251,s[68:71],s64 offen offset:1088 lds\n" \
M_"v[60:63],a[28:31],a[68:71],v[60:63]\n" \
S_"lgkmcnt(0)\n" \
"LT97_%=:\n" \
"s_memrealtime s[18:19]\n" \
S_"lgkmcnt(0)\n" \
"s_sub_u32 s18,s18,s20\n" \
"s_cmp_ge_u32 s18,1159\n" \
"s_cbranch_scc1 LT97d_%=\n" \
"s_sleep 1\n" \
"s_branch LT97_%=\n" \
"LT97d_%=:\n" \
R_"a[68:71],v237 offset:43008\n" \
M_"v[64:67],a[0:3],a[72:75],v[64:67]\n" \
T_"a[46:47],v238 offset:9032\n" \
M_"v[68:71],a[4:7],a[72:75],v[68:71]\n" \
T_"a[48:49],v238 offset:8832\n" \
M_"v[72:75],a[8:11],a[72:75],v[72:75]\n" \
T_"a[50:51],v238 offset:9088\n" \
L_"v252,s[68:71],s64 offen offset:2176 lds\n" \
M_"v[76:79],a[12:15],a[72:75],v[76:79]\n" \
T_"a[52:53],v238 offset:8840\n" \
M_"v[80:83],a[16:19],a[72:75],v[80:83]\n" \
T_"a[54:55],v238 offset:9096\n" \
M_"v[84:87],a[20:23],a[72:75],v[84:87]\n" \
T_"a[56:57],v238 offset:8896\n" \
M_"v[88:91],a[24:27],a[72:75],v[88:91]\n" \
T_"a[58:59],v238 offset:9152\n" \
L_"v253,s[68:71],s64 offen offset:3264 lds\n" \
M_"v[92:95],a[28:31],a[72:75],v[92:95]\n" \
R_"a[72:75],v237 offset:45056\n" \
M_"v[96:99],a[0:3],a[76:79],v[96:99]\n" \
T_"a[60:61],v238 offset:8904\n" \
M_"v[100:103],a[4:7],a[76:79],v[100:103]\n" \
T_"a[62:63],v238 offset:9160\n" \
M_"v[104:107],a[8:11],a[76:79],v[104:107]\n" \
A_"m0,s83,4360\n" \
A_"s60,s64,65536\n" \
M_"v[108:111],a[12:15],a[76:79],v[108:111]\n" \
L_"v250,s[68:71],s60 offen lds\n" \
M_"v[112:115],a[16:19],a[76:79],v[112:115]\n" \
M_"v[116:119],a[20:23],a[76:79],v[116:119]\n" \
M_"v[120:123],a[24:27],a[76:79],v[120:123]\n" \
L_"v251,s[68:71],s60 offen offset:1088 lds\n" \
M_"v[124:127],a[28:31],a[76:79],v[124:127]\n" \
S_"lgkmcnt(0)\n" \
"LT98_%=:\n" \
"s_memrealtime s[18:19]\n" \
S_"lgkmcnt(0)\n" \
"s_sub_u32 s18,s18,s20\n" \
"s_cmp_ge_u32 s18,1182\n" \
"s_cbranch_scc1 LT98d_%=\n" \
"s_sleep 1\n" \
"s_branch LT98_%=\n" \
"LT98d_%=:\n" \
R_"a[76:79],v237 offset:47104\n" \
M_"v[128:131],a[0:3],a[80:83],v[128:131]\n" \
M_"v[132:135],a[4:7],a[80:83],v[132:135]\n" \
M_"v[136:139],a[8:11],a[80:83],v[136:139]\n" \
L_"v252,s[68:71],s60 offen offset:2176 lds\n" \
M_"v[140:143],a[12:15],a[80:83],v[140:143]\n" \
M_"v[144:147],a[16:19],a[80:83],v[144:147]\n" \
M_"v[148:151],a[20:23],a[80:83],v[148:151]\n" \
M_"v[152:155],a[24:27],a[80:83],v[152:155]\n" \
L_"v253,s[68:71],s60 offen offset:3264 lds\n" \
M_"v[156:159],a[28:31],a[80:83],v[156:159]\n" \
R_"a[80:83],v237 offset:49152\n" \
M_"v[160:163],a[0:3],a[84:87],v[160:163]\n" \
M_"v[164:167],a[4:7],a[84:87],v[164:167]\n" \
M_"v[168:171],a[8:11],a[84:87],v[168:171]\n" \
M_"v[172:175],a[12:15],a[84:87],v[172:175]\n" \
M_"v[176:179],a[16:19],a[84:87],v[176:179]\n" \
M_"v[180:183],a[20:23],a[84:87],v[180:183]\n" \
M_"v[184:187],a[24:27],a[84:87],v[184:187]\n" \
M_"v[188:191],a[28:31],a[84:87],v[188:191]\n" \
S_"lgkmcnt(0)\n" \
"LT99_%=:\n" \
"s_memrealtime s[18:19]\n" \
S_"lgkmcnt(0)\n" \
"s_sub_u32 s18,s18,s20\n" \
"s_cmp_ge_u32 s18,1205\n" \
"s_cbranch_scc1 LT99d_%=\n" \
"s_sleep 1\n" \
"s_branch LT99_%=\n" \
"LT99d_%=:\n" \
R_"a[84:87],v237 offset:51200\n" \
M_"v[192:195],a[0:3],a[88:91],v[192:195]\n" \
M_"v[196:199],a[4:7],a[88:91],v[196:199]\n" \
M_"v[200:203],a[8:11],a[88:91],v[200:203]\n" \
M_"v[204:207],a[12:15],a[88:91],v[204:207]\n" \
M_"v[208:211],a[16:19],a[88:91],v[208:211]\n" \
M_"v[212:215],a[20:23],a[88:91],v[212:215]\n" \
M_"v[216:219],a[24:27],a[88:91],v[216:219]\n" \
M_"v[220:223],a[28:31],a[88:91],v[220:223]\n" \
R_"a[88:91],v237 offset:53248\n" \
S_"lgkmcnt(4)\n" \
M_"v[0:3],a[32:35],a[64:67],v[0:3]\n" \
M_"v[4:7],a[36:39],a[64:67],v[4:7]\n" \
M_"v[8:11],a[40:43],a[64:67],v[8:11]\n" \
M_"v[12:15],a[44:47],a[64:67],v[12:15]\n" \
S_"lgkmcnt(0)\n" \
"LT100_%=:\n" \
"s_memrealtime s[18:19]\n" \
S_"lgkmcnt(0)\n" \
"s_sub_u32 s18,s18,s20\n" \
"s_cmp_ge_u32 s18,1229\n" \
"s_cbranch_scc1 LT100d_%=\n" \
"s_sleep 1\n" \
"s_branch LT100_%=\n" \
"LT100d_%=:\n" \
M_"v[16:19],a[48:51],a[64:67],v[16:19]\n" \
M_"v[20:23],a[52:55],a[64:67],v[20:23]\n" \
M_"v[24:27],a[56:59],a[64:67],v[24:27]\n" \
M_"v[28:31],a[60:63],a[64:67],v[28:31]\n" \
"v_mov_b32 v247,7\n" \
S_"vmcnt(8)\n" \
"ds_write_b32 v234,v247 offset:0\n" \
M_"v[32:35],a[32:35],a[68:71],v[32:35]\n" \
T_"a[0:1],v238 offset:17408\n" \
A_"s64,s64,262144\n" \
"s_cmp_eq_u32 s64,4718592\n" \
"s_cselect_b32 s64,0,s64\n" \
A_"m0,s83,8704\n" \
M_"v[36:39],a[36:39],a[68:71],v[36:39]\n" \
T_"a[2:3],v238 offset:17664\n" \
L_"v250,s[68:71],s64 offen lds\n" \
M_"v[40:43],a[40:43],a[68:71],v[40:43]\n" \
T_"a[4:5],v238 offset:17416\n" \
M_"v[44:47],a[44:47],a[68:71],v[44:47]\n" \
T_"a[6:7],v238 offset:17672\n" \
L_"v251,s[68:71],s64 offen offset:1088 lds\n" \
M_"v[48:51],a[48:51],a[68:71],v[48:51]\n" \
T_"a[8:9],v238 offset:17472\n" \
M_"v[52:55],a[52:55],a[68:71],v[52:55]\n" \
T_"a[10:11],v238 offset:17728\n" \
L_"v252,s[68:71],s64 offen offset:2176 lds\n" \
M_"v[56:59],a[56:59],a[68:71],v[56:59]\n" \
T_"a[12:13],v238 offset:17480\n" \
M_"v[60:63],a[60:63],a[68:71],v[60:63]\n" \
S_"lgkmcnt(0)\n" \
"LT101_%=:\n" \
"s_memrealtime s[18:19]\n" \
S_"lgkmcnt(0)\n" \
"s_sub_u32 s18,s18,s20\n" \
"s_cmp_ge_u32 s18,1252\n" \
"s_cbranch_scc1 LT101d_%=\n" \
"s_sleep 1\n" \
"s_branch LT101_%=\n" \
"LT101d_%=:\n" \
T_"a[14:15],v238 offset:17736\n" \
L_"v253,s[68:71],s64 offen offset:3264 lds\n" \
M_"v[64:67],a[32:35],a[72:75],v[64:67]\n" \
"ds_read_b32 v248,v235 offset:0\n" \
A_"m0,s83,13064\n" \
A_"s60,s64,65536\n" \
M_"v[68:71],a[36:39],a[72:75],v[68:71]\n" \
T_"a[16:17],v238 offset:17536\n" \
M_"v[72:75],a[40:43],a[72:75],v[72:75]\n" \
T_"a[18:19],v238 offset:17792\n" \
L_"v250,s[68:71],s60 offen lds\n" \
M_"v[76:79],a[44:47],a[72:75],v[76:79]\n" \
T_"a[20:21],v238 offset:17544\n" \
M_"v[80:83],a[48:51],a[72:75],v[80:83]\n" \
T_"a[22:23],v238 offset:17800\n" \
L_"v251,s[68:71],s60 offen offset:1088 lds\n" \
M_"v[84:87],a[52:55],a[72:75],v[84:87]\n" \
T_"a[24:25],v238 offset:17600\n" \
S_"lgkmcnt(5)\n" \
"v_readfirstlane_b32 s63,v248\n" \
M_"v[88:91],a[56:59],a[72:75],v[88:91]\n" \
T_"a[26:27],v238 offset:17856\n" \
"s_cmp_ge_u32 s63,7\n" \
"s_cbranch_scc0 LPS6_%=\n" \
"LPS6d_%=:\n" \
M_"v[92:95],a[60:63],a[72:75],v[92:95]\n" \
T_"a[28:29],v238 offset:17608\n" \
L_"v252,s[68:71],s60 offen offset:2176 lds\n" \
M_"v[96:99],a[32:35],a[76:79],v[96:99]\n" \
T_"a[30:31],v238 offset:17864\n" \
L_"v253,s[68:71],s60 offen offset:3264 lds\n" \
M_"v[100:103],a[36:39],a[76:79],v[100:103]\n" \
R_"a[64:67],v236 offset:0\n" \
M_"v[104:107],a[40:43],a[76:79],v[104:107]\n" \
R_"a[68:71],v236 offset:2048\n" \
M_"v[108:111],a[44:47],a[76:79],v[108:111]\n" \
R_"a[72:75],v236 offset:4096\n" \
M_"v[112:115],a[48:51],a[76:79],v[112:115]\n" \
A_"s65,s65,128\n" \
"s_cmp_eq_u32 s65,1152\n" \
"s_cselect_b32 s65,0,s65\n" \
A_"m0,s73,40960\n" \
M_"v[116:119],a[52:55],a[76:79],v[116:119]\n" \
L_"v240,s[76:79],s65 offen lds\n" \
M_"v[120:123],a[56:59],a[76:79],v[120:123]\n" \
M_"v[124:127],a[60:63],a[76:79],v[124:127]\n" \
S_"lgkmcnt(0)\n" \
"LT102_%=:\n" \
"s_memrealtime s[18:19]\n" \
S_"lgkmcnt(0)\n" \
"s_sub_u32 s18,s18,s20\n" \
"s_cmp_ge_u32 s18,1275\n" \
"s_cbranch_scc1 LT102d_%=\n" \
"s_sleep 1\n" \
"s_branch LT102_%=\n" \
"LT102d_%=:\n" \
R_"a[76:79],v236 offset:6144\n" \
M_"v[128:131],a[32:35],a[80:83],v[128:131]\n" \
L_"v241,s[76:79],s65 offen offset:1024 lds\n" \
M_"v[132:135],a[36:39],a[80:83],v[132:135]\n" \
M_"v[136:139],a[40:43],a[80:83],v[136:139]\n" \
M_"v[140:143],a[44:47],a[80:83],v[140:143]\n" \
L_"v242,s[76:79],s65 offen offset:2048 lds\n" \
M_"v[144:147],a[48:51],a[80:83],v[144:147]\n" \
M_"v[148:151],a[52:55],a[80:83],v[148:151]\n" \
M_"v[152:155],a[56:59],a[80:83],v[152:155]\n" \
L_"v249,s[76:79],s65 offen offset:3072 lds\n" \
M_"v[156:159],a[60:63],a[80:83],v[156:159]\n" \
R_"a[80:83],v236 offset:8192\n" \
M_"v[160:163],a[32:35],a[84:87],v[160:163]\n" \
A_"m0,s73,45056\n" \
M_"v[164:167],a[36:39],a[84:87],v[164:167]\n" \
L_"v243,s[76:79],s65 offen lds\n" \
M_"v[168:171],a[40:43],a[84:87],v[168:171]\n" \
M_"v[172:175],a[44:47],a[84:87],v[172:175]\n" \
M_"v[176:179],a[48:51],a[84:87],v[176:179]\n" \
L_"v244,s[76:79],s65 offen offset:1024 lds\n" \
M_"v[180:183],a[52:55],a[84:87],v[180:183]\n" \
M_"v[184:187],a[56:59],a[84:87],v[184:187]\n" \
M_"v[188:191],a[60:63],a[84:87],v[188:191]\n" \
S_"lgkmcnt(0)\n" \
"LT103_%=:\n" \
"s_memrealtime s[18:19]\n" \
S_"lgkmcnt(0)\n" \
"s_sub_u32 s18,s18,s20\n" \
"s_cmp_ge_u32 s18,1298\n" \
"s_cbranch_scc1 LT103d_%=\n" \
"s_sleep 1\n" \
"s_branch LT103_%=\n" \
"LT103d_%=:\n" \
R_"a[84:87],v236 offset:10240\n" \
M_"v[192:195],a[32:35],a[88:91],v[192:195]\n" \
L_"v245,s[76:79],s65 offen offset:2048 lds\n" \
M_"v[196:199],a[36:39],a[88:91],v[196:199]\n" \
M_"v[200:203],a[40:43],a[88:91],v[200:203]\n" \
M_"v[204:207],a[44:47],a[88:91],v[204:207]\n" \
M_"v[208:211],a[48:51],a[88:91],v[208:211]\n" \
M_"v[212:215],a[52:55],a[88:91],v[212:215]\n" \
M_"v[216:219],a[56:59],a[88:91],v[216:219]\n" \
M_"v[220:223],a[60:63],a[88:91],v[220:223]\n" \
R_"a[88:91],v236 offset:12288\n" \
S_"lgkmcnt(4)\n" \
M_"v[0:3],a[0:3],a[64:67],v[0:3]\n" \
M_"v[4:7],a[4:7],a[64:67],v[4:7]\n" \
M_"v[8:11],a[8:11],a[64:67],v[8:11]\n" \
M_"v[12:15],a[12:15],a[64:67],v[12:15]\n" \
S_"lgkmcnt(0)\n" \
"LT104_%=:\n" \
"s_memrealtime s[18:19]\n" \
S_"lgkmcnt(0)\n" \
"s_sub_u32 s18,s18,s20\n" \
"s_cmp_ge_u32 s18,1322\n" \
"s_cbranch_scc1 LT104d_%=\n" \
"s_sleep 1\n" \
"s_branch LT104_%=\n" \
"LT104d_%=:\n" \
M_"v[16:19],a[16:19],a[64:67],v[16:19]\n" \
M_"v[20:23],a[20:23],a[64:67],v[20:23]\n" \
M_"v[24:27],a[24:27],a[64:67],v[24:27]\n" \
M_"v[28:31],a[28:31],a[64:67],v[28:31]\n" \
S_"vmcnt(15)\n" \
R_"a[64:67],v237 offset:0\n" \
M_"v[32:35],a[0:3],a[68:71],v[32:35]\n" \
T_"a[32:33],v238 offset:0\n" \
M_"v[36:39],a[4:7],a[68:71],v[36:39]\n" \
T_"a[34:35],v238 offset:256\n" \
A_"s64,s64,262144\n" \
"s_cmp_eq_u32 s64,4718592\n" \
"s_cselect_b32 s64,0,s64\n" \
A_"m0,s83,17408\n" \
M_"v[40:43],a[8:11],a[68:71],v[40:43]\n" \
T_"a[36:37],v238 offset:8\n" \
L_"v250,s[68:71],s64 offen lds\n" \
M_"v[44:47],a[12:15],a[68:71],v[44:47]\n" \
T_"a[38:39],v238 offset:264\n" \
M_"v[48:51],a[16:19],a[68:71],v[48:51]\n" \
T_"a[40:41],v238 offset:64\n" \
M_"v[52:55],a[20:23],a[68:71],v[52:55]\n" \
T_"a[42:43],v238 offset:320\n" \
M_"v[56:59],a[24:27],a[68:71],v[56:59]\n" \
T_"a[44:45],v238 offset:72\n" \
L_"v251,s[68:71],s64 offen offset:1088 lds\n" \
M_"v[60:63],a[28:31],a[68:71],v[60:63]\n" \
S_"lgkmcnt(0)\n" \
"LT105_%=:\n" \
"s_memrealtime s[18:19]\n" \
S_"lgkmcnt(0)\n" \
"s_sub_u32 s18,s18,s20\n" \
"s_cmp_ge_u32 s18,1345\n" \
"s_cbranch_scc1 LT105d_%=\n" \
"s_sleep 1\n" \
"s_branch LT105_%=\n" \
"LT105d_%=:\n" \
R_"a[68:71],v237 offset:2048\n" \
M_"v[64:67],a[0:3],a[72:75],v[64:67]\n" \
T_"a[46:47],v238 offset:328\n" \
M_"v[68:71],a[4:7],a[72:75],v[68:71]\n" \
T_"a[48:49],v238 offset:128\n" \
M_"v[72:75],a[8:11],a[72:75],v[72:75]\n" \
T_"a[50:51],v238 offset:384\n" \
L_"v252,s[68:71],s64 offen offset:2176 lds\n" \
M_"v[76:79],a[12:15],a[72:75],v[76:79]\n" \
T_"a[52:53],v238 offset:136\n" \
M_"v[80:83],a[16:19],a[72:75],v[80:83]\n" \
T_"a[54:55],v238 offset:392\n" \
M_"v[84:87],a[20:23],a[72:75],v[84:87]\n" \
T_"a[56:57],v238 offset:192\n" \
M_"v[88:91],a[24:27],a[72:75],v[88:91]\n" \
T_"a[58:59],v238 offset:448\n" \
L_"v253,s[68:71],s64 offen offset:3264 lds\n" \
M_"v[92:95],a[28:31],a[72:75],v[92:95]\n" \
R_"a[72:75],v237 offset:4096\n" \
M_"v[96:99],a[0:3],a[76:79],v[96:99]\n" \
T_"a[60:61],v238 offset:200\n" \
M_"v[100:103],a[4:7],a[76:79],v[100:103]\n" \
T_"a[62:63],v238 offset:456\n" \
M_"v[104:107],a[8:11],a[76:79],v[104:107]\n" \
A_"m0,s83,21768\n" \
A_"s60,s64,65536\n" \
M_"v[108:111],a[12:15],a[76:79],v[108:111]\n" \
L_"v250,s[68:71],s60 offen lds\n" \
M_"v[112:115],a[16:19],a[76:79],v[112:115]\n" \
M_"v[116:119],a[20:23],a[76:79],v[116:119]\n" \
M_"v[120:123],a[24:27],a[76:79],v[120:123]\n" \
L_"v251,s[68:71],s60 offen offset:1088 lds\n" \
M_"v[124:127],a[28:31],a[76:79],v[124:127]\n" \
S_"lgkmcnt(0)\n" \
"LT106_%=:\n" \
"s_memrealtime s[18:19]\n" \
S_"lgkmcnt(0)\n" \
"s_sub_u32 s18,s18,s20\n" \
"s_cmp_ge_u32 s18,1368\n" \
"s_cbranch_scc1 LT106d_%=\n" \
"s_sleep 1\n" \
"s_branch LT106_%=\n" \
"LT106d_%=:\n" \
R_"a[76:79],v237 offset:6144\n" \
M_"v[128:131],a[0:3],a[80:83],v[128:131]\n" \
M_"v[132:135],a[4:7],a[80:83],v[132:135]\n" \
M_"v[136:139],a[8:11],a[80:83],v[136:139]\n" \
L_"v252,s[68:71],s60 offen offset:2176 lds\n" \
M_"v[140:143],a[12:15],a[80:83],v[140:143]\n" \
M_"v[144:147],a[16:19],a[80:83],v[144:147]\n" \
M_"v[148:151],a[20:23],a[80:83],v[148:151]\n" \
M_"v[152:155],a[24:27],a[80:83],v[152:155]\n" \
L_"v253,s[68:71],s60 offen offset:3264 lds\n" \
M_"v[156:159],a[28:31],a[80:83],v[156:159]\n" \
R_"a[80:83],v237 offset:8192\n" \
M_"v[160:163],a[0:3],a[84:87],v[160:163]\n" \
M_"v[164:167],a[4:7],a[84:87],v[164:167]\n" \
M_"v[168:171],a[8:11],a[84:87],v[168:171]\n" \
M_"v[172:175],a[12:15],a[84:87],v[172:175]\n" \
M_"v[176:179],a[16:19],a[84:87],v[176:179]\n" \
M_"v[180:183],a[20:23],a[84:87],v[180:183]\n" \
M_"v[184:187],a[24:27],a[84:87],v[184:187]\n" \
M_"v[188:191],a[28:31],a[84:87],v[188:191]\n" \
S_"lgkmcnt(0)\n" \
"LT107_%=:\n" \
"s_memrealtime s[18:19]\n" \
S_"lgkmcnt(0)\n" \
"s_sub_u32 s18,s18,s20\n" \
"s_cmp_ge_u32 s18,1391\n" \
"s_cbranch_scc1 LT107d_%=\n" \
"s_sleep 1\n" \
"s_branch LT107_%=\n" \
"LT107d_%=:\n" \
R_"a[84:87],v237 offset:10240\n" \
M_"v[192:195],a[0:3],a[88:91],v[192:195]\n" \
M_"v[196:199],a[4:7],a[88:91],v[196:199]\n" \
M_"v[200:203],a[8:11],a[88:91],v[200:203]\n" \
M_"v[204:207],a[12:15],a[88:91],v[204:207]\n" \
M_"v[208:211],a[16:19],a[88:91],v[208:211]\n" \
M_"v[212:215],a[20:23],a[88:91],v[212:215]\n" \
M_"v[216:219],a[24:27],a[88:91],v[216:219]\n" \
M_"v[220:223],a[28:31],a[88:91],v[220:223]\n" \
R_"a[88:91],v237 offset:12288\n" \
S_"lgkmcnt(4)\n" \
M_"v[0:3],a[32:35],a[64:67],v[0:3]\n" \
M_"v[4:7],a[36:39],a[64:67],v[4:7]\n" \
M_"v[8:11],a[40:43],a[64:67],v[8:11]\n" \
M_"v[12:15],a[44:47],a[64:67],v[12:15]\n" \
S_"lgkmcnt(0)\n" \
"LT108_%=:\n" \
"s_memrealtime s[18:19]\n" \
S_"lgkmcnt(0)\n" \
"s_sub_u32 s18,s18,s20\n" \
"s_cmp_ge_u32 s18,1415\n" \
"s_cbranch_scc1 LT108d_%=\n" \
"s_sleep 1\n" \
"s_branch LT108_%=\n" \
"LT108d_%=:\n" \
M_"v[16:19],a[48:51],a[64:67],v[16:19]\n" \
M_"v[20:23],a[52:55],a[64:67],v[20:23]\n" \
M_"v[24:27],a[56:59],a[64:67],v[24:27]\n" \
M_"v[28:31],a[60:63],a[64:67],v[28:31]\n" \
"v_mov_b32 v247,8\n" \
S_"vmcnt(8)\n" \
"ds_write_b32 v234,v247 offset:0\n" \
M_"v[32:35],a[32:35],a[68:71],v[32:35]\n" \
T_"a[0:1],v238 offset:8704\n" \
M_"v[36:39],a[36:39],a[68:71],v[36:39]\n" \
T_"a[2:3],v238 offset:8960\n" \
M_"v[40:43],a[40:43],a[68:71],v[40:43]\n" \
T_"a[4:5],v238 offset:8712\n" \
M_"v[44:47],a[44:47],a[68:71],v[44:47]\n" \
T_"a[6:7],v238 offset:8968\n" \
M_"v[48:51],a[48:51],a[68:71],v[48:51]\n" \
T_"a[8:9],v238 offset:8768\n" \
M_"v[52:55],a[52:55],a[68:71],v[52:55]\n" \
T_"a[10:11],v238 offset:9024\n" \
M_"v[56:59],a[56:59],a[68:71],v[56:59]\n" \
T_"a[12:13],v238 offset:8776\n" \
M_"v[60:63],a[60:63],a[68:71],v[60:63]\n" \
S_"lgkmcnt(0)\n" \
"LT109_%=:\n" \
"s_memrealtime s[18:19]\n" \
S_"lgkmcnt(0)\n" \
"s_sub_u32 s18,s18,s20\n" \
"s_cmp_ge_u32 s18,1438\n" \
"s_cbranch_scc1 LT109d_%=\n" \
"s_sleep 1\n" \
"s_branch LT109_%=\n" \
"LT109d_%=:\n" \
T_"a[14:15],v238 offset:9032\n" \
M_"v[64:67],a[32:35],a[72:75],v[64:67]\n" \
"ds_read_b32 v248,v235 offset:0\n" \
M_"v[68:71],a[36:39],a[72:75],v[68:71]\n" \
T_"a[16:17],v238 offset:8832\n" \
M_"v[72:75],a[40:43],a[72:75],v[72:75]\n" \
T_"a[18:19],v238 offset:9088\n" \
M_"v[76:79],a[44:47],a[72:75],v[76:79]\n" \
T_"a[20:21],v238 offset:8840\n" \
M_"v[80:83],a[48:51],a[72:75],v[80:83]\n" \
T_"a[22:23],v238 offset:9096\n" \
M_"v[84:87],a[52:55],a[72:75],v[84:87]\n" \
T_"a[24:25],v238 offset:8896\n" \
S_"lgkmcnt(5)\n" \
"v_readfirstlane_b32 s63,v248\n" \
M_"v[88:91],a[56:59],a[72:75],v[88:91]\n" \
T_"a[26:27],v238 offset:9152\n" \
"s_cmp_ge_u32 s63,8\n" \
"s_cbranch_scc0 LPS7_%=\n" \
"LPS7d_%=:\n" \
M_"v[92:95],a[60:63],a[72:75],v[92:95]\n" \
T_"a[28:29],v238 offset:8904\n" \
M_"v[96:99],a[32:35],a[76:79],v[96:99]\n" \
T_"a[30:31],v238 offset:9160\n" \
M_"v[100:103],a[36:39],a[76:79],v[100:103]\n" \
R_"a[64:67],v236 offset:40960\n" \
M_"v[104:107],a[40:43],a[76:79],v[104:107]\n" \
R_"a[68:71],v236 offset:43008\n" \
M_"v[108:111],a[44:47],a[76:79],v[108:111]\n" \
R_"a[72:75],v236 offset:45056\n" \
M_"v[112:115],a[48:51],a[76:79],v[112:115]\n" \
M_"v[116:119],a[52:55],a[76:79],v[116:119]\n" \
M_"v[120:123],a[56:59],a[76:79],v[120:123]\n" \
M_"v[124:127],a[60:63],a[76:79],v[124:127]\n" \
S_"lgkmcnt(0)\n" \
"LT110_%=:\n" \
"s_memrealtime s[18:19]\n" \
S_"lgkmcnt(0)\n" \
"s_sub_u32 s18,s18,s20\n" \
"s_cmp_ge_u32 s18,1461\n" \
"s_cbranch_scc1 LT110d_%=\n" \
"s_sleep 1\n" \
"s_branch LT110_%=\n" \
"LT110d_%=:\n" \
R_"a[76:79],v236 offset:47104\n" \
M_"v[128:131],a[32:35],a[80:83],v[128:131]\n" \
M_"v[132:135],a[36:39],a[80:83],v[132:135]\n" \
M_"v[136:139],a[40:43],a[80:83],v[136:139]\n" \
M_"v[140:143],a[44:47],a[80:83],v[140:143]\n" \
M_"v[144:147],a[48:51],a[80:83],v[144:147]\n" \
M_"v[148:151],a[52:55],a[80:83],v[148:151]\n" \
M_"v[152:155],a[56:59],a[80:83],v[152:155]\n" \
M_"v[156:159],a[60:63],a[80:83],v[156:159]\n" \
R_"a[80:83],v236 offset:49152\n" \
M_"v[160:163],a[32:35],a[84:87],v[160:163]\n" \
M_"v[164:167],a[36:39],a[84:87],v[164:167]\n" \
M_"v[168:171],a[40:43],a[84:87],v[168:171]\n" \
M_"v[172:175],a[44:47],a[84:87],v[172:175]\n" \
M_"v[176:179],a[48:51],a[84:87],v[176:179]\n" \
M_"v[180:183],a[52:55],a[84:87],v[180:183]\n" \
M_"v[184:187],a[56:59],a[84:87],v[184:187]\n" \
M_"v[188:191],a[60:63],a[84:87],v[188:191]\n" \
S_"lgkmcnt(0)\n" \
"LT111_%=:\n" \
"s_memrealtime s[18:19]\n" \
S_"lgkmcnt(0)\n" \
"s_sub_u32 s18,s18,s20\n" \
"s_cmp_ge_u32 s18,1484\n" \
"s_cbranch_scc1 LT111d_%=\n" \
"s_sleep 1\n" \
"s_branch LT111_%=\n" \
"LT111d_%=:\n" \
R_"a[84:87],v236 offset:51200\n" \
M_"v[192:195],a[32:35],a[88:91],v[192:195]\n" \
M_"v[196:199],a[36:39],a[88:91],v[196:199]\n" \
M_"v[200:203],a[40:43],a[88:91],v[200:203]\n" \
M_"v[204:207],a[44:47],a[88:91],v[204:207]\n" \
M_"v[208:211],a[48:51],a[88:91],v[208:211]\n" \
M_"v[212:215],a[52:55],a[88:91],v[212:215]\n" \
M_"v[216:219],a[56:59],a[88:91],v[216:219]\n" \
M_"v[220:223],a[60:63],a[88:91],v[220:223]\n" \
R_"a[88:91],v236 offset:53248\n" \
S_"lgkmcnt(4)\n" \
M_"v[0:3],a[0:3],a[64:67],v[0:3]\n" \
M_"v[4:7],a[4:7],a[64:67],v[4:7]\n" \
M_"v[8:11],a[8:11],a[64:67],v[8:11]\n" \
M_"v[12:15],a[12:15],a[64:67],v[12:15]\n" \
S_"lgkmcnt(0)\n" \
"LT112_%=:\n" \
"s_memrealtime s[18:19]\n" \
S_"lgkmcnt(0)\n" \
"s_sub_u32 s18,s18,s20\n" \
"s_cmp_ge_u32 s18,1508\n" \
"s_cbranch_scc1 LT112d_%=\n" \
"s_sleep 1\n" \
"s_branch LT112_%=\n" \
"LT112d_%=:\n" \
M_"v[16:19],a[16:19],a[64:67],v[16:19]\n" \
"v_lshlrev_b32 v239,4,v255\n" \
"v_add_u32 v239,s83,v239\n" \
M_"v[20:23],a[20:23],a[64:67],v[20:23]\n" \
M_"v[24:27],a[24:27],a[64:67],v[24:27]\n" \
M_"v[28:31],a[28:31],a[64:67],v[28:31]\n" \
S_"vmcnt(0)\n" \
R_"a[64:67],v237 offset:40960\n" \
"ds_write_b128 v239,a[96:99] offset:0\n" \
M_"v[32:35],a[0:3],a[68:71],v[32:35]\n" \
T_"a[32:33],v238 offset:17408\n" \
M_"v[36:39],a[4:7],a[68:71],v[36:39]\n" \
T_"a[34:35],v238 offset:17664\n" \
M_"v[40:43],a[8:11],a[68:71],v[40:43]\n" \
T_"a[36:37],v238 offset:17416\n" \
"ds_write_b128 v239,a[100:103] offset:1056\n" \
M_"v[44:47],a[12:15],a[68:71],v[44:47]\n" \
T_"a[38:39],v238 offset:17672\n" \
M_"v[48:51],a[16:19],a[68:71],v[48:51]\n" \
T_"a[40:41],v238 offset:17472\n" \
M_"v[52:55],a[20:23],a[68:71],v[52:55]\n" \
T_"a[42:43],v238 offset:17728\n" \
"ds_write_b128 v239,a[104:107] offset:2112\n" \
M_"v[56:59],a[24:27],a[68:71],v[56:59]\n" \
T_"a[44:45],v238 offset:17480\n" \
M_"v[60:63],a[28:31],a[68:71],v[60:63]\n" \
S_"lgkmcnt(0)\n" \
"LT113_%=:\n" \
"s_memrealtime s[18:19]\n" \
S_"lgkmcnt(0)\n" \
"s_sub_u32 s18,s18,s20\n" \
"s_cmp_ge_u32 s18,1531\n" \
"s_cbranch_scc1 LT113d_%=\n" \
"s_sleep 1\n" \
"s_branch LT113_%=\n" \
"LT113d_%=:\n" \
R_"a[68:71],v237 offset:43008\n" \
M_"v[64:67],a[0:3],a[72:75],v[64:67]\n" \
T_"a[46:47],v238 offset:17736\n" \
"ds_write_b128 v239,a[108:111] offset:3168\n" \
M_"v[68:71],a[4:7],a[72:75],v[68:71]\n" \
T_"a[48:49],v238 offset:17536\n" \
M_"v[72:75],a[8:11],a[72:75],v[72:75]\n" \
T_"a[50:51],v238 offset:17792\n" \
M_"v[76:79],a[12:15],a[72:75],v[76:79]\n" \
T_"a[52:53],v238 offset:17544\n" \
"ds_write_b128 v239,a[112:115] offset:4224\n" \
M_"v[80:83],a[16:19],a[72:75],v[80:83]\n" \
T_"a[54:55],v238 offset:17800\n" \
M_"v[84:87],a[20:23],a[72:75],v[84:87]\n" \
T_"a[56:57],v238 offset:17600\n" \
M_"v[88:91],a[24:27],a[72:75],v[88:91]\n" \
T_"a[58:59],v238 offset:17856\n" \
"ds_write_b128 v239,a[116:119] offset:5280\n" \
M_"v[92:95],a[28:31],a[72:75],v[92:95]\n" \
R_"a[72:75],v237 offset:45056\n" \
M_"v[96:99],a[0:3],a[76:79],v[96:99]\n" \
T_"a[60:61],v238 offset:17608\n" \
M_"v[100:103],a[4:7],a[76:79],v[100:103]\n" \
T_"a[62:63],v238 offset:17864\n" \
"ds_write_b128 v239,a[120:123] offset:6336\n" \
M_"v[104:107],a[8:11],a[76:79],v[104:107]\n" \
M_"v[108:111],a[12:15],a[76:79],v[108:111]\n" \
M_"v[112:115],a[16:19],a[76:79],v[112:115]\n" \
"ds_write_b128 v239,a[124:127] offset:7392\n" \
M_"v[116:119],a[20:23],a[76:79],v[116:119]\n" \
M_"v[120:123],a[24:27],a[76:79],v[120:123]\n" \
M_"v[124:127],a[28:31],a[76:79],v[124:127]\n" \
S_"lgkmcnt(0)\n" \
"LT114_%=:\n" \
"s_memrealtime s[18:19]\n" \
S_"lgkmcnt(0)\n" \
"s_sub_u32 s18,s18,s20\n" \
"s_cmp_ge_u32 s18,1554\n" \
"s_cbranch_scc1 LT114d_%=\n" \
"s_sleep 1\n" \
"s_branch LT114_%=\n" \
"LT114d_%=:\n" \
R_"a[76:79],v237 offset:47104\n" \
"ds_write_b128 v239,a[128:131] offset:8704\n" \
M_"v[128:131],a[0:3],a[80:83],v[128:131]\n" \
M_"v[132:135],a[4:7],a[80:83],v[132:135]\n" \
M_"v[136:139],a[8:11],a[80:83],v[136:139]\n" \
"ds_write_b128 v239,a[132:135] offset:9760\n" \
M_"v[140:143],a[12:15],a[80:83],v[140:143]\n" \
M_"v[144:147],a[16:19],a[80:83],v[144:147]\n" \
M_"v[148:151],a[20:23],a[80:83],v[148:151]\n" \
"ds_write_b128 v239,a[136:139] offset:10816\n" \
M_"v[152:155],a[24:27],a[80:83],v[152:155]\n" \
M_"v[156:159],a[28:31],a[80:83],v[156:159]\n" \
R_"a[80:83],v237 offset:49152\n" \
M_"v[160:163],a[0:3],a[84:87],v[160:163]\n" \
"ds_write_b128 v239,a[140:143] offset:11872\n" \
M_"v[164:167],a[4:7],a[84:87],v[164:167]\n" \
M_"v[168:171],a[8:11],a[84:87],v[168:171]\n" \
M_"v[172:175],a[12:15],a[84:87],v[172:175]\n" \
"ds_write_b128 v239,a[144:147] offset:12928\n" \
M_"v[176:179],a[16:19],a[84:87],v[176:179]\n" \
M_"v[180:183],a[20:23],a[84:87],v[180:183]\n" \
M_"v[184:187],a[24:27],a[84:87],v[184:187]\n" \
"ds_write_b128 v239,a[148:151] offset:13984\n" \
M_"v[188:191],a[28:31],a[84:87],v[188:191]\n" \
S_"lgkmcnt(0)\n" \
"LT115_%=:\n" \
"s_memrealtime s[18:19]\n" \
S_"lgkmcnt(0)\n" \
"s_sub_u32 s18,s18,s20\n" \
"s_cmp_ge_u32 s18,1577\n" \
"s_cbranch_scc1 LT115d_%=\n" \
"s_sleep 1\n" \
"s_branch LT115_%=\n" \
"LT115d_%=:\n" \
R_"a[84:87],v237 offset:51200\n" \
M_"v[192:195],a[0:3],a[88:91],v[192:195]\n" \
M_"v[196:199],a[4:7],a[88:91],v[196:199]\n" \
"ds_write_b128 v239,a[152:155] offset:15040\n" \
M_"v[200:203],a[8:11],a[88:91],v[200:203]\n" \
M_"v[204:207],a[12:15],a[88:91],v[204:207]\n" \
M_"v[208:211],a[16:19],a[88:91],v[208:211]\n" \
"ds_write_b128 v239,a[156:159] offset:16096\n" \
M_"v[212:215],a[20:23],a[88:91],v[212:215]\n" \
M_"v[216:219],a[24:27],a[88:91],v[216:219]\n" \
M_"v[220:223],a[28:31],a[88:91],v[220:223]\n" \
R_"a[88:91],v237 offset:53248\n" \
S_"lgkmcnt(4)\n" \
M_"v[0:3],a[32:35],a[64:67],v[0:3]\n" \
M_"v[4:7],a[36:39],a[64:67],v[4:7]\n" \
M_"v[8:11],a[40:43],a[64:67],v[8:11]\n" \
M_"v[12:15],a[44:47],a[64:67],v[12:15]\n" \
S_"lgkmcnt(0)\n" \
"LT116_%=:\n" \
"s_memrealtime s[18:19]\n" \
S_"lgkmcnt(0)\n" \
"s_sub_u32 s18,s18,s20\n" \
"s_cmp_ge_u32 s18,1601\n" \
"s_cbranch_scc1 LT116d_%=\n" \
"s_sleep 1\n" \
"s_branch LT116_%=\n" \
"LT116d_%=:\n" \
M_"v[16:19],a[48:51],a[64:67],v[16:19]\n" \
"ds_write_b128 v239,a[160:163] offset:17408\n" \
M_"v[20:23],a[52:55],a[64:67],v[20:23]\n" \
M_"v[24:27],a[56:59],a[64:67],v[24:27]\n" \
M_"v[28:31],a[60:63],a[64:67],v[28:31]\n" \
"v_mov_b32 v247,9\n" \
S_"lgkmcnt(0)\n" \
"ds_write_b32 v234,v247 offset:0\n" \
"ds_write_b128 v239,a[164:167] offset:18464\n" \
M_"v[32:35],a[32:35],a[68:71],v[32:35]\n" \
M_"v[36:39],a[36:39],a[68:71],v[36:39]\n" \
P_"v[0:1],v[0:1],s[80:81]\n" \
P_"v[2:3],v[2:3],s[80:81]\n" \
M_"v[40:43],a[40:43],a[68:71],v[40:43]\n" \
"ds_write_b128 v239,a[168:171] offset:19520\n" \
P_"v[4:5],v[4:5],s[80:81]\n" \
M_"v[44:47],a[44:47],a[68:71],v[44:47]\n" \
P_"v[6:7],v[6:7],s[80:81]\n" \
C_"v0,v0,v1\n" \
M_"v[48:51],a[48:51],a[68:71],v[48:51]\n" \
C_"v1,v2,v3\n" \
C_"v2,v4,v5\n" \
M_"v[52:55],a[52:55],a[68:71],v[52:55]\n" \
"ds_write_b128 v239,a[172:175] offset:20576\n" \
C_"v3,v6,v7\n" \
M_"v[56:59],a[56:59],a[68:71],v[56:59]\n" \
P_"v[8:9],v[8:9],s[80:81]\n" \
P_"v[10:11],v[10:11],s[80:81]\n" \
M_"v[60:63],a[60:63],a[68:71],v[60:63]\n" \
S_"lgkmcnt(0)\n" \
"LT117_%=:\n" \
"s_memrealtime s[18:19]\n" \
S_"lgkmcnt(0)\n" \
"s_sub_u32 s18,s18,s20\n" \
"s_cmp_ge_u32 s18,1624\n" \
"s_cbranch_scc1 LT117d_%=\n" \
"s_sleep 1\n" \
"s_branch LT117_%=\n" \
"LT117d_%=:\n" \
M_"v[64:67],a[32:35],a[72:75],v[64:67]\n" \
"ds_read_b32 v248,v235 offset:0\n" \
"ds_write_b128 v239,a[176:179] offset:21632\n" \
M_"v[68:71],a[36:39],a[72:75],v[68:71]\n" \
P_"v[12:13],v[12:13],s[80:81]\n" \
P_"v[14:15],v[14:15],s[80:81]\n" \
M_"v[72:75],a[40:43],a[72:75],v[72:75]\n" \
C_"v4,v8,v9\n" \
C_"v5,v10,v11\n" \
M_"v[76:79],a[44:47],a[72:75],v[76:79]\n" \
"ds_write_b128 v239,a[180:183] offset:22688\n" \
C_"v6,v12,v13\n" \
M_"v[80:83],a[48:51],a[72:75],v[80:83]\n" \
C_"v7,v14,v15\n" \
P_"v[16:17],v[16:17],s[80:81]\n" \
M_"v[84:87],a[52:55],a[72:75],v[84:87]\n" \
S_"lgkmcnt(2)\n" \
"v_readfirstlane_b32 s63,v248\n" \
P_"v[18:19],v[18:19],s[80:81]\n" \
M_"v[88:91],a[56:59],a[72:75],v[88:91]\n" \
"s_cmp_ge_u32 s63,9\n" \
"s_cbranch_scc0 LPS8_%=\n" \
"LPS8d_%=:\n" \
"ds_write_b128 v239,a[184:187] offset:23744\n" \
M_"v[92:95],a[60:63],a[72:75],v[92:95]\n" \
P_"v[20:21],v[20:21],s[80:81]\n" \
P_"v[22:23],v[22:23],s[80:81]\n" \
M_"v[96:99],a[32:35],a[76:79],v[96:99]\n" \
C_"v8,v16,v17\n" \
C_"v9,v18,v19\n" \
M_"v[100:103],a[36:39],a[76:79],v[100:103]\n" \
"ds_write_b128 v239,a[188:191] offset:24800\n" \
C_"v10,v20,v21\n" \
M_"v[104:107],a[40:43],a[76:79],v[104:107]\n" \
C_"v11,v22,v23\n" \
P_"v[24:25],v[24:25],s[80:81]\n" \
M_"v[108:111],a[44:47],a[76:79],v[108:111]\n" \
P_"v[26:27],v[26:27],s[80:81]\n" \
P_"v[28:29],v[28:29],s[80:81]\n" \
M_"v[112:115],a[48:51],a[76:79],v[112:115]\n" \
"ds_write_b128 v239,a[192:195] offset:26112\n" \
P_"v[30:31],v[30:31],s[80:81]\n" \
M_"v[116:119],a[52:55],a[76:79],v[116:119]\n" \
C_"v12,v24,v25\n" \
C_"v13,v26,v27\n" \
M_"v[120:123],a[56:59],a[76:79],v[120:123]\n" \
C_"v14,v28,v29\n" \
C_"v15,v30,v31\n" \
M_"v[124:127],a[60:63],a[76:79],v[124:127]\n" \
S_"lgkmcnt(0)\n" \
"LT118_%=:\n" \
"s_memrealtime s[18:19]\n" \
S_"lgkmcnt(0)\n" \
"s_sub_u32 s18,s18,s20\n" \
"s_cmp_ge_u32 s18,1647\n" \
"s_cbranch_scc1 LT118d_%=\n" \
"s_sleep 1\n" \
"s_branch LT118_%=\n" \
"LT118d_%=:\n" \
"ds_write_b128 v239,a[196:199] offset:27168\n" \
M_"v[128:131],a[32:35],a[80:83],v[128:131]\n" \
P_"v[32:33],v[32:33],s[80:81]\n" \
P_"v[34:35],v[34:35],s[80:81]\n" \
M_"v[132:135],a[36:39],a[80:83],v[132:135]\n" \
P_"v[36:37],v[36:37],s[80:81]\n" \
P_"v[38:39],v[38:39],s[80:81]\n" \
M_"v[136:139],a[40:43],a[80:83],v[136:139]\n" \
"ds_write_b128 v239,a[200:203] offset:28224\n" \
C_"v16,v32,v33\n" \
M_"v[140:143],a[44:47],a[80:83],v[140:143]\n" \
C_"v17,v34,v35\n" \
C_"v18,v36,v37\n" \
M_"v[144:147],a[48:51],a[80:83],v[144:147]\n" \
C_"v19,v38,v39\n" \
P_"v[40:41],v[40:41],s[80:81]\n" \
M_"v[148:151],a[52:55],a[80:83],v[148:151]\n" \
P_"v[42:43],v[42:43],s[80:81]\n" \
P_"v[44:45],v[44:45],s[80:81]\n" \
M_"v[152:155],a[56:59],a[80:83],v[152:155]\n" \
"ds_write_b128 v239,a[204:207] offset:29280\n" \
P_"v[46:47],v[46:47],s[80:81]\n" \
M_"v[156:159],a[60:63],a[80:83],v[156:159]\n" \
C_"v20,v40,v41\n" \
C_"v21,v42,v43\n" \
M_"v[160:163],a[32:35],a[84:87],v[160:163]\n" \
C_"v22,v44,v45\n" \
C_"v23,v46,v47\n" \
M_"v[164:167],a[36:39],a[84:87],v[164:167]\n" \
"ds_write_b128 v239,a[208:211] offset:30336\n" \
P_"v[48:49],v[48:49],s[80:81]\n" \
M_"v[168:171],a[40:43],a[84:87],v[168:171]\n" \
P_"v[50:51],v[50:51],s[80:81]\n" \
P_"v[52:53],v[52:53],s[80:81]\n" \
M_"v[172:175],a[44:47],a[84:87],v[172:175]\n" \
P_"v[54:55],v[54:55],s[80:81]\n" \
C_"v24,v48,v49\n" \
M_"v[176:179],a[48:51],a[84:87],v[176:179]\n" \
"ds_write_b128 v239,a[212:215] offset:31392\n" \
C_"v25,v50,v51\n" \
M_"v[180:183],a[52:55],a[84:87],v[180:183]\n" \
C_"v26,v52,v53\n" \
C_"v27,v54,v55\n" \
M_"v[184:187],a[56:59],a[84:87],v[184:187]\n" \
P_"v[56:57],v[56:57],s[80:81]\n" \
P_"v[58:59],v[58:59],s[80:81]\n" \
M_"v[188:191],a[60:63],a[84:87],v[188:191]\n" \
S_"lgkmcnt(0)\n" \
"LT119_%=:\n" \
"s_memrealtime s[18:19]\n" \
S_"lgkmcnt(0)\n" \
"s_sub_u32 s18,s18,s20\n" \
"s_cmp_ge_u32 s18,1670\n" \
"s_cbranch_scc1 LT119d_%=\n" \
"s_sleep 1\n" \
"s_branch LT119_%=\n" \
"LT119d_%=:\n" \
M_"v[192:195],a[32:35],a[88:91],v[192:195]\n" \
"ds_write_b128 v239,a[216:219] offset:32448\n" \
P_"v[60:61],v[60:61],s[80:81]\n" \
M_"v[196:199],a[36:39],a[88:91],v[196:199]\n" \
P_"v[62:63],v[62:63],s[80:81]\n" \
C_"v28,v56,v57\n" \
M_"v[200:203],a[40:43],a[88:91],v[200:203]\n" \
C_"v29,v58,v59\n" \
C_"v30,v60,v61\n" \
M_"v[204:207],a[44:47],a[88:91],v[204:207]\n" \
"ds_write_b128 v239,a[220:223] offset:33504\n" \
C_"v31,v62,v63\n" \
M_"v[208:211],a[48:51],a[88:91],v[208:211]\n" \
P_"v[64:65],v[64:65],s[80:81]\n" \
P_"v[66:67],v[66:67],s[80:81]\n" \
M_"v[212:215],a[52:55],a[88:91],v[212:215]\n" \
P_"v[68:69],v[68:69],s[80:81]\n" \
P_"v[70:71],v[70:71],s[80:81]\n" \
M_"v[216:219],a[56:59],a[88:91],v[216:219]\n" \
C_"v32,v64,v65\n" \
C_"v33,v66,v67\n" \
M_"v[220:223],a[60:63],a[88:91],v[220:223]\n" \
C_"v34,v68,v69\n" \
C_"v35,v70,v71\n" \
P_"v[72:73],v[72:73],s[80:81]\n" \
P_"v[74:75],v[74:75],s[80:81]\n" \
P_"v[76:77],v[76:77],s[80:81]\n" \
P_"v[78:79],v[78:79],s[80:81]\n" \
C_"v36,v72,v73\n" \
C_"v37,v74,v75\n" \
C_"v38,v76,v77\n" \
C_"v39,v78,v79\n" \
P_"v[80:81],v[80:81],s[80:81]\n" \
P_"v[82:83],v[82:83],s[80:81]\n" \
P_"v[84:85],v[84:85],s[80:81]\n" \
P_"v[86:87],v[86:87],s[80:81]\n" \
C_"v40,v80,v81\n" \
C_"v41,v82,v83\n" \
C_"v42,v84,v85\n" \
C_"v43,v86,v87\n" \
P_"v[88:89],v[88:89],s[80:81]\n" \
P_"v[90:91],v[90:91],s[80:81]\n" \
P_"v[92:93],v[92:93],s[80:81]\n" \
P_"v[94:95],v[94:95],s[80:81]\n" \
C_"v44,v88,v89\n" \
C_"v45,v90,v91\n" \
C_"v46,v92,v93\n" \
C_"v47,v94,v95\n" \
P_"v[96:97],v[96:97],s[80:81]\n" \
P_"v[98:99],v[98:99],s[80:81]\n" \
P_"v[100:101],v[100:101],s[80:81]\n" \
P_"v[102:103],v[102:103],s[80:81]\n" \
C_"v48,v96,v97\n" \
C_"v49,v98,v99\n" \
C_"v50,v100,v101\n" \
C_"v51,v102,v103\n" \
P_"v[104:105],v[104:105],s[80:81]\n" \
P_"v[106:107],v[106:107],s[80:81]\n" \
P_"v[108:109],v[108:109],s[80:81]\n" \
P_"v[110:111],v[110:111],s[80:81]\n" \
C_"v52,v104,v105\n" \
C_"v53,v106,v107\n" \
C_"v54,v108,v109\n" \
C_"v55,v110,v111\n" \
P_"v[112:113],v[112:113],s[80:81]\n" \
P_"v[114:115],v[114:115],s[80:81]\n" \
P_"v[116:117],v[116:117],s[80:81]\n" \
P_"v[118:119],v[118:119],s[80:81]\n" \
C_"v56,v112,v113\n" \
C_"v57,v114,v115\n" \
C_"v58,v116,v117\n" \
C_"v59,v118,v119\n" \
P_"v[120:121],v[120:121],s[80:81]\n" \
P_"v[122:123],v[122:123],s[80:81]\n" \
P_"v[124:125],v[124:125],s[80:81]\n" \
P_"v[126:127],v[126:127],s[80:81]\n" \
C_"v60,v120,v121\n" \
C_"v61,v122,v123\n" \
C_"v62,v124,v125\n" \
C_"v63,v126,v127\n" \
P_"v[128:129],v[128:129],s[80:81]\n" \
P_"v[130:131],v[130:131],s[80:81]\n" \
P_"v[132:133],v[132:133],s[80:81]\n" \
P_"v[134:135],v[134:135],s[80:81]\n" \
C_"v64,v128,v129\n" \
C_"v65,v130,v131\n" \
C_"v66,v132,v133\n" \
C_"v67,v134,v135\n" \
P_"v[136:137],v[136:137],s[80:81]\n" \
P_"v[138:139],v[138:139],s[80:81]\n" \
P_"v[140:141],v[140:141],s[80:81]\n" \
P_"v[142:143],v[142:143],s[80:81]\n" \
C_"v68,v136,v137\n" \
C_"v69,v138,v139\n" \
C_"v70,v140,v141\n" \
C_"v71,v142,v143\n" \
P_"v[144:145],v[144:145],s[80:81]\n" \
P_"v[146:147],v[146:147],s[80:81]\n" \
P_"v[148:149],v[148:149],s[80:81]\n" \
P_"v[150:151],v[150:151],s[80:81]\n" \
C_"v72,v144,v145\n" \
C_"v73,v146,v147\n" \
C_"v74,v148,v149\n" \
C_"v75,v150,v151\n" \
P_"v[152:153],v[152:153],s[80:81]\n" \
P_"v[154:155],v[154:155],s[80:81]\n" \
P_"v[156:157],v[156:157],s[80:81]\n" \
P_"v[158:159],v[158:159],s[80:81]\n" \
C_"v76,v152,v153\n" \
C_"v77,v154,v155\n" \
C_"v78,v156,v157\n" \
C_"v79,v158,v159\n" \
P_"v[160:161],v[160:161],s[80:81]\n" \
P_"v[162:163],v[162:163],s[80:81]\n" \
P_"v[164:165],v[164:165],s[80:81]\n" \
P_"v[166:167],v[166:167],s[80:81]\n" \
C_"v80,v160,v161\n" \
C_"v81,v162,v163\n" \
C_"v82,v164,v165\n" \
C_"v83,v166,v167\n" \
P_"v[168:169],v[168:169],s[80:81]\n" \
P_"v[170:171],v[170:171],s[80:81]\n" \
P_"v[172:173],v[172:173],s[80:81]\n" \
P_"v[174:175],v[174:175],s[80:81]\n" \
C_"v84,v168,v169\n" \
C_"v85,v170,v171\n" \
C_"v86,v172,v173\n" \
C_"v87,v174,v175\n" \
P_"v[176:177],v[176:177],s[80:81]\n" \
P_"v[178:179],v[178:179],s[80:81]\n" \
P_"v[180:181],v[180:181],s[80:81]\n" \
P_"v[182:183],v[182:183],s[80:81]\n" \
C_"v88,v176,v177\n" \
C_"v89,v178,v179\n" \
C_"v90,v180,v181\n" \
C_"v91,v182,v183\n" \
P_"v[184:185],v[184:185],s[80:81]\n" \
P_"v[186:187],v[186:187],s[80:81]\n" \
P_"v[188:189],v[188:189],s[80:81]\n" \
P_"v[190:191],v[190:191],s[80:81]\n" \
C_"v92,v184,v185\n" \
C_"v93,v186,v187\n" \
C_"v94,v188,v189\n" \
C_"v95,v190,v191\n" \
P_"v[192:193],v[192:193],s[80:81]\n" \
P_"v[194:195],v[194:195],s[80:81]\n" \
P_"v[196:197],v[196:197],s[80:81]\n" \
P_"v[198:199],v[198:199],s[80:81]\n" \
C_"v96,v192,v193\n" \
C_"v97,v194,v195\n" \
C_"v98,v196,v197\n" \
C_"v99,v198,v199\n" \
P_"v[200:201],v[200:201],s[80:81]\n" \
P_"v[202:203],v[202:203],s[80:81]\n" \
P_"v[204:205],v[204:205],s[80:81]\n" \
P_"v[206:207],v[206:207],s[80:81]\n" \
C_"v100,v200,v201\n" \
C_"v101,v202,v203\n" \
C_"v102,v204,v205\n" \
C_"v103,v206,v207\n" \
P_"v[208:209],v[208:209],s[80:81]\n" \
P_"v[210:211],v[210:211],s[80:81]\n" \
P_"v[212:213],v[212:213],s[80:81]\n" \
P_"v[214:215],v[214:215],s[80:81]\n" \
C_"v104,v208,v209\n" \
C_"v105,v210,v211\n" \
C_"v106,v212,v213\n" \
C_"v107,v214,v215\n" \
P_"v[216:217],v[216:217],s[80:81]\n" \
P_"v[218:219],v[218:219],s[80:81]\n" \
P_"v[220:221],v[220:221],s[80:81]\n" \
P_"v[222:223],v[222:223],s[80:81]\n" \
C_"v108,v216,v217\n" \
C_"v109,v218,v219\n" \
C_"v110,v220,v221\n" \
C_"v111,v222,v223\n" \
W_"a160,0\n" \
W_"a161,0\n" \
W_"a162,0\n" \
W_"a163,0\n" \
W_"a164,0\n" \
W_"a165,0\n" \
W_"a166,0\n" \
W_"a167,0\n" \
W_"a168,0\n" \
W_"a169,0\n" \
W_"a170,0\n" \
W_"a171,0\n" \
W_"a172,0\n" \
W_"a173,0\n" \
W_"a174,0\n" \
W_"a175,0\n" \
W_"a176,0\n" \
W_"a177,0\n" \
W_"a178,0\n" \
W_"a179,0\n" \
W_"a180,0\n" \
W_"a181,0\n" \
W_"a182,0\n" \
W_"a183,0\n" \
W_"a184,0\n" \
W_"a185,0\n" \
W_"a186,0\n" \
W_"a187,0\n" \
W_"a188,0\n" \
W_"a189,0\n" \
W_"a190,0\n" \
W_"a191,0\n" \
W_"a192,0\n" \
W_"a193,0\n" \
W_"a194,0\n" \
W_"a195,0\n" \
W_"a196,0\n" \
W_"a197,0\n" \
W_"a198,0\n" \
W_"a199,0\n" \
W_"a200,0\n" \
W_"a201,0\n" \
W_"a202,0\n" \
W_"a203,0\n" \
W_"a204,0\n" \
W_"a205,0\n" \
W_"a206,0\n" \
W_"a207,0\n" \
W_"a208,0\n" \
W_"a209,0\n" \
W_"a210,0\n" \
W_"a211,0\n" \
W_"a212,0\n" \
W_"a213,0\n" \
W_"a214,0\n" \
W_"a215,0\n" \
W_"a216,0\n" \
W_"a217,0\n" \
W_"a218,0\n" \
W_"a219,0\n" \
W_"a220,0\n" \
W_"a221,0\n" \
W_"a222,0\n" \
W_"a223,0\n" \
"v_mov_b32 v239,v246\n" \
"v_lshlrev_b32 v240,4,v255\n" \
A_"s74,s83,28672\n" \
"v_lshrrev_b32 v236,2,v255\n" \
"v_mul_u32_u24 v242,1152,v236\n" \
"v_and_b32 v237,3,v255\n" \
"v_lshl_add_u32 v242,v237,4,v242\n" \
"v_add_u32 v241,s74,v240\n" \
"v_lshrrev_b32 v236,4,v255\n" \
"v_lshlrev_b32 v250,10,v236\n" \
"v_bfe_u32 v237,v255,2,2\n" \
"v_lshl_add_u32 v250,v237,7,v250\n" \
"v_and_b32 v238,3,v255\n" \
"v_lshl_add_u32 v250,v238,4,v250\n" \
"v_add_u32 v250,s74,v250\n" \
"v_lshrrev_b32 v237,1,v237\n" \
"v_xor_b32 v238,1,v237\n" \
"v_lshl_add_u32 v251,v238,6,v250\n" \
"v_lshl_add_u32 v250,v237,6,v250\n" \
"v_add_u32 v249,17408,v242\n" \
"v_add_u32 v243,34816,v242\n" \
"v_add_u32 v244,52224,v242\n" \
"s_lshl_b32 s15,s56,5\n" \
A_"s15,s15,s89\n" \
"s_lshl_b32 s15,s15,2\n" \
A_"s15,s15,1024\n" \
"v_lshlrev_b32 v246,9,v255\n" \
"v_add_u32 v246,s15,v246\n" \
"v_subrev_u32 v236,0,v255\n" \
"v_cmp_gt_u32 vcc,8,v236\n" \
"v_mov_b32 v236,0x40000000\n" \
"s_nop 0\n" \
"v_cndmask_b32 v246,v236,v246,vcc\n" \
"s_lshl_b32 s15,s89,2\n" \
A_"s15,s15,s56\n" \
"s_lshl_b32 s15,s15,7\n" \
A_"s15,s15,1024\n" \
"v_and_b32 v248,7,v255\n" \
"v_cmp_eq_u32 vcc,s89,v248\n" \
"v_xor_b32 v236,1,v248\n" \
"s_nop 0\n" \
"v_cndmask_b32 v248,v248,v236,vcc\n" \
"v_lshlrev_b32 v248,2,v248\n" \
"v_add_u32 v248,s15,v248\n" \
"v_and_b32 v245,15,v255\n" \
"v_mul_u32_u24 v245,2304,v245\n" \
"v_lshrrev_b32 v236,4,v255\n" \
"v_lshl_add_u32 v245,v236,4,v245\n" \
"s_cmp_lt_u32 s89,7\n" \
"s_cselect_b32 s15,0x40000000,0\n" \
"v_add_u32 v247,s15,v240\n" \
A_"s15,s75,6\n" \
"s_lshl_b32 s68,s15,7\n" \
"s_mul_i32 s76,s15,14336\n" \
"s_lshl_b32 s15,s75,6\n" \
A_"s69,s15,1024\n" \
"s_lshl_b32 s77,s75,10\n" \
A_"s77,s77,114688\n" \
"s_mul_i32 s15,s89,129024\n" \
A_"s71,s76,s15\n" \
A_"s72,s77,s15\n" \
"s_mul_i32 s15,s89,36864\n" \
A_"s79,s15,s68\n" \
S_"lgkmcnt(0)\n" \
"LT120_%=:\n" \
"s_memrealtime s[18:19]\n" \
S_"lgkmcnt(0)\n" \
"s_sub_u32 s18,s18,s20\n" \
"s_cmp_ge_u32 s18,1725\n" \
"s_cbranch_scc1 LT120d_%=\n" \
"s_sleep 1\n" \
"s_branch LT120_%=\n" \
"LT120d_%=:\n" \
"; ---- attent\n" \
"LATT_%=:\n" \
"s_mov_b32 s97,0\n" \
"v_mov_b32 v253,v234\n" \
"v_mov_b32 v252,64\n" \
S_"lgkmcnt(0)\n" \
"s_memrealtime s[18:19]\n" \
S_"lgkmcnt(0)\n" \
"s_mov_b32 s16,122\n" \
"s_mov_b32 s17,260\n" \
"s_cmp_lt_u32 s56,2\n" \
"s_cselect_b32 s31,s16,122\n" \
"s_cselect_b32 s17,s17,0\n" \
"s_cselect_b32 s16,s16,119\n" \
A_"s31,s31,s18\n" \
"; ---- attent2\n" \
"LATT2_%=:\n" \
S_"vmcnt(24)\n" \
R_"a[224:227],v232 offset:0\n" \
R_"a[228:231],v232 offset:64\n" \
R_"a[232:235],v232 offset:128\n" \
R_"a[236:239],v232 offset:192\n" \
R_"a[240:243],v232 offset:512\n" \
R_"a[244:247],v232 offset:576\n" \
R_"a[248:251],v232 offset:640\n" \
R_"a[252:255],v232 offset:704\n" \
"s_mov_b32 s59,6\n" \
"; ---- att0\n" \
S_"lgkmcnt(7)\n" \
M_"v[144:147],a[224:227],v[0:3],0\n" \
A_"s66,s66,8192\n" \
"v_mov_b32 v216,0\n" \
S_"lgkmcnt(6)\n" \
M_"v[144:147],a[228:231],v[4:7],v[144:147]\n" \
"s_and_b32 s66,s66,131071\n" \
"v_mov_b32 v217,0\n" \
S_"lgkmcnt(5)\n" \
M_"v[144:147],a[232:235],v[8:11],v[144:147]\n" \
A_"m0,s83,0\n" \
"v_mov_b32 v218,0\n" \
S_"lgkmcnt(4)\n" \
M_"v[144:147],a[236:239],v[12:15],v[144:147]\n" \
A_"s67,s66,4096\n" \
"v_mov_b32 v219,0\n" \
S_"lgkmcnt(3)\n" \
M_"v[148:151],a[240:243],v[0:3],0\n" \
S_"lgkmcnt(2)\n" \
M_"v[148:151],a[244:247],v[4:7],v[148:151]\n" \
S_"lgkmcnt(0)\n" \
M_"v[148:151],a[248:251],v[8:11],v[148:151]\n" \
M_"v[148:151],a[252:255],v[12:15],v[148:151]\n" \
L_"v228,s[40:43],s66 offen " KVP " lds\n" \
L_"v229,s[40:43],s66 offen offset:1056 " KVP " lds\n" \
L_"v230,s[40:43],s66 offen offset:2112 " KVP " lds\n" \
L_"v231,s[40:43],s66 offen offset:3168 " KVP " lds\n" \
"s_nop 7\n" \
M_"v[152:155],a[224:227],v[16:19],0\n" \
"v_max3_f32 v234,v144,v145,v146\n" \
"v_max3_f32 v235,v147,v148,v149\n" \
"v_max3_f32 v234,v234,v150,v151\n" \
"v_max_f32 v234,v234,v235\n" \
"v_mov_b32 v235,v234\n" \
"s_nop 1\n" \
"v_permlane32_swap_b32 v234,v235\n" \
M_"v[152:155],a[228:231],v[20:23],v[152:155]\n" \
"v_max_f32 v234,v234,v235\n" \
"v_mov_b32 v235,v234\n" \
"s_nop 1\n" \
"v_permlane16_swap_b32 v234,v235\n" \
"v_max_f32 v234,v234,v235\n" \
"v_sub_f32 v168,0,v234\n" \
"v_sub_f32 v169,0,v234\n" \
M_"v[152:155],a[232:235],v[24:27],v[152:155]\n" \
"v_sub_f32 v170,0,v234\n" \
"v_sub_f32 v171,0,v234\n" \
"v_sub_f32 v144,v144,v234\n" \
"v_sub_f32 v145,v145,v234\n" \
"v_sub_f32 v146,v146,v234\n" \
"v_sub_f32 v147,v147,v234\n" \
"v_sub_f32 v148,v148,v234\n" \
M_"v[152:155],a[236:239],v[28:31],v[152:155]\n" \
"v_sub_f32 v149,v149,v234\n" \
"v_sub_f32 v150,v150,v234\n" \
"v_sub_f32 v151,v151,v234\n" \
"v_exp_f32 v144,v144\n" \
"v_exp_f32 v145,v145\n" \
M_"v[156:159],a[240:243],v[16:19],0\n" \
"v_exp_f32 v146,v146\n" \
"v_exp_f32 v147,v147\n" \
C_"v160,v144,v145\n" \
"v_exp_f32 v148,v148\n" \
M_"v[156:159],a[244:247],v[20:23],v[156:159]\n" \
"v_exp_f32 v149,v149\n" \
C_"v161,v146,v147\n" \
"v_exp_f32 v150,v150\n" \
"v_exp_f32 v151,v151\n" \
M_"v[156:159],a[248:251],v[24:27],v[156:159]\n" \
C_"v162,v148,v149\n" \
C_"v163,v150,v151\n" \
"v_mov_b32 v220,0\n" \
"v_mov_b32 v221,0\n" \
"v_mov_b32 v222,0\n" \
"v_mov_b32 v223,0\n" \
M_"v[156:159],a[252:255],v[28:31],v[156:159]\n" \
S_"vmcnt(20)\n" \
T_"v[112:113],v233 offset:8704\n" \
T_"v[114:115],v233 offset:9216\n" \
T_"v[116:117],v233 offset:8736\n" \
T_"v[118:119],v233 offset:9248\n" \
T_"v[120:121],v233 offset:8768\n" \
T_"v[122:123],v233 offset:9280\n" \
T_"v[124:125],v233 offset:8800\n" \
T_"v[126:127],v233 offset:9312\n" \
M_"v[144:147],a[224:227],v[32:35],0\n" \
T_"v[128:129],v233 offset:8832\n" \
"v_max3_f32 v234,v152,v153,v154\n" \
"v_max3_f32 v235,v155,v156,v157\n" \
M_"v[144:147],a[228:231],v[36:39],v[144:147]\n" \
T_"v[130:131],v233 offset:9344\n" \
"v_max3_f32 v234,v234,v158,v159\n" \
"v_max_f32 v234,v234,v235\n" \
M_"v[144:147],a[232:235],v[40:43],v[144:147]\n" \
T_"v[132:133],v233 offset:8864\n" \
"v_mov_b32 v235,v234\n" \
"s_nop 1\n" \
M_"v[144:147],a[236:239],v[44:47],v[144:147]\n" \
T_"v[134:135],v233 offset:9376\n" \
"v_permlane32_swap_b32 v234,v235\n" \
"v_max_f32 v234,v234,v235\n" \
M_"v[148:151],a[240:243],v[32:35],0\n" \
T_"v[136:137],v233 offset:8896\n" \
"v_mov_b32 v235,v234\n" \
"s_nop 1\n" \
M_"v[148:151],a[244:247],v[36:39],v[148:151]\n" \
T_"v[138:139],v233 offset:9408\n" \
"v_permlane16_swap_b32 v234,v235\n" \
"v_max_f32 v234,v234,v235\n" \
M_"v[148:151],a[248:251],v[40:43],v[148:151]\n" \
T_"v[140:141],v233 offset:8928\n" \
"v_sub_f32 v172,0,v234\n" \
"v_sub_f32 v173,0,v234\n" \
M_"v[148:151],a[252:255],v[44:47],v[148:151]\n" \
T_"v[142:143],v233 offset:9440\n" \
"v_sub_f32 v174,0,v234\n" \
"v_sub_f32 v175,0,v234\n" \
S_"lgkmcnt(6)\n" \
M_"a[0:3],v[112:115],v[160:163],0\n" \
"v_sub_f32 v152,v152,v234\n" \
"v_sub_f32 v153,v153,v234\n" \
"v_sub_f32 v154,v154,v234\n" \
M_"a[4:7],v[116:119],v[160:163],0\n" \
"v_sub_f32 v155,v155,v234\n" \
"v_sub_f32 v156,v156,v234\n" \
"v_sub_f32 v157,v157,v234\n" \
M_"a[8:11],v[120:123],v[160:163],0\n" \
"v_sub_f32 v158,v158,v234\n" \
"v_sub_f32 v159,v159,v234\n" \
M_"a[12:15],v[124:127],v[160:163],0\n" \
"v_exp_f32 v152,v152\n" \
"v_exp_f32 v153,v153\n" \
M_"a[16:19],v[128:131],v[160:163],0\n" \
"v_exp_f32 v154,v154\n" \
S_"lgkmcnt(1)\n" \
M_"a[20:23],v[132:135],v[160:163],0\n" \
"v_exp_f32 v155,v155\n" \
C_"v164,v152,v153\n" \
M_"a[24:27],v[136:139],v[160:163],0\n" \
"v_exp_f32 v156,v156\n" \
"v_exp_f32 v157,v157\n" \
S_"lgkmcnt(0)\n" \
M_"a[28:31],v[140:143],v[160:163],0\n" \
C_"v165,v154,v155\n" \
"v_exp_f32 v158,v158\n" \
M_"v[196:199],v[224:227],v[160:163],0\n" \
"v_exp_f32 v159,v159\n" \
C_"v166,v156,v157\n" \
C_"v167,v158,v159\n" \
M_"v[152:155],a[224:227],v[48:51],0\n" \
A_"m0,s83,4224\n" \
"v_max3_f32 v234,v144,v145,v146\n" \
M_"v[152:155],a[228:231],v[52:55],v[152:155]\n" \
L_"v228,s[40:43],s67 offen " KVP " lds\n" \
"v_max3_f32 v235,v147,v148,v149\n" \
"v_max3_f32 v234,v234,v150,v151\n" \
M_"v[152:155],a[232:235],v[56:59],v[152:155]\n" \
L_"v229,s[40:43],s67 offen offset:1056 " KVP " lds\n" \
"v_max_f32 v234,v234,v235\n" \
"v_mov_b32 v235,v234\n" \
M_"v[152:155],a[236:239],v[60:63],v[152:155]\n" \
L_"v230,s[40:43],s67 offen offset:2112 " KVP " lds\n" \
"s_nop 1\n" \
"v_permlane32_swap_b32 v234,v235\n" \
M_"v[156:159],a[240:243],v[48:51],0\n" \
L_"v231,s[40:43],s67 offen offset:3168 " KVP " lds\n" \
"v_max_f32 v234,v234,v235\n" \
"v_mov_b32 v235,v234\n" \
M_"v[156:159],a[244:247],v[52:55],v[156:159]\n" \
"s_nop 1\n" \
"v_permlane16_swap_b32 v234,v235\n" \
"v_max_f32 v234,v234,v235\n" \
M_"v[156:159],a[248:251],v[56:59],v[156:159]\n" \
"v_sub_f32 v176,0,v234\n" \
"v_sub_f32 v177,0,v234\n" \
"v_sub_f32 v178,0,v234\n" \
M_"v[156:159],a[252:255],v[60:63],v[156:159]\n" \
"v_sub_f32 v179,0,v234\n" \
"v_sub_f32 v144,v144,v234\n" \
"v_sub_f32 v145,v145,v234\n" \
M_"a[32:35],v[112:115],v[164:167],0\n" \
"v_sub_f32 v146,v146,v234\n" \
"v_sub_f32 v147,v147,v234\n" \
M_"a[36:39],v[116:119],v[164:167],0\n" \
"v_sub_f32 v148,v148,v234\n" \
"v_sub_f32 v149,v149,v234\n" \
"v_sub_f32 v150,v150,v234\n" \
M_"a[40:43],v[120:123],v[164:167],0\n" \
"v_sub_f32 v151,v151,v234\n" \
"v_exp_f32 v144,v144\n" \
M_"a[44:47],v[124:127],v[164:167],0\n" \
"v_exp_f32 v145,v145\n" \
M_"a[48:51],v[128:131],v[164:167],0\n" \
"v_exp_f32 v146,v146\n" \
"v_exp_f32 v147,v147\n" \
M_"a[52:55],v[132:135],v[164:167],0\n" \
C_"v160,v144,v145\n" \
"v_exp_f32 v148,v148\n" \
M_"a[56:59],v[136:139],v[164:167],0\n" \
"v_exp_f32 v149,v149\n" \
C_"v161,v146,v147\n" \
M_"a[60:63],v[140:143],v[164:167],0\n" \
"v_exp_f32 v150,v150\n" \
M_"v[200:203],v[224:227],v[164:167],0\n" \
"v_exp_f32 v151,v151\n" \
C_"v162,v148,v149\n" \
C_"v163,v150,v151\n" \
M_"v[144:147],a[224:227],v[64:67],0\n" \
A_"m0,s83,8704\n" \
"v_max3_f32 v234,v152,v153,v154\n" \
M_"v[144:147],a[228:231],v[68:71],v[144:147]\n" \
L_"v228,s[44:47],s66 offen " KVP " lds\n" \
"v_max3_f32 v235,v155,v156,v157\n" \
"v_max3_f32 v234,v234,v158,v159\n" \
M_"v[144:147],a[232:235],v[72:75],v[144:147]\n" \
L_"v229,s[44:47],s66 offen offset:1056 " KVP " lds\n" \
"v_max_f32 v234,v234,v235\n" \
"v_mov_b32 v235,v234\n" \
M_"v[144:147],a[236:239],v[76:79],v[144:147]\n" \
L_"v230,s[44:47],s66 offen offset:2112 " KVP " lds\n" \
"s_nop 1\n" \
"v_permlane32_swap_b32 v234,v235\n" \
M_"v[148:151],a[240:243],v[64:67],0\n" \
L_"v231,s[44:47],s66 offen offset:3168 " KVP " lds\n" \
"v_max_f32 v234,v234,v235\n" \
"v_mov_b32 v235,v234\n" \
M_"v[148:151],a[244:247],v[68:71],v[148:151]\n" \
"s_nop 1\n" \
"v_permlane16_swap_b32 v234,v235\n" \
"v_max_f32 v234,v234,v235\n" \
M_"v[148:151],a[248:251],v[72:75],v[148:151]\n" \
"v_sub_f32 v180,0,v234\n" \
"v_sub_f32 v181,0,v234\n" \
"v_sub_f32 v182,0,v234\n" \
M_"v[148:151],a[252:255],v[76:79],v[148:151]\n" \
"v_sub_f32 v183,0,v234\n" \
"v_sub_f32 v152,v152,v234\n" \
"v_sub_f32 v153,v153,v234\n" \
M_"a[64:67],v[112:115],v[160:163],0\n" \
"v_sub_f32 v154,v154,v234\n" \
"v_sub_f32 v155,v155,v234\n" \
M_"a[68:71],v[116:119],v[160:163],0\n" \
"v_sub_f32 v156,v156,v234\n" \
"v_sub_f32 v157,v157,v234\n" \
"v_sub_f32 v158,v158,v234\n" \
M_"a[72:75],v[120:123],v[160:163],0\n" \
"v_sub_f32 v159,v159,v234\n" \
"v_exp_f32 v152,v152\n" \
M_"a[76:79],v[124:127],v[160:163],0\n" \
"v_exp_f32 v153,v153\n" \
M_"a[80:83],v[128:131],v[160:163],0\n" \
"v_exp_f32 v154,v154\n" \
"v_exp_f32 v155,v155\n" \
M_"a[84:87],v[132:135],v[160:163],0\n" \
C_"v164,v152,v153\n" \
"v_exp_f32 v156,v156\n" \
M_"a[88:91],v[136:139],v[160:163],0\n" \
"v_exp_f32 v157,v157\n" \
C_"v165,v154,v155\n" \
M_"a[92:95],v[140:143],v[160:163],0\n" \
"v_exp_f32 v158,v158\n" \
M_"v[204:207],v[224:227],v[160:163],0\n" \
"v_exp_f32 v159,v159\n" \
C_"v166,v156,v157\n" \
C_"v167,v158,v159\n" \
M_"v[152:155],a[224:227],v[80:83],0\n" \
A_"m0,s83,12928\n" \
"v_max3_f32 v234,v144,v145,v146\n" \
M_"v[152:155],a[228:231],v[84:87],v[152:155]\n" \
L_"v228,s[44:47],s67 offen " KVP " lds\n" \
"v_max3_f32 v235,v147,v148,v149\n" \
"v_max3_f32 v234,v234,v150,v151\n" \
M_"v[152:155],a[232:235],v[88:91],v[152:155]\n" \
L_"v229,s[44:47],s67 offen offset:1056 " KVP " lds\n" \
"v_max_f32 v234,v234,v235\n" \
"v_mov_b32 v235,v234\n" \
M_"v[152:155],a[236:239],v[92:95],v[152:155]\n" \
L_"v230,s[44:47],s67 offen offset:2112 " KVP " lds\n" \
"s_nop 1\n" \
"v_permlane32_swap_b32 v234,v235\n" \
M_"v[156:159],a[240:243],v[80:83],0\n" \
L_"v231,s[44:47],s67 offen offset:3168 " KVP " lds\n" \
"v_max_f32 v234,v234,v235\n" \
"v_mov_b32 v235,v234\n" \
M_"v[156:159],a[244:247],v[84:87],v[156:159]\n" \
"s_nop 1\n" \
"v_permlane16_swap_b32 v234,v235\n" \
"v_max_f32 v234,v234,v235\n" \
M_"v[156:159],a[248:251],v[88:91],v[156:159]\n" \
"v_sub_f32 v184,0,v234\n" \
"v_sub_f32 v185,0,v234\n" \
"v_sub_f32 v186,0,v234\n" \
M_"v[156:159],a[252:255],v[92:95],v[156:159]\n" \
"v_sub_f32 v187,0,v234\n" \
"v_sub_f32 v144,v144,v234\n" \
"v_sub_f32 v145,v145,v234\n" \
M_"a[96:99],v[112:115],v[164:167],0\n" \
"v_sub_f32 v146,v146,v234\n" \
"v_sub_f32 v147,v147,v234\n" \
M_"a[100:103],v[116:119],v[164:167],0\n" \
"v_sub_f32 v148,v148,v234\n" \
"v_sub_f32 v149,v149,v234\n" \
"v_sub_f32 v150,v150,v234\n" \
M_"a[104:107],v[120:123],v[164:167],0\n" \
"v_sub_f32 v151,v151,v234\n" \
"v_exp_f32 v144,v144\n" \
M_"a[108:111],v[124:127],v[164:167],0\n" \
"v_exp_f32 v145,v145\n" \
M_"a[112:115],v[128:131],v[164:167],0\n" \
"v_exp_f32 v146,v146\n" \
"v_exp_f32 v147,v147\n" \
M_"a[116:119],v[132:135],v[164:167],0\n" \
C_"v160,v144,v145\n" \
"v_exp_f32 v148,v148\n" \
M_"a[120:123],v[136:139],v[164:167],0\n" \
"v_exp_f32 v149,v149\n" \
C_"v161,v146,v147\n" \
M_"a[124:127],v[140:143],v[164:167],0\n" \
"v_exp_f32 v150,v150\n" \
M_"v[208:211],v[224:227],v[164:167],0\n" \
"v_exp_f32 v151,v151\n" \
C_"v162,v148,v149\n" \
C_"v163,v150,v151\n" \
M_"v[144:147],a[224:227],v[96:99],0\n" \
S_"vmcnt(24)\n" \
"v_max3_f32 v234,v152,v153,v154\n" \
"v_max3_f32 v235,v155,v156,v157\n" \
M_"v[144:147],a[228:231],v[100:103],v[144:147]\n" \
R_"a[224:227],v232 offset:17408\n" \
"v_max3_f32 v234,v234,v158,v159\n" \
"v_max_f32 v234,v234,v235\n" \
M_"v[144:147],a[232:235],v[104:107],v[144:147]\n" \
R_"a[228:231],v232 offset:17472\n" \
"v_mov_b32 v235,v234\n" \
"s_nop 1\n" \
M_"v[144:147],a[236:239],v[108:111],v[144:147]\n" \
R_"a[232:235],v232 offset:17536\n" \
"v_permlane32_swap_b32 v234,v235\n" \
"v_max_f32 v234,v234,v235\n" \
M_"v[148:151],a[240:243],v[96:99],0\n" \
R_"a[236:239],v232 offset:17600\n" \
"v_mov_b32 v235,v234\n" \
"s_nop 1\n" \
M_"v[148:151],a[244:247],v[100:103],v[148:151]\n" \
R_"a[240:243],v232 offset:17920\n" \
"v_permlane16_swap_b32 v234,v235\n" \
"v_max_f32 v234,v234,v235\n" \
M_"v[148:151],a[248:251],v[104:107],v[148:151]\n" \
R_"a[244:247],v232 offset:17984\n" \
"v_sub_f32 v188,0,v234\n" \
"v_sub_f32 v189,0,v234\n" \
M_"v[148:151],a[252:255],v[108:111],v[148:151]\n" \
R_"a[248:251],v232 offset:18048\n" \
"v_sub_f32 v190,0,v234\n" \
"v_sub_f32 v191,0,v234\n" \
M_"a[128:131],v[112:115],v[160:163],0\n" \
R_"a[252:255],v232 offset:18112\n" \
"v_sub_f32 v152,v152,v234\n" \
"v_sub_f32 v153,v153,v234\n" \
"v_sub_f32 v154,v154,v234\n" \
M_"a[132:135],v[116:119],v[160:163],0\n" \
"v_sub_f32 v155,v155,v234\n" \
"v_sub_f32 v156,v156,v234\n" \
"v_sub_f32 v157,v157,v234\n" \
M_"a[136:139],v[120:123],v[160:163],0\n" \
"v_sub_f32 v158,v158,v234\n" \
"v_sub_f32 v159,v159,v234\n" \
M_"a[140:143],v[124:127],v[160:163],0\n" \
"v_exp_f32 v152,v152\n" \
"v_exp_f32 v153,v153\n" \
M_"a[144:147],v[128:131],v[160:163],0\n" \
"v_exp_f32 v154,v154\n" \
M_"a[148:151],v[132:135],v[160:163],0\n" \
"v_exp_f32 v155,v155\n" \
C_"v164,v152,v153\n" \
M_"a[152:155],v[136:139],v[160:163],0\n" \
"v_exp_f32 v156,v156\n" \
"v_exp_f32 v157,v157\n" \
M_"a[156:159],v[140:143],v[160:163],0\n" \
C_"v165,v154,v155\n" \
"v_exp_f32 v158,v158\n" \
M_"v[212:215],v[224:227],v[160:163],0\n" \
"v_exp_f32 v159,v159\n" \
C_"v166,v156,v157\n" \
C_"v167,v158,v159\n" \
"v_max3_f32 v234,v144,v145,v146\n" \
"v_max3_f32 v235,v147,v148,v149\n" \
"v_max3_f32 v234,v234,v150,v151\n" \
"v_max_f32 v234,v234,v235\n" \
"v_mov_b32 v235,v234\n" \
"s_nop 1\n" \
"v_permlane32_swap_b32 v234,v235\n" \
"v_max_f32 v234,v234,v235\n" \
"v_mov_b32 v235,v234\n" \
"s_nop 1\n" \
"v_permlane16_swap_b32 v234,v235\n" \
"v_max_f32 v234,v234,v235\n" \
"v_sub_f32 v192,0,v234\n" \
"v_sub_f32 v193,0,v234\n" \
"v_sub_f32 v194,0,v234\n" \
"v_sub_f32 v195,0,v234\n" \
"v_sub_f32 v144,v144,v234\n" \
"v_sub_f32 v145,v145,v234\n" \
"v_sub_f32 v146,v146,v234\n" \
"v_sub_f32 v147,v147,v234\n" \
"v_sub_f32 v148,v148,v234\n" \
"v_sub_f32 v149,v149,v234\n" \
"v_sub_f32 v150,v150,v234\n" \
"v_sub_f32 v151,v151,v234\n" \
"s_cmp_eq_u32 s97,0\n" \
"s_cbranch_scc0 LA_%=\n" \
"s_memrealtime s[18:19]\n" \
"; ---- fatt\n" \
"LF_%=:\n" \
S_"lgkmcnt(0)\n" \
"s_sub_u32 s60,s18,s31\n" \
"s_cmp_lt_i32 s60,0\n" \
"s_cbranch_scc1 LCa_%=\n" \
"LCad_%=:\n" \
A_"s31,s18,s16\n" \
M_"v[152:155],a[224:227],v[0:3],v[168:171]\n" \
"v_exp_f32 v144,v144\n" \
M_"v[152:155],a[228:231],v[4:7],v[152:155]\n" \
A_"s66,s66,8192\n" \
M_"v[152:155],a[232:235],v[8:11],v[152:155]\n" \
"v_exp_f32 v145,v145\n" \
M_"v[152:155],a[236:239],v[12:15],v[152:155]\n" \
"s_and_b32 s66,s66,131071\n" \
M_"v[156:159],a[240:243],v[0:3],v[168:171]\n" \
"v_exp_f32 v146,v146\n" \
M_"v[156:159],a[244:247],v[4:7],v[156:159]\n" \
A_"m0,s83,17408\n" \
A_"s67,s66,4096\n" \
M_"v[156:159],a[248:251],v[8:11],v[156:159]\n" \
"v_exp_f32 v147,v147\n" \
M_"v[156:159],a[252:255],v[12:15],v[156:159]\n" \
L_"v228,s[40:43],s66 offen " KVP " lds\n" \
M_"a[160:163],v[112:115],v[164:167],a[160:163]\n" \
C_"v160,v144,v145\n" \
C_"v161,v146,v147\n" \
M_"a[164:167],v[116:119],v[164:167],a[164:167]\n" \
"v_exp_f32 v148,v148\n" \
M_"a[168:171],v[120:123],v[164:167],a[168:171]\n" \
L_"v229,s[40:43],s66 offen offset:1056 " KVP " lds\n" \
M_"a[172:175],v[124:127],v[164:167],a[172:175]\n" \
"v_exp_f32 v149,v149\n" \
M_"a[176:179],v[128:131],v[164:167],a[176:179]\n" \
L_"v230,s[40:43],s66 offen offset:2112 " KVP " lds\n" \
M_"a[180:183],v[132:135],v[164:167],a[180:183]\n" \
"v_exp_f32 v150,v150\n" \
M_"a[184:187],v[136:139],v[164:167],a[184:187]\n" \
L_"v231,s[40:43],s66 offen offset:3168 " KVP " lds\n" \
M_"a[188:191],v[140:143],v[164:167],a[188:191]\n" \
"v_exp_f32 v151,v151\n" \
M_"v[216:219],v[224:227],v[164:167],v[216:219]\n" \
C_"v162,v148,v149\n" \
C_"v163,v150,v151\n" \
M_"v[144:147],a[224:227],v[16:19],v[172:175]\n" \
"v_exp_f32 v152,v152\n" \
M_"v[144:147],a[228:231],v[20:23],v[144:147]\n" \
"v_exp_f32 v153,v153\n" \
M_"v[144:147],a[232:235],v[24:27],v[144:147]\n" \
"v_exp_f32 v154,v154\n" \
M_"v[144:147],a[236:239],v[28:31],v[144:147]\n" \
"v_exp_f32 v155,v155\n" \
M_"v[148:151],a[240:243],v[16:19],v[172:175]\n" \
C_"v164,v152,v153\n" \
C_"v165,v154,v155\n" \
M_"v[148:151],a[244:247],v[20:23],v[148:151]\n" \
"v_exp_f32 v156,v156\n" \
M_"v[148:151],a[248:251],v[24:27],v[148:151]\n" \
"v_exp_f32 v157,v157\n" \
M_"v[148:151],a[252:255],v[28:31],v[148:151]\n" \
"v_exp_f32 v158,v158\n" \
M_"a[192:195],v[112:115],v[160:163],a[192:195]\n" \
"v_exp_f32 v159,v159\n" \
M_"a[196:199],v[116:119],v[160:163],a[196:199]\n" \
S_"vmcnt(20)\n" \
T_"v[112:113],v233 offset:26112\n" \
M_"a[200:203],v[120:123],v[160:163],a[200:203]\n" \
T_"v[114:115],v233 offset:26624\n" \
M_"a[204:207],v[124:127],v[160:163],a[204:207]\n" \
T_"v[116:117],v233 offset:26144\n" \
T_"v[118:119],v233 offset:26656\n" \
M_"a[208:211],v[128:131],v[160:163],a[208:211]\n" \
C_"v166,v156,v157\n" \
T_"v[120:121],v233 offset:26176\n" \
M_"a[212:215],v[132:135],v[160:163],a[212:215]\n" \
T_"v[122:123],v233 offset:26688\n" \
M_"a[216:219],v[136:139],v[160:163],a[216:219]\n" \
C_"v167,v158,v159\n" \
T_"v[124:125],v233 offset:26208\n" \
M_"a[220:223],v[140:143],v[160:163],a[220:223]\n" \
T_"v[126:127],v233 offset:26720\n" \
M_"v[220:223],v[224:227],v[160:163],v[220:223]\n" \
M_"v[152:155],a[224:227],v[32:35],v[176:179]\n" \
T_"v[128:129],v233 offset:26240\n" \
T_"v[130:131],v233 offset:26752\n" \
M_"v[152:155],a[228:231],v[36:39],v[152:155]\n" \
T_"v[132:133],v233 offset:26272\n" \
T_"v[134:135],v233 offset:26784\n" \
M_"v[152:155],a[232:235],v[40:43],v[152:155]\n" \
T_"v[136:137],v233 offset:26304\n" \
T_"v[138:139],v233 offset:26816\n" \
M_"v[152:155],a[236:239],v[44:47],v[152:155]\n" \
T_"v[140:141],v233 offset:26336\n" \
T_"v[142:143],v233 offset:26848\n" \
M_"v[156:159],a[240:243],v[32:35],v[176:179]\n" \
"v_exp_f32 v144,v144\n" \
M_"v[156:159],a[244:247],v[36:39],v[156:159]\n" \
"v_exp_f32 v145,v145\n" \
M_"v[156:159],a[248:251],v[40:43],v[156:159]\n" \
"v_exp_f32 v146,v146\n" \
M_"v[156:159],a[252:255],v[44:47],v[156:159]\n" \
S_"lgkmcnt(8)\n" \
M_"a[0:3],v[112:115],v[164:167],a[0:3]\n" \
"v_exp_f32 v147,v147\n" \
M_"a[4:7],v[116:119],v[164:167],a[4:7]\n" \
C_"v160,v144,v145\n" \
C_"v161,v146,v147\n" \
M_"a[8:11],v[120:123],v[164:167],a[8:11]\n" \
"v_exp_f32 v148,v148\n" \
M_"a[12:15],v[124:127],v[164:167],a[12:15]\n" \
S_"lgkmcnt(0)\n" \
M_"a[16:19],v[128:131],v[164:167],a[16:19]\n" \
"v_exp_f32 v149,v149\n" \
M_"a[20:23],v[132:135],v[164:167],a[20:23]\n" \
"v_exp_f32 v150,v150\n" \
M_"a[24:27],v[136:139],v[164:167],a[24:27]\n" \
"v_exp_f32 v151,v151\n" \
M_"a[28:31],v[140:143],v[164:167],a[28:31]\n" \
M_"v[196:199],v[224:227],v[164:167],v[196:199]\n" \
C_"v162,v148,v149\n" \
C_"v163,v150,v151\n" \
M_"v[144:147],a[224:227],v[48:51],v[180:183]\n" \
"v_exp_f32 v152,v152\n" \
M_"v[144:147],a[228:231],v[52:55],v[144:147]\n" \
A_"m0,s83,21632\n" \
M_"v[144:147],a[232:235],v[56:59],v[144:147]\n" \
"v_exp_f32 v153,v153\n" \
M_"v[144:147],a[236:239],v[60:63],v[144:147]\n" \
L_"v228,s[40:43],s67 offen " KVP " lds\n" \
M_"v[148:151],a[240:243],v[48:51],v[180:183]\n" \
"v_exp_f32 v154,v154\n" \
M_"v[148:151],a[244:247],v[52:55],v[148:151]\n" \
L_"v229,s[40:43],s67 offen offset:1056 " KVP " lds\n" \
M_"v[148:151],a[248:251],v[56:59],v[148:151]\n" \
"v_exp_f32 v155,v155\n" \
M_"v[148:151],a[252:255],v[60:63],v[148:151]\n" \
L_"v230,s[40:43],s67 offen offset:2112 " KVP " lds\n" \
M_"a[32:35],v[112:115],v[160:163],a[32:35]\n" \
C_"v164,v152,v153\n" \
C_"v165,v154,v155\n" \
M_"a[36:39],v[116:119],v[160:163],a[36:39]\n" \
"v_exp_f32 v156,v156\n" \
M_"a[40:43],v[120:123],v[160:163],a[40:43]\n" \
L_"v231,s[40:43],s67 offen offset:3168 " KVP " lds\n" \
M_"a[44:47],v[124:127],v[160:163],a[44:47]\n" \
"v_exp_f32 v157,v157\n" \
M_"a[48:51],v[128:131],v[160:163],a[48:51]\n" \
M_"a[52:55],v[132:135],v[160:163],a[52:55]\n" \
"v_exp_f32 v158,v158\n" \
M_"a[56:59],v[136:139],v[160:163],a[56:59]\n" \
M_"a[60:63],v[140:143],v[160:163],a[60:63]\n" \
"v_exp_f32 v159,v159\n" \
M_"v[200:203],v[224:227],v[160:163],v[200:203]\n" \
C_"v166,v156,v157\n" \
C_"v167,v158,v159\n" \
M_"v[152:155],a[224:227],v[64:67],v[184:187]\n" \
"v_exp_f32 v144,v144\n" \
M_"v[152:155],a[228:231],v[68:71],v[152:155]\n" \
A_"m0,s83,26112\n" \
M_"v[152:155],a[232:235],v[72:75],v[152:155]\n" \
"v_exp_f32 v145,v145\n" \
M_"v[152:155],a[236:239],v[76:79],v[152:155]\n" \
L_"v228,s[44:47],s66 offen " KVP " lds\n" \
M_"v[156:159],a[240:243],v[64:67],v[184:187]\n" \
"v_exp_f32 v146,v146\n" \
M_"v[156:159],a[244:247],v[68:71],v[156:159]\n" \
L_"v229,s[44:47],s66 offen offset:1056 " KVP " lds\n" \
M_"v[156:159],a[248:251],v[72:75],v[156:159]\n" \
"v_exp_f32 v147,v147\n" \
M_"v[156:159],a[252:255],v[76:79],v[156:159]\n" \
L_"v230,s[44:47],s66 offen offset:2112 " KVP " lds\n" \
M_"a[64:67],v[112:115],v[164:167],a[64:67]\n" \
C_"v160,v144,v145\n" \
C_"v161,v146,v147\n" \
M_"a[68:71],v[116:119],v[164:167],a[68:71]\n" \
"v_exp_f32 v148,v148\n" \
M_"a[72:75],v[120:123],v[164:167],a[72:75]\n" \
L_"v231,s[44:47],s66 offen offset:3168 " KVP " lds\n" \
M_"a[76:79],v[124:127],v[164:167],a[76:79]\n" \
"v_exp_f32 v149,v149\n" \
M_"a[80:83],v[128:131],v[164:167],a[80:83]\n" \
M_"a[84:87],v[132:135],v[164:167],a[84:87]\n" \
"v_exp_f32 v150,v150\n" \
M_"a[88:91],v[136:139],v[164:167],a[88:91]\n" \
M_"a[92:95],v[140:143],v[164:167],a[92:95]\n" \
"v_exp_f32 v151,v151\n" \
M_"v[204:207],v[224:227],v[164:167],v[204:207]\n" \
C_"v162,v148,v149\n" \
C_"v163,v150,v151\n" \
M_"v[144:147],a[224:227],v[80:83],v[188:191]\n" \
"v_exp_f32 v152,v152\n" \
M_"v[144:147],a[228:231],v[84:87],v[144:147]\n" \
A_"m0,s83,30336\n" \
M_"v[144:147],a[232:235],v[88:91],v[144:147]\n" \
"v_exp_f32 v153,v153\n" \
M_"v[144:147],a[236:239],v[92:95],v[144:147]\n" \
L_"v228,s[44:47],s67 offen " KVP " lds\n" \
M_"v[148:151],a[240:243],v[80:83],v[188:191]\n" \
"v_exp_f32 v154,v154\n" \
M_"v[148:151],a[244:247],v[84:87],v[148:151]\n" \
L_"v229,s[44:47],s67 offen offset:1056 " KVP " lds\n" \
M_"v[148:151],a[248:251],v[88:91],v[148:151]\n" \
"v_exp_f32 v155,v155\n" \
M_"v[148:151],a[252:255],v[92:95],v[148:151]\n" \
L_"v230,s[44:47],s67 offen offset:2112 " KVP " lds\n" \
M_"a[96:99],v[112:115],v[160:163],a[96:99]\n" \
C_"v164,v152,v153\n" \
C_"v165,v154,v155\n" \
M_"a[100:103],v[116:119],v[160:163],a[100:103]\n" \
"v_exp_f32 v156,v156\n" \
M_"a[104:107],v[120:123],v[160:163],a[104:107]\n" \
L_"v231,s[44:47],s67 offen offset:3168 " KVP " lds\n" \
M_"a[108:111],v[124:127],v[160:163],a[108:111]\n" \
"v_exp_f32 v157,v157\n" \
M_"a[112:115],v[128:131],v[160:163],a[112:115]\n" \
"v_add_u32 v252,1,v252\n" \
M_"a[116:119],v[132:135],v[160:163],a[116:119]\n" \
"v_exp_f32 v158,v158\n" \
M_"a[120:123],v[136:139],v[160:163],a[120:123]\n" \
"ds_write_b32 v253,v252 offset:0\n" \
M_"a[124:127],v[140:143],v[160:163],a[124:127]\n" \
"v_exp_f32 v159,v159\n" \
M_"v[208:211],v[224:227],v[160:163],v[208:211]\n" \
C_"v166,v156,v157\n" \
C_"v167,v158,v159\n" \
M_"v[152:155],a[224:227],v[96:99],v[192:195]\n" \
S_"vmcnt(24)\n" \
M_"v[152:155],a[228:231],v[100:103],v[152:155]\n" \
R_"a[224:227],v232 offset:0\n" \
M_"v[152:155],a[232:235],v[104:107],v[152:155]\n" \
R_"a[228:231],v232 offset:64\n" \
R_"a[232:235],v232 offset:128\n" \
M_"v[152:155],a[236:239],v[108:111],v[152:155]\n" \
"v_exp_f32 v144,v144\n" \
M_"v[156:159],a[240:243],v[96:99],v[192:195]\n" \
R_"a[236:239],v232 offset:192\n" \
R_"a[240:243],v232 offset:512\n" \
M_"v[156:159],a[244:247],v[100:103],v[156:159]\n" \
"v_exp_f32 v145,v145\n" \
M_"v[156:159],a[248:251],v[104:107],v[156:159]\n" \
R_"a[244:247],v232 offset:576\n" \
M_"v[156:159],a[252:255],v[108:111],v[156:159]\n" \
R_"a[248:251],v232 offset:640\n" \
R_"a[252:255],v232 offset:704\n" \
M_"a[128:131],v[112:115],v[164:167],a[128:131]\n" \
"v_exp_f32 v146,v146\n" \
M_"a[132:135],v[116:119],v[164:167],a[132:135]\n" \
"v_exp_f32 v147,v147\n" \
M_"a[136:139],v[120:123],v[164:167],a[136:139]\n" \
C_"v160,v144,v145\n" \
C_"v161,v146,v147\n" \
M_"a[140:143],v[124:127],v[164:167],a[140:143]\n" \
"v_exp_f32 v148,v148\n" \
M_"a[144:147],v[128:131],v[164:167],a[144:147]\n" \
"v_exp_f32 v149,v149\n" \
M_"a[148:151],v[132:135],v[164:167],a[148:151]\n" \
"v_exp_f32 v150,v150\n" \
M_"a[152:155],v[136:139],v[164:167],a[152:155]\n" \
"v_exp_f32 v151,v151\n" \
M_"a[156:159],v[140:143],v[164:167],a[156:159]\n" \
"s_memrealtime s[18:19]\n" \
"s_cmp_eq_u32 s59,0\n" \
M_"v[212:215],v[224:227],v[164:167],v[212:215]\n" \
C_"v162,v148,v149\n" \
C_"v163,v150,v151\n" \
"s_cselect_b32 s42,0,s42\n" \
"s_cselect_b32 s46,0,s46\n" \
"s_cselect_b32 s16,s17,s16\n" \
S_"lgkmcnt(0)\n" \
"s_sub_u32 s60,s18,s31\n" \
"s_cmp_lt_i32 s60,0\n" \
"s_cbranch_scc1 LCb_%=\n" \
"LCbd_%=:\n" \
A_"s31,s18,s16\n" \
M_"v[144:147],a[224:227],v[0:3],v[168:171]\n" \
"v_exp_f32 v152,v152\n" \
M_"v[144:147],a[228:231],v[4:7],v[144:147]\n" \
A_"s66,s66,8192\n" \
M_"v[144:147],a[232:235],v[8:11],v[144:147]\n" \
"v_exp_f32 v153,v153\n" \
M_"v[144:147],a[236:239],v[12:15],v[144:147]\n" \
"s_and_b32 s66,s66,131071\n" \
M_"v[148:151],a[240:243],v[0:3],v[168:171]\n" \
"v_exp_f32 v154,v154\n" \
M_"v[148:151],a[244:247],v[4:7],v[148:151]\n" \
A_"m0,s83,0\n" \
A_"s67,s66,4096\n" \
M_"v[148:151],a[248:251],v[8:11],v[148:151]\n" \
"v_exp_f32 v155,v155\n" \
M_"v[148:151],a[252:255],v[12:15],v[148:151]\n" \
L_"v228,s[40:43],s66 offen " KVP " lds\n" \
M_"a[160:163],v[112:115],v[160:163],a[160:163]\n" \
C_"v164,v152,v153\n" \
C_"v165,v154,v155\n" \
M_"a[164:167],v[116:119],v[160:163],a[164:167]\n" \
"v_exp_f32 v156,v156\n" \
M_"a[168:171],v[120:123],v[160:163],a[168:171]\n" \
L_"v229,s[40:43],s66 offen offset:1056 " KVP " lds\n" \
M_"a[172:175],v[124:127],v[160:163],a[172:175]\n" \
"v_exp_f32 v157,v157\n" \
M_"a[176:179],v[128:131],v[160:163],a[176:179]\n" \
L_"v230,s[40:43],s66 offen offset:2112 " KVP " lds\n" \
M_"a[180:183],v[132:135],v[160:163],a[180:183]\n" \
"v_exp_f32 v158,v158\n" \
M_"a[184:187],v[136:139],v[160:163],a[184:187]\n" \
L_"v231,s[40:43],s66 offen offset:3168 " KVP " lds\n" \
M_"a[188:191],v[140:143],v[160:163],a[188:191]\n" \
"v_exp_f32 v159,v159\n" \
M_"v[216:219],v[224:227],v[160:163],v[216:219]\n" \
C_"v166,v156,v157\n" \
C_"v167,v158,v159\n" \
M_"v[152:155],a[224:227],v[16:19],v[172:175]\n" \
"v_exp_f32 v144,v144\n" \
M_"v[152:155],a[228:231],v[20:23],v[152:155]\n" \
"v_exp_f32 v145,v145\n" \
M_"v[152:155],a[232:235],v[24:27],v[152:155]\n" \
"v_exp_f32 v146,v146\n" \
M_"v[152:155],a[236:239],v[28:31],v[152:155]\n" \
"v_exp_f32 v147,v147\n" \
M_"v[156:159],a[240:243],v[16:19],v[172:175]\n" \
C_"v160,v144,v145\n" \
C_"v161,v146,v147\n" \
M_"v[156:159],a[244:247],v[20:23],v[156:159]\n" \
"v_exp_f32 v148,v148\n" \
M_"v[156:159],a[248:251],v[24:27],v[156:159]\n" \
"v_exp_f32 v149,v149\n" \
M_"v[156:159],a[252:255],v[28:31],v[156:159]\n" \
"v_exp_f32 v150,v150\n" \
M_"a[192:195],v[112:115],v[164:167],a[192:195]\n" \
"v_exp_f32 v151,v151\n" \
M_"a[196:199],v[116:119],v[164:167],a[196:199]\n" \
S_"vmcnt(20)\n" \
T_"v[112:113],v233 offset:8704\n" \
M_"a[200:203],v[120:123],v[164:167],a[200:203]\n" \
T_"v[114:115],v233 offset:9216\n" \
M_"a[204:207],v[124:127],v[164:167],a[204:207]\n" \
T_"v[116:117],v233 offset:8736\n" \
T_"v[118:119],v233 offset:9248\n" \
M_"a[208:211],v[128:131],v[164:167],a[208:211]\n" \
C_"v162,v148,v149\n" \
T_"v[120:121],v233 offset:8768\n" \
M_"a[212:215],v[132:135],v[164:167],a[212:215]\n" \
T_"v[122:123],v233 offset:9280\n" \
M_"a[216:219],v[136:139],v[164:167],a[216:219]\n" \
C_"v163,v150,v151\n" \
T_"v[124:125],v233 offset:8800\n" \
M_"a[220:223],v[140:143],v[164:167],a[220:223]\n" \
T_"v[126:127],v233 offset:9312\n" \
M_"v[220:223],v[224:227],v[164:167],v[220:223]\n" \
M_"v[144:147],a[224:227],v[32:35],v[176:179]\n" \
T_"v[128:129],v233 offset:8832\n" \
T_"v[130:131],v233 offset:9344\n" \
M_"v[144:147],a[228:231],v[36:39],v[144:147]\n" \
T_"v[132:133],v233 offset:8864\n" \
T_"v[134:135],v233 offset:9376\n" \
M_"v[144:147],a[232:235],v[40:43],v[144:147]\n" \
T_"v[136:137],v233 offset:8896\n" \
T_"v[138:139],v233 offset:9408\n" \
M_"v[144:147],a[236:239],v[44:47],v[144:147]\n" \
T_"v[140:141],v233 offset:8928\n" \
T_"v[142:143],v233 offset:9440\n" \
M_"v[148:151],a[240:243],v[32:35],v[176:179]\n" \
"v_exp_f32 v152,v152\n" \
M_"v[148:151],a[244:247],v[36:39],v[148:151]\n" \
"v_exp_f32 v153,v153\n" \
M_"v[148:151],a[248:251],v[40:43],v[148:151]\n" \
"v_exp_f32 v154,v154\n" \
M_"v[148:151],a[252:255],v[44:47],v[148:151]\n" \
S_"lgkmcnt(8)\n" \
M_"a[0:3],v[112:115],v[160:163],a[0:3]\n" \
"v_exp_f32 v155,v155\n" \
M_"a[4:7],v[116:119],v[160:163],a[4:7]\n" \
C_"v164,v152,v153\n" \
C_"v165,v154,v155\n" \
M_"a[8:11],v[120:123],v[160:163],a[8:11]\n" \
"v_exp_f32 v156,v156\n" \
M_"a[12:15],v[124:127],v[160:163],a[12:15]\n" \
S_"lgkmcnt(0)\n" \
M_"a[16:19],v[128:131],v[160:163],a[16:19]\n" \
"v_exp_f32 v157,v157\n" \
M_"a[20:23],v[132:135],v[160:163],a[20:23]\n" \
"v_exp_f32 v158,v158\n" \
M_"a[24:27],v[136:139],v[160:163],a[24:27]\n" \
"v_exp_f32 v159,v159\n" \
M_"a[28:31],v[140:143],v[160:163],a[28:31]\n" \
M_"v[196:199],v[224:227],v[160:163],v[196:199]\n" \
C_"v166,v156,v157\n" \
C_"v167,v158,v159\n" \
M_"v[152:155],a[224:227],v[48:51],v[180:183]\n" \
"v_exp_f32 v144,v144\n" \
M_"v[152:155],a[228:231],v[52:55],v[152:155]\n" \
A_"m0,s83,4224\n" \
M_"v[152:155],a[232:235],v[56:59],v[152:155]\n" \
"v_exp_f32 v145,v145\n" \
M_"v[152:155],a[236:239],v[60:63],v[152:155]\n" \
L_"v228,s[40:43],s67 offen " KVP " lds\n" \
M_"v[156:159],a[240:243],v[48:51],v[180:183]\n" \
"v_exp_f32 v146,v146\n" \
M_"v[156:159],a[244:247],v[52:55],v[156:159]\n" \
L_"v229,s[40:43],s67 offen offset:1056 " KVP " lds\n" \
M_"v[156:159],a[248:251],v[56:59],v[156:159]\n" \
"v_exp_f32 v147,v147\n" \
M_"v[156:159],a[252:255],v[60:63],v[156:159]\n" \
L_"v230,s[40:43],s67 offen offset:2112 " KVP " lds\n" \
M_"a[32:35],v[112:115],v[164:167],a[32:35]\n" \
C_"v160,v144,v145\n" \
C_"v161,v146,v147\n" \
M_"a[36:39],v[116:119],v[164:167],a[36:39]\n" \
"v_exp_f32 v148,v148\n" \
M_"a[40:43],v[120:123],v[164:167],a[40:43]\n" \
L_"v231,s[40:43],s67 offen offset:3168 " KVP " lds\n" \
M_"a[44:47],v[124:127],v[164:167],a[44:47]\n" \
"v_exp_f32 v149,v149\n" \
M_"a[48:51],v[128:131],v[164:167],a[48:51]\n" \
M_"a[52:55],v[132:135],v[164:167],a[52:55]\n" \
"v_exp_f32 v150,v150\n" \
M_"a[56:59],v[136:139],v[164:167],a[56:59]\n" \
M_"a[60:63],v[140:143],v[164:167],a[60:63]\n" \
"v_exp_f32 v151,v151\n" \
M_"v[200:203],v[224:227],v[164:167],v[200:203]\n" \
C_"v162,v148,v149\n" \
C_"v163,v150,v151\n" \
M_"v[144:147],a[224:227],v[64:67],v[184:187]\n" \
"v_exp_f32 v152,v152\n" \
M_"v[144:147],a[228:231],v[68:71],v[144:147]\n" \
A_"m0,s83,8704\n" \
M_"v[144:147],a[232:235],v[72:75],v[144:147]\n" \
"v_exp_f32 v153,v153\n" \
M_"v[144:147],a[236:239],v[76:79],v[144:147]\n" \
L_"v228,s[44:47],s66 offen " KVP " lds\n" \
M_"v[148:151],a[240:243],v[64:67],v[184:187]\n" \
"v_exp_f32 v154,v154\n" \
M_"v[148:151],a[244:247],v[68:71],v[148:151]\n" \
L_"v229,s[44:47],s66 offen offset:1056 " KVP " lds\n" \
M_"v[148:151],a[248:251],v[72:75],v[148:151]\n" \
"v_exp_f32 v155,v155\n" \
M_"v[148:151],a[252:255],v[76:79],v[148:151]\n" \
L_"v230,s[44:47],s66 offen offset:2112 " KVP " lds\n" \
M_"a[64:67],v[112:115],v[160:163],a[64:67]\n" \
C_"v164,v152,v153\n" \
C_"v165,v154,v155\n" \
M_"a[68:71],v[116:119],v[160:163],a[68:71]\n" \
"v_exp_f32 v156,v156\n" \
M_"a[72:75],v[120:123],v[160:163],a[72:75]\n" \
L_"v231,s[44:47],s66 offen offset:3168 " KVP " lds\n" \
M_"a[76:79],v[124:127],v[160:163],a[76:79]\n" \
"v_exp_f32 v157,v157\n" \
M_"a[80:83],v[128:131],v[160:163],a[80:83]\n" \
M_"a[84:87],v[132:135],v[160:163],a[84:87]\n" \
"v_exp_f32 v158,v158\n" \
M_"a[88:91],v[136:139],v[160:163],a[88:91]\n" \
M_"a[92:95],v[140:143],v[160:163],a[92:95]\n" \
"v_exp_f32 v159,v159\n" \
M_"v[204:207],v[224:227],v[160:163],v[204:207]\n" \
C_"v166,v156,v157\n" \
C_"v167,v158,v159\n" \
M_"v[152:155],a[224:227],v[80:83],v[188:191]\n" \
"v_exp_f32 v144,v144\n" \
M_"v[152:155],a[228:231],v[84:87],v[152:155]\n" \
A_"m0,s83,12928\n" \
M_"v[152:155],a[232:235],v[88:91],v[152:155]\n" \
"v_exp_f32 v145,v145\n" \
M_"v[152:155],a[236:239],v[92:95],v[152:155]\n" \
L_"v228,s[44:47],s67 offen " KVP " lds\n" \
M_"v[156:159],a[240:243],v[80:83],v[188:191]\n" \
"v_exp_f32 v146,v146\n" \
M_"v[156:159],a[244:247],v[84:87],v[156:159]\n" \
L_"v229,s[44:47],s67 offen offset:1056 " KVP " lds\n" \
M_"v[156:159],a[248:251],v[88:91],v[156:159]\n" \
"v_exp_f32 v147,v147\n" \
M_"v[156:159],a[252:255],v[92:95],v[156:159]\n" \
L_"v230,s[44:47],s67 offen offset:2112 " KVP " lds\n" \
M_"a[96:99],v[112:115],v[164:167],a[96:99]\n" \
C_"v160,v144,v145\n" \
C_"v161,v146,v147\n" \
M_"a[100:103],v[116:119],v[164:167],a[100:103]\n" \
"v_exp_f32 v148,v148\n" \
M_"a[104:107],v[120:123],v[164:167],a[104:107]\n" \
L_"v231,s[44:47],s67 offen offset:3168 " KVP " lds\n" \
M_"a[108:111],v[124:127],v[164:167],a[108:111]\n" \
"v_exp_f32 v149,v149\n" \
M_"a[112:115],v[128:131],v[164:167],a[112:115]\n" \
"v_add_u32 v252,1,v252\n" \
M_"a[116:119],v[132:135],v[164:167],a[116:119]\n" \
"v_exp_f32 v150,v150\n" \
M_"a[120:123],v[136:139],v[164:167],a[120:123]\n" \
"ds_write_b32 v253,v252 offset:0\n" \
M_"a[124:127],v[140:143],v[164:167],a[124:127]\n" \
"v_exp_f32 v151,v151\n" \
M_"v[208:211],v[224:227],v[164:167],v[208:211]\n" \
C_"v162,v148,v149\n" \
C_"v163,v150,v151\n" \
M_"v[144:147],a[224:227],v[96:99],v[192:195]\n" \
S_"vmcnt(24)\n" \
M_"v[144:147],a[228:231],v[100:103],v[144:147]\n" \
R_"a[224:227],v232 offset:17408\n" \
M_"v[144:147],a[232:235],v[104:107],v[144:147]\n" \
R_"a[228:231],v232 offset:17472\n" \
R_"a[232:235],v232 offset:17536\n" \
M_"v[144:147],a[236:239],v[108:111],v[144:147]\n" \
"v_exp_f32 v152,v152\n" \
M_"v[148:151],a[240:243],v[96:99],v[192:195]\n" \
R_"a[236:239],v232 offset:17600\n" \
R_"a[240:243],v232 offset:17920\n" \
M_"v[148:151],a[244:247],v[100:103],v[148:151]\n" \
"v_exp_f32 v153,v153\n" \
M_"v[148:151],a[248:251],v[104:107],v[148:151]\n" \
R_"a[244:247],v232 offset:17984\n" \
M_"v[148:151],a[252:255],v[108:111],v[148:151]\n" \
R_"a[248:251],v232 offset:18048\n" \
R_"a[252:255],v232 offset:18112\n" \
M_"a[128:131],v[112:115],v[160:163],a[128:131]\n" \
"v_exp_f32 v154,v154\n" \
M_"a[132:135],v[116:119],v[160:163],a[132:135]\n" \
"v_exp_f32 v155,v155\n" \
M_"a[136:139],v[120:123],v[160:163],a[136:139]\n" \
C_"v164,v152,v153\n" \
C_"v165,v154,v155\n" \
M_"a[140:143],v[124:127],v[160:163],a[140:143]\n" \
"v_exp_f32 v156,v156\n" \
M_"a[144:147],v[128:131],v[160:163],a[144:147]\n" \
"v_exp_f32 v157,v157\n" \
M_"a[148:151],v[132:135],v[160:163],a[148:151]\n" \
"v_exp_f32 v158,v158\n" \
M_"a[152:155],v[136:139],v[160:163],a[152:155]\n" \
"v_exp_f32 v159,v159\n" \
M_"a[156:159],v[140:143],v[160:163],a[156:159]\n" \
"s_memrealtime s[18:19]\n" \
"s_sub_u32 s59,s59,1\n" \
M_"v[212:215],v[224:227],v[160:163],v[212:215]\n" \
C_"v166,v156,v157\n" \
C_"v167,v158,v159\n" \
"s_cbranch_scc0 LF_%=\n" \
"; ---- fatt15\n" \
"LF15_%=:\n" \
S_"lgkmcnt(0)\n" \
"s_sub_u32 s60,s18,s31\n" \
"s_cmp_lt_i32 s60,0\n" \
"s_cbranch_scc1 LCc_%=\n" \
"LCcd_%=:\n" \
A_"s31,s18,s16\n" \
M_"v[152:155],a[224:227],v[0:3],v[168:171]\n" \
"v_exp_f32 v144,v144\n" \
M_"v[152:155],a[228:231],v[4:7],v[152:155]\n" \
M_"v[152:155],a[232:235],v[8:11],v[152:155]\n" \
"v_exp_f32 v145,v145\n" \
M_"v[152:155],a[236:239],v[12:15],v[152:155]\n" \
M_"v[156:159],a[240:243],v[0:3],v[168:171]\n" \
"v_exp_f32 v146,v146\n" \
M_"v[156:159],a[244:247],v[4:7],v[156:159]\n" \
M_"v[156:159],a[248:251],v[8:11],v[156:159]\n" \
"v_exp_f32 v147,v147\n" \
M_"v[156:159],a[252:255],v[12:15],v[156:159]\n" \
M_"a[160:163],v[112:115],v[164:167],a[160:163]\n" \
C_"v160,v144,v145\n" \
C_"v161,v146,v147\n" \
M_"a[164:167],v[116:119],v[164:167],a[164:167]\n" \
"v_exp_f32 v148,v148\n" \
M_"a[168:171],v[120:123],v[164:167],a[168:171]\n" \
M_"a[172:175],v[124:127],v[164:167],a[172:175]\n" \
"v_exp_f32 v149,v149\n" \
M_"a[176:179],v[128:131],v[164:167],a[176:179]\n" \
M_"a[180:183],v[132:135],v[164:167],a[180:183]\n" \
"v_exp_f32 v150,v150\n" \
M_"a[184:187],v[136:139],v[164:167],a[184:187]\n" \
M_"a[188:191],v[140:143],v[164:167],a[188:191]\n" \
"v_exp_f32 v151,v151\n" \
M_"v[216:219],v[224:227],v[164:167],v[216:219]\n" \
C_"v162,v148,v149\n" \
C_"v163,v150,v151\n" \
M_"v[144:147],a[224:227],v[16:19],v[172:175]\n" \
"v_exp_f32 v152,v152\n" \
M_"v[144:147],a[228:231],v[20:23],v[144:147]\n" \
"v_exp_f32 v153,v153\n" \
M_"v[144:147],a[232:235],v[24:27],v[144:147]\n" \
"v_exp_f32 v154,v154\n" \
M_"v[144:147],a[236:239],v[28:31],v[144:147]\n" \
"v_exp_f32 v155,v155\n" \
M_"v[148:151],a[240:243],v[16:19],v[172:175]\n" \
C_"v164,v152,v153\n" \
C_"v165,v154,v155\n" \
M_"v[148:151],a[244:247],v[20:23],v[148:151]\n" \
"v_exp_f32 v156,v156\n" \
M_"v[148:151],a[248:251],v[24:27],v[148:151]\n" \
"v_exp_f32 v157,v157\n" \
M_"v[148:151],a[252:255],v[28:31],v[148:151]\n" \
"v_exp_f32 v158,v158\n" \
M_"a[192:195],v[112:115],v[160:163],a[192:195]\n" \
"v_exp_f32 v159,v159\n" \
M_"a[196:199],v[116:119],v[160:163],a[196:199]\n" \
S_"vmcnt(16)\n" \
T_"v[112:113],v233 offset:26112\n" \
M_"a[200:203],v[120:123],v[160:163],a[200:203]\n" \
T_"v[114:115],v233 offset:26624\n" \
M_"a[204:207],v[124:127],v[160:163],a[204:207]\n" \
T_"v[116:117],v233 offset:26144\n" \
T_"v[118:119],v233 offset:26656\n" \
M_"a[208:211],v[128:131],v[160:163],a[208:211]\n" \
C_"v166,v156,v157\n" \
T_"v[120:121],v233 offset:26176\n" \
M_"a[212:215],v[132:135],v[160:163],a[212:215]\n" \
T_"v[122:123],v233 offset:26688\n" \
M_"a[216:219],v[136:139],v[160:163],a[216:219]\n" \
C_"v167,v158,v159\n" \
T_"v[124:125],v233 offset:26208\n" \
M_"a[220:223],v[140:143],v[160:163],a[220:223]\n" \
T_"v[126:127],v233 offset:26720\n" \
M_"v[220:223],v[224:227],v[160:163],v[220:223]\n" \
M_"v[152:155],a[224:227],v[32:35],v[176:179]\n" \
T_"v[128:129],v233 offset:26240\n" \
T_"v[130:131],v233 offset:26752\n" \
M_"v[152:155],a[228:231],v[36:39],v[152:155]\n" \
T_"v[132:133],v233 offset:26272\n" \
T_"v[134:135],v233 offset:26784\n" \
M_"v[152:155],a[232:235],v[40:43],v[152:155]\n" \
T_"v[136:137],v233 offset:26304\n" \
T_"v[138:139],v233 offset:26816\n" \
M_"v[152:155],a[236:239],v[44:47],v[152:155]\n" \
T_"v[140:141],v233 offset:26336\n" \
T_"v[142:143],v233 offset:26848\n" \
M_"v[156:159],a[240:243],v[32:35],v[176:179]\n" \
"v_exp_f32 v144,v144\n" \
M_"v[156:159],a[244:247],v[36:39],v[156:159]\n" \
"v_exp_f32 v145,v145\n" \
M_"v[156:159],a[248:251],v[40:43],v[156:159]\n" \
"v_exp_f32 v146,v146\n" \
M_"v[156:159],a[252:255],v[44:47],v[156:159]\n" \
S_"lgkmcnt(8)\n" \
M_"a[0:3],v[112:115],v[164:167],a[0:3]\n" \
"v_exp_f32 v147,v147\n" \
M_"a[4:7],v[116:119],v[164:167],a[4:7]\n" \
C_"v160,v144,v145\n" \
C_"v161,v146,v147\n" \
M_"a[8:11],v[120:123],v[164:167],a[8:11]\n" \
"v_exp_f32 v148,v148\n" \
M_"a[12:15],v[124:127],v[164:167],a[12:15]\n" \
S_"lgkmcnt(0)\n" \
M_"a[16:19],v[128:131],v[164:167],a[16:19]\n" \
"v_exp_f32 v149,v149\n" \
M_"a[20:23],v[132:135],v[164:167],a[20:23]\n" \
"v_exp_f32 v150,v150\n" \
M_"a[24:27],v[136:139],v[164:167],a[24:27]\n" \
"v_exp_f32 v151,v151\n" \
M_"a[28:31],v[140:143],v[164:167],a[28:31]\n" \
M_"v[196:199],v[224:227],v[164:167],v[196:199]\n" \
C_"v162,v148,v149\n" \
C_"v163,v150,v151\n" \
M_"v[144:147],a[224:227],v[48:51],v[180:183]\n" \
"v_exp_f32 v152,v152\n" \
M_"v[144:147],a[228:231],v[52:55],v[144:147]\n" \
M_"v[144:147],a[232:235],v[56:59],v[144:147]\n" \
"v_exp_f32 v153,v153\n" \
M_"v[144:147],a[236:239],v[60:63],v[144:147]\n" \
M_"v[148:151],a[240:243],v[48:51],v[180:183]\n" \
"v_exp_f32 v154,v154\n" \
M_"v[148:151],a[244:247],v[52:55],v[148:151]\n" \
M_"v[148:151],a[248:251],v[56:59],v[148:151]\n" \
"v_exp_f32 v155,v155\n" \
M_"v[148:151],a[252:255],v[60:63],v[148:151]\n" \
M_"a[32:35],v[112:115],v[160:163],a[32:35]\n" \
C_"v164,v152,v153\n" \
C_"v165,v154,v155\n" \
M_"a[36:39],v[116:119],v[160:163],a[36:39]\n" \
"v_exp_f32 v156,v156\n" \
M_"a[40:43],v[120:123],v[160:163],a[40:43]\n" \
M_"a[44:47],v[124:127],v[160:163],a[44:47]\n" \
"v_exp_f32 v157,v157\n" \
M_"a[48:51],v[128:131],v[160:163],a[48:51]\n" \
M_"a[52:55],v[132:135],v[160:163],a[52:55]\n" \
"v_exp_f32 v158,v158\n" \
M_"a[56:59],v[136:139],v[160:163],a[56:59]\n" \
M_"a[60:63],v[140:143],v[160:163],a[60:63]\n" \
"v_exp_f32 v159,v159\n" \
M_"v[200:203],v[224:227],v[160:163],v[200:203]\n" \
C_"v166,v156,v157\n" \
C_"v167,v158,v159\n" \
M_"v[152:155],a[224:227],v[64:67],v[184:187]\n" \
"v_exp_f32 v144,v144\n" \
M_"v[152:155],a[228:231],v[68:71],v[152:155]\n" \
M_"v[152:155],a[232:235],v[72:75],v[152:155]\n" \
"v_exp_f32 v145,v145\n" \
M_"v[152:155],a[236:239],v[76:79],v[152:155]\n" \
M_"v[156:159],a[240:243],v[64:67],v[184:187]\n" \
"v_exp_f32 v146,v146\n" \
M_"v[156:159],a[244:247],v[68:71],v[156:159]\n" \
M_"v[156:159],a[248:251],v[72:75],v[156:159]\n" \
"v_exp_f32 v147,v147\n" \
M_"v[156:159],a[252:255],v[76:79],v[156:159]\n" \
M_"a[64:67],v[112:115],v[164:167],a[64:67]\n" \
C_"v160,v144,v145\n" \
C_"v161,v146,v147\n" \
M_"a[68:71],v[116:119],v[164:167],a[68:71]\n" \
"v_exp_f32 v148,v148\n" \
M_"a[72:75],v[120:123],v[164:167],a[72:75]\n" \
M_"a[76:79],v[124:127],v[164:167],a[76:79]\n" \
"v_exp_f32 v149,v149\n" \
M_"a[80:83],v[128:131],v[164:167],a[80:83]\n" \
M_"a[84:87],v[132:135],v[164:167],a[84:87]\n" \
"v_exp_f32 v150,v150\n" \
M_"a[88:91],v[136:139],v[164:167],a[88:91]\n" \
M_"a[92:95],v[140:143],v[164:167],a[92:95]\n" \
"v_exp_f32 v151,v151\n" \
M_"v[204:207],v[224:227],v[164:167],v[204:207]\n" \
C_"v162,v148,v149\n" \
C_"v163,v150,v151\n" \
M_"v[144:147],a[224:227],v[80:83],v[188:191]\n" \
"v_exp_f32 v152,v152\n" \
M_"v[144:147],a[228:231],v[84:87],v[144:147]\n" \
"v_add_u32 v252,1,v252\n" \
M_"v[144:147],a[232:235],v[88:91],v[144:147]\n" \
"v_exp_f32 v153,v153\n" \
M_"v[144:147],a[236:239],v[92:95],v[144:147]\n" \
"ds_write_b32 v253,v252 offset:0\n" \
M_"v[148:151],a[240:243],v[80:83],v[188:191]\n" \
"v_exp_f32 v154,v154\n" \
M_"v[148:151],a[244:247],v[84:87],v[148:151]\n" \
M_"v[148:151],a[248:251],v[88:91],v[148:151]\n" \
"v_exp_f32 v155,v155\n" \
M_"v[148:151],a[252:255],v[92:95],v[148:151]\n" \
M_"a[96:99],v[112:115],v[160:163],a[96:99]\n" \
C_"v164,v152,v153\n" \
C_"v165,v154,v155\n" \
M_"a[100:103],v[116:119],v[160:163],a[100:103]\n" \
"v_exp_f32 v156,v156\n" \
M_"a[104:107],v[120:123],v[160:163],a[104:107]\n" \
M_"a[108:111],v[124:127],v[160:163],a[108:111]\n" \
"v_exp_f32 v157,v157\n" \
M_"a[112:115],v[128:131],v[160:163],a[112:115]\n" \
M_"a[116:119],v[132:135],v[160:163],a[116:119]\n" \
"v_exp_f32 v158,v158\n" \
M_"a[120:123],v[136:139],v[160:163],a[120:123]\n" \
M_"a[124:127],v[140:143],v[160:163],a[124:127]\n" \
"v_exp_f32 v159,v159\n" \
M_"v[208:211],v[224:227],v[160:163],v[208:211]\n" \
C_"v166,v156,v157\n" \
C_"v167,v158,v159\n" \
M_"v[152:155],a[224:227],v[96:99],v[192:195]\n" \
M_"v[152:155],a[228:231],v[100:103],v[152:155]\n" \
M_"v[152:155],a[232:235],v[104:107],v[152:155]\n" \
M_"v[152:155],a[236:239],v[108:111],v[152:155]\n" \
"v_exp_f32 v144,v144\n" \
M_"v[156:159],a[240:243],v[96:99],v[192:195]\n" \
M_"v[156:159],a[244:247],v[100:103],v[156:159]\n" \
"v_exp_f32 v145,v145\n" \
M_"v[156:159],a[248:251],v[104:107],v[156:159]\n" \
M_"v[156:159],a[252:255],v[108:111],v[156:159]\n" \
M_"a[128:131],v[112:115],v[164:167],a[128:131]\n" \
"v_exp_f32 v146,v146\n" \
M_"a[132:135],v[116:119],v[164:167],a[132:135]\n" \
"v_exp_f32 v147,v147\n" \
M_"a[136:139],v[120:123],v[164:167],a[136:139]\n" \
C_"v160,v144,v145\n" \
C_"v161,v146,v147\n" \
M_"a[140:143],v[124:127],v[164:167],a[140:143]\n" \
"v_exp_f32 v148,v148\n" \
M_"a[144:147],v[128:131],v[164:167],a[144:147]\n" \
"v_exp_f32 v149,v149\n" \
M_"a[148:151],v[132:135],v[164:167],a[148:151]\n" \
"v_exp_f32 v150,v150\n" \
M_"a[152:155],v[136:139],v[164:167],a[152:155]\n" \
"v_exp_f32 v151,v151\n" \
M_"a[156:159],v[140:143],v[164:167],a[156:159]\n" \
M_"v[212:215],v[224:227],v[164:167],v[212:215]\n" \
C_"v162,v148,v149\n" \
C_"v163,v150,v151\n" \
"; ---- fdrain\n" \
"s_nop 0\n" \
"v_exp_f32 v152,v152\n" \
M_"a[160:163],v[112:115],v[160:163],a[160:163]\n" \
"v_exp_f32 v153,v153\n" \
M_"a[164:167],v[116:119],v[160:163],a[164:167]\n" \
"v_exp_f32 v154,v154\n" \
M_"a[168:171],v[120:123],v[160:163],a[168:171]\n" \
"v_exp_f32 v155,v155\n" \
M_"a[172:175],v[124:127],v[160:163],a[172:175]\n" \
"v_exp_f32 v156,v156\n" \
M_"a[176:179],v[128:131],v[160:163],a[176:179]\n" \
"v_exp_f32 v157,v157\n" \
M_"a[180:183],v[132:135],v[160:163],a[180:183]\n" \
"v_exp_f32 v158,v158\n" \
M_"a[184:187],v[136:139],v[160:163],a[184:187]\n" \
"v_exp_f32 v159,v159\n" \
M_"a[188:191],v[140:143],v[160:163],a[188:191]\n" \
C_"v164,v152,v153\n" \
C_"v165,v154,v155\n" \
M_"v[216:219],v[224:227],v[160:163],v[216:219]\n" \
C_"v166,v156,v157\n" \
C_"v167,v158,v159\n" \
"s_nop 1\n" \
M_"a[192:195],v[112:115],v[164:167],a[192:195]\n" \
M_"a[196:199],v[116:119],v[164:167],a[196:199]\n" \
M_"a[200:203],v[120:123],v[164:167],a[200:203]\n" \
M_"a[204:207],v[124:127],v[164:167],a[204:207]\n" \
M_"a[208:211],v[128:131],v[164:167],a[208:211]\n" \
M_"a[212:215],v[132:135],v[164:167],a[212:215]\n" \
M_"a[216:219],v[136:139],v[164:167],a[216:219]\n" \
M_"a[220:223],v[140:143],v[164:167],a[220:223]\n" \
M_"v[220:223],v[224:227],v[164:167],v[220:223]\n" \
"s_nop 7\n" \
"v_mov_b32 v236,0x53800000\n" \
"v_add_f32 v235,v196,v200\n" \
"v_add_f32 v235,v235,v204\n" \
"v_add_f32 v235,v235,v208\n" \
"v_add_f32 v235,v235,v212\n" \
"v_add_f32 v235,v235,v216\n" \
"v_add_f32 v235,v235,v220\n" \
"v_cmp_nle_f32 vcc,v235,v236\n" \
"s_cbranch_vccnz LREDO_%=\n" \
"LATTD_%=:\n" \
"; ---- outc\n" \
"v_mov_b32 v252,100\n" \
"ds_write_b32 v253,v252 offset:0\n" \
"s_cmp_lt_u32 s56,2\n" \
"s_cbranch_scc0 LOUTB_%=\n" \
"; ---- outa\n" \
"v_lshlrev_b32 v240,4,v255\n" \
A_"s74,s83,28672\n" \
"v_lshrrev_b32 v236,2,v255\n" \
"v_mul_u32_u24 v242,1152,v236\n" \
"v_and_b32 v237,3,v255\n" \
"v_lshl_add_u32 v242,v237,4,v242\n" \
"v_add_u32 v241,s74,v240\n" \
"v_lshrrev_b32 v236,4,v255\n" \
"v_lshlrev_b32 v250,10,v236\n" \
"v_bfe_u32 v237,v255,2,2\n" \
"v_lshl_add_u32 v250,v237,7,v250\n" \
"v_and_b32 v238,3,v255\n" \
"v_lshl_add_u32 v250,v238,4,v250\n" \
"v_add_u32 v250,s74,v250\n" \
"v_lshrrev_b32 v237,1,v237\n" \
"v_xor_b32 v238,1,v237\n" \
"v_lshl_add_u32 v251,v238,6,v250\n" \
"v_lshl_add_u32 v250,v237,6,v250\n" \
"v_add_u32 v249,17408,v242\n" \
"v_add_u32 v243,34816,v242\n" \
"v_add_u32 v244,52224,v242\n" \
"s_lshl_b32 s15,s56,5\n" \
A_"s15,s15,s89\n" \
"s_lshl_b32 s15,s15,2\n" \
A_"s15,s15,1024\n" \
"v_lshlrev_b32 v246,9,v255\n" \
"v_add_u32 v246,s15,v246\n" \
"v_subrev_u32 v236,0,v255\n" \
"v_cmp_gt_u32 vcc,8,v236\n" \
"v_mov_b32 v236,0x40000000\n" \
"s_nop 0\n" \
"v_cndmask_b32 v246,v236,v246,vcc\n" \
"s_lshl_b32 s15,s89,2\n" \
A_"s15,s15,s56\n" \
"s_lshl_b32 s15,s15,7\n" \
A_"s15,s15,1024\n" \
"v_and_b32 v248,7,v255\n" \
"v_cmp_eq_u32 vcc,s89,v248\n" \
"v_xor_b32 v236,1,v248\n" \
"s_nop 0\n" \
"v_cndmask_b32 v248,v248,v236,vcc\n" \
"v_lshlrev_b32 v248,2,v248\n" \
"v_add_u32 v248,s15,v248\n" \
"v_and_b32 v245,15,v255\n" \
"v_mul_u32_u24 v245,2304,v245\n" \
"v_lshrrev_b32 v236,4,v255\n" \
"v_lshl_add_u32 v245,v236,4,v245\n" \
"s_cmp_lt_u32 s89,7\n" \
"s_cselect_b32 s15,0x40000000,0\n" \
"v_add_u32 v247,s15,v240\n" \
"v_lshrrev_b32 v236,3,v255\n" \
"v_lshrrev_b32 v237,2,v236\n" \
"v_lshlrev_b32 v237,4,v237\n" \
"v_and_b32 v238,3,v236\n" \
"v_add_u32 v237,v237,v238\n" \
"v_mul_u32_u24 v228,1152,v237\n" \
"v_bfe_u32 v237,v255,4,1\n" \
"v_lshlrev_b32 v237,2,v237\n" \
"v_and_b32 v238,7,v255\n" \
"v_xor_b32 v238,v238,v237\n" \
"v_lshl_add_u32 v228,v238,4,v228\n" \
"v_add_u32 v229,3584,v228\n" \
"v_add_u32 v230,7168,v228\n" \
"v_add_u32 v231,10752,v228\n" \
"s_xor_b32 s60,s56,0\n" \
"s_mul_i32 s64,s60,147456\n" \
"s_mul_i32 s61,s60,40960\n" \
A_"s61,s61,s57\n" \
"v_add_u32 v224,s61,v240\n" \
"s_xor_b32 s60,s56,1\n" \
"s_mul_i32 s65,s60,147456\n" \
"s_mul_i32 s61,s60,40960\n" \
A_"s61,s61,s57\n" \
"v_add_u32 v225,s61,v240\n" \
"s_mov_b32 s60,2\n" \
"s_mul_i32 s66,s60,147456\n" \
"s_mul_i32 s61,s60,40960\n" \
A_"s61,s61,s57\n" \
"v_add_u32 v226,s61,v240\n" \
"s_mov_b32 s60,3\n" \
"s_mul_i32 s67,s60,147456\n" \
"s_mul_i32 s61,s60,40960\n" \
A_"s61,s61,s57\n" \
"v_add_u32 v227,s61,v240\n" \
"s_mul_i32 s63,s89,129024\n" \
"s_mul_i32 s62,s89,36864\n" \
"s_mul_i32 s60,s56,3\n" \
A_"s60,s60,0\n" \
"s_lshl_b32 s68,s60,7\n" \
"s_mul_i32 s76,s60,14336\n" \
A_"s71,s76,s63\n" \
A_"s79,s62,s68\n" \
"s_mul_i32 s60,s56,3\n" \
A_"s60,s60,1\n" \
"s_lshl_b32 s69,s60,7\n" \
"s_mul_i32 s77,s60,14336\n" \
A_"s72,s77,s63\n" \
A_"s90,s62,s69\n" \
"s_mul_i32 s60,s56,3\n" \
A_"s60,s60,2\n" \
"s_lshl_b32 s70,s60,7\n" \
"s_mul_i32 s78,s60,14336\n" \
A_"s73,s78,s63\n" \
A_"s91,s62,s70\n" \
"s_lshl_b32 s62,s75,8\n" \
"s_lshl_b32 s63,s89,5\n" \
A_"s62,s62,s63\n" \
A_"s62,s62,256\n" \
"s_lshl_b32 s63,s75,5\n" \
A_"s63,s63,96\n" \
"v_mov_b32 v236,s62\n" \
"v_mov_b32 v237,s63\n" \
"v_cmp_gt_u32 vcc,8,v255\n" \
"s_nop 1\n" \
"v_cndmask_b32 v233,v237,v236,vcc\n" \
"v_lshl_add_u32 v233,v255,2,v233\n" \
"v_subrev_u32 v236,0,v255\n" \
"v_cmp_gt_u32 vcc,16,v236\n" \
"v_mov_b32 v236,0x40000000\n" \
"s_nop 0\n" \
"v_cndmask_b32 v233,v236,v233,vcc\n" \
"s_lshl_b32 s62,s75,8\n" \
"s_lshl_b32 s63,s89,2\n" \
A_"s62,s62,s63\n" \
"v_lshlrev_b32 v234,5,v255\n" \
"v_add_u32 v234,s62,v234\n" \
"v_subrev_u32 v236,8,v255\n" \
"v_cmp_gt_u32 vcc,8,v236\n" \
"v_mov_b32 v236,0x40000000\n" \
"s_nop 0\n" \
"v_cndmask_b32 v234,v236,v234,vcc\n" \
S_"vmcnt(0) lgkmcnt(0)\n" \
"buffer_load_dword v235,v233,s[84:87],0 offen sc1\n" \
"s_lshl_b32 s62,s89,2\n" \
A_"s62,s62,s75\n" \
"s_lshl_b32 s62,s62,7\n" \
A_"s62,s62,1024\n" \
"v_and_b32 v238,15,v255\n" \
"v_lshlrev_b32 v238,2,v238\n" \
"v_lshrrev_b32 v239,4,v255\n" \
"v_lshl_add_u32 v238,v239,8,v238\n" \
"v_add_u32 v238,s62,v238\n" \
"v_subrev_u32 v239,0,v255\n" \
"v_cmp_gt_u32 vcc,32,v239\n" \
"v_mov_b32 v239,0x40000000\n" \
"s_nop 0\n" \
"v_cndmask_b32 v238,v239,v238,vcc\n" \
"v_mov_b32 v239,0\n" \
"buffer_store_dword v239,v238,s[84:87],0 offen sc1\n" \
A_"m0,s74,0\n" \
A_"s60,s64,s68\n" \
A_"s60,s60,0\n" \
L_"v228,s[48:51],s60 offen nt lds\n" \
L_"v229,s[48:51],s60 offen offset:1024 nt lds\n" \
L_"v230,s[48:51],s60 offen offset:2048 nt lds\n" \
L_"v231,s[48:51],s60 offen offset:3072 nt lds\n" \
A_"m0,s74,4096\n" \
A_"s60,s64,s68\n" \
A_"s60,s60,36864\n" \
L_"v228,s[48:51],s60 offen nt lds\n" \
L_"v229,s[48:51],s60 offen offset:1024 nt lds\n" \
L_"v230,s[48:51],s60 offen offset:2048 nt lds\n" \
L_"v231,s[48:51],s60 offen offset:3072 nt lds\n" \
A_"m0,s74,8192\n" \
A_"s60,s64,s68\n" \
A_"s60,s60,73728\n" \
L_"v228,s[48:51],s60 offen nt lds\n" \
L_"v229,s[48:51],s60 offen offset:1024 nt lds\n" \
L_"v230,s[48:51],s60 offen offset:2048 nt lds\n" \
L_"v231,s[48:51],s60 offen offset:3072 nt lds\n" \
"v_add_u32 v160,s83,v240\n" \
"v_rcp_f32 v196,v196\n" \
"v_rcp_f32 v200,v200\n" \
"v_rcp_f32 v204,v204\n" \
"v_rcp_f32 v208,v208\n" \
"v_rcp_f32 v212,v212\n" \
"v_rcp_f32 v216,v216\n" \
"v_rcp_f32 v220,v220\n" \
"v_mov_b32 v197,v196\n" \
"v_mov_b32 v201,v200\n" \
"v_mov_b32 v205,v204\n" \
"v_mov_b32 v209,v208\n" \
"v_mov_b32 v213,v212\n" \
"v_mov_b32 v217,v216\n" \
"v_mov_b32 v221,v220\n" \
G_"v144,a0\n" \
G_"v145,a1\n" \
G_"v146,a2\n" \
G_"v147,a3\n" \
G_"v148,a4\n" \
G_"v149,a5\n" \
G_"v150,a6\n" \
G_"v151,a7\n" \
P_"v[144:145],v[144:145],v[196:197]\n" \
P_"v[146:147],v[146:147],v[196:197]\n" \
P_"v[148:149],v[148:149],v[196:197]\n" \
P_"v[150:151],v[150:151],v[196:197]\n" \
C_"v0,v144,v145\n" \
C_"v1,v146,v147\n" \
C_"v2,v148,v149\n" \
C_"v3,v150,v151\n" \
"ds_write_b128 v160,v[0:3] offset:0\n" \
G_"v152,a32\n" \
G_"v153,a33\n" \
G_"v154,a34\n" \
G_"v155,a35\n" \
G_"v156,a36\n" \
G_"v157,a37\n" \
G_"v158,a38\n" \
G_"v159,a39\n" \
P_"v[152:153],v[152:153],v[200:201]\n" \
P_"v[154:155],v[154:155],v[200:201]\n" \
P_"v[156:157],v[156:157],v[200:201]\n" \
P_"v[158:159],v[158:159],v[200:201]\n" \
C_"v16,v152,v153\n" \
C_"v17,v154,v155\n" \
C_"v18,v156,v157\n" \
C_"v19,v158,v159\n" \
"ds_write_b128 v160,v[16:19] offset:1024\n" \
G_"v144,a64\n" \
G_"v145,a65\n" \
G_"v146,a66\n" \
G_"v147,a67\n" \
G_"v148,a68\n" \
G_"v149,a69\n" \
G_"v150,a70\n" \
G_"v151,a71\n" \
P_"v[144:145],v[144:145],v[204:205]\n" \
P_"v[146:147],v[146:147],v[204:205]\n" \
P_"v[148:149],v[148:149],v[204:205]\n" \
P_"v[150:151],v[150:151],v[204:205]\n" \
C_"v32,v144,v145\n" \
C_"v33,v146,v147\n" \
C_"v34,v148,v149\n" \
C_"v35,v150,v151\n" \
"ds_write_b128 v160,v[32:35] offset:2048\n" \
G_"v152,a96\n" \
G_"v153,a97\n" \
G_"v154,a98\n" \
G_"v155,a99\n" \
G_"v156,a100\n" \
G_"v157,a101\n" \
G_"v158,a102\n" \
G_"v159,a103\n" \
P_"v[152:153],v[152:153],v[208:209]\n" \
P_"v[154:155],v[154:155],v[208:209]\n" \
P_"v[156:157],v[156:157],v[208:209]\n" \
P_"v[158:159],v[158:159],v[208:209]\n" \
C_"v48,v152,v153\n" \
C_"v49,v154,v155\n" \
C_"v50,v156,v157\n" \
C_"v51,v158,v159\n" \
"ds_write_b128 v160,v[48:51] offset:3072\n" \
G_"v144,a128\n" \
G_"v145,a129\n" \
G_"v146,a130\n" \
G_"v147,a131\n" \
G_"v148,a132\n" \
G_"v149,a133\n" \
G_"v150,a134\n" \
G_"v151,a135\n" \
P_"v[144:145],v[144:145],v[212:213]\n" \
P_"v[146:147],v[146:147],v[212:213]\n" \
P_"v[148:149],v[148:149],v[212:213]\n" \
P_"v[150:151],v[150:151],v[212:213]\n" \
C_"v64,v144,v145\n" \
C_"v65,v146,v147\n" \
C_"v66,v148,v149\n" \
C_"v67,v150,v151\n" \
"ds_write_b128 v160,v[64:67] offset:4096\n" \
G_"v152,a160\n" \
G_"v153,a161\n" \
G_"v154,a162\n" \
G_"v155,a163\n" \
G_"v156,a164\n" \
G_"v157,a165\n" \
G_"v158,a166\n" \
G_"v159,a167\n" \
P_"v[152:153],v[152:153],v[216:217]\n" \
P_"v[154:155],v[154:155],v[216:217]\n" \
P_"v[156:157],v[156:157],v[216:217]\n" \
P_"v[158:159],v[158:159],v[216:217]\n" \
C_"v80,v152,v153\n" \
C_"v81,v154,v155\n" \
C_"v82,v156,v157\n" \
C_"v83,v158,v159\n" \
"ds_write_b128 v160,v[80:83] offset:5120\n" \
G_"v144,a192\n" \
G_"v145,a193\n" \
G_"v146,a194\n" \
G_"v147,a195\n" \
G_"v148,a196\n" \
G_"v149,a197\n" \
G_"v150,a198\n" \
G_"v151,a199\n" \
P_"v[144:145],v[144:145],v[220:221]\n" \
P_"v[146:147],v[146:147],v[220:221]\n" \
P_"v[148:149],v[148:149],v[220:221]\n" \
P_"v[150:151],v[150:151],v[220:221]\n" \
C_"v96,v144,v145\n" \
C_"v97,v146,v147\n" \
C_"v98,v148,v149\n" \
C_"v99,v150,v151\n" \
"ds_write_b128 v160,v[96:99] offset:6144\n" \
G_"v152,a8\n" \
G_"v153,a9\n" \
G_"v154,a10\n" \
G_"v155,a11\n" \
G_"v156,a12\n" \
G_"v157,a13\n" \
G_"v158,a14\n" \
G_"v159,a15\n" \
P_"v[152:153],v[152:153],v[196:197]\n" \
P_"v[154:155],v[154:155],v[196:197]\n" \
P_"v[156:157],v[156:157],v[196:197]\n" \
P_"v[158:159],v[158:159],v[196:197]\n" \
C_"v4,v152,v153\n" \
C_"v5,v154,v155\n" \
C_"v6,v156,v157\n" \
C_"v7,v158,v159\n" \
"ds_write_b128 v160,v[4:7] offset:7168\n" \
G_"v144,a40\n" \
G_"v145,a41\n" \
G_"v146,a42\n" \
G_"v147,a43\n" \
G_"v148,a44\n" \
G_"v149,a45\n" \
G_"v150,a46\n" \
G_"v151,a47\n" \
P_"v[144:145],v[144:145],v[200:201]\n" \
P_"v[146:147],v[146:147],v[200:201]\n" \
P_"v[148:149],v[148:149],v[200:201]\n" \
P_"v[150:151],v[150:151],v[200:201]\n" \
C_"v20,v144,v145\n" \
C_"v21,v146,v147\n" \
C_"v22,v148,v149\n" \
C_"v23,v150,v151\n" \
"ds_write_b128 v160,v[20:23] offset:8192\n" \
G_"v152,a72\n" \
G_"v153,a73\n" \
G_"v154,a74\n" \
G_"v155,a75\n" \
G_"v156,a76\n" \
G_"v157,a77\n" \
G_"v158,a78\n" \
G_"v159,a79\n" \
P_"v[152:153],v[152:153],v[204:205]\n" \
P_"v[154:155],v[154:155],v[204:205]\n" \
P_"v[156:157],v[156:157],v[204:205]\n" \
P_"v[158:159],v[158:159],v[204:205]\n" \
C_"v36,v152,v153\n" \
C_"v37,v154,v155\n" \
C_"v38,v156,v157\n" \
C_"v39,v158,v159\n" \
"ds_write_b128 v160,v[36:39] offset:9216\n" \
G_"v144,a104\n" \
G_"v145,a105\n" \
G_"v146,a106\n" \
G_"v147,a107\n" \
G_"v148,a108\n" \
G_"v149,a109\n" \
G_"v150,a110\n" \
G_"v151,a111\n" \
P_"v[144:145],v[144:145],v[208:209]\n" \
P_"v[146:147],v[146:147],v[208:209]\n" \
P_"v[148:149],v[148:149],v[208:209]\n" \
P_"v[150:151],v[150:151],v[208:209]\n" \
C_"v52,v144,v145\n" \
C_"v53,v146,v147\n" \
C_"v54,v148,v149\n" \
C_"v55,v150,v151\n" \
"ds_write_b128 v160,v[52:55] offset:10240\n" \
G_"v152,a136\n" \
G_"v153,a137\n" \
G_"v154,a138\n" \
G_"v155,a139\n" \
G_"v156,a140\n" \
G_"v157,a141\n" \
G_"v158,a142\n" \
G_"v159,a143\n" \
P_"v[152:153],v[152:153],v[212:213]\n" \
P_"v[154:155],v[154:155],v[212:213]\n" \
P_"v[156:157],v[156:157],v[212:213]\n" \
P_"v[158:159],v[158:159],v[212:213]\n" \
C_"v68,v152,v153\n" \
C_"v69,v154,v155\n" \
C_"v70,v156,v157\n" \
C_"v71,v158,v159\n" \
"ds_write_b128 v160,v[68:71] offset:11264\n" \
G_"v144,a168\n" \
G_"v145,a169\n" \
G_"v146,a170\n" \
G_"v147,a171\n" \
G_"v148,a172\n" \
G_"v149,a173\n" \
G_"v150,a174\n" \
G_"v151,a175\n" \
P_"v[144:145],v[144:145],v[216:217]\n" \
P_"v[146:147],v[146:147],v[216:217]\n" \
P_"v[148:149],v[148:149],v[216:217]\n" \
P_"v[150:151],v[150:151],v[216:217]\n" \
C_"v84,v144,v145\n" \
C_"v85,v146,v147\n" \
C_"v86,v148,v149\n" \
C_"v87,v150,v151\n" \
"ds_write_b128 v160,v[84:87] offset:12288\n" \
G_"v152,a200\n" \
G_"v153,a201\n" \
G_"v154,a202\n" \
G_"v155,a203\n" \
G_"v156,a204\n" \
G_"v157,a205\n" \
G_"v158,a206\n" \
G_"v159,a207\n" \
P_"v[152:153],v[152:153],v[220:221]\n" \
P_"v[154:155],v[154:155],v[220:221]\n" \
P_"v[156:157],v[156:157],v[220:221]\n" \
P_"v[158:159],v[158:159],v[220:221]\n" \
C_"v100,v152,v153\n" \
C_"v101,v154,v155\n" \
C_"v102,v156,v157\n" \
C_"v103,v158,v159\n" \
"ds_write_b128 v160,v[100:103] offset:13312\n" \
G_"v144,a16\n" \
G_"v145,a17\n" \
G_"v146,a18\n" \
G_"v147,a19\n" \
G_"v148,a20\n" \
G_"v149,a21\n" \
G_"v150,a22\n" \
G_"v151,a23\n" \
P_"v[144:145],v[144:145],v[196:197]\n" \
P_"v[146:147],v[146:147],v[196:197]\n" \
P_"v[148:149],v[148:149],v[196:197]\n" \
P_"v[150:151],v[150:151],v[196:197]\n" \
C_"v8,v144,v145\n" \
C_"v9,v146,v147\n" \
C_"v10,v148,v149\n" \
C_"v11,v150,v151\n" \
"ds_write_b128 v160,v[8:11] offset:14336\n" \
G_"v152,a48\n" \
G_"v153,a49\n" \
G_"v154,a50\n" \
G_"v155,a51\n" \
G_"v156,a52\n" \
G_"v157,a53\n" \
G_"v158,a54\n" \
G_"v159,a55\n" \
P_"v[152:153],v[152:153],v[200:201]\n" \
P_"v[154:155],v[154:155],v[200:201]\n" \
P_"v[156:157],v[156:157],v[200:201]\n" \
P_"v[158:159],v[158:159],v[200:201]\n" \
C_"v24,v152,v153\n" \
C_"v25,v154,v155\n" \
C_"v26,v156,v157\n" \
C_"v27,v158,v159\n" \
"ds_write_b128 v160,v[24:27] offset:15360\n" \
G_"v144,a80\n" \
G_"v145,a81\n" \
G_"v146,a82\n" \
G_"v147,a83\n" \
G_"v148,a84\n" \
G_"v149,a85\n" \
G_"v150,a86\n" \
G_"v151,a87\n" \
P_"v[144:145],v[144:145],v[204:205]\n" \
P_"v[146:147],v[146:147],v[204:205]\n" \
P_"v[148:149],v[148:149],v[204:205]\n" \
P_"v[150:151],v[150:151],v[204:205]\n" \
C_"v40,v144,v145\n" \
C_"v41,v146,v147\n" \
C_"v42,v148,v149\n" \
C_"v43,v150,v151\n" \
"ds_write_b128 v160,v[40:43] offset:16384\n" \
G_"v152,a112\n" \
G_"v153,a113\n" \
G_"v154,a114\n" \
G_"v155,a115\n" \
G_"v156,a116\n" \
G_"v157,a117\n" \
G_"v158,a118\n" \
G_"v159,a119\n" \
P_"v[152:153],v[152:153],v[208:209]\n" \
P_"v[154:155],v[154:155],v[208:209]\n" \
P_"v[156:157],v[156:157],v[208:209]\n" \
P_"v[158:159],v[158:159],v[208:209]\n" \
C_"v56,v152,v153\n" \
C_"v57,v154,v155\n" \
C_"v58,v156,v157\n" \
C_"v59,v158,v159\n" \
"ds_write_b128 v160,v[56:59] offset:17408\n" \
G_"v144,a144\n" \
G_"v145,a145\n" \
G_"v146,a146\n" \
G_"v147,a147\n" \
G_"v148,a148\n" \
G_"v149,a149\n" \
G_"v150,a150\n" \
G_"v151,a151\n" \
P_"v[144:145],v[144:145],v[212:213]\n" \
P_"v[146:147],v[146:147],v[212:213]\n" \
P_"v[148:149],v[148:149],v[212:213]\n" \
P_"v[150:151],v[150:151],v[212:213]\n" \
C_"v72,v144,v145\n" \
C_"v73,v146,v147\n" \
C_"v74,v148,v149\n" \
C_"v75,v150,v151\n" \
"ds_write_b128 v160,v[72:75] offset:18432\n" \
G_"v152,a176\n" \
G_"v153,a177\n" \
G_"v154,a178\n" \
G_"v155,a179\n" \
G_"v156,a180\n" \
G_"v157,a181\n" \
G_"v158,a182\n" \
G_"v159,a183\n" \
P_"v[152:153],v[152:153],v[216:217]\n" \
P_"v[154:155],v[154:155],v[216:217]\n" \
P_"v[156:157],v[156:157],v[216:217]\n" \
P_"v[158:159],v[158:159],v[216:217]\n" \
C_"v88,v152,v153\n" \
C_"v89,v154,v155\n" \
C_"v90,v156,v157\n" \
C_"v91,v158,v159\n" \
"ds_write_b128 v160,v[88:91] offset:19456\n" \
G_"v144,a208\n" \
G_"v145,a209\n" \
G_"v146,a210\n" \
G_"v147,a211\n" \
G_"v148,a212\n" \
G_"v149,a213\n" \
G_"v150,a214\n" \
G_"v151,a215\n" \
P_"v[144:145],v[144:145],v[220:221]\n" \
P_"v[146:147],v[146:147],v[220:221]\n" \
P_"v[148:149],v[148:149],v[220:221]\n" \
P_"v[150:151],v[150:151],v[220:221]\n" \
C_"v104,v144,v145\n" \
C_"v105,v146,v147\n" \
C_"v106,v148,v149\n" \
C_"v107,v150,v151\n" \
"ds_write_b128 v160,v[104:107] offset:20480\n" \
G_"v152,a24\n" \
G_"v153,a25\n" \
G_"v154,a26\n" \
G_"v155,a27\n" \
G_"v156,a28\n" \
G_"v157,a29\n" \
G_"v158,a30\n" \
G_"v159,a31\n" \
P_"v[152:153],v[152:153],v[196:197]\n" \
P_"v[154:155],v[154:155],v[196:197]\n" \
P_"v[156:157],v[156:157],v[196:197]\n" \
P_"v[158:159],v[158:159],v[196:197]\n" \
C_"v12,v152,v153\n" \
C_"v13,v154,v155\n" \
C_"v14,v156,v157\n" \
C_"v15,v158,v159\n" \
"ds_write_b128 v160,v[12:15] offset:21504\n" \
G_"v144,a56\n" \
G_"v145,a57\n" \
G_"v146,a58\n" \
G_"v147,a59\n" \
G_"v148,a60\n" \
G_"v149,a61\n" \
G_"v150,a62\n" \
G_"v151,a63\n" \
P_"v[144:145],v[144:145],v[200:201]\n" \
P_"v[146:147],v[146:147],v[200:201]\n" \
P_"v[148:149],v[148:149],v[200:201]\n" \
P_"v[150:151],v[150:151],v[200:201]\n" \
C_"v28,v144,v145\n" \
C_"v29,v146,v147\n" \
C_"v30,v148,v149\n" \
C_"v31,v150,v151\n" \
"ds_write_b128 v160,v[28:31] offset:22528\n" \
G_"v152,a88\n" \
G_"v153,a89\n" \
G_"v154,a90\n" \
G_"v155,a91\n" \
G_"v156,a92\n" \
G_"v157,a93\n" \
G_"v158,a94\n" \
G_"v159,a95\n" \
P_"v[152:153],v[152:153],v[204:205]\n" \
P_"v[154:155],v[154:155],v[204:205]\n" \
P_"v[156:157],v[156:157],v[204:205]\n" \
P_"v[158:159],v[158:159],v[204:205]\n" \
C_"v44,v152,v153\n" \
C_"v45,v154,v155\n" \
C_"v46,v156,v157\n" \
C_"v47,v158,v159\n" \
"ds_write_b128 v160,v[44:47] offset:23552\n" \
G_"v144,a120\n" \
G_"v145,a121\n" \
G_"v146,a122\n" \
G_"v147,a123\n" \
G_"v148,a124\n" \
G_"v149,a125\n" \
G_"v150,a126\n" \
G_"v151,a127\n" \
P_"v[144:145],v[144:145],v[208:209]\n" \
P_"v[146:147],v[146:147],v[208:209]\n" \
P_"v[148:149],v[148:149],v[208:209]\n" \
P_"v[150:151],v[150:151],v[208:209]\n" \
C_"v60,v144,v145\n" \
C_"v61,v146,v147\n" \
C_"v62,v148,v149\n" \
C_"v63,v150,v151\n" \
"ds_write_b128 v160,v[60:63] offset:24576\n" \
G_"v152,a152\n" \
G_"v153,a153\n" \
G_"v154,a154\n" \
G_"v155,a155\n" \
G_"v156,a156\n" \
G_"v157,a157\n" \
G_"v158,a158\n" \
G_"v159,a159\n" \
P_"v[152:153],v[152:153],v[212:213]\n" \
P_"v[154:155],v[154:155],v[212:213]\n" \
P_"v[156:157],v[156:157],v[212:213]\n" \
P_"v[158:159],v[158:159],v[212:213]\n" \
C_"v76,v152,v153\n" \
C_"v77,v154,v155\n" \
C_"v78,v156,v157\n" \
C_"v79,v158,v159\n" \
"ds_write_b128 v160,v[76:79] offset:25600\n" \
G_"v144,a184\n" \
G_"v145,a185\n" \
G_"v146,a186\n" \
G_"v147,a187\n" \
G_"v148,a188\n" \
G_"v149,a189\n" \
G_"v150,a190\n" \
G_"v151,a191\n" \
P_"v[144:145],v[144:145],v[216:217]\n" \
P_"v[146:147],v[146:147],v[216:217]\n" \
P_"v[148:149],v[148:149],v[216:217]\n" \
P_"v[150:151],v[150:151],v[216:217]\n" \
C_"v92,v144,v145\n" \
C_"v93,v146,v147\n" \
C_"v94,v148,v149\n" \
C_"v95,v150,v151\n" \
"ds_write_b128 v160,v[92:95] offset:26624\n" \
G_"v152,a216\n" \
G_"v153,a217\n" \
G_"v154,a218\n" \
G_"v155,a219\n" \
G_"v156,a220\n" \
G_"v157,a221\n" \
G_"v158,a222\n" \
G_"v159,a223\n" \
P_"v[152:153],v[152:153],v[220:221]\n" \
P_"v[154:155],v[154:155],v[220:221]\n" \
P_"v[156:157],v[156:157],v[220:221]\n" \
P_"v[158:159],v[158:159],v[220:221]\n" \
C_"v108,v152,v153\n" \
C_"v109,v154,v155\n" \
C_"v110,v156,v157\n" \
C_"v111,v158,v159\n" \
"ds_write_b128 v160,v[108:111] offset:27648\n" \
"s_lshl_b32 s62,s75,5\n" \
"s_lshl_b32 s63,s89,2\n" \
A_"s62,s62,s63\n" \
A_"s62,s62,128\n" \
"v_mov_b32 v238,s62\n" \
"v_subrev_u32 v239,0,v255\n" \
"v_cmp_gt_u32 vcc,1,v239\n" \
"v_mov_b32 v239,0x40000000\n" \
"s_nop 0\n" \
"v_cndmask_b32 v238,v239,v238,vcc\n" \
S_"vmcnt(0)\n" \
"v_readfirstlane_b32 s88,v235\n" \
"LNON_%=:\n" \
A_"s88,s88,1\n" \
"s_nop 1\n" \
"v_cmp_eq_u32 vcc,s88,v235\n" \
"s_cbranch_vccnz LNON_%=\n" \
"v_mov_b32 v239,s88\n" \
"buffer_store_dword v239,v238,s[84:87],0 offen sc1\n" \
"s_lshl_b32 s60,s56,2\n" \
A_"s60,s60,s57\n" \
A_"s60,s60,163776\n" \
"v_mov_b32 v236,s60\n" \
"s_xor_b32 s60,s60,4\n" \
"v_mov_b32 v232,s60\n" \
"v_mov_b32 v237,128\n" \
S_"lgkmcnt(0)\n" \
"ds_write_b32 v236,v237 offset:0\n" \
"v_xor_b32 v236,8,v236\n" \
S_"lgkmcnt(0)\n" \
"s_memrealtime s[20:21]\n" \
S_"lgkmcnt(0)\n" \
T_"a[112:113],v250 offset:0\n" \
T_"a[114:115],v250 offset:512\n" \
T_"a[116:117],v250 offset:8\n" \
T_"a[118:119],v250 offset:520\n" \
T_"a[120:121],v251 offset:0\n" \
T_"a[122:123],v251 offset:512\n" \
T_"a[124:125],v251 offset:8\n" \
T_"a[126:127],v251 offset:520\n" \
R_"a[144:147],v224 offset:0\n" \
R_"a[148:151],v224 offset:1024\n" \
R_"a[152:155],v224 offset:2048\n" \
R_"a[156:159],v224 offset:3072\n" \
R_"a[160:163],v224 offset:4096\n" \
R_"a[164:167],v224 offset:5120\n" \
R_"a[168:171],v224 offset:6144\n" \
S_"lgkmcnt(6)\n" \
M_"a[0:3],a[112:115],a[144:147],0\n" \
A_"m0,s74,0\n" \
A_"s60,s64,s68\n" \
A_"s60,s60,110592\n" \
M_"a[4:7],a[116:119],a[144:147],0\n" \
L_"v228,s[48:51],s60 offen nt lds\n" \
S_"lgkmcnt(0)\n" \
"LT2_%=:\n" \
"s_memrealtime s[18:19]\n" \
S_"lgkmcnt(0)\n" \
"s_sub_u32 s18,s18,s20\n" \
"s_cmp_ge_u32 s18,0\n" \
"s_cbranch_scc1 LT2d_%=\n" \
"s_sleep 1\n" \
"s_branch LT2_%=\n" \
"LT2d_%=:\n" \
M_"a[8:11],a[120:123],a[144:147],0\n" \
L_"v229,s[48:51],s60 offen offset:1024 nt lds\n" \
M_"a[12:15],a[124:127],a[144:147],0\n" \
R_"a[144:147],v224 offset:7168\n" \
M_"a[16:19],a[112:115],a[148:151],0\n" \
L_"v230,s[48:51],s60 offen offset:2048 nt lds\n" \
T_"a[128:129],v250 offset:4096\n" \
M_"a[20:23],a[116:119],a[148:151],0\n" \
L_"v231,s[48:51],s60 offen offset:3072 nt lds\n" \
T_"a[130:131],v250 offset:4608\n" \
M_"a[24:27],a[120:123],a[148:151],0\n" \
T_"a[132:133],v250 offset:4104\n" \
M_"a[28:31],a[124:127],a[148:151],0\n" \
R_"a[148:151],v224 offset:8192\n" \
M_"a[32:35],a[112:115],a[152:155],0\n" \
T_"a[134:135],v250 offset:4616\n" \
M_"a[36:39],a[116:119],a[152:155],0\n" \
T_"a[136:137],v251 offset:4096\n" \
M_"a[40:43],a[120:123],a[152:155],0\n" \
T_"a[138:139],v251 offset:4608\n" \
M_"a[44:47],a[124:127],a[152:155],0\n" \
R_"a[152:155],v224 offset:9216\n" \
M_"a[48:51],a[112:115],a[156:159],0\n" \
T_"a[140:141],v251 offset:4104\n" \
M_"a[52:55],a[116:119],a[156:159],0\n" \
T_"a[142:143],v251 offset:4616\n" \
M_"a[56:59],a[120:123],a[156:159],0\n" \
M_"a[60:63],a[124:127],a[156:159],0\n" \
R_"a[156:159],v224 offset:10240\n" \
S_"lgkmcnt(0)\n" \
"LT3_%=:\n" \
"s_memrealtime s[18:19]\n" \
S_"lgkmcnt(0)\n" \
"s_sub_u32 s18,s18,s20\n" \
"s_cmp_ge_u32 s18,22\n" \
"s_cbranch_scc1 LT3d_%=\n" \
"s_sleep 1\n" \
"s_branch LT3_%=\n" \
"LT3d_%=:\n" \
M_"a[64:67],a[112:115],a[160:163],0\n" \
M_"a[68:71],a[116:119],a[160:163],0\n" \
M_"a[72:75],a[120:123],a[160:163],0\n" \
M_"a[76:79],a[124:127],a[160:163],0\n" \
R_"a[160:163],v224 offset:11264\n" \
M_"a[80:83],a[112:115],a[164:167],0\n" \
M_"a[84:87],a[116:119],a[164:167],0\n" \
M_"a[88:91],a[120:123],a[164:167],0\n" \
M_"a[92:95],a[124:127],a[164:167],0\n" \
R_"a[164:167],v224 offset:12288\n" \
M_"a[96:99],a[112:115],a[168:171],0\n" \
M_"a[100:103],a[116:119],a[168:171],0\n" \
M_"a[104:107],a[120:123],a[168:171],0\n" \
M_"a[108:111],a[124:127],a[168:171],0\n" \
R_"a[168:171],v224 offset:13312\n" \
M_"a[0:3],a[128:131],a[144:147],a[0:3]\n" \
A_"m0,s74,4096\n" \
A_"s60,s64,s69\n" \
A_"s60,s60,0\n" \
M_"a[4:7],a[132:135],a[144:147],a[4:7]\n" \
L_"v228,s[48:51],s60 offen nt lds\n" \
S_"lgkmcnt(0)\n" \
"LT4_%=:\n" \
"s_memrealtime s[18:19]\n" \
S_"lgkmcnt(0)\n" \
"s_sub_u32 s18,s18,s20\n" \
"s_cmp_ge_u32 s18,45\n" \
"s_cbranch_scc1 LT4d_%=\n" \
"s_sleep 1\n" \
"s_branch LT4_%=\n" \
"LT4d_%=:\n" \
M_"a[8:11],a[136:139],a[144:147],a[8:11]\n" \
L_"v229,s[48:51],s60 offen offset:1024 nt lds\n" \
M_"a[12:15],a[140:143],a[144:147],a[12:15]\n" \
R_"a[144:147],v224 offset:14336\n" \
M_"a[16:19],a[128:131],a[148:151],a[16:19]\n" \
L_"v230,s[48:51],s60 offen offset:2048 nt lds\n" \
T_"a[172:173],v250 offset:8192\n" \
M_"a[20:23],a[132:135],a[148:151],a[20:23]\n" \
L_"v231,s[48:51],s60 offen offset:3072 nt lds\n" \
T_"a[174:175],v250 offset:8704\n" \
M_"a[24:27],a[136:139],a[148:151],a[24:27]\n" \
T_"a[176:177],v250 offset:8200\n" \
M_"a[28:31],a[140:143],a[148:151],a[28:31]\n" \
R_"a[148:151],v224 offset:15360\n" \
M_"a[32:35],a[128:131],a[152:155],a[32:35]\n" \
T_"a[178:179],v250 offset:8712\n" \
M_"a[36:39],a[132:135],a[152:155],a[36:39]\n" \
T_"a[180:181],v251 offset:8192\n" \
M_"a[40:43],a[136:139],a[152:155],a[40:43]\n" \
T_"a[182:183],v251 offset:8704\n" \
M_"a[44:47],a[140:143],a[152:155],a[44:47]\n" \
R_"a[152:155],v224 offset:16384\n" \
M_"a[48:51],a[128:131],a[156:159],a[48:51]\n" \
T_"a[184:185],v251 offset:8200\n" \
M_"a[52:55],a[132:135],a[156:159],a[52:55]\n" \
T_"a[186:187],v251 offset:8712\n" \
M_"a[56:59],a[136:139],a[156:159],a[56:59]\n" \
S_"vmcnt(0)\n" \
M_"a[60:63],a[140:143],a[156:159],a[60:63]\n" \
R_"a[156:159],v224 offset:17408\n" \
S_"lgkmcnt(0)\n" \
"LT5_%=:\n" \
"s_memrealtime s[18:19]\n" \
S_"lgkmcnt(0)\n" \
"s_sub_u32 s18,s18,s20\n" \
"s_cmp_ge_u32 s18,67\n" \
"s_cbranch_scc1 LT5d_%=\n" \
"s_sleep 1\n" \
"s_branch LT5_%=\n" \
"LT5d_%=:\n" \
M_"a[64:67],a[128:131],a[160:163],a[64:67]\n" \
M_"a[68:71],a[132:135],a[160:163],a[68:71]\n" \
M_"a[72:75],a[136:139],a[160:163],a[72:75]\n" \
M_"a[76:79],a[140:143],a[160:163],a[76:79]\n" \
R_"a[160:163],v224 offset:18432\n" \
M_"a[80:83],a[128:131],a[164:167],a[80:83]\n" \
M_"a[84:87],a[132:135],a[164:167],a[84:87]\n" \
M_"a[88:91],a[136:139],a[164:167],a[88:91]\n" \
M_"a[92:95],a[140:143],a[164:167],a[92:95]\n" \
R_"a[164:167],v224 offset:19456\n" \
M_"a[96:99],a[128:131],a[168:171],a[96:99]\n" \
M_"a[100:103],a[132:135],a[168:171],a[100:103]\n" \
M_"a[104:107],a[136:139],a[168:171],a[104:107]\n" \
M_"a[108:111],a[140:143],a[168:171],a[108:111]\n" \
R_"a[168:171],v224 offset:20480\n" \
M_"a[0:3],a[172:175],a[144:147],a[0:3]\n" \
A_"m0,s74,8192\n" \
A_"s60,s64,s69\n" \
A_"s60,s60,36864\n" \
M_"a[4:7],a[176:179],a[144:147],a[4:7]\n" \
L_"v228,s[48:51],s60 offen nt lds\n" \
S_"lgkmcnt(0)\n" \
"LT6_%=:\n" \
"s_memrealtime s[18:19]\n" \
S_"lgkmcnt(0)\n" \
"s_sub_u32 s18,s18,s20\n" \
"s_cmp_ge_u32 s18,90\n" \
"s_cbranch_scc1 LT6d_%=\n" \
"s_sleep 1\n" \
"s_branch LT6_%=\n" \
"LT6d_%=:\n" \
M_"a[8:11],a[180:183],a[144:147],a[8:11]\n" \
L_"v229,s[48:51],s60 offen offset:1024 nt lds\n" \
M_"a[12:15],a[184:187],a[144:147],a[12:15]\n" \
R_"a[144:147],v224 offset:21504\n" \
M_"a[16:19],a[172:175],a[148:151],a[16:19]\n" \
L_"v230,s[48:51],s60 offen offset:2048 nt lds\n" \
T_"a[188:189],v250 offset:0\n" \
M_"a[20:23],a[176:179],a[148:151],a[20:23]\n" \
L_"v231,s[48:51],s60 offen offset:3072 nt lds\n" \
T_"a[190:191],v250 offset:512\n" \
M_"a[24:27],a[180:183],a[148:151],a[24:27]\n" \
T_"a[192:193],v250 offset:8\n" \
M_"a[28:31],a[184:187],a[148:151],a[28:31]\n" \
R_"a[148:151],v224 offset:22528\n" \
M_"a[32:35],a[172:175],a[152:155],a[32:35]\n" \
T_"a[194:195],v250 offset:520\n" \
M_"a[36:39],a[176:179],a[152:155],a[36:39]\n" \
T_"a[196:197],v251 offset:0\n" \
M_"a[40:43],a[180:183],a[152:155],a[40:43]\n" \
T_"a[198:199],v251 offset:512\n" \
M_"a[44:47],a[184:187],a[152:155],a[44:47]\n" \
R_"a[152:155],v224 offset:23552\n" \
M_"a[48:51],a[172:175],a[156:159],a[48:51]\n" \
T_"a[200:201],v251 offset:8\n" \
M_"a[52:55],a[176:179],a[156:159],a[52:55]\n" \
T_"a[202:203],v251 offset:520\n" \
M_"a[56:59],a[180:183],a[156:159],a[56:59]\n" \
M_"a[60:63],a[184:187],a[156:159],a[60:63]\n" \
R_"a[156:159],v224 offset:24576\n" \
S_"lgkmcnt(0)\n" \
"LT7_%=:\n" \
"s_memrealtime s[18:19]\n" \
S_"lgkmcnt(0)\n" \
"s_sub_u32 s18,s18,s20\n" \
"s_cmp_ge_u32 s18,112\n" \
"s_cbranch_scc1 LT7d_%=\n" \
"s_sleep 1\n" \
"s_branch LT7_%=\n" \
"LT7d_%=:\n" \
M_"a[64:67],a[172:175],a[160:163],a[64:67]\n" \
M_"a[68:71],a[176:179],a[160:163],a[68:71]\n" \
M_"a[72:75],a[180:183],a[160:163],a[72:75]\n" \
M_"a[76:79],a[184:187],a[160:163],a[76:79]\n" \
R_"a[160:163],v224 offset:25600\n" \
M_"a[80:83],a[172:175],a[164:167],a[80:83]\n" \
M_"a[84:87],a[176:179],a[164:167],a[84:87]\n" \
M_"a[88:91],a[180:183],a[164:167],a[88:91]\n" \
M_"a[92:95],a[184:187],a[164:167],a[92:95]\n" \
R_"a[164:167],v224 offset:26624\n" \
M_"a[96:99],a[172:175],a[168:171],a[96:99]\n" \
M_"a[100:103],a[176:179],a[168:171],a[100:103]\n" \
M_"a[104:107],a[180:183],a[168:171],a[104:107]\n" \
M_"a[108:111],a[184:187],a[168:171],a[108:111]\n" \
R_"a[168:171],v224 offset:27648\n" \
M_"a[0:3],a[188:191],a[144:147],a[0:3]\n" \
A_"m0,s74,0\n" \
A_"s60,s64,s69\n" \
A_"s60,s60,73728\n" \
M_"a[4:7],a[192:195],a[144:147],a[4:7]\n" \
L_"v228,s[48:51],s60 offen nt lds\n" \
S_"lgkmcnt(0)\n" \
"LT8_%=:\n" \
"s_memrealtime s[18:19]\n" \
S_"lgkmcnt(0)\n" \
"s_sub_u32 s18,s18,s20\n" \
"s_cmp_ge_u32 s18,135\n" \
"s_cbranch_scc1 LT8d_%=\n" \
"s_sleep 1\n" \
"s_branch LT8_%=\n" \
"LT8d_%=:\n" \
M_"a[8:11],a[196:199],a[144:147],a[8:11]\n" \
L_"v229,s[48:51],s60 offen offset:1024 nt lds\n" \
M_"a[12:15],a[200:203],a[144:147],a[12:15]\n" \
R_"a[144:147],v224 offset:0\n" \
M_"a[16:19],a[188:191],a[148:151],a[16:19]\n" \
L_"v230,s[48:51],s60 offen offset:2048 nt lds\n" \
T_"a[204:205],v250 offset:4096\n" \
M_"a[20:23],a[192:195],a[148:151],a[20:23]\n" \
L_"v231,s[48:51],s60 offen offset:3072 nt lds\n" \
T_"a[206:207],v250 offset:4608\n" \
M_"a[24:27],a[196:199],a[148:151],a[24:27]\n" \
T_"a[208:209],v250 offset:4104\n" \
M_"a[28:31],a[200:203],a[148:151],a[28:31]\n" \
R_"a[148:151],v224 offset:1024\n" \
M_"a[32:35],a[188:191],a[152:155],a[32:35]\n" \
T_"a[210:211],v250 offset:4616\n" \
M_"a[36:39],a[192:195],a[152:155],a[36:39]\n" \
T_"a[212:213],v251 offset:4096\n" \
M_"a[40:43],a[196:199],a[152:155],a[40:43]\n" \
T_"a[214:215],v251 offset:4608\n" \
M_"a[44:47],a[200:203],a[152:155],a[44:47]\n" \
R_"a[152:155],v224 offset:2048\n" \
M_"a[48:51],a[188:191],a[156:159],a[48:51]\n" \
T_"a[216:217],v251 offset:4104\n" \
M_"a[52:55],a[192:195],a[156:159],a[52:55]\n" \
T_"a[218:219],v251 offset:4616\n" \
M_"a[56:59],a[196:199],a[156:159],a[56:59]\n" \
M_"a[60:63],a[200:203],a[156:159],a[60:63]\n" \
R_"a[156:159],v224 offset:3072\n" \
S_"lgkmcnt(0)\n" \
"LT9_%=:\n" \
"s_memrealtime s[18:19]\n" \
S_"lgkmcnt(0)\n" \
"s_sub_u32 s18,s18,s20\n" \
"s_cmp_ge_u32 s18,157\n" \
"s_cbranch_scc1 LT9d_%=\n" \
"s_sleep 1\n" \
"s_branch LT9_%=\n" \
"LT9d_%=:\n" \
M_"a[64:67],a[188:191],a[160:163],a[64:67]\n" \
M_"a[68:71],a[192:195],a[160:163],a[68:71]\n" \
M_"a[72:75],a[196:199],a[160:163],a[72:75]\n" \
M_"a[76:79],a[200:203],a[160:163],a[76:79]\n" \
R_"a[160:163],v224 offset:4096\n" \
M_"a[80:83],a[188:191],a[164:167],a[80:83]\n" \
M_"a[84:87],a[192:195],a[164:167],a[84:87]\n" \
M_"a[88:91],a[196:199],a[164:167],a[88:91]\n" \
M_"a[92:95],a[200:203],a[164:167],a[92:95]\n" \
R_"a[164:167],v224 offset:5120\n" \
M_"a[96:99],a[188:191],a[168:171],a[96:99]\n" \
M_"a[100:103],a[192:195],a[168:171],a[100:103]\n" \
M_"a[104:107],a[196:199],a[168:171],a[104:107]\n" \
M_"a[108:111],a[200:203],a[168:171],a[108:111]\n" \
R_"a[168:171],v224 offset:6144\n" \
M_"v[0:3],a[204:207],a[144:147],0\n" \
A_"m0,s74,4096\n" \
A_"s60,s64,s69\n" \
A_"s60,s60,110592\n" \
M_"v[4:7],a[208:211],a[144:147],0\n" \
L_"v228,s[48:51],s60 offen nt lds\n" \
S_"lgkmcnt(0)\n" \
"LT10_%=:\n" \
"s_memrealtime s[18:19]\n" \
S_"lgkmcnt(0)\n" \
"s_sub_u32 s18,s18,s20\n" \
"s_cmp_ge_u32 s18,180\n" \
"s_cbranch_scc1 LT10d_%=\n" \
"s_sleep 1\n" \
"s_branch LT10_%=\n" \
"LT10d_%=:\n" \
M_"v[8:11],a[212:215],a[144:147],0\n" \
L_"v229,s[48:51],s60 offen offset:1024 nt lds\n" \
M_"v[12:15],a[216:219],a[144:147],0\n" \
R_"a[144:147],v224 offset:7168\n" \
M_"v[16:19],a[204:207],a[148:151],0\n" \
L_"v230,s[48:51],s60 offen offset:2048 nt lds\n" \
S_"vmcnt(7)\n" \
T_"a[220:221],v250 offset:8192\n" \
M_"v[20:23],a[208:211],a[148:151],0\n" \
L_"v231,s[48:51],s60 offen offset:3072 nt lds\n" \
T_"a[222:223],v250 offset:8704\n" \
M_"v[24:27],a[212:215],a[148:151],0\n" \
T_"a[224:225],v250 offset:8200\n" \
M_"v[28:31],a[216:219],a[148:151],0\n" \
R_"a[148:151],v224 offset:8192\n" \
M_"v[32:35],a[204:207],a[152:155],0\n" \
T_"a[226:227],v250 offset:8712\n" \
M_"v[36:39],a[208:211],a[152:155],0\n" \
T_"a[228:229],v251 offset:8192\n" \
M_"v[40:43],a[212:215],a[152:155],0\n" \
T_"a[230:231],v251 offset:8704\n" \
M_"v[44:47],a[216:219],a[152:155],0\n" \
R_"a[152:155],v224 offset:9216\n" \
M_"v[48:51],a[204:207],a[156:159],0\n" \
T_"a[232:233],v251 offset:8200\n" \
M_"v[52:55],a[208:211],a[156:159],0\n" \
T_"a[234:235],v251 offset:8712\n" \
M_"v[56:59],a[212:215],a[156:159],0\n" \
M_"v[60:63],a[216:219],a[156:159],0\n" \
R_"a[156:159],v224 offset:10240\n" \
S_"lgkmcnt(0)\n" \
"LT11_%=:\n" \
"s_memrealtime s[18:19]\n" \
S_"lgkmcnt(0)\n" \
"s_sub_u32 s18,s18,s20\n" \
"s_cmp_ge_u32 s18,202\n" \
"s_cbranch_scc1 LT11d_%=\n" \
"s_sleep 1\n" \
"s_branch LT11_%=\n" \
"LT11d_%=:\n" \
M_"v[64:67],a[204:207],a[160:163],0\n" \
M_"v[68:71],a[208:211],a[160:163],0\n" \
M_"v[72:75],a[212:215],a[160:163],0\n" \
M_"v[76:79],a[216:219],a[160:163],0\n" \
R_"a[160:163],v224 offset:11264\n" \
M_"v[80:83],a[204:207],a[164:167],0\n" \
M_"v[84:87],a[208:211],a[164:167],0\n" \
M_"v[88:91],a[212:215],a[164:167],0\n" \
M_"v[92:95],a[216:219],a[164:167],0\n" \
R_"a[164:167],v224 offset:12288\n" \
M_"v[96:99],a[204:207],a[168:171],0\n" \
M_"v[100:103],a[208:211],a[168:171],0\n" \
M_"v[104:107],a[212:215],a[168:171],0\n" \
M_"v[108:111],a[216:219],a[168:171],0\n" \
R_"a[168:171],v224 offset:13312\n" \
M_"v[0:3],a[220:223],a[144:147],v[0:3]\n" \
A_"m0,s74,8192\n" \
A_"s60,s64,s70\n" \
A_"s60,s60,0\n" \
M_"v[4:7],a[224:227],a[144:147],v[4:7]\n" \
L_"v228,s[48:51],s60 offen nt lds\n" \
S_"lgkmcnt(0)\n" \
"LT12_%=:\n" \
"s_memrealtime s[18:19]\n" \
S_"lgkmcnt(0)\n" \
"s_sub_u32 s18,s18,s20\n" \
"s_cmp_ge_u32 s18,225\n" \
"s_cbranch_scc1 LT12d_%=\n" \
"s_sleep 1\n" \
"s_branch LT12_%=\n" \
"LT12d_%=:\n" \
M_"v[8:11],a[228:231],a[144:147],v[8:11]\n" \
L_"v229,s[48:51],s60 offen offset:1024 nt lds\n" \
M_"v[12:15],a[232:235],a[144:147],v[12:15]\n" \
R_"a[144:147],v224 offset:14336\n" \
M_"v[16:19],a[220:223],a[148:151],v[16:19]\n" \
L_"v230,s[48:51],s60 offen offset:2048 nt lds\n" \
S_"vmcnt(7)\n" \
T_"a[236:237],v250 offset:0\n" \
M_"v[20:23],a[224:227],a[148:151],v[20:23]\n" \
L_"v231,s[48:51],s60 offen offset:3072 nt lds\n" \
T_"a[238:239],v250 offset:512\n" \
M_"v[24:27],a[228:231],a[148:151],v[24:27]\n" \
T_"a[240:241],v250 offset:8\n" \
M_"v[28:31],a[232:235],a[148:151],v[28:31]\n" \
R_"a[148:151],v224 offset:15360\n" \
M_"v[32:35],a[220:223],a[152:155],v[32:35]\n" \
T_"a[242:243],v250 offset:520\n" \
M_"v[36:39],a[224:227],a[152:155],v[36:39]\n" \
T_"a[244:245],v251 offset:0\n" \
M_"v[40:43],a[228:231],a[152:155],v[40:43]\n" \
T_"a[246:247],v251 offset:512\n" \
M_"v[44:47],a[232:235],a[152:155],v[44:47]\n" \
R_"a[152:155],v224 offset:16384\n" \
M_"v[48:51],a[220:223],a[156:159],v[48:51]\n" \
T_"a[248:249],v251 offset:8\n" \
M_"v[52:55],a[224:227],a[156:159],v[52:55]\n" \
T_"a[250:251],v251 offset:520\n" \
M_"v[56:59],a[228:231],a[156:159],v[56:59]\n" \
M_"v[60:63],a[232:235],a[156:159],v[60:63]\n" \
R_"a[156:159],v224 offset:17408\n" \
S_"lgkmcnt(0)\n" \
"LT13_%=:\n" \
"s_memrealtime s[18:19]\n" \
S_"lgkmcnt(0)\n" \
"s_sub_u32 s18,s18,s20\n" \
"s_cmp_ge_u32 s18,247\n" \
"s_cbranch_scc1 LT13d_%=\n" \
"s_sleep 1\n" \
"s_branch LT13_%=\n" \
"LT13d_%=:\n" \
M_"v[64:67],a[220:223],a[160:163],v[64:67]\n" \
M_"v[68:71],a[224:227],a[160:163],v[68:71]\n" \
M_"v[72:75],a[228:231],a[160:163],v[72:75]\n" \
M_"v[76:79],a[232:235],a[160:163],v[76:79]\n" \
R_"a[160:163],v224 offset:18432\n" \
M_"v[80:83],a[220:223],a[164:167],v[80:83]\n" \
M_"v[84:87],a[224:227],a[164:167],v[84:87]\n" \
M_"v[88:91],a[228:231],a[164:167],v[88:91]\n" \
M_"v[92:95],a[232:235],a[164:167],v[92:95]\n" \
R_"a[164:167],v224 offset:19456\n" \
M_"v[96:99],a[220:223],a[168:171],v[96:99]\n" \
M_"v[100:103],a[224:227],a[168:171],v[100:103]\n" \
M_"v[104:107],a[228:231],a[168:171],v[104:107]\n" \
M_"v[108:111],a[232:235],a[168:171],v[108:111]\n" \
R_"a[168:171],v224 offset:20480\n" \
M_"v[0:3],a[236:239],a[144:147],v[0:3]\n" \
A_"m0,s74,0\n" \
A_"s60,s64,s70\n" \
A_"s60,s60,36864\n" \
M_"v[4:7],a[240:243],a[144:147],v[4:7]\n" \
L_"v228,s[48:51],s60 offen nt lds\n" \
S_"lgkmcnt(0)\n" \
"LT14_%=:\n" \
"s_memrealtime s[18:19]\n" \
S_"lgkmcnt(0)\n" \
"s_sub_u32 s18,s18,s20\n" \
"s_cmp_ge_u32 s18,270\n" \
"s_cbranch_scc1 LT14d_%=\n" \
"s_sleep 1\n" \
"s_branch LT14_%=\n" \
"LT14d_%=:\n" \
M_"v[8:11],a[244:247],a[144:147],v[8:11]\n" \
L_"v229,s[48:51],s60 offen offset:1024 nt lds\n" \
M_"v[12:15],a[248:251],a[144:147],v[12:15]\n" \
R_"a[144:147],v224 offset:21504\n" \
M_"v[16:19],a[236:239],a[148:151],v[16:19]\n" \
L_"v230,s[48:51],s60 offen offset:2048 nt lds\n" \
S_"vmcnt(7)\n" \
T_"a[112:113],v250 offset:4096\n" \
M_"v[20:23],a[240:243],a[148:151],v[20:23]\n" \
L_"v231,s[48:51],s60 offen offset:3072 nt lds\n" \
T_"a[114:115],v250 offset:4608\n" \
M_"v[24:27],a[244:247],a[148:151],v[24:27]\n" \
T_"a[116:117],v250 offset:4104\n" \
M_"v[28:31],a[248:251],a[148:151],v[28:31]\n" \
R_"a[148:151],v224 offset:22528\n" \
M_"v[32:35],a[236:239],a[152:155],v[32:35]\n" \
T_"a[118:119],v250 offset:4616\n" \
M_"v[36:39],a[240:243],a[152:155],v[36:39]\n" \
T_"a[120:121],v251 offset:4096\n" \
M_"v[40:43],a[244:247],a[152:155],v[40:43]\n" \
T_"a[122:123],v251 offset:4608\n" \
M_"v[44:47],a[248:251],a[152:155],v[44:47]\n" \
R_"a[152:155],v224 offset:23552\n" \
M_"v[48:51],a[236:239],a[156:159],v[48:51]\n" \
T_"a[124:125],v251 offset:4104\n" \
M_"v[52:55],a[240:243],a[156:159],v[52:55]\n" \
T_"a[126:127],v251 offset:4616\n" \
M_"v[56:59],a[244:247],a[156:159],v[56:59]\n" \
M_"v[60:63],a[248:251],a[156:159],v[60:63]\n" \
R_"a[156:159],v224 offset:24576\n" \
S_"lgkmcnt(0)\n" \
"LT15_%=:\n" \
"s_memrealtime s[18:19]\n" \
S_"lgkmcnt(0)\n" \
"s_sub_u32 s18,s18,s20\n" \
"s_cmp_ge_u32 s18,292\n" \
"s_cbranch_scc1 LT15d_%=\n" \
"s_sleep 1\n" \
"s_branch LT15_%=\n" \
"LT15d_%=:\n" \
M_"v[64:67],a[236:239],a[160:163],v[64:67]\n" \
M_"v[68:71],a[240:243],a[160:163],v[68:71]\n" \
M_"v[72:75],a[244:247],a[160:163],v[72:75]\n" \
M_"v[76:79],a[248:251],a[160:163],v[76:79]\n" \
R_"a[160:163],v224 offset:25600\n" \
M_"v[80:83],a[236:239],a[164:167],v[80:83]\n" \
M_"v[84:87],a[240:243],a[164:167],v[84:87]\n" \
M_"v[88:91],a[244:247],a[164:167],v[88:91]\n" \
M_"v[92:95],a[248:251],a[164:167],v[92:95]\n" \
R_"a[164:167],v224 offset:26624\n" \
M_"v[96:99],a[236:239],a[168:171],v[96:99]\n" \
M_"v[100:103],a[240:243],a[168:171],v[100:103]\n" \
M_"v[104:107],a[244:247],a[168:171],v[104:107]\n" \
M_"v[108:111],a[248:251],a[168:171],v[108:111]\n" \
R_"a[168:171],v224 offset:27648\n" \
M_"v[0:3],a[112:115],a[144:147],v[0:3]\n" \
A_"m0,s74,4096\n" \
A_"s60,s64,s70\n" \
A_"s60,s60,73728\n" \
M_"v[4:7],a[116:119],a[144:147],v[4:7]\n" \
L_"v228,s[48:51],s60 offen nt lds\n" \
S_"lgkmcnt(0)\n" \
"LT16_%=:\n" \
"s_memrealtime s[18:19]\n" \
S_"lgkmcnt(0)\n" \
"s_sub_u32 s18,s18,s20\n" \
"s_cmp_ge_u32 s18,315\n" \
"s_cbranch_scc1 LT16d_%=\n" \
"s_sleep 1\n" \
"s_branch LT16_%=\n" \
"LT16d_%=:\n" \
M_"v[8:11],a[120:123],a[144:147],v[8:11]\n" \
L_"v229,s[48:51],s60 offen offset:1024 nt lds\n" \
M_"v[12:15],a[124:127],a[144:147],v[12:15]\n" \
R_"a[144:147],v224 offset:0\n" \
M_"v[16:19],a[112:115],a[148:151],v[16:19]\n" \
L_"v230,s[48:51],s60 offen offset:2048 nt lds\n" \
S_"vmcnt(7)\n" \
T_"a[128:129],v250 offset:8192\n" \
M_"v[20:23],a[116:119],a[148:151],v[20:23]\n" \
L_"v231,s[48:51],s60 offen offset:3072 nt lds\n" \
T_"a[130:131],v250 offset:8704\n" \
M_"v[24:27],a[120:123],a[148:151],v[24:27]\n" \
T_"a[132:133],v250 offset:8200\n" \
M_"v[28:31],a[124:127],a[148:151],v[28:31]\n" \
R_"a[148:151],v224 offset:1024\n" \
M_"v[32:35],a[112:115],a[152:155],v[32:35]\n" \
T_"a[134:135],v250 offset:8712\n" \
M_"v[36:39],a[116:119],a[152:155],v[36:39]\n" \
T_"a[136:137],v251 offset:8192\n" \
M_"v[40:43],a[120:123],a[152:155],v[40:43]\n" \
T_"a[138:139],v251 offset:8704\n" \
M_"v[44:47],a[124:127],a[152:155],v[44:47]\n" \
R_"a[152:155],v224 offset:2048\n" \
M_"v[48:51],a[112:115],a[156:159],v[48:51]\n" \
T_"a[140:141],v251 offset:8200\n" \
M_"v[52:55],a[116:119],a[156:159],v[52:55]\n" \
T_"a[142:143],v251 offset:8712\n" \
M_"v[56:59],a[120:123],a[156:159],v[56:59]\n" \
M_"v[60:63],a[124:127],a[156:159],v[60:63]\n" \
R_"a[156:159],v224 offset:3072\n" \
S_"lgkmcnt(0)\n" \
"LT17_%=:\n" \
"s_memrealtime s[18:19]\n" \
S_"lgkmcnt(0)\n" \
"s_sub_u32 s18,s18,s20\n" \
"s_cmp_ge_u32 s18,337\n" \
"s_cbranch_scc1 LT17d_%=\n" \
"s_sleep 1\n" \
"s_branch LT17_%=\n" \
"LT17d_%=:\n" \
M_"v[64:67],a[112:115],a[160:163],v[64:67]\n" \
M_"v[68:71],a[116:119],a[160:163],v[68:71]\n" \
M_"v[72:75],a[120:123],a[160:163],v[72:75]\n" \
M_"v[76:79],a[124:127],a[160:163],v[76:79]\n" \
R_"a[160:163],v224 offset:4096\n" \
M_"v[80:83],a[112:115],a[164:167],v[80:83]\n" \
M_"v[84:87],a[116:119],a[164:167],v[84:87]\n" \
M_"v[88:91],a[120:123],a[164:167],v[88:91]\n" \
M_"v[92:95],a[124:127],a[164:167],v[92:95]\n" \
R_"a[164:167],v224 offset:5120\n" \
M_"v[96:99],a[112:115],a[168:171],v[96:99]\n" \
M_"v[100:103],a[116:119],a[168:171],v[100:103]\n" \
M_"v[104:107],a[120:123],a[168:171],v[104:107]\n" \
M_"v[108:111],a[124:127],a[168:171],v[108:111]\n" \
R_"a[168:171],v224 offset:6144\n" \
M_"v[112:115],a[128:131],a[144:147],0\n" \
A_"m0,s74,8192\n" \
A_"s60,s64,s70\n" \
A_"s60,s60,110592\n" \
M_"v[116:119],a[132:135],a[144:147],0\n" \
L_"v228,s[48:51],s60 offen nt lds\n" \
S_"lgkmcnt(0)\n" \
"LT18_%=:\n" \
"s_memrealtime s[18:19]\n" \
S_"lgkmcnt(0)\n" \
"s_sub_u32 s18,s18,s20\n" \
"s_cmp_ge_u32 s18,360\n" \
"s_cbranch_scc1 LT18d_%=\n" \
"s_sleep 1\n" \
"s_branch LT18_%=\n" \
"LT18d_%=:\n" \
M_"v[120:123],a[136:139],a[144:147],0\n" \
L_"v229,s[48:51],s60 offen offset:1024 nt lds\n" \
M_"v[124:127],a[140:143],a[144:147],0\n" \
R_"a[144:147],v224 offset:7168\n" \
M_"v[128:131],a[128:131],a[148:151],0\n" \
L_"v230,s[48:51],s60 offen offset:2048 nt lds\n" \
S_"vmcnt(7)\n" \
T_"a[172:173],v250 offset:0\n" \
M_"v[132:135],a[132:135],a[148:151],0\n" \
L_"v231,s[48:51],s60 offen offset:3072 nt lds\n" \
T_"a[174:175],v250 offset:512\n" \
M_"v[136:139],a[136:139],a[148:151],0\n" \
T_"a[176:177],v250 offset:8\n" \
M_"v[140:143],a[140:143],a[148:151],0\n" \
R_"a[148:151],v224 offset:8192\n" \
M_"v[144:147],a[128:131],a[152:155],0\n" \
T_"a[178:179],v250 offset:520\n" \
M_"v[148:151],a[132:135],a[152:155],0\n" \
T_"a[180:181],v251 offset:0\n" \
M_"v[152:155],a[136:139],a[152:155],0\n" \
T_"a[182:183],v251 offset:512\n" \
M_"v[156:159],a[140:143],a[152:155],0\n" \
R_"a[152:155],v224 offset:9216\n" \
M_"v[160:163],a[128:131],a[156:159],0\n" \
T_"a[184:185],v251 offset:8\n" \
M_"v[164:167],a[132:135],a[156:159],0\n" \
T_"a[186:187],v251 offset:520\n" \
M_"v[168:171],a[136:139],a[156:159],0\n" \
M_"v[172:175],a[140:143],a[156:159],0\n" \
R_"a[156:159],v224 offset:10240\n" \
S_"lgkmcnt(0)\n" \
"LT19_%=:\n" \
"s_memrealtime s[18:19]\n" \
S_"lgkmcnt(0)\n" \
"s_sub_u32 s18,s18,s20\n" \
"s_cmp_ge_u32 s18,382\n" \
"s_cbranch_scc1 LT19d_%=\n" \
"s_sleep 1\n" \
"s_branch LT19_%=\n" \
"LT19d_%=:\n" \
M_"v[176:179],a[128:131],a[160:163],0\n" \
M_"v[180:183],a[132:135],a[160:163],0\n" \
M_"v[184:187],a[136:139],a[160:163],0\n" \
M_"v[188:191],a[140:143],a[160:163],0\n" \
R_"a[160:163],v224 offset:11264\n" \
M_"v[192:195],a[128:131],a[164:167],0\n" \
M_"v[196:199],a[132:135],a[164:167],0\n" \
M_"v[200:203],a[136:139],a[164:167],0\n" \
M_"v[204:207],a[140:143],a[164:167],0\n" \
R_"a[164:167],v224 offset:12288\n" \
M_"v[208:211],a[128:131],a[168:171],0\n" \
M_"v[212:215],a[132:135],a[168:171],0\n" \
M_"v[216:219],a[136:139],a[168:171],0\n" \
M_"v[220:223],a[140:143],a[168:171],0\n" \
R_"a[168:171],v224 offset:13312\n" \
M_"v[112:115],a[172:175],a[144:147],v[112:115]\n" \
A_"m0,s74,0\n" \
A_"s60,s65,s68\n" \
A_"s60,s60,0\n" \
M_"v[116:119],a[176:179],a[144:147],v[116:119]\n" \
L_"v228,s[48:51],s60 offen nt lds\n" \
S_"lgkmcnt(0)\n" \
"LT20_%=:\n" \
"s_memrealtime s[18:19]\n" \
S_"lgkmcnt(0)\n" \
"s_sub_u32 s18,s18,s20\n" \
"s_cmp_ge_u32 s18,405\n" \
"s_cbranch_scc1 LT20d_%=\n" \
"s_sleep 1\n" \
"s_branch LT20_%=\n" \
"LT20d_%=:\n" \
M_"v[120:123],a[180:183],a[144:147],v[120:123]\n" \
L_"v229,s[48:51],s60 offen offset:1024 nt lds\n" \
M_"v[124:127],a[184:187],a[144:147],v[124:127]\n" \
R_"a[144:147],v224 offset:14336\n" \
M_"v[128:131],a[172:175],a[148:151],v[128:131]\n" \
L_"v230,s[48:51],s60 offen offset:2048 nt lds\n" \
S_"vmcnt(7)\n" \
T_"a[188:189],v250 offset:4096\n" \
M_"v[132:135],a[176:179],a[148:151],v[132:135]\n" \
L_"v231,s[48:51],s60 offen offset:3072 nt lds\n" \
T_"a[190:191],v250 offset:4608\n" \
M_"v[136:139],a[180:183],a[148:151],v[136:139]\n" \
T_"a[192:193],v250 offset:4104\n" \
M_"v[140:143],a[184:187],a[148:151],v[140:143]\n" \
R_"a[148:151],v224 offset:15360\n" \
M_"v[144:147],a[172:175],a[152:155],v[144:147]\n" \
T_"a[194:195],v250 offset:4616\n" \
M_"v[148:151],a[176:179],a[152:155],v[148:151]\n" \
T_"a[196:197],v251 offset:4096\n" \
M_"v[152:155],a[180:183],a[152:155],v[152:155]\n" \
T_"a[198:199],v251 offset:4608\n" \
M_"v[156:159],a[184:187],a[152:155],v[156:159]\n" \
R_"a[152:155],v224 offset:16384\n" \
M_"v[160:163],a[172:175],a[156:159],v[160:163]\n" \
T_"a[200:201],v251 offset:4104\n" \
M_"v[164:167],a[176:179],a[156:159],v[164:167]\n" \
T_"a[202:203],v251 offset:4616\n" \
M_"v[168:171],a[180:183],a[156:159],v[168:171]\n" \
"buffer_load_dword v235,v233,s[84:87],0 offen sc1\n" \
M_"v[172:175],a[184:187],a[156:159],v[172:175]\n" \
R_"a[156:159],v224 offset:17408\n" \
S_"lgkmcnt(0)\n" \
"LT21_%=:\n" \
"s_memrealtime s[18:19]\n" \
S_"lgkmcnt(0)\n" \
"s_sub_u32 s18,s18,s20\n" \
"s_cmp_ge_u32 s18,427\n" \
"s_cbranch_scc1 LT21d_%=\n" \
"s_sleep 1\n" \
"s_branch LT21_%=\n" \
"LT21d_%=:\n" \
M_"v[176:179],a[172:175],a[160:163],v[176:179]\n" \
M_"v[180:183],a[176:179],a[160:163],v[180:183]\n" \
M_"v[184:187],a[180:183],a[160:163],v[184:187]\n" \
M_"v[188:191],a[184:187],a[160:163],v[188:191]\n" \
R_"a[160:163],v224 offset:18432\n" \
M_"v[192:195],a[172:175],a[164:167],v[192:195]\n" \
M_"v[196:199],a[176:179],a[164:167],v[196:199]\n" \
M_"v[200:203],a[180:183],a[164:167],v[200:203]\n" \
M_"v[204:207],a[184:187],a[164:167],v[204:207]\n" \
R_"a[164:167],v224 offset:19456\n" \
M_"v[208:211],a[172:175],a[168:171],v[208:211]\n" \
M_"v[212:215],a[176:179],a[168:171],v[212:215]\n" \
M_"v[216:219],a[180:183],a[168:171],v[216:219]\n" \
M_"v[220:223],a[184:187],a[168:171],v[220:223]\n" \
R_"a[168:171],v224 offset:20480\n" \
M_"v[112:115],a[188:191],a[144:147],v[112:115]\n" \
A_"m0,s74,4096\n" \
A_"s60,s65,s68\n" \
A_"s60,s60,36864\n" \
M_"v[116:119],a[192:195],a[144:147],v[116:119]\n" \
L_"v228,s[48:51],s60 offen nt lds\n" \
S_"lgkmcnt(0)\n" \
"LT22_%=:\n" \
"s_memrealtime s[18:19]\n" \
S_"lgkmcnt(0)\n" \
"s_sub_u32 s18,s18,s20\n" \
"s_cmp_ge_u32 s18,450\n" \
"s_cbranch_scc1 LT22d_%=\n" \
"s_sleep 1\n" \
"s_branch LT22_%=\n" \
"LT22d_%=:\n" \
M_"v[120:123],a[196:199],a[144:147],v[120:123]\n" \
L_"v229,s[48:51],s60 offen offset:1024 nt lds\n" \
M_"v[124:127],a[200:203],a[144:147],v[124:127]\n" \
R_"a[144:147],v224 offset:21504\n" \
M_"v[128:131],a[188:191],a[148:151],v[128:131]\n" \
L_"v230,s[48:51],s60 offen offset:2048 nt lds\n" \
S_"vmcnt(8)\n" \
T_"a[204:205],v250 offset:8192\n" \
M_"v[132:135],a[192:195],a[148:151],v[132:135]\n" \
L_"v231,s[48:51],s60 offen offset:3072 nt lds\n" \
T_"a[206:207],v250 offset:8704\n" \
M_"v[136:139],a[196:199],a[148:151],v[136:139]\n" \
T_"a[208:209],v250 offset:8200\n" \
M_"v[140:143],a[200:203],a[148:151],v[140:143]\n" \
R_"a[148:151],v224 offset:22528\n" \
M_"v[144:147],a[188:191],a[152:155],v[144:147]\n" \
T_"a[210:211],v250 offset:8712\n" \
M_"v[148:151],a[192:195],a[152:155],v[148:151]\n" \
T_"a[212:213],v251 offset:8192\n" \
M_"v[152:155],a[196:199],a[152:155],v[152:155]\n" \
T_"a[214:215],v251 offset:8704\n" \
M_"v[156:159],a[200:203],a[152:155],v[156:159]\n" \
R_"a[152:155],v224 offset:23552\n" \
M_"v[160:163],a[188:191],a[156:159],v[160:163]\n" \
T_"a[216:217],v251 offset:8200\n" \
M_"v[164:167],a[192:195],a[156:159],v[164:167]\n" \
T_"a[218:219],v251 offset:8712\n" \
M_"v[168:171],a[196:199],a[156:159],v[168:171]\n" \
M_"v[172:175],a[200:203],a[156:159],v[172:175]\n" \
R_"a[156:159],v224 offset:24576\n" \
S_"lgkmcnt(0)\n" \
"LT23_%=:\n" \
"s_memrealtime s[18:19]\n" \
S_"lgkmcnt(0)\n" \
"s_sub_u32 s18,s18,s20\n" \
"s_cmp_ge_u32 s18,472\n" \
"s_cbranch_scc1 LT23d_%=\n" \
"s_sleep 1\n" \
"s_branch LT23_%=\n" \
"LT23d_%=:\n" \
M_"v[176:179],a[188:191],a[160:163],v[176:179]\n" \
M_"v[180:183],a[192:195],a[160:163],v[180:183]\n" \
M_"v[184:187],a[196:199],a[160:163],v[184:187]\n" \
M_"v[188:191],a[200:203],a[160:163],v[188:191]\n" \
R_"a[160:163],v224 offset:25600\n" \
M_"v[192:195],a[188:191],a[164:167],v[192:195]\n" \
M_"v[196:199],a[192:195],a[164:167],v[196:199]\n" \
M_"v[200:203],a[196:199],a[164:167],v[200:203]\n" \
M_"v[204:207],a[200:203],a[164:167],v[204:207]\n" \
R_"a[164:167],v224 offset:26624\n" \
M_"v[208:211],a[188:191],a[168:171],v[208:211]\n" \
M_"v[212:215],a[192:195],a[168:171],v[212:215]\n" \
M_"v[216:219],a[196:199],a[168:171],v[216:219]\n" \
M_"v[220:223],a[200:203],a[168:171],v[220:223]\n" \
R_"a[168:171],v224 offset:27648\n" \
M_"v[112:115],a[204:207],a[144:147],v[112:115]\n" \
M_"v[116:119],a[208:211],a[144:147],v[116:119]\n" \
S_"lgkmcnt(0)\n" \
"LT25_%=:\n" \
"s_memrealtime s[18:19]\n" \
S_"lgkmcnt(0)\n" \
"s_sub_u32 s18,s18,s20\n" \
"s_cmp_ge_u32 s18,495\n" \
"s_cbranch_scc1 LT25d_%=\n" \
"s_sleep 1\n" \
"s_branch LT25_%=\n" \
"LT25d_%=:\n" \
M_"v[120:123],a[212:215],a[144:147],v[120:123]\n" \
"LG24_%=:\n" \
"ds_read_b32 v253,v232 offset:0\n" \
S_"lgkmcnt(0)\n" \
"v_readfirstlane_b32 s63,v253\n" \
"s_nop 0\n" \
"s_cmp_ge_u32 s63,128\n" \
"s_cbranch_scc1 LG24d_%=\n" \
"s_sleep 1\n" \
"s_branch LG24_%=\n" \
"LG24d_%=:\n" \
M_"v[124:127],a[216:219],a[144:147],v[124:127]\n" \
R_"a[144:147],v225 offset:0\n" \
M_"v[128:131],a[204:207],a[148:151],v[128:131]\n" \
A_"m0,s74,8192\n" \
A_"s60,s65,s68\n" \
A_"s60,s60,73728\n" \
L_"v228,s[48:51],s60 offen nt lds\n" \
S_"vmcnt(6)\n" \
T_"a[220:221],v250 offset:0\n" \
M_"v[132:135],a[208:211],a[148:151],v[132:135]\n" \
L_"v229,s[48:51],s60 offen offset:1024 nt lds\n" \
L_"v230,s[48:51],s60 offen offset:2048 nt lds\n" \
T_"a[222:223],v250 offset:512\n" \
M_"v[136:139],a[212:215],a[148:151],v[136:139]\n" \
L_"v231,s[48:51],s60 offen offset:3072 nt lds\n" \
T_"a[224:225],v250 offset:8\n" \
M_"v[140:143],a[216:219],a[148:151],v[140:143]\n" \
R_"a[148:151],v225 offset:1024\n" \
M_"v[144:147],a[204:207],a[152:155],v[144:147]\n" \
T_"a[226:227],v250 offset:520\n" \
M_"v[148:151],a[208:211],a[152:155],v[148:151]\n" \
T_"a[228:229],v251 offset:0\n" \
M_"v[152:155],a[212:215],a[152:155],v[152:155]\n" \
T_"a[230:231],v251 offset:512\n" \
M_"v[156:159],a[216:219],a[152:155],v[156:159]\n" \
R_"a[152:155],v225 offset:2048\n" \
M_"v[160:163],a[204:207],a[156:159],v[160:163]\n" \
T_"a[232:233],v251 offset:8\n" \
M_"v[164:167],a[208:211],a[156:159],v[164:167]\n" \
T_"a[234:235],v251 offset:520\n" \
M_"v[168:171],a[212:215],a[156:159],v[168:171]\n" \
M_"v[172:175],a[216:219],a[156:159],v[172:175]\n" \
R_"a[156:159],v225 offset:3072\n" \
S_"lgkmcnt(0)\n" \
"LT26_%=:\n" \
"s_memrealtime s[18:19]\n" \
S_"lgkmcnt(0)\n" \
"s_sub_u32 s18,s18,s20\n" \
"s_cmp_ge_u32 s18,517\n" \
"s_cbranch_scc1 LT26d_%=\n" \
"s_sleep 1\n" \
"s_branch LT26_%=\n" \
"LT26d_%=:\n" \
M_"v[176:179],a[204:207],a[160:163],v[176:179]\n" \
M_"v[180:183],a[208:211],a[160:163],v[180:183]\n" \
M_"v[184:187],a[212:215],a[160:163],v[184:187]\n" \
M_"v[188:191],a[216:219],a[160:163],v[188:191]\n" \
R_"a[160:163],v225 offset:4096\n" \
M_"v[192:195],a[204:207],a[164:167],v[192:195]\n" \
M_"v[196:199],a[208:211],a[164:167],v[196:199]\n" \
M_"v[200:203],a[212:215],a[164:167],v[200:203]\n" \
M_"v[204:207],a[216:219],a[164:167],v[204:207]\n" \
R_"a[164:167],v225 offset:5120\n" \
M_"v[208:211],a[204:207],a[168:171],v[208:211]\n" \
M_"v[212:215],a[208:211],a[168:171],v[212:215]\n" \
M_"v[216:219],a[212:215],a[168:171],v[216:219]\n" \
M_"v[220:223],a[216:219],a[168:171],v[220:223]\n" \
R_"a[168:171],v225 offset:6144\n" \
M_"a[0:3],a[220:223],a[144:147],a[0:3]\n" \
A_"m0,s74,0\n" \
A_"s60,s65,s68\n" \
A_"s60,s60,110592\n" \
M_"a[4:7],a[224:227],a[144:147],a[4:7]\n" \
L_"v228,s[48:51],s60 offen nt lds\n" \
S_"lgkmcnt(0)\n" \
"LT27_%=:\n" \
"s_memrealtime s[18:19]\n" \
S_"lgkmcnt(0)\n" \
"s_sub_u32 s18,s18,s20\n" \
"s_cmp_ge_u32 s18,540\n" \
"s_cbranch_scc1 LT27d_%=\n" \
"s_sleep 1\n" \
"s_branch LT27_%=\n" \
"LT27d_%=:\n" \
M_"a[8:11],a[228:231],a[144:147],a[8:11]\n" \
L_"v229,s[48:51],s60 offen offset:1024 nt lds\n" \
M_"a[12:15],a[232:235],a[144:147],a[12:15]\n" \
R_"a[144:147],v225 offset:7168\n" \
M_"a[16:19],a[220:223],a[148:151],a[16:19]\n" \
L_"v230,s[48:51],s60 offen offset:2048 nt lds\n" \
S_"vmcnt(7)\n" \
T_"a[236:237],v250 offset:4096\n" \
M_"a[20:23],a[224:227],a[148:151],a[20:23]\n" \
L_"v231,s[48:51],s60 offen offset:3072 nt lds\n" \
T_"a[238:239],v250 offset:4608\n" \
M_"a[24:27],a[228:231],a[148:151],a[24:27]\n" \
T_"a[240:241],v250 offset:4104\n" \
M_"a[28:31],a[232:235],a[148:151],a[28:31]\n" \
R_"a[148:151],v225 offset:8192\n" \
M_"a[32:35],a[220:223],a[152:155],a[32:35]\n" \
T_"a[242:243],v250 offset:4616\n" \
M_"a[36:39],a[224:227],a[152:155],a[36:39]\n" \
T_"a[244:245],v251 offset:4096\n" \
M_"a[40:43],a[228:231],a[152:155],a[40:43]\n" \
T_"a[246:247],v251 offset:4608\n" \
M_"a[44:47],a[232:235],a[152:155],a[44:47]\n" \
R_"a[152:155],v225 offset:9216\n" \
M_"a[48:51],a[220:223],a[156:159],a[48:51]\n" \
T_"a[248:249],v251 offset:4104\n" \
M_"a[52:55],a[224:227],a[156:159],a[52:55]\n" \
T_"a[250:251],v251 offset:4616\n" \
M_"a[56:59],a[228:231],a[156:159],a[56:59]\n" \
"buffer_store_dword v235,v234,s[84:87],0 offen sc1\n" \
M_"a[60:63],a[232:235],a[156:159],a[60:63]\n" \
R_"a[156:159],v225 offset:10240\n" \
S_"lgkmcnt(0)\n" \
"LT28_%=:\n" \
"s_memrealtime s[18:19]\n" \
S_"lgkmcnt(0)\n" \
"s_sub_u32 s18,s18,s20\n" \
"s_cmp_ge_u32 s18,562\n" \
"s_cbranch_scc1 LT28d_%=\n" \
"s_sleep 1\n" \
"s_branch LT28_%=\n" \
"LT28d_%=:\n" \
M_"a[64:67],a[220:223],a[160:163],a[64:67]\n" \
M_"a[68:71],a[224:227],a[160:163],a[68:71]\n" \
M_"a[72:75],a[228:231],a[160:163],a[72:75]\n" \
M_"a[76:79],a[232:235],a[160:163],a[76:79]\n" \
R_"a[160:163],v225 offset:11264\n" \
M_"a[80:83],a[220:223],a[164:167],a[80:83]\n" \
M_"a[84:87],a[224:227],a[164:167],a[84:87]\n" \
M_"a[88:91],a[228:231],a[164:167],a[88:91]\n" \
M_"a[92:95],a[232:235],a[164:167],a[92:95]\n" \
R_"a[164:167],v225 offset:12288\n" \
M_"a[96:99],a[220:223],a[168:171],a[96:99]\n" \
M_"a[100:103],a[224:227],a[168:171],a[100:103]\n" \
M_"a[104:107],a[228:231],a[168:171],a[104:107]\n" \
M_"a[108:111],a[232:235],a[168:171],a[108:111]\n" \
R_"a[168:171],v225 offset:13312\n" \
M_"a[0:3],a[236:239],a[144:147],a[0:3]\n" \
A_"m0,s74,4096\n" \
A_"s60,s65,s69\n" \
A_"s60,s60,0\n" \
M_"a[4:7],a[240:243],a[144:147],a[4:7]\n" \
L_"v228,s[48:51],s60 offen nt lds\n" \
S_"lgkmcnt(0)\n" \
"LT29_%=:\n" \
"s_memrealtime s[18:19]\n" \
S_"lgkmcnt(0)\n" \
"s_sub_u32 s18,s18,s20\n" \
"s_cmp_ge_u32 s18,585\n" \
"s_cbranch_scc1 LT29d_%=\n" \
"s_sleep 1\n" \
"s_branch LT29_%=\n" \
"LT29d_%=:\n" \
M_"a[8:11],a[244:247],a[144:147],a[8:11]\n" \
L_"v229,s[48:51],s60 offen offset:1024 nt lds\n" \
M_"a[12:15],a[248:251],a[144:147],a[12:15]\n" \
R_"a[144:147],v225 offset:14336\n" \
M_"a[16:19],a[236:239],a[148:151],a[16:19]\n" \
L_"v230,s[48:51],s60 offen offset:2048 nt lds\n" \
S_"vmcnt(8)\n" \
T_"a[112:113],v250 offset:8192\n" \
M_"a[20:23],a[240:243],a[148:151],a[20:23]\n" \
L_"v231,s[48:51],s60 offen offset:3072 nt lds\n" \
T_"a[114:115],v250 offset:8704\n" \
M_"a[24:27],a[244:247],a[148:151],a[24:27]\n" \
T_"a[116:117],v250 offset:8200\n" \
M_"a[28:31],a[248:251],a[148:151],a[28:31]\n" \
R_"a[148:151],v225 offset:15360\n" \
M_"a[32:35],a[236:239],a[152:155],a[32:35]\n" \
T_"a[118:119],v250 offset:8712\n" \
M_"a[36:39],a[240:243],a[152:155],a[36:39]\n" \
T_"a[120:121],v251 offset:8192\n" \
M_"a[40:43],a[244:247],a[152:155],a[40:43]\n" \
T_"a[122:123],v251 offset:8704\n" \
M_"a[44:47],a[248:251],a[152:155],a[44:47]\n" \
R_"a[152:155],v225 offset:16384\n" \
M_"a[48:51],a[236:239],a[156:159],a[48:51]\n" \
T_"a[124:125],v251 offset:8200\n" \
M_"a[52:55],a[240:243],a[156:159],a[52:55]\n" \
T_"a[126:127],v251 offset:8712\n" \
M_"a[56:59],a[244:247],a[156:159],a[56:59]\n" \
M_"a[60:63],a[248:251],a[156:159],a[60:63]\n" \
R_"a[156:159],v225 offset:17408\n" \
S_"lgkmcnt(0)\n" \
"LT30_%=:\n" \
"s_memrealtime s[18:19]\n" \
S_"lgkmcnt(0)\n" \
"s_sub_u32 s18,s18,s20\n" \
"s_cmp_ge_u32 s18,607\n" \
"s_cbranch_scc1 LT30d_%=\n" \
"s_sleep 1\n" \
"s_branch LT30_%=\n" \
"LT30d_%=:\n" \
M_"a[64:67],a[236:239],a[160:163],a[64:67]\n" \
M_"a[68:71],a[240:243],a[160:163],a[68:71]\n" \
M_"a[72:75],a[244:247],a[160:163],a[72:75]\n" \
M_"a[76:79],a[248:251],a[160:163],a[76:79]\n" \
R_"a[160:163],v225 offset:18432\n" \
M_"a[80:83],a[236:239],a[164:167],a[80:83]\n" \
M_"a[84:87],a[240:243],a[164:167],a[84:87]\n" \
M_"a[88:91],a[244:247],a[164:167],a[88:91]\n" \
M_"a[92:95],a[248:251],a[164:167],a[92:95]\n" \
R_"a[164:167],v225 offset:19456\n" \
M_"a[96:99],a[236:239],a[168:171],a[96:99]\n" \
M_"a[100:103],a[240:243],a[168:171],a[100:103]\n" \
M_"a[104:107],a[244:247],a[168:171],a[104:107]\n" \
M_"a[108:111],a[248:251],a[168:171],a[108:111]\n" \
R_"a[168:171],v225 offset:20480\n" \
M_"a[0:3],a[112:115],a[144:147],a[0:3]\n" \
A_"m0,s74,8192\n" \
A_"s60,s65,s69\n" \
A_"s60,s60,36864\n" \
M_"a[4:7],a[116:119],a[144:147],a[4:7]\n" \
L_"v228,s[48:51],s60 offen nt lds\n" \
S_"lgkmcnt(0)\n" \
"LT31_%=:\n" \
"s_memrealtime s[18:19]\n" \
S_"lgkmcnt(0)\n" \
"s_sub_u32 s18,s18,s20\n" \
"s_cmp_ge_u32 s18,630\n" \
"s_cbranch_scc1 LT31d_%=\n" \
"s_sleep 1\n" \
"s_branch LT31_%=\n" \
"LT31d_%=:\n" \
M_"a[8:11],a[120:123],a[144:147],a[8:11]\n" \
L_"v229,s[48:51],s60 offen offset:1024 nt lds\n" \
M_"a[12:15],a[124:127],a[144:147],a[12:15]\n" \
R_"a[144:147],v225 offset:21504\n" \
M_"a[16:19],a[112:115],a[148:151],a[16:19]\n" \
L_"v230,s[48:51],s60 offen offset:2048 nt lds\n" \
S_"vmcnt(8)\n" \
T_"a[128:129],v250 offset:0\n" \
M_"a[20:23],a[116:119],a[148:151],a[20:23]\n" \
L_"v231,s[48:51],s60 offen offset:3072 nt lds\n" \
T_"a[130:131],v250 offset:512\n" \
M_"a[24:27],a[120:123],a[148:151],a[24:27]\n" \
T_"a[132:133],v250 offset:8\n" \
M_"a[28:31],a[124:127],a[148:151],a[28:31]\n" \
R_"a[148:151],v225 offset:22528\n" \
M_"a[32:35],a[112:115],a[152:155],a[32:35]\n" \
T_"a[134:135],v250 offset:520\n" \
M_"a[36:39],a[116:119],a[152:155],a[36:39]\n" \
T_"a[136:137],v251 offset:0\n" \
M_"a[40:43],a[120:123],a[152:155],a[40:43]\n" \
T_"a[138:139],v251 offset:512\n" \
M_"a[44:47],a[124:127],a[152:155],a[44:47]\n" \
R_"a[152:155],v225 offset:23552\n" \
M_"a[48:51],a[112:115],a[156:159],a[48:51]\n" \
T_"a[140:141],v251 offset:8\n" \
M_"a[52:55],a[116:119],a[156:159],a[52:55]\n" \
T_"a[142:143],v251 offset:520\n" \
M_"a[56:59],a[120:123],a[156:159],a[56:59]\n" \
M_"a[60:63],a[124:127],a[156:159],a[60:63]\n" \
R_"a[156:159],v225 offset:24576\n" \
S_"lgkmcnt(0)\n" \
"LT32_%=:\n" \
"s_memrealtime s[18:19]\n" \
S_"lgkmcnt(0)\n" \
"s_sub_u32 s18,s18,s20\n" \
"s_cmp_ge_u32 s18,652\n" \
"s_cbranch_scc1 LT32d_%=\n" \
"s_sleep 1\n" \
"s_branch LT32_%=\n" \
"LT32d_%=:\n" \
M_"a[64:67],a[112:115],a[160:163],a[64:67]\n" \
M_"a[68:71],a[116:119],a[160:163],a[68:71]\n" \
M_"a[72:75],a[120:123],a[160:163],a[72:75]\n" \
M_"a[76:79],a[124:127],a[160:163],a[76:79]\n" \
R_"a[160:163],v225 offset:25600\n" \
M_"a[80:83],a[112:115],a[164:167],a[80:83]\n" \
M_"a[84:87],a[116:119],a[164:167],a[84:87]\n" \
M_"a[88:91],a[120:123],a[164:167],a[88:91]\n" \
M_"a[92:95],a[124:127],a[164:167],a[92:95]\n" \
R_"a[164:167],v225 offset:26624\n" \
M_"a[96:99],a[112:115],a[168:171],a[96:99]\n" \
M_"a[100:103],a[116:119],a[168:171],a[100:103]\n" \
M_"a[104:107],a[120:123],a[168:171],a[104:107]\n" \
M_"a[108:111],a[124:127],a[168:171],a[108:111]\n" \
R_"a[168:171],v225 offset:27648\n" \
M_"a[0:3],a[128:131],a[144:147],a[0:3]\n" \
A_"m0,s74,0\n" \
A_"s60,s65,s69\n" \
A_"s60,s60,73728\n" \
M_"a[4:7],a[132:135],a[144:147],a[4:7]\n" \
L_"v228,s[48:51],s60 offen nt lds\n" \
S_"lgkmcnt(0)\n" \
"LT33_%=:\n" \
"s_memrealtime s[18:19]\n" \
S_"lgkmcnt(0)\n" \
"s_sub_u32 s18,s18,s20\n" \
"s_cmp_ge_u32 s18,675\n" \
"s_cbranch_scc1 LT33d_%=\n" \
"s_sleep 1\n" \
"s_branch LT33_%=\n" \
"LT33d_%=:\n" \
M_"a[8:11],a[136:139],a[144:147],a[8:11]\n" \
L_"v229,s[48:51],s60 offen offset:1024 nt lds\n" \
M_"a[12:15],a[140:143],a[144:147],a[12:15]\n" \
R_"a[144:147],v225 offset:0\n" \
M_"a[16:19],a[128:131],a[148:151],a[16:19]\n" \
L_"v230,s[48:51],s60 offen offset:2048 nt lds\n" \
S_"vmcnt(7)\n" \
T_"a[172:173],v250 offset:4096\n" \
M_"a[20:23],a[132:135],a[148:151],a[20:23]\n" \
L_"v231,s[48:51],s60 offen offset:3072 nt lds\n" \
T_"a[174:175],v250 offset:4608\n" \
M_"a[24:27],a[136:139],a[148:151],a[24:27]\n" \
T_"a[176:177],v250 offset:4104\n" \
M_"a[28:31],a[140:143],a[148:151],a[28:31]\n" \
R_"a[148:151],v225 offset:1024\n" \
M_"a[32:35],a[128:131],a[152:155],a[32:35]\n" \
T_"a[178:179],v250 offset:4616\n" \
M_"a[36:39],a[132:135],a[152:155],a[36:39]\n" \
T_"a[180:181],v251 offset:4096\n" \
M_"a[40:43],a[136:139],a[152:155],a[40:43]\n" \
T_"a[182:183],v251 offset:4608\n" \
M_"a[44:47],a[140:143],a[152:155],a[44:47]\n" \
R_"a[152:155],v225 offset:2048\n" \
M_"a[48:51],a[128:131],a[156:159],a[48:51]\n" \
T_"a[184:185],v251 offset:4104\n" \
M_"a[52:55],a[132:135],a[156:159],a[52:55]\n" \
T_"a[186:187],v251 offset:4616\n" \
M_"a[56:59],a[136:139],a[156:159],a[56:59]\n" \
M_"a[60:63],a[140:143],a[156:159],a[60:63]\n" \
R_"a[156:159],v225 offset:3072\n" \
S_"lgkmcnt(0)\n" \
"LT34_%=:\n" \
"s_memrealtime s[18:19]\n" \
S_"lgkmcnt(0)\n" \
"s_sub_u32 s18,s18,s20\n" \
"s_cmp_ge_u32 s18,697\n" \
"s_cbranch_scc1 LT34d_%=\n" \
"s_sleep 1\n" \
"s_branch LT34_%=\n" \
"LT34d_%=:\n" \
M_"a[64:67],a[128:131],a[160:163],a[64:67]\n" \
M_"a[68:71],a[132:135],a[160:163],a[68:71]\n" \
M_"a[72:75],a[136:139],a[160:163],a[72:75]\n" \
M_"a[76:79],a[140:143],a[160:163],a[76:79]\n" \
R_"a[160:163],v225 offset:4096\n" \
M_"a[80:83],a[128:131],a[164:167],a[80:83]\n" \
M_"a[84:87],a[132:135],a[164:167],a[84:87]\n" \
M_"a[88:91],a[136:139],a[164:167],a[88:91]\n" \
M_"a[92:95],a[140:143],a[164:167],a[92:95]\n" \
R_"a[164:167],v225 offset:5120\n" \
M_"a[96:99],a[128:131],a[168:171],a[96:99]\n" \
M_"a[100:103],a[132:135],a[168:171],a[100:103]\n" \
M_"a[104:107],a[136:139],a[168:171],a[104:107]\n" \
M_"a[108:111],a[140:143],a[168:171],a[108:111]\n" \
R_"a[168:171],v225 offset:6144\n" \
M_"v[0:3],a[172:175],a[144:147],v[0:3]\n" \
A_"m0,s74,4096\n" \
A_"s60,s65,s69\n" \
A_"s60,s60,110592\n" \
M_"v[4:7],a[176:179],a[144:147],v[4:7]\n" \
L_"v228,s[48:51],s60 offen nt lds\n" \
S_"lgkmcnt(0)\n" \
"LT35_%=:\n" \
"s_memrealtime s[18:19]\n" \
S_"lgkmcnt(0)\n" \
"s_sub_u32 s18,s18,s20\n" \
"s_cmp_ge_u32 s18,720\n" \
"s_cbranch_scc1 LT35d_%=\n" \
"s_sleep 1\n" \
"s_branch LT35_%=\n" \
"LT35d_%=:\n" \
M_"v[8:11],a[180:183],a[144:147],v[8:11]\n" \
L_"v229,s[48:51],s60 offen offset:1024 nt lds\n" \
M_"v[12:15],a[184:187],a[144:147],v[12:15]\n" \
R_"a[144:147],v225 offset:7168\n" \
M_"v[16:19],a[172:175],a[148:151],v[16:19]\n" \
L_"v230,s[48:51],s60 offen offset:2048 nt lds\n" \
S_"vmcnt(7)\n" \
T_"a[188:189],v250 offset:8192\n" \
M_"v[20:23],a[176:179],a[148:151],v[20:23]\n" \
L_"v231,s[48:51],s60 offen offset:3072 nt lds\n" \
T_"a[190:191],v250 offset:8704\n" \
M_"v[24:27],a[180:183],a[148:151],v[24:27]\n" \
T_"a[192:193],v250 offset:8200\n" \
M_"v[28:31],a[184:187],a[148:151],v[28:31]\n" \
R_"a[148:151],v225 offset:8192\n" \
M_"v[32:35],a[172:175],a[152:155],v[32:35]\n" \
T_"a[194:195],v250 offset:8712\n" \
M_"v[36:39],a[176:179],a[152:155],v[36:39]\n" \
T_"a[196:197],v251 offset:8192\n" \
M_"v[40:43],a[180:183],a[152:155],v[40:43]\n" \
T_"a[198:199],v251 offset:8704\n" \
M_"v[44:47],a[184:187],a[152:155],v[44:47]\n" \
R_"a[152:155],v225 offset:9216\n" \
M_"v[48:51],a[172:175],a[156:159],v[48:51]\n" \
T_"a[200:201],v251 offset:8200\n" \
M_"v[52:55],a[176:179],a[156:159],v[52:55]\n" \
T_"a[202:203],v251 offset:8712\n" \
M_"v[56:59],a[180:183],a[156:159],v[56:59]\n" \
M_"v[60:63],a[184:187],a[156:159],v[60:63]\n" \
R_"a[156:159],v225 offset:10240\n" \
S_"lgkmcnt(0)\n" \
"LT36_%=:\n" \
"s_memrealtime s[18:19]\n" \
S_"lgkmcnt(0)\n" \
"s_sub_u32 s18,s18,s20\n" \
"s_cmp_ge_u32 s18,742\n" \
"s_cbranch_scc1 LT36d_%=\n" \
"s_sleep 1\n" \
"s_branch LT36_%=\n" \
"LT36d_%=:\n" \
M_"v[64:67],a[172:175],a[160:163],v[64:67]\n" \
M_"v[68:71],a[176:179],a[160:163],v[68:71]\n" \
M_"v[72:75],a[180:183],a[160:163],v[72:75]\n" \
M_"v[76:79],a[184:187],a[160:163],v[76:79]\n" \
R_"a[160:163],v225 offset:11264\n" \
M_"v[80:83],a[172:175],a[164:167],v[80:83]\n" \
M_"v[84:87],a[176:179],a[164:167],v[84:87]\n" \
M_"v[88:91],a[180:183],a[164:167],v[88:91]\n" \
M_"v[92:95],a[184:187],a[164:167],v[92:95]\n" \
R_"a[164:167],v225 offset:12288\n" \
M_"v[96:99],a[172:175],a[168:171],v[96:99]\n" \
M_"v[100:103],a[176:179],a[168:171],v[100:103]\n" \
M_"v[104:107],a[180:183],a[168:171],v[104:107]\n" \
M_"v[108:111],a[184:187],a[168:171],v[108:111]\n" \
R_"a[168:171],v225 offset:13312\n" \
M_"v[0:3],a[188:191],a[144:147],v[0:3]\n" \
A_"m0,s74,8192\n" \
A_"s60,s65,s70\n" \
A_"s60,s60,0\n" \
M_"v[4:7],a[192:195],a[144:147],v[4:7]\n" \
L_"v228,s[48:51],s60 offen nt lds\n" \
S_"lgkmcnt(0)\n" \
"LT37_%=:\n" \
"s_memrealtime s[18:19]\n" \
S_"lgkmcnt(0)\n" \
"s_sub_u32 s18,s18,s20\n" \
"s_cmp_ge_u32 s18,765\n" \
"s_cbranch_scc1 LT37d_%=\n" \
"s_sleep 1\n" \
"s_branch LT37_%=\n" \
"LT37d_%=:\n" \
M_"v[8:11],a[196:199],a[144:147],v[8:11]\n" \
L_"v229,s[48:51],s60 offen offset:1024 nt lds\n" \
M_"v[12:15],a[200:203],a[144:147],v[12:15]\n" \
R_"a[144:147],v225 offset:14336\n" \
M_"v[16:19],a[188:191],a[148:151],v[16:19]\n" \
L_"v230,s[48:51],s60 offen offset:2048 nt lds\n" \
S_"vmcnt(7)\n" \
T_"a[204:205],v250 offset:0\n" \
M_"v[20:23],a[192:195],a[148:151],v[20:23]\n" \
L_"v231,s[48:51],s60 offen offset:3072 nt lds\n" \
T_"a[206:207],v250 offset:512\n" \
M_"v[24:27],a[196:199],a[148:151],v[24:27]\n" \
T_"a[208:209],v250 offset:8\n" \
M_"v[28:31],a[200:203],a[148:151],v[28:31]\n" \
R_"a[148:151],v225 offset:15360\n" \
M_"v[32:35],a[188:191],a[152:155],v[32:35]\n" \
T_"a[210:211],v250 offset:520\n" \
M_"v[36:39],a[192:195],a[152:155],v[36:39]\n" \
T_"a[212:213],v251 offset:0\n" \
M_"v[40:43],a[196:199],a[152:155],v[40:43]\n" \
T_"a[214:215],v251 offset:512\n" \
M_"v[44:47],a[200:203],a[152:155],v[44:47]\n" \
R_"a[152:155],v225 offset:16384\n" \
M_"v[48:51],a[188:191],a[156:159],v[48:51]\n" \
T_"a[216:217],v251 offset:8\n" \
M_"v[52:55],a[192:195],a[156:159],v[52:55]\n" \
T_"a[218:219],v251 offset:520\n" \
M_"v[56:59],a[196:199],a[156:159],v[56:59]\n" \
M_"v[60:63],a[200:203],a[156:159],v[60:63]\n" \
R_"a[156:159],v225 offset:17408\n" \
S_"lgkmcnt(0)\n" \
"LT38_%=:\n" \
"s_memrealtime s[18:19]\n" \
S_"lgkmcnt(0)\n" \
"s_sub_u32 s18,s18,s20\n" \
"s_cmp_ge_u32 s18,787\n" \
"s_cbranch_scc1 LT38d_%=\n" \
"s_sleep 1\n" \
"s_branch LT38_%=\n" \
"LT38d_%=:\n" \
M_"v[64:67],a[188:191],a[160:163],v[64:67]\n" \
M_"v[68:71],a[192:195],a[160:163],v[68:71]\n" \
M_"v[72:75],a[196:199],a[160:163],v[72:75]\n" \
M_"v[76:79],a[200:203],a[160:163],v[76:79]\n" \
R_"a[160:163],v225 offset:18432\n" \
M_"v[80:83],a[188:191],a[164:167],v[80:83]\n" \
M_"v[84:87],a[192:195],a[164:167],v[84:87]\n" \
M_"v[88:91],a[196:199],a[164:167],v[88:91]\n" \
M_"v[92:95],a[200:203],a[164:167],v[92:95]\n" \
R_"a[164:167],v225 offset:19456\n" \
M_"v[96:99],a[188:191],a[168:171],v[96:99]\n" \
M_"v[100:103],a[192:195],a[168:171],v[100:103]\n" \
M_"v[104:107],a[196:199],a[168:171],v[104:107]\n" \
M_"v[108:111],a[200:203],a[168:171],v[108:111]\n" \
R_"a[168:171],v225 offset:20480\n" \
M_"v[0:3],a[204:207],a[144:147],v[0:3]\n" \
A_"m0,s74,0\n" \
A_"s60,s65,s70\n" \
A_"s60,s60,36864\n" \
M_"v[4:7],a[208:211],a[144:147],v[4:7]\n" \
L_"v228,s[48:51],s60 offen nt lds\n" \
S_"lgkmcnt(0)\n" \
"LT39_%=:\n" \
"s_memrealtime s[18:19]\n" \
S_"lgkmcnt(0)\n" \
"s_sub_u32 s18,s18,s20\n" \
"s_cmp_ge_u32 s18,810\n" \
"s_cbranch_scc1 LT39d_%=\n" \
"s_sleep 1\n" \
"s_branch LT39_%=\n" \
"LT39d_%=:\n" \
M_"v[8:11],a[212:215],a[144:147],v[8:11]\n" \
L_"v229,s[48:51],s60 offen offset:1024 nt lds\n" \
M_"v[12:15],a[216:219],a[144:147],v[12:15]\n" \
R_"a[144:147],v225 offset:21504\n" \
M_"v[16:19],a[204:207],a[148:151],v[16:19]\n" \
L_"v230,s[48:51],s60 offen offset:2048 nt lds\n" \
S_"vmcnt(7)\n" \
T_"a[220:221],v250 offset:4096\n" \
M_"v[20:23],a[208:211],a[148:151],v[20:23]\n" \
L_"v231,s[48:51],s60 offen offset:3072 nt lds\n" \
T_"a[222:223],v250 offset:4608\n" \
M_"v[24:27],a[212:215],a[148:151],v[24:27]\n" \
T_"a[224:225],v250 offset:4104\n" \
M_"v[28:31],a[216:219],a[148:151],v[28:31]\n" \
R_"a[148:151],v225 offset:22528\n" \
M_"v[32:35],a[204:207],a[152:155],v[32:35]\n" \
T_"a[226:227],v250 offset:4616\n" \
M_"v[36:39],a[208:211],a[152:155],v[36:39]\n" \
T_"a[228:229],v251 offset:4096\n" \
M_"v[40:43],a[212:215],a[152:155],v[40:43]\n" \
T_"a[230:231],v251 offset:4608\n" \
M_"v[44:47],a[216:219],a[152:155],v[44:47]\n" \
R_"a[152:155],v225 offset:23552\n" \
M_"v[48:51],a[204:207],a[156:159],v[48:51]\n" \
T_"a[232:233],v251 offset:4104\n" \
M_"v[52:55],a[208:211],a[156:159],v[52:55]\n" \
T_"a[234:235],v251 offset:4616\n" \
M_"v[56:59],a[212:215],a[156:159],v[56:59]\n" \
M_"v[60:63],a[216:219],a[156:159],v[60:63]\n" \
R_"a[156:159],v225 offset:24576\n" \
S_"lgkmcnt(0)\n" \
"LT40_%=:\n" \
"s_memrealtime s[18:19]\n" \
S_"lgkmcnt(0)\n" \
"s_sub_u32 s18,s18,s20\n" \
"s_cmp_ge_u32 s18,832\n" \
"s_cbranch_scc1 LT40d_%=\n" \
"s_sleep 1\n" \
"s_branch LT40_%=\n" \
"LT40d_%=:\n" \
M_"v[64:67],a[204:207],a[160:163],v[64:67]\n" \
M_"v[68:71],a[208:211],a[160:163],v[68:71]\n" \
M_"v[72:75],a[212:215],a[160:163],v[72:75]\n" \
M_"v[76:79],a[216:219],a[160:163],v[76:79]\n" \
R_"a[160:163],v225 offset:25600\n" \
M_"v[80:83],a[204:207],a[164:167],v[80:83]\n" \
M_"v[84:87],a[208:211],a[164:167],v[84:87]\n" \
M_"v[88:91],a[212:215],a[164:167],v[88:91]\n" \
M_"v[92:95],a[216:219],a[164:167],v[92:95]\n" \
R_"a[164:167],v225 offset:26624\n" \
M_"v[96:99],a[204:207],a[168:171],v[96:99]\n" \
M_"v[100:103],a[208:211],a[168:171],v[100:103]\n" \
M_"v[104:107],a[212:215],a[168:171],v[104:107]\n" \
M_"v[108:111],a[216:219],a[168:171],v[108:111]\n" \
R_"a[168:171],v225 offset:27648\n" \
M_"v[0:3],a[220:223],a[144:147],v[0:3]\n" \
A_"m0,s74,4096\n" \
A_"s60,s65,s70\n" \
A_"s60,s60,73728\n" \
M_"v[4:7],a[224:227],a[144:147],v[4:7]\n" \
L_"v228,s[48:51],s60 offen nt lds\n" \
S_"lgkmcnt(0)\n" \
"LT41_%=:\n" \
"s_memrealtime s[18:19]\n" \
S_"lgkmcnt(0)\n" \
"s_sub_u32 s18,s18,s20\n" \
"s_cmp_ge_u32 s18,855\n" \
"s_cbranch_scc1 LT41d_%=\n" \
"s_sleep 1\n" \
"s_branch LT41_%=\n" \
"LT41d_%=:\n" \
M_"v[8:11],a[228:231],a[144:147],v[8:11]\n" \
L_"v229,s[48:51],s60 offen offset:1024 nt lds\n" \
M_"v[12:15],a[232:235],a[144:147],v[12:15]\n" \
R_"a[144:147],v225 offset:0\n" \
M_"v[16:19],a[220:223],a[148:151],v[16:19]\n" \
L_"v230,s[48:51],s60 offen offset:2048 nt lds\n" \
S_"vmcnt(7)\n" \
T_"a[236:237],v250 offset:8192\n" \
M_"v[20:23],a[224:227],a[148:151],v[20:23]\n" \
L_"v231,s[48:51],s60 offen offset:3072 nt lds\n" \
T_"a[238:239],v250 offset:8704\n" \
M_"v[24:27],a[228:231],a[148:151],v[24:27]\n" \
T_"a[240:241],v250 offset:8200\n" \
M_"v[28:31],a[232:235],a[148:151],v[28:31]\n" \
R_"a[148:151],v225 offset:1024\n" \
M_"v[32:35],a[220:223],a[152:155],v[32:35]\n" \
T_"a[242:243],v250 offset:8712\n" \
M_"v[36:39],a[224:227],a[152:155],v[36:39]\n" \
T_"a[244:245],v251 offset:8192\n" \
M_"v[40:43],a[228:231],a[152:155],v[40:43]\n" \
T_"a[246:247],v251 offset:8704\n" \
M_"v[44:47],a[232:235],a[152:155],v[44:47]\n" \
R_"a[152:155],v225 offset:2048\n" \
M_"v[48:51],a[220:223],a[156:159],v[48:51]\n" \
T_"a[248:249],v251 offset:8200\n" \
M_"v[52:55],a[224:227],a[156:159],v[52:55]\n" \
T_"a[250:251],v251 offset:8712\n" \
M_"v[56:59],a[228:231],a[156:159],v[56:59]\n" \
M_"v[60:63],a[232:235],a[156:159],v[60:63]\n" \
R_"a[156:159],v225 offset:3072\n" \
S_"lgkmcnt(0)\n" \
"LT42_%=:\n" \
"s_memrealtime s[18:19]\n" \
S_"lgkmcnt(0)\n" \
"s_sub_u32 s18,s18,s20\n" \
"s_cmp_ge_u32 s18,877\n" \
"s_cbranch_scc1 LT42d_%=\n" \
"s_sleep 1\n" \
"s_branch LT42_%=\n" \
"LT42d_%=:\n" \
M_"v[64:67],a[220:223],a[160:163],v[64:67]\n" \
M_"v[68:71],a[224:227],a[160:163],v[68:71]\n" \
M_"v[72:75],a[228:231],a[160:163],v[72:75]\n" \
M_"v[76:79],a[232:235],a[160:163],v[76:79]\n" \
R_"a[160:163],v225 offset:4096\n" \
M_"v[80:83],a[220:223],a[164:167],v[80:83]\n" \
M_"v[84:87],a[224:227],a[164:167],v[84:87]\n" \
M_"v[88:91],a[228:231],a[164:167],v[88:91]\n" \
M_"v[92:95],a[232:235],a[164:167],v[92:95]\n" \
R_"a[164:167],v225 offset:5120\n" \
M_"v[96:99],a[220:223],a[168:171],v[96:99]\n" \
M_"v[100:103],a[224:227],a[168:171],v[100:103]\n" \
M_"v[104:107],a[228:231],a[168:171],v[104:107]\n" \
M_"v[108:111],a[232:235],a[168:171],v[108:111]\n" \
R_"a[168:171],v225 offset:6144\n" \
M_"v[112:115],a[236:239],a[144:147],v[112:115]\n" \
A_"m0,s74,8192\n" \
A_"s60,s65,s70\n" \
A_"s60,s60,110592\n" \
M_"v[116:119],a[240:243],a[144:147],v[116:119]\n" \
L_"v228,s[48:51],s60 offen nt lds\n" \
S_"lgkmcnt(0)\n" \
"LT43_%=:\n" \
"s_memrealtime s[18:19]\n" \
S_"lgkmcnt(0)\n" \
"s_sub_u32 s18,s18,s20\n" \
"s_cmp_ge_u32 s18,900\n" \
"s_cbranch_scc1 LT43d_%=\n" \
"s_sleep 1\n" \
"s_branch LT43_%=\n" \
"LT43d_%=:\n" \
M_"v[120:123],a[244:247],a[144:147],v[120:123]\n" \
L_"v229,s[48:51],s60 offen offset:1024 nt lds\n" \
M_"v[124:127],a[248:251],a[144:147],v[124:127]\n" \
R_"a[144:147],v225 offset:7168\n" \
M_"v[128:131],a[236:239],a[148:151],v[128:131]\n" \
L_"v230,s[48:51],s60 offen offset:2048 nt lds\n" \
S_"vmcnt(7)\n" \
T_"a[112:113],v250 offset:0\n" \
M_"v[132:135],a[240:243],a[148:151],v[132:135]\n" \
L_"v231,s[48:51],s60 offen offset:3072 nt lds\n" \
T_"a[114:115],v250 offset:512\n" \
M_"v[136:139],a[244:247],a[148:151],v[136:139]\n" \
T_"a[116:117],v250 offset:8\n" \
M_"v[140:143],a[248:251],a[148:151],v[140:143]\n" \
R_"a[148:151],v225 offset:8192\n" \
M_"v[144:147],a[236:239],a[152:155],v[144:147]\n" \
T_"a[118:119],v250 offset:520\n" \
M_"v[148:151],a[240:243],a[152:155],v[148:151]\n" \
T_"a[120:121],v251 offset:0\n" \
M_"v[152:155],a[244:247],a[152:155],v[152:155]\n" \
T_"a[122:123],v251 offset:512\n" \
M_"v[156:159],a[248:251],a[152:155],v[156:159]\n" \
R_"a[152:155],v225 offset:9216\n" \
M_"v[160:163],a[236:239],a[156:159],v[160:163]\n" \
T_"a[124:125],v251 offset:8\n" \
M_"v[164:167],a[240:243],a[156:159],v[164:167]\n" \
T_"a[126:127],v251 offset:520\n" \
M_"v[168:171],a[244:247],a[156:159],v[168:171]\n" \
M_"v[172:175],a[248:251],a[156:159],v[172:175]\n" \
R_"a[156:159],v225 offset:10240\n" \
S_"lgkmcnt(0)\n" \
"LT44_%=:\n" \
"s_memrealtime s[18:19]\n" \
S_"lgkmcnt(0)\n" \
"s_sub_u32 s18,s18,s20\n" \
"s_cmp_ge_u32 s18,922\n" \
"s_cbranch_scc1 LT44d_%=\n" \
"s_sleep 1\n" \
"s_branch LT44_%=\n" \
"LT44d_%=:\n" \
M_"v[176:179],a[236:239],a[160:163],v[176:179]\n" \
M_"v[180:183],a[240:243],a[160:163],v[180:183]\n" \
M_"v[184:187],a[244:247],a[160:163],v[184:187]\n" \
M_"v[188:191],a[248:251],a[160:163],v[188:191]\n" \
R_"a[160:163],v225 offset:11264\n" \
M_"v[192:195],a[236:239],a[164:167],v[192:195]\n" \
M_"v[196:199],a[240:243],a[164:167],v[196:199]\n" \
M_"v[200:203],a[244:247],a[164:167],v[200:203]\n" \
M_"v[204:207],a[248:251],a[164:167],v[204:207]\n" \
R_"a[164:167],v225 offset:12288\n" \
M_"v[208:211],a[236:239],a[168:171],v[208:211]\n" \
M_"v[212:215],a[240:243],a[168:171],v[212:215]\n" \
M_"v[216:219],a[244:247],a[168:171],v[216:219]\n" \
M_"v[220:223],a[248:251],a[168:171],v[220:223]\n" \
R_"a[168:171],v225 offset:13312\n" \
M_"v[112:115],a[112:115],a[144:147],v[112:115]\n" \
A_"m0,s74,0\n" \
A_"s60,s66,s68\n" \
A_"s60,s60,0\n" \
M_"v[116:119],a[116:119],a[144:147],v[116:119]\n" \
L_"v228,s[48:51],s60 offen lds\n" \
S_"lgkmcnt(0)\n" \
"LT45_%=:\n" \
"s_memrealtime s[18:19]\n" \
S_"lgkmcnt(0)\n" \
"s_sub_u32 s18,s18,s20\n" \
"s_cmp_ge_u32 s18,945\n" \
"s_cbranch_scc1 LT45d_%=\n" \
"s_sleep 1\n" \
"s_branch LT45_%=\n" \
"LT45d_%=:\n" \
M_"v[120:123],a[120:123],a[144:147],v[120:123]\n" \
L_"v229,s[48:51],s60 offen offset:1024 lds\n" \
M_"v[124:127],a[124:127],a[144:147],v[124:127]\n" \
R_"a[144:147],v225 offset:14336\n" \
M_"v[128:131],a[112:115],a[148:151],v[128:131]\n" \
L_"v230,s[48:51],s60 offen offset:2048 lds\n" \
S_"vmcnt(7)\n" \
T_"a[128:129],v250 offset:4096\n" \
M_"v[132:135],a[116:119],a[148:151],v[132:135]\n" \
L_"v231,s[48:51],s60 offen offset:3072 lds\n" \
T_"a[130:131],v250 offset:4608\n" \
M_"v[136:139],a[120:123],a[148:151],v[136:139]\n" \
T_"a[132:133],v250 offset:4104\n" \
M_"v[140:143],a[124:127],a[148:151],v[140:143]\n" \
R_"a[148:151],v225 offset:15360\n" \
M_"v[144:147],a[112:115],a[152:155],v[144:147]\n" \
T_"a[134:135],v250 offset:4616\n" \
M_"v[148:151],a[116:119],a[152:155],v[148:151]\n" \
T_"a[136:137],v251 offset:4096\n" \
M_"v[152:155],a[120:123],a[152:155],v[152:155]\n" \
T_"a[138:139],v251 offset:4608\n" \
M_"v[156:159],a[124:127],a[152:155],v[156:159]\n" \
R_"a[152:155],v225 offset:16384\n" \
M_"v[160:163],a[112:115],a[156:159],v[160:163]\n" \
T_"a[140:141],v251 offset:4104\n" \
M_"v[164:167],a[116:119],a[156:159],v[164:167]\n" \
T_"a[142:143],v251 offset:4616\n" \
M_"v[168:171],a[120:123],a[156:159],v[168:171]\n" \
M_"v[172:175],a[124:127],a[156:159],v[172:175]\n" \
R_"a[156:159],v225 offset:17408\n" \
S_"lgkmcnt(0)\n" \
"LT46_%=:\n" \
"s_memrealtime s[18:19]\n" \
S_"lgkmcnt(0)\n" \
"s_sub_u32 s18,s18,s20\n" \
"s_cmp_ge_u32 s18,967\n" \
"s_cbranch_scc1 LT46d_%=\n" \
"s_sleep 1\n" \
"s_branch LT46_%=\n" \
"LT46d_%=:\n" \
M_"v[176:179],a[112:115],a[160:163],v[176:179]\n" \
M_"v[180:183],a[116:119],a[160:163],v[180:183]\n" \
M_"v[184:187],a[120:123],a[160:163],v[184:187]\n" \
M_"v[188:191],a[124:127],a[160:163],v[188:191]\n" \
R_"a[160:163],v225 offset:18432\n" \
M_"v[192:195],a[112:115],a[164:167],v[192:195]\n" \
M_"v[196:199],a[116:119],a[164:167],v[196:199]\n" \
M_"v[200:203],a[120:123],a[164:167],v[200:203]\n" \
M_"v[204:207],a[124:127],a[164:167],v[204:207]\n" \
R_"a[164:167],v225 offset:19456\n" \
M_"v[208:211],a[112:115],a[168:171],v[208:211]\n" \
M_"v[212:215],a[116:119],a[168:171],v[212:215]\n" \
M_"v[216:219],a[120:123],a[168:171],v[216:219]\n" \
M_"v[220:223],a[124:127],a[168:171],v[220:223]\n" \
R_"a[168:171],v225 offset:20480\n" \
M_"v[112:115],a[128:131],a[144:147],v[112:115]\n" \
A_"m0,s74,4096\n" \
A_"s60,s66,s68\n" \
A_"s60,s60,36864\n" \
M_"v[116:119],a[132:135],a[144:147],v[116:119]\n" \
L_"v228,s[48:51],s60 offen lds\n" \
S_"lgkmcnt(0)\n" \
"LT47_%=:\n" \
"s_memrealtime s[18:19]\n" \
S_"lgkmcnt(0)\n" \
"s_sub_u32 s18,s18,s20\n" \
"s_cmp_ge_u32 s18,990\n" \
"s_cbranch_scc1 LT47d_%=\n" \
"s_sleep 1\n" \
"s_branch LT47_%=\n" \
"LT47d_%=:\n" \
M_"v[120:123],a[136:139],a[144:147],v[120:123]\n" \
L_"v229,s[48:51],s60 offen offset:1024 lds\n" \
M_"v[124:127],a[140:143],a[144:147],v[124:127]\n" \
R_"a[144:147],v225 offset:21504\n" \
M_"v[128:131],a[128:131],a[148:151],v[128:131]\n" \
L_"v230,s[48:51],s60 offen offset:2048 lds\n" \
S_"vmcnt(7)\n" \
T_"a[172:173],v250 offset:8192\n" \
M_"v[132:135],a[132:135],a[148:151],v[132:135]\n" \
L_"v231,s[48:51],s60 offen offset:3072 lds\n" \
T_"a[174:175],v250 offset:8704\n" \
M_"v[136:139],a[136:139],a[148:151],v[136:139]\n" \
T_"a[176:177],v250 offset:8200\n" \
M_"v[140:143],a[140:143],a[148:151],v[140:143]\n" \
R_"a[148:151],v225 offset:22528\n" \
M_"v[144:147],a[128:131],a[152:155],v[144:147]\n" \
T_"a[178:179],v250 offset:8712\n" \
M_"v[148:151],a[132:135],a[152:155],v[148:151]\n" \
T_"a[180:181],v251 offset:8192\n" \
M_"v[152:155],a[136:139],a[152:155],v[152:155]\n" \
T_"a[182:183],v251 offset:8704\n" \
M_"v[156:159],a[140:143],a[152:155],v[156:159]\n" \
R_"a[152:155],v225 offset:23552\n" \
M_"v[160:163],a[128:131],a[156:159],v[160:163]\n" \
T_"a[184:185],v251 offset:8200\n" \
M_"v[164:167],a[132:135],a[156:159],v[164:167]\n" \
T_"a[186:187],v251 offset:8712\n" \
M_"v[168:171],a[136:139],a[156:159],v[168:171]\n" \
M_"v[172:175],a[140:143],a[156:159],v[172:175]\n" \
R_"a[156:159],v225 offset:24576\n" \
S_"lgkmcnt(0)\n" \
"LT48_%=:\n" \
"s_memrealtime s[18:19]\n" \
S_"lgkmcnt(0)\n" \
"s_sub_u32 s18,s18,s20\n" \
"s_cmp_ge_u32 s18,1012\n" \
"s_cbranch_scc1 LT48d_%=\n" \
"s_sleep 1\n" \
"s_branch LT48_%=\n" \
"LT48d_%=:\n" \
M_"v[176:179],a[128:131],a[160:163],v[176:179]\n" \
M_"v[180:183],a[132:135],a[160:163],v[180:183]\n" \
M_"v[184:187],a[136:139],a[160:163],v[184:187]\n" \
M_"v[188:191],a[140:143],a[160:163],v[188:191]\n" \
R_"a[160:163],v225 offset:25600\n" \
M_"v[192:195],a[128:131],a[164:167],v[192:195]\n" \
M_"v[196:199],a[132:135],a[164:167],v[196:199]\n" \
M_"v[200:203],a[136:139],a[164:167],v[200:203]\n" \
M_"v[204:207],a[140:143],a[164:167],v[204:207]\n" \
R_"a[164:167],v225 offset:26624\n" \
M_"v[208:211],a[128:131],a[168:171],v[208:211]\n" \
M_"v[212:215],a[132:135],a[168:171],v[212:215]\n" \
M_"v[216:219],a[136:139],a[168:171],v[216:219]\n" \
M_"v[220:223],a[140:143],a[168:171],v[220:223]\n" \
R_"a[168:171],v225 offset:27648\n" \
M_"v[112:115],a[172:175],a[144:147],v[112:115]\n" \
M_"v[116:119],a[176:179],a[144:147],v[116:119]\n" \
M_"v[120:123],a[180:183],a[144:147],v[120:123]\n" \
"LHS_%=:\n" \
"buffer_load_dword v235,v233,s[84:87],0 offen sc1\n" \
S_"vmcnt(0)\n" \
"buffer_store_dword v235,v234,s[84:87],0 offen sc1\n" \
"v_cmp_ne_u32 vcc,s88,v235\n" \
"s_nop 1\n" \
"s_and_b32 s97,vcc_lo,0xff\n" \
"s_cbranch_scc0 LHSd_%=\n" \
"s_cmp_eq_u32 s96,0\n" \
"s_cbranch_scc1 LHSd_%=\n" \
"s_sub_u32 s96,s96,1\n" \
"s_sleep 4\n" \
"s_branch LHS_%=\n" \
"LHSd_%=:\n" \
"buffer_load_dword v235,v233,s[84:87],0 offen sc1\n" \
"LG1_%=:\n" \
"ds_read_b32 v253,v236 offset:0\n" \
S_"lgkmcnt(0)\n" \
"v_readfirstlane_b32 s63,v253\n" \
"s_nop 0\n" \
"s_cmp_ge_u32 s63,78\n" \
"s_cbranch_scc1 LG1d_%=\n" \
"s_sleep 1\n" \
"s_branch LG1_%=\n" \
"LG1d_%=:\n" \
"s_mov_b32 s97,s30\n" \
L_"a[252:255],v240,s[48:51],s97 offen\n" \
L_"a[252:255],v240,s[48:51],s97 offen offset:1024\n" \
L_"a[252:255],v240,s[48:51],s97 offen offset:2048\n" \
L_"a[252:255],v240,s[48:51],s97 offen offset:3072\n" \
A_"s97,s97,4096\n" \
L_"a[252:255],v240,s[48:51],s97 offen\n" \
L_"a[252:255],v240,s[48:51],s97 offen offset:1024\n" \
L_"a[252:255],v240,s[48:51],s97 offen offset:2048\n" \
L_"a[252:255],v240,s[48:51],s97 offen offset:3072\n" \
A_"s97,s97,4096\n" \
L_"a[252:255],v240,s[48:51],s97 offen\n" \
L_"a[252:255],v240,s[48:51],s97 offen offset:1024\n" \
L_"a[252:255],v240,s[48:51],s97 offen offset:2048\n" \
L_"a[252:255],v240,s[48:51],s97 offen offset:3072\n" \
A_"s97,s97,4096\n" \
L_"a[252:255],v240,s[48:51],s97 offen\n" \
L_"a[252:255],v240,s[48:51],s97 offen offset:1024\n" \
L_"a[252:255],v240,s[48:51],s97 offen offset:2048\n" \
L_"a[252:255],v240,s[48:51],s97 offen offset:3072\n" \
A_"s97,s97,4096\n" \
L_"a[252:255],v240,s[48:51],s97 offen\n" \
L_"a[252:255],v240,s[48:51],s97 offen offset:1024\n" \
A_"m0,s74,8192\n" \
A_"s60,s66,s68\n" \
A_"s60,s60,73728\n" \
L_"v228,s[48:51],s60 offen lds\n" \
L_"v229,s[48:51],s60 offen offset:1024 lds\n" \
L_"v230,s[48:51],s60 offen offset:2048 lds\n" \
L_"v231,s[48:51],s60 offen offset:3072 lds\n" \
T_"a[188:189],v250 offset:0\n" \
T_"a[190:191],v250 offset:512\n" \
T_"a[192:193],v250 offset:8\n" \
T_"a[194:195],v250 offset:520\n" \
T_"a[196:197],v251 offset:0\n" \
T_"a[198:199],v251 offset:512\n" \
T_"a[200:201],v251 offset:8\n" \
T_"a[202:203],v251 offset:520\n" \
A_"m0,s74,0\n" \
A_"s60,s66,s68\n" \
A_"s60,s60,110592\n" \
S_"lgkmcnt(0)\n" \
L_"v228,s[48:51],s60 offen lds\n" \
L_"v229,s[48:51],s60 offen offset:1024 lds\n" \
L_"v230,s[48:51],s60 offen offset:2048 lds\n" \
L_"v231,s[48:51],s60 offen offset:3072 lds\n" \
T_"a[204:205],v250 offset:4096\n" \
T_"a[206:207],v250 offset:4608\n" \
T_"a[208:209],v250 offset:4104\n" \
T_"a[210:211],v250 offset:4616\n" \
T_"a[212:213],v251 offset:4096\n" \
T_"a[214:215],v251 offset:4608\n" \
T_"a[216:217],v251 offset:4104\n" \
T_"a[218:219],v251 offset:4616\n" \
A_"m0,s74,4096\n" \
A_"s60,s67,s68\n" \
A_"s60,s60,0\n" \
S_"lgkmcnt(0)\n" \
L_"v228,s[48:51],s60 offen lds\n" \
L_"v229,s[48:51],s60 offen offset:1024 lds\n" \
L_"v230,s[48:51],s60 offen offset:2048 lds\n" \
L_"v231,s[48:51],s60 offen offset:3072 lds\n" \
S_"vmcnt(8)\n" \
T_"a[220:221],v250 offset:8192\n" \
T_"a[222:223],v250 offset:8704\n" \
T_"a[224:225],v250 offset:8200\n" \
T_"a[226:227],v250 offset:8712\n" \
T_"a[228:229],v251 offset:8192\n" \
T_"a[230:231],v251 offset:8704\n" \
T_"a[232:233],v251 offset:8200\n" \
T_"a[234:235],v251 offset:8712\n" \
A_"m0,s74,8192\n" \
A_"s60,s67,s68\n" \
A_"s60,s60,36864\n" \
S_"lgkmcnt(0)\n" \
L_"v228,s[48:51],s60 offen lds\n" \
L_"v229,s[48:51],s60 offen offset:1024 lds\n" \
L_"v230,s[48:51],s60 offen offset:2048 lds\n" \
L_"v231,s[48:51],s60 offen offset:3072 lds\n" \
S_"vmcnt(8)\n" \
T_"a[236:237],v250 offset:0\n" \
T_"a[238:239],v250 offset:512\n" \
T_"a[240:241],v250 offset:8\n" \
T_"a[242:243],v250 offset:520\n" \
T_"a[244:245],v251 offset:0\n" \
T_"a[246:247],v251 offset:512\n" \
T_"a[248:249],v251 offset:8\n" \
T_"a[250:251],v251 offset:520\n" \
A_"m0,s74,0\n" \
A_"s60,s67,s68\n" \
A_"s60,s60,73728\n" \
S_"lgkmcnt(0)\n" \
L_"v228,s[48:51],s60 offen lds\n" \
L_"v229,s[48:51],s60 offen offset:1024 lds\n" \
L_"v230,s[48:51],s60 offen offset:2048 lds\n" \
L_"v231,s[48:51],s60 offen offset:3072 lds\n" \
S_"vmcnt(8)\n" \
T_"a[112:113],v250 offset:4096\n" \
T_"a[114:115],v250 offset:4608\n" \
T_"a[116:117],v250 offset:4104\n" \
T_"a[118:119],v250 offset:4616\n" \
T_"a[120:121],v251 offset:4096\n" \
T_"a[122:123],v251 offset:4608\n" \
T_"a[124:125],v251 offset:4104\n" \
T_"a[126:127],v251 offset:4616\n" \
A_"m0,s74,4096\n" \
A_"s60,s67,s68\n" \
A_"s60,s60,110592\n" \
S_"lgkmcnt(0)\n" \
L_"v228,s[48:51],s60 offen lds\n" \
L_"v229,s[48:51],s60 offen offset:1024 lds\n" \
L_"v230,s[48:51],s60 offen offset:2048 lds\n" \
L_"v231,s[48:51],s60 offen offset:3072 lds\n" \
S_"vmcnt(8)\n" \
T_"a[128:129],v250 offset:8192\n" \
T_"a[130:131],v250 offset:8704\n" \
T_"a[132:133],v250 offset:8200\n" \
T_"a[134:135],v250 offset:8712\n" \
T_"a[136:137],v251 offset:8192\n" \
T_"a[138:139],v251 offset:8704\n" \
T_"a[140:141],v251 offset:8200\n" \
T_"a[142:143],v251 offset:8712\n" \
A_"m0,s74,8192\n" \
A_"s60,s66,s69\n" \
A_"s60,s60,0\n" \
S_"lgkmcnt(0)\n" \
L_"v228,s[48:51],s60 offen lds\n" \
L_"v229,s[48:51],s60 offen offset:1024 lds\n" \
L_"v230,s[48:51],s60 offen offset:2048 lds\n" \
L_"v231,s[48:51],s60 offen offset:3072 lds\n" \
"buffer_store_dword v235,v234,s[84:87],0 offen sc1\n" \
"s_barrier\n" \
M_"v[124:127],a[184:187],a[144:147],v[124:127]\n" \
R_"a[144:147],v226 offset:0\n" \
M_"v[128:131],a[172:175],a[148:151],v[128:131]\n" \
M_"v[132:135],a[176:179],a[148:151],v[132:135]\n" \
M_"v[136:139],a[180:183],a[148:151],v[136:139]\n" \
M_"v[140:143],a[184:187],a[148:151],v[140:143]\n" \
R_"a[148:151],v226 offset:1024\n" \
M_"v[144:147],a[172:175],a[152:155],v[144:147]\n" \
M_"v[148:151],a[176:179],a[152:155],v[148:151]\n" \
M_"v[152:155],a[180:183],a[152:155],v[152:155]\n" \
M_"v[156:159],a[184:187],a[152:155],v[156:159]\n" \
R_"a[152:155],v226 offset:2048\n" \
M_"v[160:163],a[172:175],a[156:159],v[160:163]\n" \
M_"v[164:167],a[176:179],a[156:159],v[164:167]\n" \
M_"v[168:171],a[180:183],a[156:159],v[168:171]\n" \
M_"v[172:175],a[184:187],a[156:159],v[172:175]\n" \
R_"a[156:159],v226 offset:3072\n" \
M_"v[176:179],a[172:175],a[160:163],v[176:179]\n" \
M_"v[180:183],a[176:179],a[160:163],v[180:183]\n" \
M_"v[184:187],a[180:183],a[160:163],v[184:187]\n" \
M_"v[188:191],a[184:187],a[160:163],v[188:191]\n" \
R_"a[160:163],v226 offset:4096\n" \
M_"v[192:195],a[172:175],a[164:167],v[192:195]\n" \
M_"v[196:199],a[176:179],a[164:167],v[196:199]\n" \
M_"v[200:203],a[180:183],a[164:167],v[200:203]\n" \
M_"v[204:207],a[184:187],a[164:167],v[204:207]\n" \
R_"a[164:167],v226 offset:5120\n" \
M_"v[208:211],a[172:175],a[168:171],v[208:211]\n" \
M_"v[212:215],a[176:179],a[168:171],v[212:215]\n" \
M_"v[216:219],a[180:183],a[168:171],v[216:219]\n" \
M_"v[220:223],a[184:187],a[168:171],v[220:223]\n" \
R_"a[168:171],v226 offset:6144\n" \
S_"lgkmcnt(2)\n" \
M_"a[0:3],a[188:191],a[144:147],a[0:3]\n" \
M_"a[4:7],a[192:195],a[144:147],a[4:7]\n" \
M_"a[8:11],a[196:199],a[144:147],a[8:11]\n" \
M_"a[12:15],a[200:203],a[144:147],a[12:15]\n" \
R_"a[144:147],v226 offset:7168\n" \
M_"a[16:19],a[188:191],a[148:151],a[16:19]\n" \
S_"vmcnt(9)\n" \
T_"a[172:173],v250 offset:0\n" \
M_"a[20:23],a[192:195],a[148:151],a[20:23]\n" \
T_"a[174:175],v250 offset:512\n" \
M_"a[24:27],a[196:199],a[148:151],a[24:27]\n" \
T_"a[176:177],v250 offset:8\n" \
M_"a[28:31],a[200:203],a[148:151],a[28:31]\n" \
R_"a[148:151],v226 offset:8192\n" \
M_"a[32:35],a[188:191],a[152:155],a[32:35]\n" \
T_"a[178:179],v250 offset:520\n" \
M_"a[36:39],a[192:195],a[152:155],a[36:39]\n" \
T_"a[180:181],v251 offset:0\n" \
M_"a[40:43],a[196:199],a[152:155],a[40:43]\n" \
T_"a[182:183],v251 offset:512\n" \
M_"a[44:47],a[200:203],a[152:155],a[44:47]\n" \
R_"a[152:155],v226 offset:9216\n" \
M_"a[48:51],a[188:191],a[156:159],a[48:51]\n" \
T_"a[184:185],v251 offset:8\n" \
M_"a[52:55],a[192:195],a[156:159],a[52:55]\n" \
T_"a[186:187],v251 offset:520\n" \
M_"a[56:59],a[196:199],a[156:159],a[56:59]\n" \
M_"a[60:63],a[200:203],a[156:159],a[60:63]\n" \
R_"a[156:159],v226 offset:10240\n" \
M_"a[64:67],a[188:191],a[160:163],a[64:67]\n" \
M_"a[68:71],a[192:195],a[160:163],a[68:71]\n" \
M_"a[72:75],a[196:199],a[160:163],a[72:75]\n" \
M_"a[76:79],a[200:203],a[160:163],a[76:79]\n" \
R_"a[160:163],v226 offset:11264\n" \
S_"lgkmcnt(2)\n" \
M_"a[80:83],a[188:191],a[164:167],a[80:83]\n" \
M_"a[84:87],a[192:195],a[164:167],a[84:87]\n" \
M_"a[88:91],a[196:199],a[164:167],a[88:91]\n" \
M_"a[92:95],a[200:203],a[164:167],a[92:95]\n" \
R_"a[164:167],v226 offset:12288\n" \
M_"a[96:99],a[188:191],a[168:171],a[96:99]\n" \
M_"a[100:103],a[192:195],a[168:171],a[100:103]\n" \
M_"a[104:107],a[196:199],a[168:171],a[104:107]\n" \
M_"a[108:111],a[200:203],a[168:171],a[108:111]\n" \
R_"a[168:171],v226 offset:13312\n" \
M_"a[0:3],a[204:207],a[144:147],a[0:3]\n" \
A_"m0,s74,0\n" \
A_"s60,s66,s69\n" \
A_"s60,s60,36864\n" \
M_"a[4:7],a[208:211],a[144:147],a[4:7]\n" \
L_"v228,s[48:51],s60 offen lds\n" \
M_"a[8:11],a[212:215],a[144:147],a[8:11]\n" \
L_"v229,s[48:51],s60 offen offset:1024 lds\n" \
M_"a[12:15],a[216:219],a[144:147],a[12:15]\n" \
R_"a[144:147],v226 offset:14336\n" \
M_"a[16:19],a[204:207],a[148:151],a[16:19]\n" \
L_"v230,s[48:51],s60 offen offset:2048 lds\n" \
S_"vmcnt(8)\n" \
T_"a[188:189],v250 offset:4096\n" \
M_"a[20:23],a[208:211],a[148:151],a[20:23]\n" \
L_"v231,s[48:51],s60 offen offset:3072 lds\n" \
T_"a[190:191],v250 offset:4608\n" \
M_"a[24:27],a[212:215],a[148:151],a[24:27]\n" \
T_"a[192:193],v250 offset:4104\n" \
M_"a[28:31],a[216:219],a[148:151],a[28:31]\n" \
R_"a[148:151],v226 offset:15360\n" \
M_"a[32:35],a[204:207],a[152:155],a[32:35]\n" \
T_"a[194:195],v250 offset:4616\n" \
M_"a[36:39],a[208:211],a[152:155],a[36:39]\n" \
T_"a[196:197],v251 offset:4096\n" \
M_"a[40:43],a[212:215],a[152:155],a[40:43]\n" \
T_"a[198:199],v251 offset:4608\n" \
M_"a[44:47],a[216:219],a[152:155],a[44:47]\n" \
R_"a[152:155],v226 offset:16384\n" \
S_"lgkmcnt(6)\n" \
M_"a[48:51],a[204:207],a[156:159],a[48:51]\n" \
T_"a[200:201],v251 offset:4104\n" \
M_"a[52:55],a[208:211],a[156:159],a[52:55]\n" \
T_"a[202:203],v251 offset:4616\n" \
M_"a[56:59],a[212:215],a[156:159],a[56:59]\n" \
M_"a[60:63],a[216:219],a[156:159],a[60:63]\n" \
R_"a[156:159],v226 offset:17408\n" \
M_"a[64:67],a[204:207],a[160:163],a[64:67]\n" \
M_"a[68:71],a[208:211],a[160:163],a[68:71]\n" \
M_"a[72:75],a[212:215],a[160:163],a[72:75]\n" \
M_"a[76:79],a[216:219],a[160:163],a[76:79]\n" \
R_"a[160:163],v226 offset:18432\n" \
M_"a[80:83],a[204:207],a[164:167],a[80:83]\n" \
M_"a[84:87],a[208:211],a[164:167],a[84:87]\n" \
M_"a[88:91],a[212:215],a[164:167],a[88:91]\n" \
M_"a[92:95],a[216:219],a[164:167],a[92:95]\n" \
R_"a[164:167],v226 offset:19456\n" \
M_"a[96:99],a[204:207],a[168:171],a[96:99]\n" \
M_"a[100:103],a[208:211],a[168:171],a[100:103]\n" \
M_"a[104:107],a[212:215],a[168:171],a[104:107]\n" \
M_"a[108:111],a[216:219],a[168:171],a[108:111]\n" \
R_"a[168:171],v226 offset:20480\n" \
M_"a[0:3],a[220:223],a[144:147],a[0:3]\n" \
A_"m0,s74,4096\n" \
A_"s60,s66,s69\n" \
A_"s60,s60,73728\n" \
M_"a[4:7],a[224:227],a[144:147],a[4:7]\n" \
S_"lgkmcnt(2)\n" \
L_"v228,s[48:51],s60 offen lds\n" \
M_"a[8:11],a[228:231],a[144:147],a[8:11]\n" \
L_"v229,s[48:51],s60 offen offset:1024 lds\n" \
M_"a[12:15],a[232:235],a[144:147],a[12:15]\n" \
R_"a[144:147],v226 offset:21504\n" \
M_"a[16:19],a[220:223],a[148:151],a[16:19]\n" \
L_"v230,s[48:51],s60 offen offset:2048 lds\n" \
S_"vmcnt(8)\n" \
T_"a[204:205],v250 offset:8192\n" \
M_"a[20:23],a[224:227],a[148:151],a[20:23]\n" \
L_"v231,s[48:51],s60 offen offset:3072 lds\n" \
T_"a[206:207],v250 offset:8704\n" \
M_"a[24:27],a[228:231],a[148:151],a[24:27]\n" \
T_"a[208:209],v250 offset:8200\n" \
M_"a[28:31],a[232:235],a[148:151],a[28:31]\n" \
R_"a[148:151],v226 offset:22528\n" \
M_"a[32:35],a[220:223],a[152:155],a[32:35]\n" \
T_"a[210:211],v250 offset:8712\n" \
M_"a[36:39],a[224:227],a[152:155],a[36:39]\n" \
T_"a[212:213],v251 offset:8192\n" \
M_"a[40:43],a[228:231],a[152:155],a[40:43]\n" \
T_"a[214:215],v251 offset:8704\n" \
M_"a[44:47],a[232:235],a[152:155],a[44:47]\n" \
R_"a[152:155],v226 offset:23552\n" \
M_"a[48:51],a[220:223],a[156:159],a[48:51]\n" \
T_"a[216:217],v251 offset:8200\n" \
M_"a[52:55],a[224:227],a[156:159],a[52:55]\n" \
T_"a[218:219],v251 offset:8712\n" \
M_"a[56:59],a[228:231],a[156:159],a[56:59]\n" \
M_"a[60:63],a[232:235],a[156:159],a[60:63]\n" \
R_"a[156:159],v226 offset:24576\n" \
M_"a[64:67],a[220:223],a[160:163],a[64:67]\n" \
M_"a[68:71],a[224:227],a[160:163],a[68:71]\n" \
M_"a[72:75],a[228:231],a[160:163],a[72:75]\n" \
M_"a[76:79],a[232:235],a[160:163],a[76:79]\n" \
R_"a[160:163],v226 offset:25600\n" \
S_"lgkmcnt(2)\n" \
M_"a[80:83],a[220:223],a[164:167],a[80:83]\n" \
M_"a[84:87],a[224:227],a[164:167],a[84:87]\n" \
M_"a[88:91],a[228:231],a[164:167],a[88:91]\n" \
M_"a[92:95],a[232:235],a[164:167],a[92:95]\n" \
R_"a[164:167],v226 offset:26624\n" \
M_"a[96:99],a[220:223],a[168:171],a[96:99]\n" \
M_"a[100:103],a[224:227],a[168:171],a[100:103]\n" \
M_"a[104:107],a[228:231],a[168:171],a[104:107]\n" \
M_"a[108:111],a[232:235],a[168:171],a[108:111]\n" \
R_"a[168:171],v226 offset:27648\n" \
M_"a[0:3],a[236:239],a[144:147],a[0:3]\n" \
A_"m0,s74,8192\n" \
A_"s60,s66,s69\n" \
A_"s60,s60,110592\n" \
M_"a[4:7],a[240:243],a[144:147],a[4:7]\n" \
L_"v228,s[48:51],s60 offen lds\n" \
M_"a[8:11],a[244:247],a[144:147],a[8:11]\n" \
L_"v229,s[48:51],s60 offen offset:1024 lds\n" \
M_"a[12:15],a[248:251],a[144:147],a[12:15]\n" \
R_"a[144:147],v227 offset:0\n" \
M_"a[16:19],a[236:239],a[148:151],a[16:19]\n" \
L_"v230,s[48:51],s60 offen offset:2048 lds\n" \
S_"vmcnt(7)\n" \
T_"a[220:221],v250 offset:0\n" \
M_"a[20:23],a[240:243],a[148:151],a[20:23]\n" \
L_"v231,s[48:51],s60 offen offset:3072 lds\n" \
T_"a[222:223],v250 offset:512\n" \
M_"a[24:27],a[244:247],a[148:151],a[24:27]\n" \
T_"a[224:225],v250 offset:8\n" \
M_"a[28:31],a[248:251],a[148:151],a[28:31]\n" \
R_"a[148:151],v227 offset:1024\n" \
M_"a[32:35],a[236:239],a[152:155],a[32:35]\n" \
T_"a[226:227],v250 offset:520\n" \
M_"a[36:39],a[240:243],a[152:155],a[36:39]\n" \
T_"a[228:229],v251 offset:0\n" \
M_"a[40:43],a[244:247],a[152:155],a[40:43]\n" \
T_"a[230:231],v251 offset:512\n" \
M_"a[44:47],a[248:251],a[152:155],a[44:47]\n" \
R_"a[152:155],v227 offset:2048\n" \
S_"lgkmcnt(6)\n" \
M_"a[48:51],a[236:239],a[156:159],a[48:51]\n" \
T_"a[232:233],v251 offset:8\n" \
M_"a[52:55],a[240:243],a[156:159],a[52:55]\n" \
T_"a[234:235],v251 offset:520\n" \
M_"a[56:59],a[244:247],a[156:159],a[56:59]\n" \
M_"a[60:63],a[248:251],a[156:159],a[60:63]\n" \
R_"a[156:159],v227 offset:3072\n" \
M_"a[64:67],a[236:239],a[160:163],a[64:67]\n" \
M_"a[68:71],a[240:243],a[160:163],a[68:71]\n" \
M_"a[72:75],a[244:247],a[160:163],a[72:75]\n" \
M_"a[76:79],a[248:251],a[160:163],a[76:79]\n" \
R_"a[160:163],v227 offset:4096\n" \
M_"a[80:83],a[236:239],a[164:167],a[80:83]\n" \
M_"a[84:87],a[240:243],a[164:167],a[84:87]\n" \
M_"a[88:91],a[244:247],a[164:167],a[88:91]\n" \
M_"a[92:95],a[248:251],a[164:167],a[92:95]\n" \
R_"a[164:167],v227 offset:5120\n" \
M_"a[96:99],a[236:239],a[168:171],a[96:99]\n" \
M_"a[100:103],a[240:243],a[168:171],a[100:103]\n" \
M_"a[104:107],a[244:247],a[168:171],a[104:107]\n" \
M_"a[108:111],a[248:251],a[168:171],a[108:111]\n" \
R_"a[168:171],v227 offset:6144\n" \
M_"a[0:3],a[112:115],a[144:147],a[0:3]\n" \
A_"m0,s74,0\n" \
A_"s60,s67,s69\n" \
A_"s60,s60,0\n" \
M_"a[4:7],a[116:119],a[144:147],a[4:7]\n" \
S_"lgkmcnt(2)\n" \
L_"v228,s[48:51],s60 offen lds\n" \
M_"a[8:11],a[120:123],a[144:147],a[8:11]\n" \
L_"v229,s[48:51],s60 offen offset:1024 lds\n" \
M_"a[12:15],a[124:127],a[144:147],a[12:15]\n" \
R_"a[144:147],v227 offset:7168\n" \
M_"a[16:19],a[112:115],a[148:151],a[16:19]\n" \
L_"v230,s[48:51],s60 offen offset:2048 lds\n" \
S_"vmcnt(7)\n" \
T_"a[236:237],v250 offset:4096\n" \
M_"a[20:23],a[116:119],a[148:151],a[20:23]\n" \
L_"v231,s[48:51],s60 offen offset:3072 lds\n" \
T_"a[238:239],v250 offset:4608\n" \
M_"a[24:27],a[120:123],a[148:151],a[24:27]\n" \
T_"a[240:241],v250 offset:4104\n" \
M_"a[28:31],a[124:127],a[148:151],a[28:31]\n" \
R_"a[148:151],v227 offset:8192\n" \
M_"a[32:35],a[112:115],a[152:155],a[32:35]\n" \
T_"a[242:243],v250 offset:4616\n" \
M_"a[36:39],a[116:119],a[152:155],a[36:39]\n" \
T_"a[244:245],v251 offset:4096\n" \
M_"a[40:43],a[120:123],a[152:155],a[40:43]\n" \
T_"a[246:247],v251 offset:4608\n" \
M_"a[44:47],a[124:127],a[152:155],a[44:47]\n" \
R_"a[152:155],v227 offset:9216\n" \
M_"a[48:51],a[112:115],a[156:159],a[48:51]\n" \
T_"a[248:249],v251 offset:4104\n" \
M_"a[52:55],a[116:119],a[156:159],a[52:55]\n" \
T_"a[250:251],v251 offset:4616\n" \
M_"a[56:59],a[120:123],a[156:159],a[56:59]\n" \
M_"a[60:63],a[124:127],a[156:159],a[60:63]\n" \
R_"a[156:159],v227 offset:10240\n" \
M_"a[64:67],a[112:115],a[160:163],a[64:67]\n" \
M_"a[68:71],a[116:119],a[160:163],a[68:71]\n" \
M_"a[72:75],a[120:123],a[160:163],a[72:75]\n" \
M_"a[76:79],a[124:127],a[160:163],a[76:79]\n" \
R_"a[160:163],v227 offset:11264\n" \
S_"lgkmcnt(2)\n" \
M_"a[80:83],a[112:115],a[164:167],a[80:83]\n" \
M_"a[84:87],a[116:119],a[164:167],a[84:87]\n" \
M_"a[88:91],a[120:123],a[164:167],a[88:91]\n" \
M_"a[92:95],a[124:127],a[164:167],a[92:95]\n" \
R_"a[164:167],v227 offset:12288\n" \
M_"a[96:99],a[112:115],a[168:171],a[96:99]\n" \
M_"a[100:103],a[116:119],a[168:171],a[100:103]\n" \
M_"a[104:107],a[120:123],a[168:171],a[104:107]\n" \
M_"a[108:111],a[124:127],a[168:171],a[108:111]\n" \
R_"a[168:171],v227 offset:13312\n" \
M_"a[0:3],a[128:131],a[144:147],a[0:3]\n" \
A_"m0,s74,4096\n" \
A_"s60,s67,s69\n" \
A_"s60,s60,36864\n" \
M_"a[4:7],a[132:135],a[144:147],a[4:7]\n" \
L_"v228,s[48:51],s60 offen lds\n" \
M_"a[8:11],a[136:139],a[144:147],a[8:11]\n" \
L_"v229,s[48:51],s60 offen offset:1024 lds\n" \
M_"a[12:15],a[140:143],a[144:147],a[12:15]\n" \
R_"a[144:147],v227 offset:14336\n" \
M_"a[16:19],a[128:131],a[148:151],a[16:19]\n" \
L_"v230,s[48:51],s60 offen offset:2048 lds\n" \
S_"vmcnt(7)\n" \
T_"a[112:113],v250 offset:8192\n" \
M_"a[20:23],a[132:135],a[148:151],a[20:23]\n" \
L_"v231,s[48:51],s60 offen offset:3072 lds\n" \
T_"a[114:115],v250 offset:8704\n" \
M_"a[24:27],a[136:139],a[148:151],a[24:27]\n" \
T_"a[116:117],v250 offset:8200\n" \
M_"a[28:31],a[140:143],a[148:151],a[28:31]\n" \
R_"a[148:151],v227 offset:15360\n" \
M_"a[32:35],a[128:131],a[152:155],a[32:35]\n" \
T_"a[118:119],v250 offset:8712\n" \
M_"a[36:39],a[132:135],a[152:155],a[36:39]\n" \
T_"a[120:121],v251 offset:8192\n" \
M_"a[40:43],a[136:139],a[152:155],a[40:43]\n" \
T_"a[122:123],v251 offset:8704\n" \
M_"a[44:47],a[140:143],a[152:155],a[44:47]\n" \
R_"a[152:155],v227 offset:16384\n" \
S_"lgkmcnt(6)\n" \
M_"a[48:51],a[128:131],a[156:159],a[48:51]\n" \
T_"a[124:125],v251 offset:8200\n" \
M_"a[52:55],a[132:135],a[156:159],a[52:55]\n" \
T_"a[126:127],v251 offset:8712\n" \
M_"a[56:59],a[136:139],a[156:159],a[56:59]\n" \
M_"a[60:63],a[140:143],a[156:159],a[60:63]\n" \
R_"a[156:159],v227 offset:17408\n" \
M_"a[64:67],a[128:131],a[160:163],a[64:67]\n" \
M_"a[68:71],a[132:135],a[160:163],a[68:71]\n" \
M_"a[72:75],a[136:139],a[160:163],a[72:75]\n" \
M_"a[76:79],a[140:143],a[160:163],a[76:79]\n" \
R_"a[160:163],v227 offset:18432\n" \
M_"a[80:83],a[128:131],a[164:167],a[80:83]\n" \
M_"a[84:87],a[132:135],a[164:167],a[84:87]\n" \
M_"a[88:91],a[136:139],a[164:167],a[88:91]\n" \
M_"a[92:95],a[140:143],a[164:167],a[92:95]\n" \
R_"a[164:167],v227 offset:19456\n" \
M_"a[96:99],a[128:131],a[168:171],a[96:99]\n" \
M_"a[100:103],a[132:135],a[168:171],a[100:103]\n" \
M_"a[104:107],a[136:139],a[168:171],a[104:107]\n" \
M_"a[108:111],a[140:143],a[168:171],a[108:111]\n" \
R_"a[168:171],v227 offset:20480\n" \
M_"a[0:3],a[172:175],a[144:147],a[0:3]\n" \
A_"m0,s74,8192\n" \
A_"s60,s67,s69\n" \
A_"s60,s60,73728\n" \
M_"a[4:7],a[176:179],a[144:147],a[4:7]\n" \
S_"lgkmcnt(2)\n" \
L_"v228,s[48:51],s60 offen lds\n" \
M_"a[8:11],a[180:183],a[144:147],a[8:11]\n" \
L_"v229,s[48:51],s60 offen offset:1024 lds\n" \
M_"a[12:15],a[184:187],a[144:147],a[12:15]\n" \
R_"a[144:147],v227 offset:21504\n" \
M_"a[16:19],a[172:175],a[148:151],a[16:19]\n" \
L_"v230,s[48:51],s60 offen offset:2048 lds\n" \
S_"vmcnt(7)\n" \
T_"a[128:129],v250 offset:0\n" \
M_"a[20:23],a[176:179],a[148:151],a[20:23]\n" \
L_"v231,s[48:51],s60 offen offset:3072 lds\n" \
T_"a[130:131],v250 offset:512\n" \
M_"a[24:27],a[180:183],a[148:151],a[24:27]\n" \
T_"a[132:133],v250 offset:8\n" \
M_"a[28:31],a[184:187],a[148:151],a[28:31]\n" \
R_"a[148:151],v227 offset:22528\n" \
M_"a[32:35],a[172:175],a[152:155],a[32:35]\n" \
T_"a[134:135],v250 offset:520\n" \
M_"a[36:39],a[176:179],a[152:155],a[36:39]\n" \
T_"a[136:137],v251 offset:0\n" \
M_"a[40:43],a[180:183],a[152:155],a[40:43]\n" \
T_"a[138:139],v251 offset:512\n" \
M_"a[44:47],a[184:187],a[152:155],a[44:47]\n" \
R_"a[152:155],v227 offset:23552\n" \
M_"a[48:51],a[172:175],a[156:159],a[48:51]\n" \
T_"a[140:141],v251 offset:8\n" \
M_"a[52:55],a[176:179],a[156:159],a[52:55]\n" \
T_"a[142:143],v251 offset:520\n" \
M_"a[56:59],a[180:183],a[156:159],a[56:59]\n" \
M_"a[60:63],a[184:187],a[156:159],a[60:63]\n" \
R_"a[156:159],v227 offset:24576\n" \
M_"a[64:67],a[172:175],a[160:163],a[64:67]\n" \
M_"a[68:71],a[176:179],a[160:163],a[68:71]\n" \
M_"a[72:75],a[180:183],a[160:163],a[72:75]\n" \
M_"a[76:79],a[184:187],a[160:163],a[76:79]\n" \
R_"a[160:163],v227 offset:25600\n" \
S_"lgkmcnt(2)\n" \
M_"a[80:83],a[172:175],a[164:167],a[80:83]\n" \
M_"a[84:87],a[176:179],a[164:167],a[84:87]\n" \
M_"a[88:91],a[180:183],a[164:167],a[88:91]\n" \
M_"a[92:95],a[184:187],a[164:167],a[92:95]\n" \
R_"a[164:167],v227 offset:26624\n" \
M_"a[96:99],a[172:175],a[168:171],a[96:99]\n" \
M_"a[100:103],a[176:179],a[168:171],a[100:103]\n" \
M_"a[104:107],a[180:183],a[168:171],a[104:107]\n" \
M_"a[108:111],a[184:187],a[168:171],a[108:111]\n" \
R_"a[168:171],v227 offset:27648\n" \
M_"a[0:3],a[188:191],a[144:147],a[0:3]\n" \
A_"m0,s74,0\n" \
A_"s60,s67,s69\n" \
A_"s60,s60,110592\n" \
M_"a[4:7],a[192:195],a[144:147],a[4:7]\n" \
L_"v228,s[48:51],s60 offen lds\n" \
M_"a[8:11],a[196:199],a[144:147],a[8:11]\n" \
L_"v229,s[48:51],s60 offen offset:1024 lds\n" \
M_"a[12:15],a[200:203],a[144:147],a[12:15]\n" \
R_"a[144:147],v226 offset:0\n" \
M_"a[16:19],a[188:191],a[148:151],a[16:19]\n" \
L_"v230,s[48:51],s60 offen offset:2048 lds\n" \
S_"vmcnt(7)\n" \
T_"a[172:173],v250 offset:4096\n" \
M_"a[20:23],a[192:195],a[148:151],a[20:23]\n" \
L_"v231,s[48:51],s60 offen offset:3072 lds\n" \
T_"a[174:175],v250 offset:4608\n" \
M_"a[24:27],a[196:199],a[148:151],a[24:27]\n" \
T_"a[176:177],v250 offset:4104\n" \
M_"a[28:31],a[200:203],a[148:151],a[28:31]\n" \
R_"a[148:151],v226 offset:1024\n" \
M_"a[32:35],a[188:191],a[152:155],a[32:35]\n" \
T_"a[178:179],v250 offset:4616\n" \
M_"a[36:39],a[192:195],a[152:155],a[36:39]\n" \
T_"a[180:181],v251 offset:4096\n" \
M_"a[40:43],a[196:199],a[152:155],a[40:43]\n" \
T_"a[182:183],v251 offset:4608\n" \
M_"a[44:47],a[200:203],a[152:155],a[44:47]\n" \
R_"a[152:155],v226 offset:2048\n" \
S_"lgkmcnt(6)\n" \
M_"a[48:51],a[188:191],a[156:159],a[48:51]\n" \
T_"a[184:185],v251 offset:4104\n" \
M_"a[52:55],a[192:195],a[156:159],a[52:55]\n" \
T_"a[186:187],v251 offset:4616\n" \
M_"a[56:59],a[196:199],a[156:159],a[56:59]\n" \
M_"a[60:63],a[200:203],a[156:159],a[60:63]\n" \
R_"a[156:159],v226 offset:3072\n" \
M_"a[64:67],a[188:191],a[160:163],a[64:67]\n" \
M_"a[68:71],a[192:195],a[160:163],a[68:71]\n" \
M_"a[72:75],a[196:199],a[160:163],a[72:75]\n" \
M_"a[76:79],a[200:203],a[160:163],a[76:79]\n" \
R_"a[160:163],v226 offset:4096\n" \
M_"a[80:83],a[188:191],a[164:167],a[80:83]\n" \
M_"a[84:87],a[192:195],a[164:167],a[84:87]\n" \
M_"a[88:91],a[196:199],a[164:167],a[88:91]\n" \
M_"a[92:95],a[200:203],a[164:167],a[92:95]\n" \
R_"a[164:167],v226 offset:5120\n" \
M_"a[96:99],a[188:191],a[168:171],a[96:99]\n" \
M_"a[100:103],a[192:195],a[168:171],a[100:103]\n" \
M_"a[104:107],a[196:199],a[168:171],a[104:107]\n" \
M_"a[108:111],a[200:203],a[168:171],a[108:111]\n" \
R_"a[168:171],v226 offset:6144\n" \
M_"v[0:3],a[204:207],a[144:147],v[0:3]\n" \
A_"m0,s74,4096\n" \
A_"s60,s66,s70\n" \
A_"s60,s60,0\n" \
M_"v[4:7],a[208:211],a[144:147],v[4:7]\n" \
S_"lgkmcnt(2)\n" \
L_"v228,s[48:51],s60 offen lds\n" \
M_"v[8:11],a[212:215],a[144:147],v[8:11]\n" \
L_"v229,s[48:51],s60 offen offset:1024 lds\n" \
M_"v[12:15],a[216:219],a[144:147],v[12:15]\n" \
R_"a[144:147],v226 offset:7168\n" \
M_"v[16:19],a[204:207],a[148:151],v[16:19]\n" \
L_"v230,s[48:51],s60 offen offset:2048 lds\n" \
S_"vmcnt(7)\n" \
T_"a[188:189],v250 offset:8192\n" \
M_"v[20:23],a[208:211],a[148:151],v[20:23]\n" \
L_"v231,s[48:51],s60 offen offset:3072 lds\n" \
T_"a[190:191],v250 offset:8704\n" \
M_"v[24:27],a[212:215],a[148:151],v[24:27]\n" \
T_"a[192:193],v250 offset:8200\n" \
G_"v236,a20\n" \
G_"v237,a21\n" \
M_"v[28:31],a[216:219],a[148:151],v[28:31]\n" \
R_"a[148:151],v226 offset:8192\n" \
G_"v238,a22\n" \
G_"v239,a23\n" \
M_"v[32:35],a[204:207],a[152:155],v[32:35]\n" \
T_"a[194:195],v250 offset:8712\n" \
G_"v232,a16\n" \
G_"v233,a17\n" \
M_"v[36:39],a[208:211],a[152:155],v[36:39]\n" \
T_"a[196:197],v251 offset:8192\n" \
G_"v234,a18\n" \
G_"v235,a19\n" \
M_"v[40:43],a[212:215],a[152:155],v[40:43]\n" \
T_"a[198:199],v251 offset:8704\n" \
C_"v232,v232,v233\n" \
C_"v233,v234,v235\n" \
M_"v[44:47],a[216:219],a[152:155],v[44:47]\n" \
R_"a[152:155],v226 offset:9216\n" \
C_"v234,v236,v237\n" \
C_"v235,v238,v239\n" \
M_"v[48:51],a[204:207],a[156:159],v[48:51]\n" \
T_"a[200:201],v251 offset:8200\n" \
A_"s61,s71,0\n" \
"buffer_store_dwordx4 v[232:235],v240,s[52:55],s61 offen offset:2048 sc1\n" \
M_"v[52:55],a[208:211],a[156:159],v[52:55]\n" \
T_"a[202:203],v251 offset:8712\n" \
G_"v236,a28\n" \
G_"v237,a29\n" \
M_"v[56:59],a[212:215],a[156:159],v[56:59]\n" \
G_"v238,a30\n" \
G_"v239,a31\n" \
G_"v232,a24\n" \
M_"v[60:63],a[216:219],a[156:159],v[60:63]\n" \
R_"a[156:159],v226 offset:10240\n" \
G_"v233,a25\n" \
G_"v234,a26\n" \
M_"v[64:67],a[204:207],a[160:163],v[64:67]\n" \
G_"v235,a27\n" \
C_"v232,v232,v233\n" \
C_"v233,v234,v235\n" \
M_"v[68:71],a[208:211],a[160:163],v[68:71]\n" \
C_"v234,v236,v237\n" \
C_"v235,v238,v239\n" \
"buffer_store_dwordx4 v[232:235],v240,s[52:55],s61 offen offset:3072 sc1\n" \
M_"v[72:75],a[212:215],a[160:163],v[72:75]\n" \
G_"v236,a36\n" \
G_"v237,a37\n" \
G_"v238,a38\n" \
M_"v[76:79],a[216:219],a[160:163],v[76:79]\n" \
R_"a[160:163],v226 offset:11264\n" \
G_"v239,a39\n" \
G_"v232,a32\n" \
S_"lgkmcnt(2)\n" \
M_"v[80:83],a[204:207],a[164:167],v[80:83]\n" \
G_"v233,a33\n" \
G_"v234,a34\n" \
G_"v235,a35\n" \
M_"v[84:87],a[208:211],a[164:167],v[84:87]\n" \
C_"v232,v232,v233\n" \
C_"v233,v234,v235\n" \
C_"v234,v236,v237\n" \
M_"v[88:91],a[212:215],a[164:167],v[88:91]\n" \
C_"v235,v238,v239\n" \
A_"s61,s71,4096\n" \
"buffer_store_dwordx4 v[232:235],v240,s[52:55],s61 offen sc1\n" \
M_"v[92:95],a[216:219],a[164:167],v[92:95]\n" \
R_"a[164:167],v226 offset:12288\n" \
G_"v236,a44\n" \
G_"v237,a45\n" \
M_"v[96:99],a[204:207],a[168:171],v[96:99]\n" \
G_"v238,a46\n" \
G_"v239,a47\n" \
G_"v232,a40\n" \
M_"v[100:103],a[208:211],a[168:171],v[100:103]\n" \
G_"v233,a41\n" \
G_"v234,a42\n" \
G_"v235,a43\n" \
M_"v[104:107],a[212:215],a[168:171],v[104:107]\n" \
C_"v232,v232,v233\n" \
C_"v233,v234,v235\n" \
C_"v234,v236,v237\n" \
M_"v[108:111],a[216:219],a[168:171],v[108:111]\n" \
R_"a[168:171],v226 offset:13312\n" \
C_"v235,v238,v239\n" \
"buffer_store_dwordx4 v[232:235],v240,s[52:55],s61 offen offset:1024 sc1\n" \
M_"v[0:3],a[220:223],a[144:147],v[0:3]\n" \
A_"m0,s74,8192\n" \
A_"s60,s66,s70\n" \
A_"s60,s60,36864\n" \
M_"v[4:7],a[224:227],a[144:147],v[4:7]\n" \
L_"v228,s[48:51],s60 offen lds\n" \
G_"v236,a52\n" \
G_"v237,a53\n" \
M_"v[8:11],a[228:231],a[144:147],v[8:11]\n" \
L_"v229,s[48:51],s60 offen offset:1024 lds\n" \
G_"v238,a54\n" \
G_"v239,a55\n" \
M_"v[12:15],a[232:235],a[144:147],v[12:15]\n" \
R_"a[144:147],v226 offset:14336\n" \
G_"v232,a48\n" \
G_"v233,a49\n" \
M_"v[16:19],a[220:223],a[148:151],v[16:19]\n" \
L_"v230,s[48:51],s60 offen offset:2048 lds\n" \
S_"vmcnt(11)\n" \
T_"a[204:205],v250 offset:0\n" \
G_"v234,a50\n" \
M_"v[20:23],a[224:227],a[148:151],v[20:23]\n" \
L_"v231,s[48:51],s60 offen offset:3072 lds\n" \
T_"a[206:207],v250 offset:512\n" \
G_"v235,a51\n" \
M_"v[24:27],a[228:231],a[148:151],v[24:27]\n" \
T_"a[208:209],v250 offset:8\n" \
C_"v232,v232,v233\n" \
C_"v233,v234,v235\n" \
M_"v[28:31],a[232:235],a[148:151],v[28:31]\n" \
R_"a[148:151],v226 offset:15360\n" \
C_"v234,v236,v237\n" \
C_"v235,v238,v239\n" \
M_"v[32:35],a[220:223],a[152:155],v[32:35]\n" \
T_"a[210:211],v250 offset:520\n" \
"buffer_store_dwordx4 v[232:235],v240,s[52:55],s61 offen offset:2048 sc1\n" \
G_"v236,a60\n" \
M_"v[36:39],a[224:227],a[152:155],v[36:39]\n" \
T_"a[212:213],v251 offset:0\n" \
G_"v237,a61\n" \
G_"v238,a62\n" \
M_"v[40:43],a[228:231],a[152:155],v[40:43]\n" \
T_"a[214:215],v251 offset:512\n" \
G_"v239,a63\n" \
G_"v232,a56\n" \
M_"v[44:47],a[232:235],a[152:155],v[44:47]\n" \
R_"a[152:155],v226 offset:16384\n" \
G_"v233,a57\n" \
G_"v234,a58\n" \
S_"lgkmcnt(6)\n" \
M_"v[48:51],a[220:223],a[156:159],v[48:51]\n" \
T_"a[216:217],v251 offset:8\n" \
G_"v235,a59\n" \
C_"v232,v232,v233\n" \
M_"v[52:55],a[224:227],a[156:159],v[52:55]\n" \
T_"a[218:219],v251 offset:520\n" \
C_"v233,v234,v235\n" \
C_"v234,v236,v237\n" \
M_"v[56:59],a[228:231],a[156:159],v[56:59]\n" \
C_"v235,v238,v239\n" \
"buffer_store_dwordx4 v[232:235],v240,s[52:55],s61 offen offset:3072 sc1\n" \
G_"v236,a68\n" \
M_"v[60:63],a[232:235],a[156:159],v[60:63]\n" \
R_"a[156:159],v226 offset:17408\n" \
G_"v237,a69\n" \
G_"v238,a70\n" \
M_"v[64:67],a[220:223],a[160:163],v[64:67]\n" \
G_"v239,a71\n" \
G_"v232,a64\n" \
G_"v233,a65\n" \
M_"v[68:71],a[224:227],a[160:163],v[68:71]\n" \
G_"v234,a66\n" \
G_"v235,a67\n" \
C_"v232,v232,v233\n" \
M_"v[72:75],a[228:231],a[160:163],v[72:75]\n" \
C_"v233,v234,v235\n" \
C_"v234,v236,v237\n" \
C_"v235,v238,v239\n" \
M_"v[76:79],a[232:235],a[160:163],v[76:79]\n" \
R_"a[160:163],v226 offset:18432\n" \
A_"s61,s71,8192\n" \
"buffer_store_dwordx4 v[232:235],v240,s[52:55],s61 offen sc1\n" \
M_"v[80:83],a[220:223],a[164:167],v[80:83]\n" \
G_"v236,a76\n" \
G_"v237,a77\n" \
G_"v238,a78\n" \
M_"v[84:87],a[224:227],a[164:167],v[84:87]\n" \
G_"v239,a79\n" \
G_"v232,a72\n" \
G_"v233,a73\n" \
M_"v[88:91],a[228:231],a[164:167],v[88:91]\n" \
G_"v234,a74\n" \
G_"v235,a75\n" \
C_"v232,v232,v233\n" \
M_"v[92:95],a[232:235],a[164:167],v[92:95]\n" \
R_"a[164:167],v226 offset:19456\n" \
C_"v233,v234,v235\n" \
C_"v234,v236,v237\n" \
M_"v[96:99],a[220:223],a[168:171],v[96:99]\n" \
C_"v235,v238,v239\n" \
"buffer_store_dwordx4 v[232:235],v240,s[52:55],s61 offen offset:1024 sc1\n" \
G_"v236,a84\n" \
M_"v[100:103],a[224:227],a[168:171],v[100:103]\n" \
G_"v237,a85\n" \
G_"v238,a86\n" \
G_"v239,a87\n" \
M_"v[104:107],a[228:231],a[168:171],v[104:107]\n" \
G_"v232,a80\n" \
G_"v233,a81\n" \
G_"v234,a82\n" \
M_"v[108:111],a[232:235],a[168:171],v[108:111]\n" \
R_"a[168:171],v226 offset:20480\n" \
G_"v235,a83\n" \
C_"v232,v232,v233\n" \
M_"v[0:3],a[236:239],a[144:147],v[0:3]\n" \
A_"m0,s74,0\n" \
A_"s60,s66,s70\n" \
A_"s60,s60,73728\n" \
M_"v[4:7],a[240:243],a[144:147],v[4:7]\n" \
S_"lgkmcnt(2)\n" \
L_"v228,s[48:51],s60 offen lds\n" \
C_"v233,v234,v235\n" \
C_"v234,v236,v237\n" \
M_"v[8:11],a[244:247],a[144:147],v[8:11]\n" \
L_"v229,s[48:51],s60 offen offset:1024 lds\n" \
C_"v235,v238,v239\n" \
"buffer_store_dwordx4 v[232:235],v240,s[52:55],s61 offen offset:2048 sc1\n" \
M_"v[12:15],a[248:251],a[144:147],v[12:15]\n" \
R_"a[144:147],v226 offset:21504\n" \
G_"v236,a92\n" \
G_"v237,a93\n" \
M_"v[16:19],a[236:239],a[148:151],v[16:19]\n" \
L_"v230,s[48:51],s60 offen offset:2048 lds\n" \
S_"vmcnt(16)\n" \
T_"a[220:221],v250 offset:4096\n" \
G_"v238,a94\n" \
M_"v[20:23],a[240:243],a[148:151],v[20:23]\n" \
L_"v231,s[48:51],s60 offen offset:3072 lds\n" \
T_"a[222:223],v250 offset:4608\n" \
G_"v239,a95\n" \
M_"v[24:27],a[244:247],a[148:151],v[24:27]\n" \
T_"a[224:225],v250 offset:4104\n" \
G_"v232,a88\n" \
G_"v233,a89\n" \
M_"v[28:31],a[248:251],a[148:151],v[28:31]\n" \
R_"a[148:151],v226 offset:22528\n" \
G_"v234,a90\n" \
G_"v235,a91\n" \
M_"v[32:35],a[236:239],a[152:155],v[32:35]\n" \
T_"a[226:227],v250 offset:4616\n" \
C_"v232,v232,v233\n" \
C_"v233,v234,v235\n" \
M_"v[36:39],a[240:243],a[152:155],v[36:39]\n" \
T_"a[228:229],v251 offset:4096\n" \
C_"v234,v236,v237\n" \
C_"v235,v238,v239\n" \
M_"v[40:43],a[244:247],a[152:155],v[40:43]\n" \
T_"a[230:231],v251 offset:4608\n" \
"buffer_store_dwordx4 v[232:235],v240,s[52:55],s61 offen offset:3072 sc1\n" \
G_"v236,a100\n" \
M_"v[44:47],a[248:251],a[152:155],v[44:47]\n" \
R_"a[152:155],v226 offset:23552\n" \
G_"v237,a101\n" \
G_"v238,a102\n" \
M_"v[48:51],a[236:239],a[156:159],v[48:51]\n" \
T_"a[232:233],v251 offset:4104\n" \
G_"v239,a103\n" \
G_"v232,a96\n" \
M_"v[52:55],a[240:243],a[156:159],v[52:55]\n" \
T_"a[234:235],v251 offset:4616\n" \
G_"v233,a97\n" \
G_"v234,a98\n" \
M_"v[56:59],a[244:247],a[156:159],v[56:59]\n" \
G_"v235,a99\n" \
C_"v232,v232,v233\n" \
C_"v233,v234,v235\n" \
M_"v[60:63],a[248:251],a[156:159],v[60:63]\n" \
R_"a[156:159],v226 offset:24576\n" \
C_"v234,v236,v237\n" \
C_"v235,v238,v239\n" \
M_"v[64:67],a[236:239],a[160:163],v[64:67]\n" \
A_"s61,s71,12288\n" \
"buffer_store_dwordx4 v[232:235],v240,s[52:55],s61 offen sc1\n" \
G_"v236,a108\n" \
M_"v[68:71],a[240:243],a[160:163],v[68:71]\n" \
G_"v237,a109\n" \
G_"v238,a110\n" \
G_"v239,a111\n" \
M_"v[72:75],a[244:247],a[160:163],v[72:75]\n" \
G_"v232,a104\n" \
G_"v233,a105\n" \
G_"v234,a106\n" \
M_"v[76:79],a[248:251],a[160:163],v[76:79]\n" \
R_"a[160:163],v226 offset:25600\n" \
G_"v235,a107\n" \
C_"v232,v232,v233\n" \
S_"lgkmcnt(2)\n" \
M_"v[80:83],a[236:239],a[164:167],v[80:83]\n" \
C_"v233,v234,v235\n" \
C_"v234,v236,v237\n" \
C_"v235,v238,v239\n" \
M_"v[84:87],a[240:243],a[164:167],v[84:87]\n" \
"buffer_store_dwordx4 v[232:235],v240,s[52:55],s61 offen offset:1024 sc1\n" \
G_"v236,a4\n" \
G_"v237,a5\n" \
M_"v[88:91],a[244:247],a[164:167],v[88:91]\n" \
G_"v238,a6\n" \
G_"v239,a7\n" \
G_"v232,a0\n" \
M_"v[92:95],a[248:251],a[164:167],v[92:95]\n" \
R_"a[164:167],v226 offset:26624\n" \
G_"v233,a1\n" \
G_"v234,a2\n" \
M_"v[96:99],a[236:239],a[168:171],v[96:99]\n" \
G_"v235,a3\n" \
C_"v232,v232,v233\n" \
C_"v233,v234,v235\n" \
M_"v[100:103],a[240:243],a[168:171],v[100:103]\n" \
C_"v234,v236,v237\n" \
C_"v235,v238,v239\n" \
A_"s61,s71,0\n" \
M_"v[104:107],a[244:247],a[168:171],v[104:107]\n" \
"buffer_store_dwordx4 v[232:235],v247,s[52:55],s61 offen sc1\n" \
G_"v236,a12\n" \
G_"v237,a13\n" \
M_"v[108:111],a[248:251],a[168:171],v[108:111]\n" \
R_"a[168:171],v226 offset:27648\n" \
G_"v238,a14\n" \
G_"v239,a15\n" \
M_"v[0:3],a[112:115],a[144:147],v[0:3]\n" \
A_"m0,s74,4096\n" \
A_"s60,s66,s70\n" \
A_"s60,s60,110592\n" \
M_"v[4:7],a[116:119],a[144:147],v[4:7]\n" \
L_"v228,s[48:51],s60 offen lds\n" \
G_"v232,a8\n" \
G_"v233,a9\n" \
M_"v[8:11],a[120:123],a[144:147],v[8:11]\n" \
L_"v229,s[48:51],s60 offen offset:1024 lds\n" \
G_"v234,a10\n" \
G_"v235,a11\n" \
M_"v[12:15],a[124:127],a[144:147],v[12:15]\n" \
R_"a[144:147],v227 offset:0\n" \
C_"v232,v232,v233\n" \
C_"v233,v234,v235\n" \
M_"v[16:19],a[112:115],a[148:151],v[16:19]\n" \
L_"v230,s[48:51],s60 offen offset:2048 lds\n" \
S_"vmcnt(16)\n" \
T_"a[236:237],v250 offset:8192\n" \
C_"v234,v236,v237\n" \
M_"v[20:23],a[116:119],a[148:151],v[20:23]\n" \
L_"v231,s[48:51],s60 offen offset:3072 lds\n" \
T_"a[238:239],v250 offset:8704\n" \
C_"v235,v238,v239\n" \
M_"v[24:27],a[120:123],a[148:151],v[24:27]\n" \
T_"a[240:241],v250 offset:8200\n" \
"buffer_store_dwordx4 v[232:235],v247,s[52:55],s61 offen offset:1024 sc1\n" \
M_"v[28:31],a[124:127],a[148:151],v[28:31]\n" \
R_"a[148:151],v227 offset:1024\n" \
M_"v[32:35],a[112:115],a[152:155],v[32:35]\n" \
T_"a[242:243],v250 offset:8712\n" \
M_"v[36:39],a[116:119],a[152:155],v[36:39]\n" \
T_"a[244:245],v251 offset:8192\n" \
M_"v[40:43],a[120:123],a[152:155],v[40:43]\n" \
T_"a[246:247],v251 offset:8704\n" \
M_"v[44:47],a[124:127],a[152:155],v[44:47]\n" \
R_"a[152:155],v227 offset:2048\n" \
S_"lgkmcnt(6)\n" \
M_"v[48:51],a[112:115],a[156:159],v[48:51]\n" \
T_"a[248:249],v251 offset:8200\n" \
M_"v[52:55],a[116:119],a[156:159],v[52:55]\n" \
T_"a[250:251],v251 offset:8712\n" \
M_"v[56:59],a[120:123],a[156:159],v[56:59]\n" \
M_"v[60:63],a[124:127],a[156:159],v[60:63]\n" \
R_"a[156:159],v227 offset:3072\n" \
M_"v[64:67],a[112:115],a[160:163],v[64:67]\n" \
M_"v[68:71],a[116:119],a[160:163],v[68:71]\n" \
M_"v[72:75],a[120:123],a[160:163],v[72:75]\n" \
M_"v[76:79],a[124:127],a[160:163],v[76:79]\n" \
R_"a[160:163],v227 offset:4096\n" \
M_"v[80:83],a[112:115],a[164:167],v[80:83]\n" \
M_"v[84:87],a[116:119],a[164:167],v[84:87]\n" \
M_"v[88:91],a[120:123],a[164:167],v[88:91]\n" \
M_"v[92:95],a[124:127],a[164:167],v[92:95]\n" \
R_"a[164:167],v227 offset:5120\n" \
M_"v[96:99],a[112:115],a[168:171],v[96:99]\n" \
M_"v[100:103],a[116:119],a[168:171],v[100:103]\n" \
M_"v[104:107],a[120:123],a[168:171],v[104:107]\n" \
M_"v[108:111],a[124:127],a[168:171],v[108:111]\n" \
R_"a[168:171],v227 offset:6144\n" \
M_"v[0:3],a[128:131],a[144:147],v[0:3]\n" \
A_"m0,s74,8192\n" \
A_"s60,s67,s70\n" \
A_"s60,s60,0\n" \
M_"v[4:7],a[132:135],a[144:147],v[4:7]\n" \
S_"lgkmcnt(2)\n" \
L_"v228,s[48:51],s60 offen lds\n" \
M_"v[8:11],a[136:139],a[144:147],v[8:11]\n" \
L_"v229,s[48:51],s60 offen offset:1024 lds\n" \
M_"v[12:15],a[140:143],a[144:147],v[12:15]\n" \
R_"a[144:147],v227 offset:7168\n" \
M_"v[16:19],a[128:131],a[148:151],v[16:19]\n" \
L_"v230,s[48:51],s60 offen offset:2048 lds\n" \
S_"vmcnt(12)\n" \
T_"a[112:113],v250 offset:0\n" \
M_"v[20:23],a[132:135],a[148:151],v[20:23]\n" \
L_"v231,s[48:51],s60 offen offset:3072 lds\n" \
T_"a[114:115],v250 offset:512\n" \
M_"v[24:27],a[136:139],a[148:151],v[24:27]\n" \
T_"a[116:117],v250 offset:8\n" \
M_"v[28:31],a[140:143],a[148:151],v[28:31]\n" \
R_"a[148:151],v227 offset:8192\n" \
M_"v[32:35],a[128:131],a[152:155],v[32:35]\n" \
T_"a[118:119],v250 offset:520\n" \
M_"v[36:39],a[132:135],a[152:155],v[36:39]\n" \
T_"a[120:121],v251 offset:0\n" \
M_"v[40:43],a[136:139],a[152:155],v[40:43]\n" \
T_"a[122:123],v251 offset:512\n" \
M_"v[44:47],a[140:143],a[152:155],v[44:47]\n" \
R_"a[152:155],v227 offset:9216\n" \
M_"v[48:51],a[128:131],a[156:159],v[48:51]\n" \
T_"a[124:125],v251 offset:8\n" \
M_"v[52:55],a[132:135],a[156:159],v[52:55]\n" \
T_"a[126:127],v251 offset:520\n" \
M_"v[56:59],a[136:139],a[156:159],v[56:59]\n" \
M_"v[60:63],a[140:143],a[156:159],v[60:63]\n" \
R_"a[156:159],v227 offset:10240\n" \
M_"v[64:67],a[128:131],a[160:163],v[64:67]\n" \
M_"v[68:71],a[132:135],a[160:163],v[68:71]\n" \
M_"v[72:75],a[136:139],a[160:163],v[72:75]\n" \
M_"v[76:79],a[140:143],a[160:163],v[76:79]\n" \
R_"a[160:163],v227 offset:11264\n" \
S_"lgkmcnt(2)\n" \
M_"v[80:83],a[128:131],a[164:167],v[80:83]\n" \
M_"v[84:87],a[132:135],a[164:167],v[84:87]\n" \
M_"v[88:91],a[136:139],a[164:167],v[88:91]\n" \
M_"v[92:95],a[140:143],a[164:167],v[92:95]\n" \
R_"a[164:167],v227 offset:12288\n" \
M_"v[96:99],a[128:131],a[168:171],v[96:99]\n" \
M_"v[100:103],a[132:135],a[168:171],v[100:103]\n" \
M_"v[104:107],a[136:139],a[168:171],v[104:107]\n" \
M_"v[108:111],a[140:143],a[168:171],v[108:111]\n" \
R_"a[168:171],v227 offset:13312\n" \
M_"v[0:3],a[172:175],a[144:147],v[0:3]\n" \
A_"m0,s74,0\n" \
A_"s60,s67,s70\n" \
A_"s60,s60,36864\n" \
M_"v[4:7],a[176:179],a[144:147],v[4:7]\n" \
L_"v228,s[48:51],s60 offen lds\n" \
M_"v[8:11],a[180:183],a[144:147],v[8:11]\n" \
L_"v229,s[48:51],s60 offen offset:1024 lds\n" \
M_"v[12:15],a[184:187],a[144:147],v[12:15]\n" \
R_"a[144:147],v227 offset:14336\n" \
M_"v[16:19],a[172:175],a[148:151],v[16:19]\n" \
L_"v230,s[48:51],s60 offen offset:2048 lds\n" \
S_"vmcnt(8)\n" \
T_"a[128:129],v250 offset:4096\n" \
M_"v[20:23],a[176:179],a[148:151],v[20:23]\n" \
L_"v231,s[48:51],s60 offen offset:3072 lds\n" \
T_"a[130:131],v250 offset:4608\n" \
M_"v[24:27],a[180:183],a[148:151],v[24:27]\n" \
T_"a[132:133],v250 offset:4104\n" \
M_"v[28:31],a[184:187],a[148:151],v[28:31]\n" \
R_"a[148:151],v227 offset:15360\n" \
M_"v[32:35],a[172:175],a[152:155],v[32:35]\n" \
T_"a[134:135],v250 offset:4616\n" \
M_"v[36:39],a[176:179],a[152:155],v[36:39]\n" \
T_"a[136:137],v251 offset:4096\n" \
M_"v[40:43],a[180:183],a[152:155],v[40:43]\n" \
T_"a[138:139],v251 offset:4608\n" \
M_"v[44:47],a[184:187],a[152:155],v[44:47]\n" \
R_"a[152:155],v227 offset:16384\n" \
S_"lgkmcnt(6)\n" \
M_"v[48:51],a[172:175],a[156:159],v[48:51]\n" \
T_"a[140:141],v251 offset:4104\n" \
M_"v[52:55],a[176:179],a[156:159],v[52:55]\n" \
T_"a[142:143],v251 offset:4616\n" \
M_"v[56:59],a[180:183],a[156:159],v[56:59]\n" \
M_"v[60:63],a[184:187],a[156:159],v[60:63]\n" \
R_"a[156:159],v227 offset:17408\n" \
M_"v[64:67],a[172:175],a[160:163],v[64:67]\n" \
M_"v[68:71],a[176:179],a[160:163],v[68:71]\n" \
M_"v[72:75],a[180:183],a[160:163],v[72:75]\n" \
M_"v[76:79],a[184:187],a[160:163],v[76:79]\n" \
R_"a[160:163],v227 offset:18432\n" \
M_"v[80:83],a[172:175],a[164:167],v[80:83]\n" \
M_"v[84:87],a[176:179],a[164:167],v[84:87]\n" \
M_"v[88:91],a[180:183],a[164:167],v[88:91]\n" \
M_"v[92:95],a[184:187],a[164:167],v[92:95]\n" \
R_"a[164:167],v227 offset:19456\n" \
M_"v[96:99],a[172:175],a[168:171],v[96:99]\n" \
M_"v[100:103],a[176:179],a[168:171],v[100:103]\n" \
M_"v[104:107],a[180:183],a[168:171],v[104:107]\n" \
M_"v[108:111],a[184:187],a[168:171],v[108:111]\n" \
R_"a[168:171],v227 offset:20480\n" \
M_"v[0:3],a[188:191],a[144:147],v[0:3]\n" \
A_"m0,s74,4096\n" \
A_"s60,s67,s70\n" \
A_"s60,s60,73728\n" \
M_"v[4:7],a[192:195],a[144:147],v[4:7]\n" \
S_"lgkmcnt(2)\n" \
L_"v228,s[48:51],s60 offen lds\n" \
M_"v[8:11],a[196:199],a[144:147],v[8:11]\n" \
L_"v229,s[48:51],s60 offen offset:1024 lds\n" \
M_"v[12:15],a[200:203],a[144:147],v[12:15]\n" \
R_"a[144:147],v227 offset:21504\n" \
M_"v[16:19],a[188:191],a[148:151],v[16:19]\n" \
L_"v230,s[48:51],s60 offen offset:2048 lds\n" \
S_"vmcnt(7)\n" \
T_"a[172:173],v250 offset:8192\n" \
M_"v[20:23],a[192:195],a[148:151],v[20:23]\n" \
L_"v231,s[48:51],s60 offen offset:3072 lds\n" \
T_"a[174:175],v250 offset:8704\n" \
M_"v[24:27],a[196:199],a[148:151],v[24:27]\n" \
T_"a[176:177],v250 offset:8200\n" \
M_"v[28:31],a[200:203],a[148:151],v[28:31]\n" \
R_"a[148:151],v227 offset:22528\n" \
M_"v[32:35],a[188:191],a[152:155],v[32:35]\n" \
T_"a[178:179],v250 offset:8712\n" \
M_"v[36:39],a[192:195],a[152:155],v[36:39]\n" \
T_"a[180:181],v251 offset:8192\n" \
M_"v[40:43],a[196:199],a[152:155],v[40:43]\n" \
T_"a[182:183],v251 offset:8704\n" \
M_"v[44:47],a[200:203],a[152:155],v[44:47]\n" \
R_"a[152:155],v227 offset:23552\n" \
M_"v[48:51],a[188:191],a[156:159],v[48:51]\n" \
T_"a[184:185],v251 offset:8200\n" \
M_"v[52:55],a[192:195],a[156:159],v[52:55]\n" \
T_"a[186:187],v251 offset:8712\n" \
M_"v[56:59],a[196:199],a[156:159],v[56:59]\n" \
M_"v[60:63],a[200:203],a[156:159],v[60:63]\n" \
R_"a[156:159],v227 offset:24576\n" \
M_"v[64:67],a[188:191],a[160:163],v[64:67]\n" \
M_"v[68:71],a[192:195],a[160:163],v[68:71]\n" \
M_"v[72:75],a[196:199],a[160:163],v[72:75]\n" \
M_"v[76:79],a[200:203],a[160:163],v[76:79]\n" \
R_"a[160:163],v227 offset:25600\n" \
S_"lgkmcnt(2)\n" \
M_"v[80:83],a[188:191],a[164:167],v[80:83]\n" \
M_"v[84:87],a[192:195],a[164:167],v[84:87]\n" \
M_"v[88:91],a[196:199],a[164:167],v[88:91]\n" \
M_"v[92:95],a[200:203],a[164:167],v[92:95]\n" \
R_"a[164:167],v227 offset:26624\n" \
M_"v[96:99],a[188:191],a[168:171],v[96:99]\n" \
M_"v[100:103],a[192:195],a[168:171],v[100:103]\n" \
M_"v[104:107],a[196:199],a[168:171],v[104:107]\n" \
M_"v[108:111],a[200:203],a[168:171],v[108:111]\n" \
R_"a[168:171],v227 offset:27648\n" \
M_"v[0:3],a[204:207],a[144:147],v[0:3]\n" \
A_"m0,s74,8192\n" \
A_"s60,s67,s70\n" \
A_"s60,s60,110592\n" \
M_"v[4:7],a[208:211],a[144:147],v[4:7]\n" \
L_"v228,s[48:51],s60 offen lds\n" \
M_"v[8:11],a[212:215],a[144:147],v[8:11]\n" \
L_"v229,s[48:51],s60 offen offset:1024 lds\n" \
M_"v[12:15],a[216:219],a[144:147],v[12:15]\n" \
R_"a[144:147],v226 offset:0\n" \
M_"v[16:19],a[204:207],a[148:151],v[16:19]\n" \
L_"v230,s[48:51],s60 offen offset:2048 lds\n" \
S_"vmcnt(7)\n" \
T_"a[188:189],v250 offset:0\n" \
M_"v[20:23],a[208:211],a[148:151],v[20:23]\n" \
L_"v231,s[48:51],s60 offen offset:3072 lds\n" \
T_"a[190:191],v250 offset:512\n" \
M_"v[24:27],a[212:215],a[148:151],v[24:27]\n" \
T_"a[192:193],v250 offset:8\n" \
M_"v[28:31],a[216:219],a[148:151],v[28:31]\n" \
R_"a[148:151],v226 offset:1024\n" \
M_"v[32:35],a[204:207],a[152:155],v[32:35]\n" \
T_"a[194:195],v250 offset:520\n" \
M_"v[36:39],a[208:211],a[152:155],v[36:39]\n" \
T_"a[196:197],v251 offset:0\n" \
M_"v[40:43],a[212:215],a[152:155],v[40:43]\n" \
T_"a[198:199],v251 offset:512\n" \
M_"v[44:47],a[216:219],a[152:155],v[44:47]\n" \
R_"a[152:155],v226 offset:2048\n" \
S_"lgkmcnt(6)\n" \
M_"v[48:51],a[204:207],a[156:159],v[48:51]\n" \
T_"a[200:201],v251 offset:8\n" \
M_"v[52:55],a[208:211],a[156:159],v[52:55]\n" \
T_"a[202:203],v251 offset:520\n" \
M_"v[56:59],a[212:215],a[156:159],v[56:59]\n" \
S_"vmcnt(0)\n" \
"v_mov_b32 v253,1\n" \
"buffer_store_dword v253,v246,s[84:87],0 offen sc1\n" \
M_"v[60:63],a[216:219],a[156:159],v[60:63]\n" \
R_"a[156:159],v226 offset:3072\n" \
M_"v[64:67],a[204:207],a[160:163],v[64:67]\n" \
M_"v[68:71],a[208:211],a[160:163],v[68:71]\n" \
M_"v[72:75],a[212:215],a[160:163],v[72:75]\n" \
M_"v[76:79],a[216:219],a[160:163],v[76:79]\n" \
R_"a[160:163],v226 offset:4096\n" \
M_"v[80:83],a[204:207],a[164:167],v[80:83]\n" \
M_"v[84:87],a[208:211],a[164:167],v[84:87]\n" \
M_"v[88:91],a[212:215],a[164:167],v[88:91]\n" \
M_"v[92:95],a[216:219],a[164:167],v[92:95]\n" \
R_"a[164:167],v226 offset:5120\n" \
M_"v[96:99],a[204:207],a[168:171],v[96:99]\n" \
M_"v[100:103],a[208:211],a[168:171],v[100:103]\n" \
M_"v[104:107],a[212:215],a[168:171],v[104:107]\n" \
M_"v[108:111],a[216:219],a[168:171],v[108:111]\n" \
R_"a[168:171],v226 offset:6144\n" \
M_"v[112:115],a[220:223],a[144:147],v[112:115]\n" \
M_"v[116:119],a[224:227],a[144:147],v[116:119]\n" \
M_"v[120:123],a[228:231],a[144:147],v[120:123]\n" \
M_"v[124:127],a[232:235],a[144:147],v[124:127]\n" \
R_"a[144:147],v226 offset:7168\n" \
S_"lgkmcnt(2)\n" \
M_"v[128:131],a[220:223],a[148:151],v[128:131]\n" \
T_"a[204:205],v250 offset:4096\n" \
M_"v[132:135],a[224:227],a[148:151],v[132:135]\n" \
T_"a[206:207],v250 offset:4608\n" \
M_"v[136:139],a[228:231],a[148:151],v[136:139]\n" \
T_"a[208:209],v250 offset:4104\n" \
C_"v16,v16,v17\n" \
C_"v17,v18,v19\n" \
M_"v[140:143],a[232:235],a[148:151],v[140:143]\n" \
R_"a[148:151],v226 offset:8192\n" \
C_"v18,v20,v21\n" \
C_"v19,v22,v23\n" \
M_"v[144:147],a[220:223],a[152:155],v[144:147]\n" \
T_"a[210:211],v250 offset:4616\n" \
A_"s61,s72,0\n" \
"buffer_store_dwordx4 v[16:19],v240,s[52:55],s61 offen offset:2048 sc1\n" \
M_"v[148:151],a[224:227],a[152:155],v[148:151]\n" \
T_"a[212:213],v251 offset:4096\n" \
C_"v24,v24,v25\n" \
C_"v25,v26,v27\n" \
M_"v[152:155],a[228:231],a[152:155],v[152:155]\n" \
T_"a[214:215],v251 offset:4608\n" \
C_"v26,v28,v29\n" \
C_"v27,v30,v31\n" \
M_"v[156:159],a[232:235],a[152:155],v[156:159]\n" \
R_"a[152:155],v226 offset:9216\n" \
M_"v[160:163],a[220:223],a[156:159],v[160:163]\n" \
T_"a[216:217],v251 offset:4104\n" \
"buffer_store_dwordx4 v[24:27],v240,s[52:55],s61 offen offset:3072 sc1\n" \
C_"v32,v32,v33\n" \
M_"v[164:167],a[224:227],a[156:159],v[164:167]\n" \
T_"a[218:219],v251 offset:4616\n" \
C_"v33,v34,v35\n" \
C_"v34,v36,v37\n" \
M_"v[168:171],a[228:231],a[156:159],v[168:171]\n" \
C_"v35,v38,v39\n" \
A_"s61,s72,4096\n" \
M_"v[172:175],a[232:235],a[156:159],v[172:175]\n" \
R_"a[156:159],v226 offset:10240\n" \
M_"v[176:179],a[220:223],a[160:163],v[176:179]\n" \
"buffer_store_dwordx4 v[32:35],v240,s[52:55],s61 offen sc1\n" \
C_"v40,v40,v41\n" \
C_"v41,v42,v43\n" \
M_"v[180:183],a[224:227],a[160:163],v[180:183]\n" \
C_"v42,v44,v45\n" \
C_"v43,v46,v47\n" \
M_"v[184:187],a[228:231],a[160:163],v[184:187]\n" \
M_"v[188:191],a[232:235],a[160:163],v[188:191]\n" \
R_"a[160:163],v226 offset:11264\n" \
M_"v[192:195],a[220:223],a[164:167],v[192:195]\n" \
"buffer_store_dwordx4 v[40:43],v240,s[52:55],s61 offen offset:1024 sc1\n" \
C_"v48,v48,v49\n" \
C_"v49,v50,v51\n" \
M_"v[196:199],a[224:227],a[164:167],v[196:199]\n" \
C_"v50,v52,v53\n" \
C_"v51,v54,v55\n" \
M_"v[200:203],a[228:231],a[164:167],v[200:203]\n" \
M_"v[204:207],a[232:235],a[164:167],v[204:207]\n" \
R_"a[164:167],v226 offset:12288\n" \
S_"lgkmcnt(2)\n" \
M_"v[208:211],a[220:223],a[168:171],v[208:211]\n" \
"buffer_store_dwordx4 v[48:51],v240,s[52:55],s61 offen offset:2048 sc1\n" \
C_"v56,v56,v57\n" \
C_"v57,v58,v59\n" \
M_"v[212:215],a[224:227],a[168:171],v[212:215]\n" \
C_"v58,v60,v61\n" \
C_"v59,v62,v63\n" \
M_"v[216:219],a[228:231],a[168:171],v[216:219]\n" \
M_"v[220:223],a[232:235],a[168:171],v[220:223]\n" \
R_"a[168:171],v226 offset:13312\n" \
M_"v[112:115],a[236:239],a[144:147],v[112:115]\n" \
"buffer_store_dwordx4 v[56:59],v240,s[52:55],s61 offen offset:3072 sc1\n" \
C_"v64,v64,v65\n" \
C_"v65,v66,v67\n" \
M_"v[116:119],a[240:243],a[144:147],v[116:119]\n" \
C_"v66,v68,v69\n" \
C_"v67,v70,v71\n" \
A_"s61,s72,8192\n" \
M_"v[120:123],a[244:247],a[144:147],v[120:123]\n" \
M_"v[124:127],a[248:251],a[144:147],v[124:127]\n" \
R_"a[144:147],v226 offset:14336\n" \
M_"v[128:131],a[236:239],a[148:151],v[128:131]\n" \
T_"a[220:221],v250 offset:8192\n" \
"buffer_store_dwordx4 v[64:67],v240,s[52:55],s61 offen sc1\n" \
C_"v72,v72,v73\n" \
M_"v[132:135],a[240:243],a[148:151],v[132:135]\n" \
T_"a[222:223],v250 offset:8704\n" \
C_"v73,v74,v75\n" \
C_"v74,v76,v77\n" \
M_"v[136:139],a[244:247],a[148:151],v[136:139]\n" \
T_"a[224:225],v250 offset:8200\n" \
C_"v75,v78,v79\n" \
M_"v[140:143],a[248:251],a[148:151],v[140:143]\n" \
R_"a[148:151],v226 offset:15360\n" \
M_"v[144:147],a[236:239],a[152:155],v[144:147]\n" \
T_"a[226:227],v250 offset:8712\n" \
"buffer_store_dwordx4 v[72:75],v240,s[52:55],s61 offen offset:1024 sc1\n" \
C_"v80,v80,v81\n" \
M_"v[148:151],a[240:243],a[152:155],v[148:151]\n" \
T_"a[228:229],v251 offset:8192\n" \
C_"v81,v82,v83\n" \
C_"v82,v84,v85\n" \
M_"v[152:155],a[244:247],a[152:155],v[152:155]\n" \
T_"a[230:231],v251 offset:8704\n" \
C_"v83,v86,v87\n" \
M_"v[156:159],a[248:251],a[152:155],v[156:159]\n" \
R_"a[152:155],v226 offset:16384\n" \
M_"v[160:163],a[236:239],a[156:159],v[160:163]\n" \
T_"a[232:233],v251 offset:8200\n" \
"buffer_store_dwordx4 v[80:83],v240,s[52:55],s61 offen offset:2048 sc1\n" \
C_"v88,v88,v89\n" \
M_"v[164:167],a[240:243],a[156:159],v[164:167]\n" \
T_"a[234:235],v251 offset:8712\n" \
C_"v89,v90,v91\n" \
C_"v90,v92,v93\n" \
M_"v[168:171],a[244:247],a[156:159],v[168:171]\n" \
"buffer_load_dword v252,v248,s[84:87],0 offen sc1\n" \
C_"v91,v94,v95\n" \
M_"v[172:175],a[248:251],a[156:159],v[172:175]\n" \
R_"a[156:159],v226 offset:17408\n" \
S_"lgkmcnt(5)\n" \
M_"v[176:179],a[236:239],a[160:163],v[176:179]\n" \
"buffer_store_dwordx4 v[88:91],v240,s[52:55],s61 offen offset:3072 sc1\n" \
C_"v96,v96,v97\n" \
C_"v97,v98,v99\n" \
M_"v[180:183],a[240:243],a[160:163],v[180:183]\n" \
C_"v98,v100,v101\n" \
C_"v99,v102,v103\n" \
A_"s61,s72,12288\n" \
M_"v[184:187],a[244:247],a[160:163],v[184:187]\n" \
M_"v[188:191],a[248:251],a[160:163],v[188:191]\n" \
R_"a[160:163],v226 offset:18432\n" \
M_"v[192:195],a[236:239],a[164:167],v[192:195]\n" \
"buffer_store_dwordx4 v[96:99],v240,s[52:55],s61 offen sc1\n" \
C_"v104,v104,v105\n" \
C_"v105,v106,v107\n" \
M_"v[196:199],a[240:243],a[164:167],v[196:199]\n" \
C_"v106,v108,v109\n" \
C_"v107,v110,v111\n" \
M_"v[200:203],a[244:247],a[164:167],v[200:203]\n" \
M_"v[204:207],a[248:251],a[164:167],v[204:207]\n" \
R_"a[164:167],v226 offset:19456\n" \
M_"v[208:211],a[236:239],a[168:171],v[208:211]\n" \
"buffer_store_dwordx4 v[104:107],v240,s[52:55],s61 offen offset:1024 sc1\n" \
C_"v232,v0,v1\n" \
C_"v233,v2,v3\n" \
M_"v[212:215],a[240:243],a[168:171],v[212:215]\n" \
C_"v234,v4,v5\n" \
C_"v235,v6,v7\n" \
A_"s61,s72,0\n" \
M_"v[216:219],a[244:247],a[168:171],v[216:219]\n" \
M_"v[220:223],a[248:251],a[168:171],v[220:223]\n" \
R_"a[168:171],v226 offset:20480\n" \
M_"v[112:115],a[112:115],a[144:147],v[112:115]\n" \
"buffer_store_dwordx4 v[232:235],v247,s[52:55],s61 offen sc1\n" \
C_"v236,v8,v9\n" \
C_"v237,v10,v11\n" \
M_"v[116:119],a[116:119],a[144:147],v[116:119]\n" \
C_"v238,v12,v13\n" \
C_"v239,v14,v15\n" \
M_"v[120:123],a[120:123],a[144:147],v[120:123]\n" \
M_"v[124:127],a[124:127],a[144:147],v[124:127]\n" \
R_"a[144:147],v226 offset:21504\n" \
M_"v[128:131],a[112:115],a[148:151],v[128:131]\n" \
"buffer_store_dwordx4 v[236:239],v247,s[52:55],s61 offen offset:1024 sc1\n" \
M_"v[132:135],a[116:119],a[148:151],v[132:135]\n" \
M_"v[136:139],a[120:123],a[148:151],v[136:139]\n" \
M_"v[140:143],a[124:127],a[148:151],v[140:143]\n" \
R_"a[148:151],v226 offset:22528\n" \
S_"lgkmcnt(2)\n" \
M_"v[144:147],a[112:115],a[152:155],v[144:147]\n" \
M_"v[148:151],a[116:119],a[152:155],v[148:151]\n" \
M_"v[152:155],a[120:123],a[152:155],v[152:155]\n" \
M_"v[156:159],a[124:127],a[152:155],v[156:159]\n" \
R_"a[152:155],v226 offset:23552\n" \
M_"v[160:163],a[112:115],a[156:159],v[160:163]\n" \
M_"v[164:167],a[116:119],a[156:159],v[164:167]\n" \
M_"v[168:171],a[120:123],a[156:159],v[168:171]\n" \
M_"v[172:175],a[124:127],a[156:159],v[172:175]\n" \
R_"a[156:159],v226 offset:24576\n" \
M_"v[176:179],a[112:115],a[160:163],v[176:179]\n" \
M_"v[180:183],a[116:119],a[160:163],v[180:183]\n" \
M_"v[184:187],a[120:123],a[160:163],v[184:187]\n" \
M_"v[188:191],a[124:127],a[160:163],v[188:191]\n" \
R_"a[160:163],v226 offset:25600\n" \
M_"v[192:195],a[112:115],a[164:167],v[192:195]\n" \
M_"v[196:199],a[116:119],a[164:167],v[196:199]\n" \
M_"v[200:203],a[120:123],a[164:167],v[200:203]\n" \
M_"v[204:207],a[124:127],a[164:167],v[204:207]\n" \
R_"a[164:167],v226 offset:26624\n" \
M_"v[208:211],a[112:115],a[168:171],v[208:211]\n" \
M_"v[212:215],a[116:119],a[168:171],v[212:215]\n" \
M_"v[216:219],a[120:123],a[168:171],v[216:219]\n" \
M_"v[220:223],a[124:127],a[168:171],v[220:223]\n" \
R_"a[168:171],v226 offset:27648\n" \
S_"lgkmcnt(2)\n" \
M_"v[112:115],a[128:131],a[144:147],v[112:115]\n" \
M_"v[116:119],a[132:135],a[144:147],v[116:119]\n" \
M_"v[120:123],a[136:139],a[144:147],v[120:123]\n" \
M_"v[124:127],a[140:143],a[144:147],v[124:127]\n" \
R_"a[144:147],v227 offset:0\n" \
M_"v[128:131],a[128:131],a[148:151],v[128:131]\n" \
M_"v[132:135],a[132:135],a[148:151],v[132:135]\n" \
M_"v[136:139],a[136:139],a[148:151],v[136:139]\n" \
M_"v[140:143],a[140:143],a[148:151],v[140:143]\n" \
R_"a[148:151],v227 offset:1024\n" \
M_"v[144:147],a[128:131],a[152:155],v[144:147]\n" \
M_"v[148:151],a[132:135],a[152:155],v[148:151]\n" \
M_"v[152:155],a[136:139],a[152:155],v[152:155]\n" \
M_"v[156:159],a[140:143],a[152:155],v[156:159]\n" \
R_"a[152:155],v227 offset:2048\n" \
M_"v[160:163],a[128:131],a[156:159],v[160:163]\n" \
M_"v[164:167],a[132:135],a[156:159],v[164:167]\n" \
M_"v[168:171],a[136:139],a[156:159],v[168:171]\n" \
S_"vmcnt(5)\n" \
"v_cmp_gt_u32 vcc,1,v252\n" \
"s_cbranch_vccnz LWA0_%=\n" \
"LWA0d_%=:\n" \
M_"v[172:175],a[140:143],a[156:159],v[172:175]\n" \
R_"a[156:159],v227 offset:3072\n" \
M_"v[176:179],a[128:131],a[160:163],v[176:179]\n" \
A_"s62,s76,s8\n" \
L_"a[16:19],v240,s[52:55],s62 offen sc1\n" \
L_"a[20:23],v240,s[52:55],s62 offen offset:1024 sc1\n" \
M_"v[180:183],a[132:135],a[160:163],v[180:183]\n" \
A_"s62,s76,s9\n" \
L_"a[24:27],v240,s[52:55],s62 offen sc1\n" \
L_"a[28:31],v240,s[52:55],s62 offen offset:1024 sc1\n" \
M_"v[184:187],a[136:139],a[160:163],v[184:187]\n" \
A_"s62,s76,s10\n" \
L_"a[32:35],v240,s[52:55],s62 offen sc1\n" \
L_"a[36:39],v240,s[52:55],s62 offen offset:1024 sc1\n" \
M_"v[188:191],a[140:143],a[160:163],v[188:191]\n" \
R_"a[160:163],v227 offset:4096\n" \
S_"lgkmcnt(2)\n" \
M_"v[192:195],a[128:131],a[164:167],v[192:195]\n" \
M_"v[196:199],a[132:135],a[164:167],v[196:199]\n" \
A_"s62,s76,s11\n" \
L_"a[40:43],v240,s[52:55],s62 offen sc1\n" \
L_"a[44:47],v240,s[52:55],s62 offen offset:1024 sc1\n" \
M_"v[200:203],a[136:139],a[164:167],v[200:203]\n" \
A_"s62,s76,s12\n" \
L_"a[48:51],v240,s[52:55],s62 offen sc1\n" \
L_"a[52:55],v240,s[52:55],s62 offen offset:1024 sc1\n" \
M_"v[204:207],a[140:143],a[164:167],v[204:207]\n" \
R_"a[164:167],v227 offset:5120\n" \
M_"v[208:211],a[128:131],a[168:171],v[208:211]\n" \
A_"s62,s76,s13\n" \
L_"a[56:59],v240,s[52:55],s62 offen sc1\n" \
L_"a[60:63],v240,s[52:55],s62 offen offset:1024 sc1\n" \
M_"v[212:215],a[132:135],a[168:171],v[212:215]\n" \
A_"s62,s76,s14\n" \
L_"a[64:67],v240,s[52:55],s62 offen sc1\n" \
L_"a[68:71],v240,s[52:55],s62 offen offset:1024 sc1\n" \
M_"v[216:219],a[136:139],a[168:171],v[216:219]\n" \
M_"v[220:223],a[140:143],a[168:171],v[220:223]\n" \
R_"a[168:171],v227 offset:6144\n" \
M_"v[112:115],a[172:175],a[144:147],v[112:115]\n" \
M_"v[116:119],a[176:179],a[144:147],v[116:119]\n" \
M_"v[120:123],a[180:183],a[144:147],v[120:123]\n" \
M_"v[124:127],a[184:187],a[144:147],v[124:127]\n" \
R_"a[144:147],v227 offset:7168\n" \
M_"v[128:131],a[172:175],a[148:151],v[128:131]\n" \
M_"v[132:135],a[176:179],a[148:151],v[132:135]\n" \
M_"v[136:139],a[180:183],a[148:151],v[136:139]\n" \
M_"v[140:143],a[184:187],a[148:151],v[140:143]\n" \
R_"a[148:151],v227 offset:8192\n" \
M_"v[144:147],a[172:175],a[152:155],v[144:147]\n" \
M_"v[148:151],a[176:179],a[152:155],v[148:151]\n" \
M_"v[152:155],a[180:183],a[152:155],v[152:155]\n" \
M_"v[156:159],a[184:187],a[152:155],v[156:159]\n" \
R_"a[152:155],v227 offset:9216\n" \
S_"lgkmcnt(2)\n" \
M_"v[160:163],a[172:175],a[156:159],v[160:163]\n" \
M_"v[164:167],a[176:179],a[156:159],v[164:167]\n" \
M_"v[168:171],a[180:183],a[156:159],v[168:171]\n" \
M_"v[172:175],a[184:187],a[156:159],v[172:175]\n" \
R_"a[156:159],v227 offset:10240\n" \
M_"v[176:179],a[172:175],a[160:163],v[176:179]\n" \
M_"v[180:183],a[176:179],a[160:163],v[180:183]\n" \
M_"v[184:187],a[180:183],a[160:163],v[184:187]\n" \
M_"v[188:191],a[184:187],a[160:163],v[188:191]\n" \
R_"a[160:163],v227 offset:11264\n" \
M_"v[192:195],a[172:175],a[164:167],v[192:195]\n" \
M_"v[196:199],a[176:179],a[164:167],v[196:199]\n" \
M_"v[200:203],a[180:183],a[164:167],v[200:203]\n" \
M_"v[204:207],a[184:187],a[164:167],v[204:207]\n" \
R_"a[164:167],v227 offset:12288\n" \
M_"v[208:211],a[172:175],a[168:171],v[208:211]\n" \
M_"v[212:215],a[176:179],a[168:171],v[212:215]\n" \
M_"v[216:219],a[180:183],a[168:171],v[216:219]\n" \
M_"v[220:223],a[184:187],a[168:171],v[220:223]\n" \
R_"a[168:171],v227 offset:13312\n" \
M_"v[112:115],a[188:191],a[144:147],v[112:115]\n" \
M_"v[116:119],a[192:195],a[144:147],v[116:119]\n" \
M_"v[120:123],a[196:199],a[144:147],v[120:123]\n" \
M_"v[124:127],a[200:203],a[144:147],v[124:127]\n" \
R_"a[144:147],v227 offset:14336\n" \
S_"lgkmcnt(2)\n" \
M_"v[128:131],a[188:191],a[148:151],v[128:131]\n" \
M_"v[132:135],a[192:195],a[148:151],v[132:135]\n" \
M_"v[136:139],a[196:199],a[148:151],v[136:139]\n" \
M_"v[140:143],a[200:203],a[148:151],v[140:143]\n" \
R_"a[148:151],v227 offset:15360\n" \
M_"v[144:147],a[188:191],a[152:155],v[144:147]\n" \
M_"v[148:151],a[192:195],a[152:155],v[148:151]\n" \
M_"v[152:155],a[196:199],a[152:155],v[152:155]\n" \
M_"v[156:159],a[200:203],a[152:155],v[156:159]\n" \
R_"a[152:155],v227 offset:16384\n" \
M_"v[160:163],a[188:191],a[156:159],v[160:163]\n" \
M_"v[164:167],a[192:195],a[156:159],v[164:167]\n" \
M_"v[168:171],a[196:199],a[156:159],v[168:171]\n" \
M_"v[172:175],a[200:203],a[156:159],v[172:175]\n" \
R_"a[156:159],v227 offset:17408\n" \
M_"v[176:179],a[188:191],a[160:163],v[176:179]\n" \
M_"v[180:183],a[192:195],a[160:163],v[180:183]\n" \
M_"v[184:187],a[196:199],a[160:163],v[184:187]\n" \
M_"v[188:191],a[200:203],a[160:163],v[188:191]\n" \
R_"a[160:163],v227 offset:18432\n" \
M_"v[192:195],a[188:191],a[164:167],v[192:195]\n" \
M_"v[196:199],a[192:195],a[164:167],v[196:199]\n" \
M_"v[200:203],a[196:199],a[164:167],v[200:203]\n" \
M_"v[204:207],a[200:203],a[164:167],v[204:207]\n" \
R_"a[164:167],v227 offset:19456\n" \
S_"lgkmcnt(2)\n" \
M_"v[208:211],a[188:191],a[168:171],v[208:211]\n" \
M_"v[212:215],a[192:195],a[168:171],v[212:215]\n" \
M_"v[216:219],a[196:199],a[168:171],v[216:219]\n" \
M_"v[220:223],a[200:203],a[168:171],v[220:223]\n" \
R_"a[168:171],v227 offset:20480\n" \
M_"v[112:115],a[204:207],a[144:147],v[112:115]\n" \
M_"v[116:119],a[208:211],a[144:147],v[116:119]\n" \
M_"v[120:123],a[212:215],a[144:147],v[120:123]\n" \
M_"v[124:127],a[216:219],a[144:147],v[124:127]\n" \
R_"a[144:147],v227 offset:21504\n" \
M_"v[128:131],a[204:207],a[148:151],v[128:131]\n" \
M_"v[132:135],a[208:211],a[148:151],v[132:135]\n" \
M_"v[136:139],a[212:215],a[148:151],v[136:139]\n" \
M_"v[140:143],a[216:219],a[148:151],v[140:143]\n" \
R_"a[148:151],v227 offset:22528\n" \
M_"v[144:147],a[204:207],a[152:155],v[144:147]\n" \
M_"v[148:151],a[208:211],a[152:155],v[148:151]\n" \
M_"v[152:155],a[212:215],a[152:155],v[152:155]\n" \
M_"v[156:159],a[216:219],a[152:155],v[156:159]\n" \
R_"a[152:155],v227 offset:23552\n" \
M_"v[160:163],a[204:207],a[156:159],v[160:163]\n" \
M_"v[164:167],a[208:211],a[156:159],v[164:167]\n" \
M_"v[168:171],a[212:215],a[156:159],v[168:171]\n" \
M_"v[172:175],a[216:219],a[156:159],v[172:175]\n" \
R_"a[156:159],v227 offset:24576\n" \
S_"lgkmcnt(2)\n" \
M_"v[176:179],a[204:207],a[160:163],v[176:179]\n" \
M_"v[180:183],a[208:211],a[160:163],v[180:183]\n" \
M_"v[184:187],a[212:215],a[160:163],v[184:187]\n" \
M_"v[188:191],a[216:219],a[160:163],v[188:191]\n" \
R_"a[160:163],v227 offset:25600\n" \
M_"v[192:195],a[204:207],a[164:167],v[192:195]\n" \
M_"v[196:199],a[208:211],a[164:167],v[196:199]\n" \
M_"v[200:203],a[212:215],a[164:167],v[200:203]\n" \
M_"v[204:207],a[216:219],a[164:167],v[204:207]\n" \
R_"a[164:167],v227 offset:26624\n" \
M_"v[208:211],a[204:207],a[168:171],v[208:211]\n" \
M_"v[212:215],a[208:211],a[168:171],v[212:215]\n" \
M_"v[216:219],a[212:215],a[168:171],v[216:219]\n" \
M_"v[220:223],a[216:219],a[168:171],v[220:223]\n" \
R_"a[168:171],v227 offset:27648\n" \
M_"v[112:115],a[220:223],a[144:147],v[112:115]\n" \
M_"v[116:119],a[224:227],a[144:147],v[116:119]\n" \
M_"v[120:123],a[228:231],a[144:147],v[120:123]\n" \
M_"v[124:127],a[232:235],a[144:147],v[124:127]\n" \
M_"v[128:131],a[220:223],a[148:151],v[128:131]\n" \
M_"v[132:135],a[224:227],a[148:151],v[132:135]\n" \
M_"v[136:139],a[228:231],a[148:151],v[136:139]\n" \
M_"v[140:143],a[232:235],a[148:151],v[140:143]\n" \
S_"lgkmcnt(0)\n" \
M_"v[144:147],a[220:223],a[152:155],v[144:147]\n" \
M_"v[148:151],a[224:227],a[152:155],v[148:151]\n" \
M_"v[152:155],a[228:231],a[152:155],v[152:155]\n" \
M_"v[156:159],a[232:235],a[152:155],v[156:159]\n" \
M_"v[160:163],a[220:223],a[156:159],v[160:163]\n" \
M_"v[164:167],a[224:227],a[156:159],v[164:167]\n" \
M_"v[168:171],a[228:231],a[156:159],v[168:171]\n" \
S_"vmcnt(14)\n" \
S_"vmcnt(0)\n" \
"v_mov_b32 v253,2\n" \
"buffer_store_dword v253,v246,s[84:87],0 offen sc1\n" \
M_"v[172:175],a[232:235],a[156:159],v[172:175]\n" \
M_"v[176:179],a[220:223],a[160:163],v[176:179]\n" \
M_"v[180:183],a[224:227],a[160:163],v[180:183]\n" \
M_"v[184:187],a[228:231],a[160:163],v[184:187]\n" \
M_"v[188:191],a[232:235],a[160:163],v[188:191]\n" \
M_"v[192:195],a[220:223],a[164:167],v[192:195]\n" \
M_"v[196:199],a[224:227],a[164:167],v[196:199]\n" \
M_"v[200:203],a[228:231],a[164:167],v[200:203]\n" \
M_"v[204:207],a[232:235],a[164:167],v[204:207]\n" \
M_"v[208:211],a[220:223],a[168:171],v[208:211]\n" \
M_"v[212:215],a[224:227],a[168:171],v[212:215]\n" \
M_"v[216:219],a[228:231],a[168:171],v[216:219]\n" \
M_"v[220:223],a[232:235],a[168:171],v[220:223]\n" \
"s_nop 7\n" \
C_"v128,v128,v129\n" \
C_"v129,v130,v131\n" \
C_"v130,v132,v133\n" \
C_"v131,v134,v135\n" \
A_"s61,s73,0\n" \
"buffer_store_dwordx4 v[128:131],v240,s[52:55],s61 offen offset:2048 sc1\n" \
C_"v136,v136,v137\n" \
C_"v137,v138,v139\n" \
C_"v138,v140,v141\n" \
C_"v139,v142,v143\n" \
"buffer_store_dwordx4 v[136:139],v240,s[52:55],s61 offen offset:3072 sc1\n" \
C_"v144,v144,v145\n" \
C_"v145,v146,v147\n" \
C_"v146,v148,v149\n" \
C_"v147,v150,v151\n" \
A_"s61,s73,4096\n" \
"buffer_store_dwordx4 v[144:147],v240,s[52:55],s61 offen sc1\n" \
C_"v152,v152,v153\n" \
C_"v153,v154,v155\n" \
C_"v154,v156,v157\n" \
C_"v155,v158,v159\n" \
"buffer_store_dwordx4 v[152:155],v240,s[52:55],s61 offen offset:1024 sc1\n" \
C_"v160,v160,v161\n" \
C_"v161,v162,v163\n" \
C_"v162,v164,v165\n" \
C_"v163,v166,v167\n" \
"buffer_store_dwordx4 v[160:163],v240,s[52:55],s61 offen offset:2048 sc1\n" \
C_"v168,v168,v169\n" \
C_"v169,v170,v171\n" \
C_"v170,v172,v173\n" \
C_"v171,v174,v175\n" \
"buffer_store_dwordx4 v[168:171],v240,s[52:55],s61 offen offset:3072 sc1\n" \
C_"v176,v176,v177\n" \
C_"v177,v178,v179\n" \
C_"v178,v180,v181\n" \
C_"v179,v182,v183\n" \
A_"s61,s73,8192\n" \
"buffer_store_dwordx4 v[176:179],v240,s[52:55],s61 offen sc1\n" \
C_"v184,v184,v185\n" \
C_"v185,v186,v187\n" \
C_"v186,v188,v189\n" \
C_"v187,v190,v191\n" \
"buffer_store_dwordx4 v[184:187],v240,s[52:55],s61 offen offset:1024 sc1\n" \
C_"v192,v192,v193\n" \
C_"v193,v194,v195\n" \
C_"v194,v196,v197\n" \
C_"v195,v198,v199\n" \
"buffer_store_dwordx4 v[192:195],v240,s[52:55],s61 offen offset:2048 sc1\n" \
C_"v200,v200,v201\n" \
C_"v201,v202,v203\n" \
C_"v202,v204,v205\n" \
C_"v203,v206,v207\n" \
"buffer_store_dwordx4 v[200:203],v240,s[52:55],s61 offen offset:3072 sc1\n" \
C_"v208,v208,v209\n" \
C_"v209,v210,v211\n" \
C_"v210,v212,v213\n" \
C_"v211,v214,v215\n" \
A_"s61,s73,12288\n" \
"buffer_store_dwordx4 v[208:211],v240,s[52:55],s61 offen sc1\n" \
C_"v216,v216,v217\n" \
C_"v217,v218,v219\n" \
C_"v218,v220,v221\n" \
C_"v219,v222,v223\n" \
"buffer_store_dwordx4 v[216:219],v240,s[52:55],s61 offen offset:1024 sc1\n" \
C_"v232,v112,v113\n" \
C_"v233,v114,v115\n" \
C_"v234,v116,v117\n" \
C_"v235,v118,v119\n" \
A_"s61,s73,0\n" \
"buffer_store_dwordx4 v[232:235],v247,s[52:55],s61 offen sc1\n" \
C_"v236,v120,v121\n" \
C_"v237,v122,v123\n" \
C_"v238,v124,v125\n" \
C_"v239,v126,v127\n" \
"buffer_store_dwordx4 v[236:239],v247,s[52:55],s61 offen offset:1024 sc1\n" \
"buffer_load_dword v252,v248,s[84:87],0 offen sc1\n" \
G_"v72,a0\n" \
G_"v73,a1\n" \
G_"v74,a2\n" \
G_"v75,a3\n" \
G_"v76,a4\n" \
G_"v77,a5\n" \
G_"v78,a6\n" \
G_"v79,a7\n" \
G_"v80,a8\n" \
G_"v81,a9\n" \
G_"v82,a10\n" \
G_"v83,a11\n" \
G_"v84,a12\n" \
G_"v85,a13\n" \
G_"v86,a14\n" \
G_"v87,a15\n" \
G_"v16,a16\n" \
G_"v17,a17\n" \
G_"v18,a18\n" \
G_"v19,a19\n" \
G_"v20,a20\n" \
G_"v21,a21\n" \
G_"v22,a22\n" \
G_"v23,a23\n" \
G_"v24,a24\n" \
G_"v25,a25\n" \
G_"v26,a26\n" \
G_"v27,a27\n" \
G_"v28,a28\n" \
G_"v29,a29\n" \
G_"v30,a30\n" \
G_"v31,a31\n" \
G_"v32,a32\n" \
G_"v33,a33\n" \
G_"v34,a34\n" \
G_"v35,a35\n" \
G_"v36,a36\n" \
G_"v37,a37\n" \
G_"v38,a38\n" \
G_"v39,a39\n" \
G_"v40,a40\n" \
G_"v41,a41\n" \
G_"v42,a42\n" \
G_"v43,a43\n" \
G_"v44,a44\n" \
G_"v45,a45\n" \
G_"v46,a46\n" \
G_"v47,a47\n" \
G_"v48,a48\n" \
G_"v49,a49\n" \
G_"v50,a50\n" \
G_"v51,a51\n" \
G_"v52,a52\n" \
G_"v53,a53\n" \
G_"v54,a54\n" \
G_"v55,a55\n" \
G_"v56,a56\n" \
G_"v57,a57\n" \
G_"v58,a58\n" \
G_"v59,a59\n" \
G_"v60,a60\n" \
G_"v61,a61\n" \
G_"v62,a62\n" \
G_"v63,a63\n" \
G_"v64,a64\n" \
G_"v65,a65\n" \
G_"v66,a66\n" \
G_"v67,a67\n" \
G_"v68,a68\n" \
G_"v69,a69\n" \
G_"v70,a70\n" \
G_"v71,a71\n" \
"v_lshlrev_b32 v164,16,v16\n" \
"v_and_b32 v165,0xffff0000,v16\n" \
"v_pk_add_f32 v[72:73],v[72:73],v[164:165]\n" \
"v_lshlrev_b32 v166,16,v17\n" \
"v_and_b32 v167,0xffff0000,v17\n" \
"v_pk_add_f32 v[74:75],v[74:75],v[166:167]\n" \
"v_lshlrev_b32 v172,16,v18\n" \
"v_and_b32 v173,0xffff0000,v18\n" \
"v_pk_add_f32 v[76:77],v[76:77],v[172:173]\n" \
"v_lshlrev_b32 v174,16,v19\n" \
"v_and_b32 v175,0xffff0000,v19\n" \
"v_pk_add_f32 v[78:79],v[78:79],v[174:175]\n" \
"v_lshlrev_b32 v164,16,v20\n" \
"v_and_b32 v165,0xffff0000,v20\n" \
"v_pk_add_f32 v[80:81],v[80:81],v[164:165]\n" \
"v_lshlrev_b32 v166,16,v21\n" \
"v_and_b32 v167,0xffff0000,v21\n" \
"v_pk_add_f32 v[82:83],v[82:83],v[166:167]\n" \
"v_lshlrev_b32 v172,16,v22\n" \
"v_and_b32 v173,0xffff0000,v22\n" \
"v_pk_add_f32 v[84:85],v[84:85],v[172:173]\n" \
"v_lshlrev_b32 v174,16,v23\n" \
"v_and_b32 v175,0xffff0000,v23\n" \
"v_pk_add_f32 v[86:87],v[86:87],v[174:175]\n" \
"v_lshlrev_b32 v164,16,v24\n" \
"v_and_b32 v165,0xffff0000,v24\n" \
"v_pk_add_f32 v[72:73],v[72:73],v[164:165]\n" \
"v_lshlrev_b32 v166,16,v25\n" \
"v_and_b32 v167,0xffff0000,v25\n" \
"v_pk_add_f32 v[74:75],v[74:75],v[166:167]\n" \
"v_lshlrev_b32 v172,16,v26\n" \
"v_and_b32 v173,0xffff0000,v26\n" \
"v_pk_add_f32 v[76:77],v[76:77],v[172:173]\n" \
"v_lshlrev_b32 v174,16,v27\n" \
"v_and_b32 v175,0xffff0000,v27\n" \
"v_pk_add_f32 v[78:79],v[78:79],v[174:175]\n" \
"v_lshlrev_b32 v164,16,v28\n" \
"v_and_b32 v165,0xffff0000,v28\n" \
"v_pk_add_f32 v[80:81],v[80:81],v[164:165]\n" \
"v_lshlrev_b32 v166,16,v29\n" \
"v_and_b32 v167,0xffff0000,v29\n" \
"v_pk_add_f32 v[82:83],v[82:83],v[166:167]\n" \
"v_lshlrev_b32 v172,16,v30\n" \
"v_and_b32 v173,0xffff0000,v30\n" \
"v_pk_add_f32 v[84:85],v[84:85],v[172:173]\n" \
"v_lshlrev_b32 v174,16,v31\n" \
"v_and_b32 v175,0xffff0000,v31\n" \
"v_pk_add_f32 v[86:87],v[86:87],v[174:175]\n" \
"v_lshlrev_b32 v164,16,v32\n" \
"v_and_b32 v165,0xffff0000,v32\n" \
"v_pk_add_f32 v[72:73],v[72:73],v[164:165]\n" \
"v_lshlrev_b32 v166,16,v33\n" \
"v_and_b32 v167,0xffff0000,v33\n" \
"v_pk_add_f32 v[74:75],v[74:75],v[166:167]\n" \
"v_lshlrev_b32 v172,16,v34\n" \
"v_and_b32 v173,0xffff0000,v34\n" \
"v_pk_add_f32 v[76:77],v[76:77],v[172:173]\n" \
"v_lshlrev_b32 v174,16,v35\n" \
"v_and_b32 v175,0xffff0000,v35\n" \
"v_pk_add_f32 v[78:79],v[78:79],v[174:175]\n" \
"v_lshlrev_b32 v164,16,v36\n" \
"v_and_b32 v165,0xffff0000,v36\n" \
"v_pk_add_f32 v[80:81],v[80:81],v[164:165]\n" \
"v_lshlrev_b32 v166,16,v37\n" \
"v_and_b32 v167,0xffff0000,v37\n" \
"v_pk_add_f32 v[82:83],v[82:83],v[166:167]\n" \
"v_lshlrev_b32 v172,16,v38\n" \
"v_and_b32 v173,0xffff0000,v38\n" \
"v_pk_add_f32 v[84:85],v[84:85],v[172:173]\n" \
"v_lshlrev_b32 v174,16,v39\n" \
"v_and_b32 v175,0xffff0000,v39\n" \
"v_pk_add_f32 v[86:87],v[86:87],v[174:175]\n" \
"v_lshlrev_b32 v164,16,v40\n" \
"v_and_b32 v165,0xffff0000,v40\n" \
"v_pk_add_f32 v[72:73],v[72:73],v[164:165]\n" \
"v_lshlrev_b32 v166,16,v41\n" \
"v_and_b32 v167,0xffff0000,v41\n" \
"v_pk_add_f32 v[74:75],v[74:75],v[166:167]\n" \
"v_lshlrev_b32 v172,16,v42\n" \
"v_and_b32 v173,0xffff0000,v42\n" \
"v_pk_add_f32 v[76:77],v[76:77],v[172:173]\n" \
"v_lshlrev_b32 v174,16,v43\n" \
"v_and_b32 v175,0xffff0000,v43\n" \
"v_pk_add_f32 v[78:79],v[78:79],v[174:175]\n" \
"v_lshlrev_b32 v164,16,v44\n" \
"v_and_b32 v165,0xffff0000,v44\n" \
"v_pk_add_f32 v[80:81],v[80:81],v[164:165]\n" \
"v_lshlrev_b32 v166,16,v45\n" \
"v_and_b32 v167,0xffff0000,v45\n" \
"v_pk_add_f32 v[82:83],v[82:83],v[166:167]\n" \
"v_lshlrev_b32 v172,16,v46\n" \
"v_and_b32 v173,0xffff0000,v46\n" \
"v_pk_add_f32 v[84:85],v[84:85],v[172:173]\n" \
"v_lshlrev_b32 v174,16,v47\n" \
"v_and_b32 v175,0xffff0000,v47\n" \
"v_pk_add_f32 v[86:87],v[86:87],v[174:175]\n" \
"v_lshlrev_b32 v164,16,v48\n" \
"v_and_b32 v165,0xffff0000,v48\n" \
"v_pk_add_f32 v[72:73],v[72:73],v[164:165]\n" \
"v_lshlrev_b32 v166,16,v49\n" \
"v_and_b32 v167,0xffff0000,v49\n" \
"v_pk_add_f32 v[74:75],v[74:75],v[166:167]\n" \
"v_lshlrev_b32 v172,16,v50\n" \
"v_and_b32 v173,0xffff0000,v50\n" \
"v_pk_add_f32 v[76:77],v[76:77],v[172:173]\n" \
"v_lshlrev_b32 v174,16,v51\n" \
"v_and_b32 v175,0xffff0000,v51\n" \
"v_pk_add_f32 v[78:79],v[78:79],v[174:175]\n" \
"v_lshlrev_b32 v164,16,v52\n" \
"v_and_b32 v165,0xffff0000,v52\n" \
"v_pk_add_f32 v[80:81],v[80:81],v[164:165]\n" \
"v_lshlrev_b32 v166,16,v53\n" \
"v_and_b32 v167,0xffff0000,v53\n" \
"v_pk_add_f32 v[82:83],v[82:83],v[166:167]\n" \
"v_lshlrev_b32 v172,16,v54\n" \
"v_and_b32 v173,0xffff0000,v54\n" \
"v_pk_add_f32 v[84:85],v[84:85],v[172:173]\n" \
"v_lshlrev_b32 v174,16,v55\n" \
"v_and_b32 v175,0xffff0000,v55\n" \
"v_pk_add_f32 v[86:87],v[86:87],v[174:175]\n" \
"v_lshlrev_b32 v164,16,v56\n" \
"v_and_b32 v165,0xffff0000,v56\n" \
"v_pk_add_f32 v[72:73],v[72:73],v[164:165]\n" \
"v_lshlrev_b32 v166,16,v57\n" \
"v_and_b32 v167,0xffff0000,v57\n" \
"v_pk_add_f32 v[74:75],v[74:75],v[166:167]\n" \
"v_lshlrev_b32 v172,16,v58\n" \
"v_and_b32 v173,0xffff0000,v58\n" \
"v_pk_add_f32 v[76:77],v[76:77],v[172:173]\n" \
"v_lshlrev_b32 v174,16,v59\n" \
"v_and_b32 v175,0xffff0000,v59\n" \
"v_pk_add_f32 v[78:79],v[78:79],v[174:175]\n" \
"v_lshlrev_b32 v164,16,v60\n" \
"v_and_b32 v165,0xffff0000,v60\n" \
"v_pk_add_f32 v[80:81],v[80:81],v[164:165]\n" \
"v_lshlrev_b32 v166,16,v61\n" \
"v_and_b32 v167,0xffff0000,v61\n" \
"v_pk_add_f32 v[82:83],v[82:83],v[166:167]\n" \
"v_lshlrev_b32 v172,16,v62\n" \
"v_and_b32 v173,0xffff0000,v62\n" \
"v_pk_add_f32 v[84:85],v[84:85],v[172:173]\n" \
"v_lshlrev_b32 v174,16,v63\n" \
"v_and_b32 v175,0xffff0000,v63\n" \
"v_pk_add_f32 v[86:87],v[86:87],v[174:175]\n" \
"v_lshlrev_b32 v164,16,v64\n" \
"v_and_b32 v165,0xffff0000,v64\n" \
"v_pk_add_f32 v[72:73],v[72:73],v[164:165]\n" \
"v_lshlrev_b32 v166,16,v65\n" \
"v_and_b32 v167,0xffff0000,v65\n" \
"v_pk_add_f32 v[74:75],v[74:75],v[166:167]\n" \
"v_lshlrev_b32 v172,16,v66\n" \
"v_and_b32 v173,0xffff0000,v66\n" \
"v_pk_add_f32 v[76:77],v[76:77],v[172:173]\n" \
"v_lshlrev_b32 v174,16,v67\n" \
"v_and_b32 v175,0xffff0000,v67\n" \
"v_pk_add_f32 v[78:79],v[78:79],v[174:175]\n" \
"v_lshlrev_b32 v164,16,v68\n" \
"v_and_b32 v165,0xffff0000,v68\n" \
"v_pk_add_f32 v[80:81],v[80:81],v[164:165]\n" \
"v_lshlrev_b32 v166,16,v69\n" \
"v_and_b32 v167,0xffff0000,v69\n" \
"v_pk_add_f32 v[82:83],v[82:83],v[166:167]\n" \
"v_lshlrev_b32 v172,16,v70\n" \
"v_and_b32 v173,0xffff0000,v70\n" \
"v_pk_add_f32 v[84:85],v[84:85],v[172:173]\n" \
"v_lshlrev_b32 v174,16,v71\n" \
"v_and_b32 v175,0xffff0000,v71\n" \
"v_pk_add_f32 v[86:87],v[86:87],v[174:175]\n" \
C_"v72,v72,v73\n" \
C_"v73,v74,v75\n" \
C_"v74,v76,v77\n" \
C_"v75,v78,v79\n" \
C_"v80,v80,v81\n" \
C_"v81,v82,v83\n" \
C_"v82,v84,v85\n" \
C_"v83,v86,v87\n" \
S_"vmcnt(1)\n" \
S_"vmcnt(0)\n" \
"v_mov_b32 v253,3\n" \
"buffer_store_dword v253,v246,s[84:87],0 offen sc1\n" \
"v_cmp_gt_u32 vcc,3,v252\n" \
"s_cbranch_vccz LFTA_%=\n" \
"v_cmp_gt_u32 vcc,2,v252\n" \
"s_cbranch_vccnz LWA1_%=\n" \
"LWA1d_%=:\n" \
A_"s62,s77,s8\n" \
L_"v[88:91],v240,s[52:55],s62 offen sc1\n" \
L_"v[92:95],v240,s[52:55],s62 offen offset:1024 sc1\n" \
A_"s62,s77,s9\n" \
L_"v[96:99],v240,s[52:55],s62 offen sc1\n" \
L_"v[100:103],v240,s[52:55],s62 offen offset:1024 sc1\n" \
A_"s62,s77,s10\n" \
L_"v[104:107],v240,s[52:55],s62 offen sc1\n" \
L_"v[108:111],v240,s[52:55],s62 offen offset:1024 sc1\n" \
A_"s62,s77,s11\n" \
L_"v[132:135],v240,s[52:55],s62 offen sc1\n" \
L_"v[140:143],v240,s[52:55],s62 offen offset:1024 sc1\n" \
A_"s62,s77,s12\n" \
L_"v[148:151],v240,s[52:55],s62 offen sc1\n" \
L_"v[156:159],v240,s[52:55],s62 offen offset:1024 sc1\n" \
A_"s62,s77,s13\n" \
L_"v[180:183],v240,s[52:55],s62 offen sc1\n" \
L_"v[188:191],v240,s[52:55],s62 offen offset:1024 sc1\n" \
A_"s62,s77,s14\n" \
L_"v[196:199],v240,s[52:55],s62 offen sc1\n" \
L_"v[204:207],v240,s[52:55],s62 offen offset:1024 sc1\n" \
"buffer_load_dword v252,v248,s[84:87],0 offen sc1\n" \
S_"vmcnt(14)\n" \
"v_lshlrev_b32 v164,16,v88\n" \
"v_and_b32 v165,0xffff0000,v88\n" \
"v_pk_add_f32 v[0:1],v[0:1],v[164:165]\n" \
"v_lshlrev_b32 v166,16,v89\n" \
"v_and_b32 v167,0xffff0000,v89\n" \
"v_pk_add_f32 v[2:3],v[2:3],v[166:167]\n" \
"v_lshlrev_b32 v172,16,v90\n" \
"v_and_b32 v173,0xffff0000,v90\n" \
"v_pk_add_f32 v[4:5],v[4:5],v[172:173]\n" \
"v_lshlrev_b32 v174,16,v91\n" \
"v_and_b32 v175,0xffff0000,v91\n" \
"v_pk_add_f32 v[6:7],v[6:7],v[174:175]\n" \
S_"vmcnt(13)\n" \
"v_lshlrev_b32 v164,16,v92\n" \
"v_and_b32 v165,0xffff0000,v92\n" \
"v_pk_add_f32 v[8:9],v[8:9],v[164:165]\n" \
"v_lshlrev_b32 v166,16,v93\n" \
"v_and_b32 v167,0xffff0000,v93\n" \
"v_pk_add_f32 v[10:11],v[10:11],v[166:167]\n" \
"v_lshlrev_b32 v172,16,v94\n" \
"v_and_b32 v173,0xffff0000,v94\n" \
"v_pk_add_f32 v[12:13],v[12:13],v[172:173]\n" \
"v_lshlrev_b32 v174,16,v95\n" \
"v_and_b32 v175,0xffff0000,v95\n" \
"v_pk_add_f32 v[14:15],v[14:15],v[174:175]\n" \
S_"vmcnt(12)\n" \
"v_lshlrev_b32 v164,16,v96\n" \
"v_and_b32 v165,0xffff0000,v96\n" \
"v_pk_add_f32 v[0:1],v[0:1],v[164:165]\n" \
"v_lshlrev_b32 v166,16,v97\n" \
"v_and_b32 v167,0xffff0000,v97\n" \
"v_pk_add_f32 v[2:3],v[2:3],v[166:167]\n" \
"v_lshlrev_b32 v172,16,v98\n" \
"v_and_b32 v173,0xffff0000,v98\n" \
"v_pk_add_f32 v[4:5],v[4:5],v[172:173]\n" \
"v_lshlrev_b32 v174,16,v99\n" \
"v_and_b32 v175,0xffff0000,v99\n" \
"v_pk_add_f32 v[6:7],v[6:7],v[174:175]\n" \
S_"vmcnt(11)\n" \
"v_lshlrev_b32 v164,16,v100\n" \
"v_and_b32 v165,0xffff0000,v100\n" \
"v_pk_add_f32 v[8:9],v[8:9],v[164:165]\n" \
"v_lshlrev_b32 v166,16,v101\n" \
"v_and_b32 v167,0xffff0000,v101\n" \
"v_pk_add_f32 v[10:11],v[10:11],v[166:167]\n" \
"v_lshlrev_b32 v172,16,v102\n" \
"v_and_b32 v173,0xffff0000,v102\n" \
"v_pk_add_f32 v[12:13],v[12:13],v[172:173]\n" \
"v_lshlrev_b32 v174,16,v103\n" \
"v_and_b32 v175,0xffff0000,v103\n" \
"v_pk_add_f32 v[14:15],v[14:15],v[174:175]\n" \
S_"vmcnt(10)\n" \
"v_lshlrev_b32 v164,16,v104\n" \
"v_and_b32 v165,0xffff0000,v104\n" \
"v_pk_add_f32 v[0:1],v[0:1],v[164:165]\n" \
"v_lshlrev_b32 v166,16,v105\n" \
"v_and_b32 v167,0xffff0000,v105\n" \
"v_pk_add_f32 v[2:3],v[2:3],v[166:167]\n" \
"v_lshlrev_b32 v172,16,v106\n" \
"v_and_b32 v173,0xffff0000,v106\n" \
"v_pk_add_f32 v[4:5],v[4:5],v[172:173]\n" \
"v_lshlrev_b32 v174,16,v107\n" \
"v_and_b32 v175,0xffff0000,v107\n" \
"v_pk_add_f32 v[6:7],v[6:7],v[174:175]\n" \
S_"vmcnt(9)\n" \
"v_lshlrev_b32 v164,16,v108\n" \
"v_and_b32 v165,0xffff0000,v108\n" \
"v_pk_add_f32 v[8:9],v[8:9],v[164:165]\n" \
"v_lshlrev_b32 v166,16,v109\n" \
"v_and_b32 v167,0xffff0000,v109\n" \
"v_pk_add_f32 v[10:11],v[10:11],v[166:167]\n" \
"v_lshlrev_b32 v172,16,v110\n" \
"v_and_b32 v173,0xffff0000,v110\n" \
"v_pk_add_f32 v[12:13],v[12:13],v[172:173]\n" \
"v_lshlrev_b32 v174,16,v111\n" \
"v_and_b32 v175,0xffff0000,v111\n" \
"v_pk_add_f32 v[14:15],v[14:15],v[174:175]\n" \
S_"vmcnt(8)\n" \
"v_lshlrev_b32 v164,16,v132\n" \
"v_and_b32 v165,0xffff0000,v132\n" \
"v_pk_add_f32 v[0:1],v[0:1],v[164:165]\n" \
"v_lshlrev_b32 v166,16,v133\n" \
"v_and_b32 v167,0xffff0000,v133\n" \
"v_pk_add_f32 v[2:3],v[2:3],v[166:167]\n" \
"v_lshlrev_b32 v172,16,v134\n" \
"v_and_b32 v173,0xffff0000,v134\n" \
"v_pk_add_f32 v[4:5],v[4:5],v[172:173]\n" \
"v_lshlrev_b32 v174,16,v135\n" \
"v_and_b32 v175,0xffff0000,v135\n" \
"v_pk_add_f32 v[6:7],v[6:7],v[174:175]\n" \
S_"vmcnt(7)\n" \
"v_lshlrev_b32 v164,16,v140\n" \
"v_and_b32 v165,0xffff0000,v140\n" \
"v_pk_add_f32 v[8:9],v[8:9],v[164:165]\n" \
"v_lshlrev_b32 v166,16,v141\n" \
"v_and_b32 v167,0xffff0000,v141\n" \
"v_pk_add_f32 v[10:11],v[10:11],v[166:167]\n" \
"v_lshlrev_b32 v172,16,v142\n" \
"v_and_b32 v173,0xffff0000,v142\n" \
"v_pk_add_f32 v[12:13],v[12:13],v[172:173]\n" \
"v_lshlrev_b32 v174,16,v143\n" \
"v_and_b32 v175,0xffff0000,v143\n" \
"v_pk_add_f32 v[14:15],v[14:15],v[174:175]\n" \
S_"vmcnt(6)\n" \
"v_lshlrev_b32 v164,16,v148\n" \
"v_and_b32 v165,0xffff0000,v148\n" \
"v_pk_add_f32 v[0:1],v[0:1],v[164:165]\n" \
"v_lshlrev_b32 v166,16,v149\n" \
"v_and_b32 v167,0xffff0000,v149\n" \
"v_pk_add_f32 v[2:3],v[2:3],v[166:167]\n" \
"v_lshlrev_b32 v172,16,v150\n" \
"v_and_b32 v173,0xffff0000,v150\n" \
"v_pk_add_f32 v[4:5],v[4:5],v[172:173]\n" \
"v_lshlrev_b32 v174,16,v151\n" \
"v_and_b32 v175,0xffff0000,v151\n" \
"v_pk_add_f32 v[6:7],v[6:7],v[174:175]\n" \
S_"vmcnt(5)\n" \
"v_lshlrev_b32 v164,16,v156\n" \
"v_and_b32 v165,0xffff0000,v156\n" \
"v_pk_add_f32 v[8:9],v[8:9],v[164:165]\n" \
"v_lshlrev_b32 v166,16,v157\n" \
"v_and_b32 v167,0xffff0000,v157\n" \
"v_pk_add_f32 v[10:11],v[10:11],v[166:167]\n" \
"v_lshlrev_b32 v172,16,v158\n" \
"v_and_b32 v173,0xffff0000,v158\n" \
"v_pk_add_f32 v[12:13],v[12:13],v[172:173]\n" \
"v_lshlrev_b32 v174,16,v159\n" \
"v_and_b32 v175,0xffff0000,v159\n" \
"v_pk_add_f32 v[14:15],v[14:15],v[174:175]\n" \
S_"vmcnt(4)\n" \
"v_lshlrev_b32 v164,16,v180\n" \
"v_and_b32 v165,0xffff0000,v180\n" \
"v_pk_add_f32 v[0:1],v[0:1],v[164:165]\n" \
"v_lshlrev_b32 v166,16,v181\n" \
"v_and_b32 v167,0xffff0000,v181\n" \
"v_pk_add_f32 v[2:3],v[2:3],v[166:167]\n" \
"v_lshlrev_b32 v172,16,v182\n" \
"v_and_b32 v173,0xffff0000,v182\n" \
"v_pk_add_f32 v[4:5],v[4:5],v[172:173]\n" \
"v_lshlrev_b32 v174,16,v183\n" \
"v_and_b32 v175,0xffff0000,v183\n" \
"v_pk_add_f32 v[6:7],v[6:7],v[174:175]\n" \
S_"vmcnt(3)\n" \
"v_lshlrev_b32 v164,16,v188\n" \
"v_and_b32 v165,0xffff0000,v188\n" \
"v_pk_add_f32 v[8:9],v[8:9],v[164:165]\n" \
"v_lshlrev_b32 v166,16,v189\n" \
"v_and_b32 v167,0xffff0000,v189\n" \
"v_pk_add_f32 v[10:11],v[10:11],v[166:167]\n" \
"v_lshlrev_b32 v172,16,v190\n" \
"v_and_b32 v173,0xffff0000,v190\n" \
"v_pk_add_f32 v[12:13],v[12:13],v[172:173]\n" \
"v_lshlrev_b32 v174,16,v191\n" \
"v_and_b32 v175,0xffff0000,v191\n" \
"v_pk_add_f32 v[14:15],v[14:15],v[174:175]\n" \
S_"vmcnt(2)\n" \
"v_lshlrev_b32 v164,16,v196\n" \
"v_and_b32 v165,0xffff0000,v196\n" \
"v_pk_add_f32 v[0:1],v[0:1],v[164:165]\n" \
"v_lshlrev_b32 v166,16,v197\n" \
"v_and_b32 v167,0xffff0000,v197\n" \
"v_pk_add_f32 v[2:3],v[2:3],v[166:167]\n" \
"v_lshlrev_b32 v172,16,v198\n" \
"v_and_b32 v173,0xffff0000,v198\n" \
"v_pk_add_f32 v[4:5],v[4:5],v[172:173]\n" \
"v_lshlrev_b32 v174,16,v199\n" \
"v_and_b32 v175,0xffff0000,v199\n" \
"v_pk_add_f32 v[6:7],v[6:7],v[174:175]\n" \
S_"vmcnt(1)\n" \
"v_lshlrev_b32 v164,16,v204\n" \
"v_and_b32 v165,0xffff0000,v204\n" \
"v_pk_add_f32 v[8:9],v[8:9],v[164:165]\n" \
"v_lshlrev_b32 v166,16,v205\n" \
"v_and_b32 v167,0xffff0000,v205\n" \
"v_pk_add_f32 v[10:11],v[10:11],v[166:167]\n" \
"v_lshlrev_b32 v172,16,v206\n" \
"v_and_b32 v173,0xffff0000,v206\n" \
"v_pk_add_f32 v[12:13],v[12:13],v[172:173]\n" \
"v_lshlrev_b32 v174,16,v207\n" \
"v_and_b32 v175,0xffff0000,v207\n" \
"v_pk_add_f32 v[14:15],v[14:15],v[174:175]\n" \
C_"v0,v0,v1\n" \
C_"v1,v2,v3\n" \
C_"v2,v4,v5\n" \
C_"v3,v6,v7\n" \
C_"v8,v8,v9\n" \
C_"v9,v10,v11\n" \
C_"v10,v12,v13\n" \
C_"v11,v14,v15\n" \
S_"vmcnt(0)\n" \
"v_cmp_gt_u32 vcc,3,v252\n" \
"s_cbranch_vccnz LWA2_%=\n" \
"LWA2d_%=:\n" \
A_"s62,s78,s8\n" \
L_"v[128:131],v240,s[52:55],s62 offen sc1\n" \
L_"v[136:139],v240,s[52:55],s62 offen offset:1024 sc1\n" \
A_"s62,s78,s9\n" \
L_"v[144:147],v240,s[52:55],s62 offen sc1\n" \
L_"v[152:155],v240,s[52:55],s62 offen offset:1024 sc1\n" \
A_"s62,s78,s10\n" \
L_"v[160:163],v240,s[52:55],s62 offen sc1\n" \
L_"v[168:171],v240,s[52:55],s62 offen offset:1024 sc1\n" \
A_"s62,s78,s11\n" \
L_"v[176:179],v240,s[52:55],s62 offen sc1\n" \
L_"v[184:187],v240,s[52:55],s62 offen offset:1024 sc1\n" \
A_"s62,s78,s12\n" \
L_"v[192:195],v240,s[52:55],s62 offen sc1\n" \
L_"v[200:203],v240,s[52:55],s62 offen offset:1024 sc1\n" \
A_"s62,s78,s13\n" \
L_"v[208:211],v240,s[52:55],s62 offen sc1\n" \
L_"v[216:219],v240,s[52:55],s62 offen offset:1024 sc1\n" \
A_"s62,s78,s14\n" \
L_"v[212:215],v240,s[52:55],s62 offen sc1\n" \
L_"v[220:223],v240,s[52:55],s62 offen offset:1024 sc1\n" \
"buffer_store_dwordx4 v[72:75],v245,s[92:95],s79 offen nt\n" \
"buffer_store_dwordx4 v[80:83],v245,s[92:95],s79 offen offset:64 nt\n" \
"buffer_store_dwordx4 v[0:3],v245,s[92:95],s90 offen nt\n" \
"buffer_store_dwordx4 v[8:11],v245,s[92:95],s90 offen offset:64 nt\n" \
S_"vmcnt(17)\n" \
"v_lshlrev_b32 v164,16,v128\n" \
"v_and_b32 v165,0xffff0000,v128\n" \
"v_pk_add_f32 v[112:113],v[112:113],v[164:165]\n" \
"v_lshlrev_b32 v166,16,v129\n" \
"v_and_b32 v167,0xffff0000,v129\n" \
"v_pk_add_f32 v[114:115],v[114:115],v[166:167]\n" \
"v_lshlrev_b32 v172,16,v130\n" \
"v_and_b32 v173,0xffff0000,v130\n" \
"v_pk_add_f32 v[116:117],v[116:117],v[172:173]\n" \
"v_lshlrev_b32 v174,16,v131\n" \
"v_and_b32 v175,0xffff0000,v131\n" \
"v_pk_add_f32 v[118:119],v[118:119],v[174:175]\n" \
S_"vmcnt(16)\n" \
"v_lshlrev_b32 v164,16,v136\n" \
"v_and_b32 v165,0xffff0000,v136\n" \
"v_pk_add_f32 v[120:121],v[120:121],v[164:165]\n" \
"v_lshlrev_b32 v166,16,v137\n" \
"v_and_b32 v167,0xffff0000,v137\n" \
"v_pk_add_f32 v[122:123],v[122:123],v[166:167]\n" \
"v_lshlrev_b32 v172,16,v138\n" \
"v_and_b32 v173,0xffff0000,v138\n" \
"v_pk_add_f32 v[124:125],v[124:125],v[172:173]\n" \
"v_lshlrev_b32 v174,16,v139\n" \
"v_and_b32 v175,0xffff0000,v139\n" \
"v_pk_add_f32 v[126:127],v[126:127],v[174:175]\n" \
S_"vmcnt(15)\n" \
"v_lshlrev_b32 v164,16,v144\n" \
"v_and_b32 v165,0xffff0000,v144\n" \
"v_pk_add_f32 v[112:113],v[112:113],v[164:165]\n" \
"v_lshlrev_b32 v166,16,v145\n" \
"v_and_b32 v167,0xffff0000,v145\n" \
"v_pk_add_f32 v[114:115],v[114:115],v[166:167]\n" \
"v_lshlrev_b32 v172,16,v146\n" \
"v_and_b32 v173,0xffff0000,v146\n" \
"v_pk_add_f32 v[116:117],v[116:117],v[172:173]\n" \
"v_lshlrev_b32 v174,16,v147\n" \
"v_and_b32 v175,0xffff0000,v147\n" \
"v_pk_add_f32 v[118:119],v[118:119],v[174:175]\n" \
S_"vmcnt(14)\n" \
"v_lshlrev_b32 v164,16,v152\n" \
"v_and_b32 v165,0xffff0000,v152\n" \
"v_pk_add_f32 v[120:121],v[120:121],v[164:165]\n" \
"v_lshlrev_b32 v166,16,v153\n" \
"v_and_b32 v167,0xffff0000,v153\n" \
"v_pk_add_f32 v[122:123],v[122:123],v[166:167]\n" \
"v_lshlrev_b32 v172,16,v154\n" \
"v_and_b32 v173,0xffff0000,v154\n" \
"v_pk_add_f32 v[124:125],v[124:125],v[172:173]\n" \
"v_lshlrev_b32 v174,16,v155\n" \
"v_and_b32 v175,0xffff0000,v155\n" \
"v_pk_add_f32 v[126:127],v[126:127],v[174:175]\n" \
S_"vmcnt(13)\n" \
"v_lshlrev_b32 v164,16,v160\n" \
"v_and_b32 v165,0xffff0000,v160\n" \
"v_pk_add_f32 v[112:113],v[112:113],v[164:165]\n" \
"v_lshlrev_b32 v166,16,v161\n" \
"v_and_b32 v167,0xffff0000,v161\n" \
"v_pk_add_f32 v[114:115],v[114:115],v[166:167]\n" \
"v_lshlrev_b32 v172,16,v162\n" \
"v_and_b32 v173,0xffff0000,v162\n" \
"v_pk_add_f32 v[116:117],v[116:117],v[172:173]\n" \
"v_lshlrev_b32 v174,16,v163\n" \
"v_and_b32 v175,0xffff0000,v163\n" \
"v_pk_add_f32 v[118:119],v[118:119],v[174:175]\n" \
S_"vmcnt(12)\n" \
"v_lshlrev_b32 v164,16,v168\n" \
"v_and_b32 v165,0xffff0000,v168\n" \
"v_pk_add_f32 v[120:121],v[120:121],v[164:165]\n" \
"v_lshlrev_b32 v166,16,v169\n" \
"v_and_b32 v167,0xffff0000,v169\n" \
"v_pk_add_f32 v[122:123],v[122:123],v[166:167]\n" \
"v_lshlrev_b32 v172,16,v170\n" \
"v_and_b32 v173,0xffff0000,v170\n" \
"v_pk_add_f32 v[124:125],v[124:125],v[172:173]\n" \
"v_lshlrev_b32 v174,16,v171\n" \
"v_and_b32 v175,0xffff0000,v171\n" \
"v_pk_add_f32 v[126:127],v[126:127],v[174:175]\n" \
S_"vmcnt(11)\n" \
"v_lshlrev_b32 v164,16,v176\n" \
"v_and_b32 v165,0xffff0000,v176\n" \
"v_pk_add_f32 v[112:113],v[112:113],v[164:165]\n" \
"v_lshlrev_b32 v166,16,v177\n" \
"v_and_b32 v167,0xffff0000,v177\n" \
"v_pk_add_f32 v[114:115],v[114:115],v[166:167]\n" \
"v_lshlrev_b32 v172,16,v178\n" \
"v_and_b32 v173,0xffff0000,v178\n" \
"v_pk_add_f32 v[116:117],v[116:117],v[172:173]\n" \
"v_lshlrev_b32 v174,16,v179\n" \
"v_and_b32 v175,0xffff0000,v179\n" \
"v_pk_add_f32 v[118:119],v[118:119],v[174:175]\n" \
S_"vmcnt(10)\n" \
"v_lshlrev_b32 v164,16,v184\n" \
"v_and_b32 v165,0xffff0000,v184\n" \
"v_pk_add_f32 v[120:121],v[120:121],v[164:165]\n" \
"v_lshlrev_b32 v166,16,v185\n" \
"v_and_b32 v167,0xffff0000,v185\n" \
"v_pk_add_f32 v[122:123],v[122:123],v[166:167]\n" \
"v_lshlrev_b32 v172,16,v186\n" \
"v_and_b32 v173,0xffff0000,v186\n" \
"v_pk_add_f32 v[124:125],v[124:125],v[172:173]\n" \
"v_lshlrev_b32 v174,16,v187\n" \
"v_and_b32 v175,0xffff0000,v187\n" \
"v_pk_add_f32 v[126:127],v[126:127],v[174:175]\n" \
S_"vmcnt(9)\n" \
"v_lshlrev_b32 v164,16,v192\n" \
"v_and_b32 v165,0xffff0000,v192\n" \
"v_pk_add_f32 v[112:113],v[112:113],v[164:165]\n" \
"v_lshlrev_b32 v166,16,v193\n" \
"v_and_b32 v167,0xffff0000,v193\n" \
"v_pk_add_f32 v[114:115],v[114:115],v[166:167]\n" \
"v_lshlrev_b32 v172,16,v194\n" \
"v_and_b32 v173,0xffff0000,v194\n" \
"v_pk_add_f32 v[116:117],v[116:117],v[172:173]\n" \
"v_lshlrev_b32 v174,16,v195\n" \
"v_and_b32 v175,0xffff0000,v195\n" \
"v_pk_add_f32 v[118:119],v[118:119],v[174:175]\n" \
S_"vmcnt(8)\n" \
"v_lshlrev_b32 v164,16,v200\n" \
"v_and_b32 v165,0xffff0000,v200\n" \
"v_pk_add_f32 v[120:121],v[120:121],v[164:165]\n" \
"v_lshlrev_b32 v166,16,v201\n" \
"v_and_b32 v167,0xffff0000,v201\n" \
"v_pk_add_f32 v[122:123],v[122:123],v[166:167]\n" \
"v_lshlrev_b32 v172,16,v202\n" \
"v_and_b32 v173,0xffff0000,v202\n" \
"v_pk_add_f32 v[124:125],v[124:125],v[172:173]\n" \
"v_lshlrev_b32 v174,16,v203\n" \
"v_and_b32 v175,0xffff0000,v203\n" \
"v_pk_add_f32 v[126:127],v[126:127],v[174:175]\n" \
S_"vmcnt(7)\n" \
"v_lshlrev_b32 v164,16,v208\n" \
"v_and_b32 v165,0xffff0000,v208\n" \
"v_pk_add_f32 v[112:113],v[112:113],v[164:165]\n" \
"v_lshlrev_b32 v166,16,v209\n" \
"v_and_b32 v167,0xffff0000,v209\n" \
"v_pk_add_f32 v[114:115],v[114:115],v[166:167]\n" \
"v_lshlrev_b32 v172,16,v210\n" \
"v_and_b32 v173,0xffff0000,v210\n" \
"v_pk_add_f32 v[116:117],v[116:117],v[172:173]\n" \
"v_lshlrev_b32 v174,16,v211\n" \
"v_and_b32 v175,0xffff0000,v211\n" \
"v_pk_add_f32 v[118:119],v[118:119],v[174:175]\n" \
S_"vmcnt(6)\n" \
"v_lshlrev_b32 v164,16,v216\n" \
"v_and_b32 v165,0xffff0000,v216\n" \
"v_pk_add_f32 v[120:121],v[120:121],v[164:165]\n" \
"v_lshlrev_b32 v166,16,v217\n" \
"v_and_b32 v167,0xffff0000,v217\n" \
"v_pk_add_f32 v[122:123],v[122:123],v[166:167]\n" \
"v_lshlrev_b32 v172,16,v218\n" \
"v_and_b32 v173,0xffff0000,v218\n" \
"v_pk_add_f32 v[124:125],v[124:125],v[172:173]\n" \
"v_lshlrev_b32 v174,16,v219\n" \
"v_and_b32 v175,0xffff0000,v219\n" \
"v_pk_add_f32 v[126:127],v[126:127],v[174:175]\n" \
S_"vmcnt(5)\n" \
"v_lshlrev_b32 v164,16,v212\n" \
"v_and_b32 v165,0xffff0000,v212\n" \
"v_pk_add_f32 v[112:113],v[112:113],v[164:165]\n" \
"v_lshlrev_b32 v166,16,v213\n" \
"v_and_b32 v167,0xffff0000,v213\n" \
"v_pk_add_f32 v[114:115],v[114:115],v[166:167]\n" \
"v_lshlrev_b32 v172,16,v214\n" \
"v_and_b32 v173,0xffff0000,v214\n" \
"v_pk_add_f32 v[116:117],v[116:117],v[172:173]\n" \
"v_lshlrev_b32 v174,16,v215\n" \
"v_and_b32 v175,0xffff0000,v215\n" \
"v_pk_add_f32 v[118:119],v[118:119],v[174:175]\n" \
S_"vmcnt(4)\n" \
"v_lshlrev_b32 v164,16,v220\n" \
"v_and_b32 v165,0xffff0000,v220\n" \
"v_pk_add_f32 v[120:121],v[120:121],v[164:165]\n" \
"v_lshlrev_b32 v166,16,v221\n" \
"v_and_b32 v167,0xffff0000,v221\n" \
"v_pk_add_f32 v[122:123],v[122:123],v[166:167]\n" \
"v_lshlrev_b32 v172,16,v222\n" \
"v_and_b32 v173,0xffff0000,v222\n" \
"v_pk_add_f32 v[124:125],v[124:125],v[172:173]\n" \
"v_lshlrev_b32 v174,16,v223\n" \
"v_and_b32 v175,0xffff0000,v223\n" \
"v_pk_add_f32 v[126:127],v[126:127],v[174:175]\n" \
C_"v112,v112,v113\n" \
C_"v113,v114,v115\n" \
C_"v114,v116,v117\n" \
C_"v115,v118,v119\n" \
C_"v120,v120,v121\n" \
C_"v121,v122,v123\n" \
C_"v122,v124,v125\n" \
C_"v123,v126,v127\n" \
"buffer_store_dwordx4 v[112:115],v245,s[92:95],s91 offen nt\n" \
"buffer_store_dwordx4 v[120:123],v245,s[92:95],s91 offen offset:64 nt\n" \
"s_branch LFIN_%=\n" \
"; ---- outb\n" \
"LOUTB_%=:\n" \
"s_xor_b32 s60,s56,0\n" \
"s_mul_i32 s64,s60,147456\n" \
"s_mul_i32 s61,s60,40960\n" \
A_"s61,s61,s57\n" \
"v_add_u32 v224,s61,v240\n" \
"s_xor_b32 s60,s75,0\n" \
"s_mul_i32 s65,s60,147456\n" \
"s_mul_i32 s61,s60,40960\n" \
A_"s61,s61,s57\n" \
"v_add_u32 v225,s61,v240\n" \
"s_xor_b32 s60,s75,1\n" \
"s_mul_i32 s66,s60,147456\n" \
"s_mul_i32 s61,s60,40960\n" \
A_"s61,s61,s57\n" \
"v_add_u32 v226,s61,v240\n" \
"s_xor_b32 s60,s56,1\n" \
"s_mul_i32 s67,s60,147456\n" \
"s_mul_i32 s61,s60,40960\n" \
A_"s61,s61,s57\n" \
"v_add_u32 v227,s61,v240\n" \
S_"vmcnt(0) lgkmcnt(0)\n" \
A_"m0,s74,0\n" \
A_"s60,s64,s69\n" \
A_"s60,s60,0\n" \
L_"v242,s[48:51],s60 offen lds\n" \
L_"v249,s[48:51],s60 offen offset:1024 lds\n" \
L_"v243,s[48:51],s60 offen offset:2048 lds\n" \
L_"v244,s[48:51],s60 offen offset:3072 lds\n" \
A_"m0,s74,4096\n" \
A_"s60,s64,s69\n" \
A_"s60,s60,73728\n" \
L_"v242,s[48:51],s60 offen lds\n" \
L_"v249,s[48:51],s60 offen offset:1024 lds\n" \
L_"v243,s[48:51],s60 offen offset:2048 lds\n" \
L_"v244,s[48:51],s60 offen offset:3072 lds\n" \
"v_add_u32 v160,s83,v240\n" \
"v_rcp_f32 v196,v196\n" \
"v_rcp_f32 v200,v200\n" \
"v_rcp_f32 v204,v204\n" \
"v_rcp_f32 v208,v208\n" \
"v_rcp_f32 v212,v212\n" \
"v_rcp_f32 v216,v216\n" \
"v_rcp_f32 v220,v220\n" \
"v_mov_b32 v197,v196\n" \
"v_mov_b32 v201,v200\n" \
"v_mov_b32 v205,v204\n" \
"v_mov_b32 v209,v208\n" \
"v_mov_b32 v213,v212\n" \
"v_mov_b32 v217,v216\n" \
"v_mov_b32 v221,v220\n" \
G_"v144,a0\n" \
G_"v145,a1\n" \
G_"v146,a2\n" \
G_"v147,a3\n" \
G_"v148,a4\n" \
G_"v149,a5\n" \
G_"v150,a6\n" \
G_"v151,a7\n" \
P_"v[144:145],v[144:145],v[196:197]\n" \
P_"v[146:147],v[146:147],v[196:197]\n" \
P_"v[148:149],v[148:149],v[196:197]\n" \
P_"v[150:151],v[150:151],v[196:197]\n" \
C_"v0,v144,v145\n" \
C_"v1,v146,v147\n" \
C_"v2,v148,v149\n" \
C_"v3,v150,v151\n" \
"ds_write_b128 v160,v[0:3] offset:0\n" \
G_"v152,a32\n" \
G_"v153,a33\n" \
G_"v154,a34\n" \
G_"v155,a35\n" \
G_"v156,a36\n" \
G_"v157,a37\n" \
G_"v158,a38\n" \
G_"v159,a39\n" \
P_"v[152:153],v[152:153],v[200:201]\n" \
P_"v[154:155],v[154:155],v[200:201]\n" \
P_"v[156:157],v[156:157],v[200:201]\n" \
P_"v[158:159],v[158:159],v[200:201]\n" \
C_"v16,v152,v153\n" \
C_"v17,v154,v155\n" \
C_"v18,v156,v157\n" \
C_"v19,v158,v159\n" \
"ds_write_b128 v160,v[16:19] offset:1024\n" \
G_"v144,a64\n" \
G_"v145,a65\n" \
G_"v146,a66\n" \
G_"v147,a67\n" \
G_"v148,a68\n" \
G_"v149,a69\n" \
G_"v150,a70\n" \
G_"v151,a71\n" \
P_"v[144:145],v[144:145],v[204:205]\n" \
P_"v[146:147],v[146:147],v[204:205]\n" \
P_"v[148:149],v[148:149],v[204:205]\n" \
P_"v[150:151],v[150:151],v[204:205]\n" \
C_"v32,v144,v145\n" \
C_"v33,v146,v147\n" \
C_"v34,v148,v149\n" \
C_"v35,v150,v151\n" \
"ds_write_b128 v160,v[32:35] offset:2048\n" \
G_"v152,a96\n" \
G_"v153,a97\n" \
G_"v154,a98\n" \
G_"v155,a99\n" \
G_"v156,a100\n" \
G_"v157,a101\n" \
G_"v158,a102\n" \
G_"v159,a103\n" \
P_"v[152:153],v[152:153],v[208:209]\n" \
P_"v[154:155],v[154:155],v[208:209]\n" \
P_"v[156:157],v[156:157],v[208:209]\n" \
P_"v[158:159],v[158:159],v[208:209]\n" \
C_"v48,v152,v153\n" \
C_"v49,v154,v155\n" \
C_"v50,v156,v157\n" \
C_"v51,v158,v159\n" \
"ds_write_b128 v160,v[48:51] offset:3072\n" \
G_"v144,a128\n" \
G_"v145,a129\n" \
G_"v146,a130\n" \
G_"v147,a131\n" \
G_"v148,a132\n" \
G_"v149,a133\n" \
G_"v150,a134\n" \
G_"v151,a135\n" \
P_"v[144:145],v[144:145],v[212:213]\n" \
P_"v[146:147],v[146:147],v[212:213]\n" \
P_"v[148:149],v[148:149],v[212:213]\n" \
P_"v[150:151],v[150:151],v[212:213]\n" \
C_"v64,v144,v145\n" \
C_"v65,v146,v147\n" \
C_"v66,v148,v149\n" \
C_"v67,v150,v151\n" \
"ds_write_b128 v160,v[64:67] offset:4096\n" \
G_"v152,a160\n" \
G_"v153,a161\n" \
G_"v154,a162\n" \
G_"v155,a163\n" \
G_"v156,a164\n" \
G_"v157,a165\n" \
G_"v158,a166\n" \
G_"v159,a167\n" \
P_"v[152:153],v[152:153],v[216:217]\n" \
P_"v[154:155],v[154:155],v[216:217]\n" \
P_"v[156:157],v[156:157],v[216:217]\n" \
P_"v[158:159],v[158:159],v[216:217]\n" \
C_"v80,v152,v153\n" \
C_"v81,v154,v155\n" \
C_"v82,v156,v157\n" \
C_"v83,v158,v159\n" \
"ds_write_b128 v160,v[80:83] offset:5120\n" \
G_"v144,a192\n" \
G_"v145,a193\n" \
G_"v146,a194\n" \
G_"v147,a195\n" \
G_"v148,a196\n" \
G_"v149,a197\n" \
G_"v150,a198\n" \
G_"v151,a199\n" \
P_"v[144:145],v[144:145],v[220:221]\n" \
P_"v[146:147],v[146:147],v[220:221]\n" \
P_"v[148:149],v[148:149],v[220:221]\n" \
P_"v[150:151],v[150:151],v[220:221]\n" \
C_"v96,v144,v145\n" \
C_"v97,v146,v147\n" \
C_"v98,v148,v149\n" \
C_"v99,v150,v151\n" \
"ds_write_b128 v160,v[96:99] offset:6144\n" \
G_"v152,a8\n" \
G_"v153,a9\n" \
G_"v154,a10\n" \
G_"v155,a11\n" \
G_"v156,a12\n" \
G_"v157,a13\n" \
G_"v158,a14\n" \
G_"v159,a15\n" \
P_"v[152:153],v[152:153],v[196:197]\n" \
P_"v[154:155],v[154:155],v[196:197]\n" \
P_"v[156:157],v[156:157],v[196:197]\n" \
P_"v[158:159],v[158:159],v[196:197]\n" \
C_"v4,v152,v153\n" \
C_"v5,v154,v155\n" \
C_"v6,v156,v157\n" \
C_"v7,v158,v159\n" \
"ds_write_b128 v160,v[4:7] offset:7168\n" \
G_"v144,a40\n" \
G_"v145,a41\n" \
G_"v146,a42\n" \
G_"v147,a43\n" \
G_"v148,a44\n" \
G_"v149,a45\n" \
G_"v150,a46\n" \
G_"v151,a47\n" \
P_"v[144:145],v[144:145],v[200:201]\n" \
P_"v[146:147],v[146:147],v[200:201]\n" \
P_"v[148:149],v[148:149],v[200:201]\n" \
P_"v[150:151],v[150:151],v[200:201]\n" \
C_"v20,v144,v145\n" \
C_"v21,v146,v147\n" \
C_"v22,v148,v149\n" \
C_"v23,v150,v151\n" \
"ds_write_b128 v160,v[20:23] offset:8192\n" \
G_"v152,a72\n" \
G_"v153,a73\n" \
G_"v154,a74\n" \
G_"v155,a75\n" \
G_"v156,a76\n" \
G_"v157,a77\n" \
G_"v158,a78\n" \
G_"v159,a79\n" \
P_"v[152:153],v[152:153],v[204:205]\n" \
P_"v[154:155],v[154:155],v[204:205]\n" \
P_"v[156:157],v[156:157],v[204:205]\n" \
P_"v[158:159],v[158:159],v[204:205]\n" \
C_"v36,v152,v153\n" \
C_"v37,v154,v155\n" \
C_"v38,v156,v157\n" \
C_"v39,v158,v159\n" \
"ds_write_b128 v160,v[36:39] offset:9216\n" \
G_"v144,a104\n" \
G_"v145,a105\n" \
G_"v146,a106\n" \
G_"v147,a107\n" \
G_"v148,a108\n" \
G_"v149,a109\n" \
G_"v150,a110\n" \
G_"v151,a111\n" \
P_"v[144:145],v[144:145],v[208:209]\n" \
P_"v[146:147],v[146:147],v[208:209]\n" \
P_"v[148:149],v[148:149],v[208:209]\n" \
P_"v[150:151],v[150:151],v[208:209]\n" \
C_"v52,v144,v145\n" \
C_"v53,v146,v147\n" \
C_"v54,v148,v149\n" \
C_"v55,v150,v151\n" \
"ds_write_b128 v160,v[52:55] offset:10240\n" \
G_"v152,a136\n" \
G_"v153,a137\n" \
G_"v154,a138\n" \
G_"v155,a139\n" \
G_"v156,a140\n" \
G_"v157,a141\n" \
G_"v158,a142\n" \
G_"v159,a143\n" \
P_"v[152:153],v[152:153],v[212:213]\n" \
P_"v[154:155],v[154:155],v[212:213]\n" \
P_"v[156:157],v[156:157],v[212:213]\n" \
P_"v[158:159],v[158:159],v[212:213]\n" \
C_"v68,v152,v153\n" \
C_"v69,v154,v155\n" \
C_"v70,v156,v157\n" \
C_"v71,v158,v159\n" \
"ds_write_b128 v160,v[68:71] offset:11264\n" \
G_"v144,a168\n" \
G_"v145,a169\n" \
G_"v146,a170\n" \
G_"v147,a171\n" \
G_"v148,a172\n" \
G_"v149,a173\n" \
G_"v150,a174\n" \
G_"v151,a175\n" \
P_"v[144:145],v[144:145],v[216:217]\n" \
P_"v[146:147],v[146:147],v[216:217]\n" \
P_"v[148:149],v[148:149],v[216:217]\n" \
P_"v[150:151],v[150:151],v[216:217]\n" \
C_"v84,v144,v145\n" \
C_"v85,v146,v147\n" \
C_"v86,v148,v149\n" \
C_"v87,v150,v151\n" \
"ds_write_b128 v160,v[84:87] offset:12288\n" \
G_"v152,a200\n" \
G_"v153,a201\n" \
G_"v154,a202\n" \
G_"v155,a203\n" \
G_"v156,a204\n" \
G_"v157,a205\n" \
G_"v158,a206\n" \
G_"v159,a207\n" \
P_"v[152:153],v[152:153],v[220:221]\n" \
P_"v[154:155],v[154:155],v[220:221]\n" \
P_"v[156:157],v[156:157],v[220:221]\n" \
P_"v[158:159],v[158:159],v[220:221]\n" \
C_"v100,v152,v153\n" \
C_"v101,v154,v155\n" \
C_"v102,v156,v157\n" \
C_"v103,v158,v159\n" \
"ds_write_b128 v160,v[100:103] offset:13312\n" \
G_"v144,a16\n" \
G_"v145,a17\n" \
G_"v146,a18\n" \
G_"v147,a19\n" \
G_"v148,a20\n" \
G_"v149,a21\n" \
G_"v150,a22\n" \
G_"v151,a23\n" \
P_"v[144:145],v[144:145],v[196:197]\n" \
P_"v[146:147],v[146:147],v[196:197]\n" \
P_"v[148:149],v[148:149],v[196:197]\n" \
P_"v[150:151],v[150:151],v[196:197]\n" \
C_"v8,v144,v145\n" \
C_"v9,v146,v147\n" \
C_"v10,v148,v149\n" \
C_"v11,v150,v151\n" \
"ds_write_b128 v160,v[8:11] offset:14336\n" \
G_"v152,a48\n" \
G_"v153,a49\n" \
G_"v154,a50\n" \
G_"v155,a51\n" \
G_"v156,a52\n" \
G_"v157,a53\n" \
G_"v158,a54\n" \
G_"v159,a55\n" \
P_"v[152:153],v[152:153],v[200:201]\n" \
P_"v[154:155],v[154:155],v[200:201]\n" \
P_"v[156:157],v[156:157],v[200:201]\n" \
P_"v[158:159],v[158:159],v[200:201]\n" \
C_"v24,v152,v153\n" \
C_"v25,v154,v155\n" \
C_"v26,v156,v157\n" \
C_"v27,v158,v159\n" \
"ds_write_b128 v160,v[24:27] offset:15360\n" \
G_"v144,a80\n" \
G_"v145,a81\n" \
G_"v146,a82\n" \
G_"v147,a83\n" \
G_"v148,a84\n" \
G_"v149,a85\n" \
G_"v150,a86\n" \
G_"v151,a87\n" \
P_"v[144:145],v[144:145],v[204:205]\n" \
P_"v[146:147],v[146:147],v[204:205]\n" \
P_"v[148:149],v[148:149],v[204:205]\n" \
P_"v[150:151],v[150:151],v[204:205]\n" \
C_"v40,v144,v145\n" \
C_"v41,v146,v147\n" \
C_"v42,v148,v149\n" \
C_"v43,v150,v151\n" \
"ds_write_b128 v160,v[40:43] offset:16384\n" \
G_"v152,a112\n" \
G_"v153,a113\n" \
G_"v154,a114\n" \
G_"v155,a115\n" \
G_"v156,a116\n" \
G_"v157,a117\n" \
G_"v158,a118\n" \
G_"v159,a119\n" \
P_"v[152:153],v[152:153],v[208:209]\n" \
P_"v[154:155],v[154:155],v[208:209]\n" \
P_"v[156:157],v[156:157],v[208:209]\n" \
P_"v[158:159],v[158:159],v[208:209]\n" \
C_"v56,v152,v153\n" \
C_"v57,v154,v155\n" \
C_"v58,v156,v157\n" \
C_"v59,v158,v159\n" \
"ds_write_b128 v160,v[56:59] offset:17408\n" \
G_"v144,a144\n" \
G_"v145,a145\n" \
G_"v146,a146\n" \
G_"v147,a147\n" \
G_"v148,a148\n" \
G_"v149,a149\n" \
G_"v150,a150\n" \
G_"v151,a151\n" \
P_"v[144:145],v[144:145],v[212:213]\n" \
P_"v[146:147],v[146:147],v[212:213]\n" \
P_"v[148:149],v[148:149],v[212:213]\n" \
P_"v[150:151],v[150:151],v[212:213]\n" \
C_"v72,v144,v145\n" \
C_"v73,v146,v147\n" \
C_"v74,v148,v149\n" \
C_"v75,v150,v151\n" \
"ds_write_b128 v160,v[72:75] offset:18432\n" \
G_"v152,a176\n" \
G_"v153,a177\n" \
G_"v154,a178\n" \
G_"v155,a179\n" \
G_"v156,a180\n" \
G_"v157,a181\n" \
G_"v158,a182\n" \
G_"v159,a183\n" \
P_"v[152:153],v[152:153],v[216:217]\n" \
P_"v[154:155],v[154:155],v[216:217]\n" \
P_"v[156:157],v[156:157],v[216:217]\n" \
P_"v[158:159],v[158:159],v[216:217]\n" \
C_"v88,v152,v153\n" \
C_"v89,v154,v155\n" \
C_"v90,v156,v157\n" \
C_"v91,v158,v159\n" \
"ds_write_b128 v160,v[88:91] offset:19456\n" \
G_"v144,a208\n" \
G_"v145,a209\n" \
G_"v146,a210\n" \
G_"v147,a211\n" \
G_"v148,a212\n" \
G_"v149,a213\n" \
G_"v150,a214\n" \
G_"v151,a215\n" \
P_"v[144:145],v[144:145],v[220:221]\n" \
P_"v[146:147],v[146:147],v[220:221]\n" \
P_"v[148:149],v[148:149],v[220:221]\n" \
P_"v[150:151],v[150:151],v[220:221]\n" \
C_"v104,v144,v145\n" \
C_"v105,v146,v147\n" \
C_"v106,v148,v149\n" \
C_"v107,v150,v151\n" \
"ds_write_b128 v160,v[104:107] offset:20480\n" \
G_"v152,a24\n" \
G_"v153,a25\n" \
G_"v154,a26\n" \
G_"v155,a27\n" \
G_"v156,a28\n" \
G_"v157,a29\n" \
G_"v158,a30\n" \
G_"v159,a31\n" \
P_"v[152:153],v[152:153],v[196:197]\n" \
P_"v[154:155],v[154:155],v[196:197]\n" \
P_"v[156:157],v[156:157],v[196:197]\n" \
P_"v[158:159],v[158:159],v[196:197]\n" \
C_"v12,v152,v153\n" \
C_"v13,v154,v155\n" \
C_"v14,v156,v157\n" \
C_"v15,v158,v159\n" \
"ds_write_b128 v160,v[12:15] offset:21504\n" \
G_"v144,a56\n" \
G_"v145,a57\n" \
G_"v146,a58\n" \
G_"v147,a59\n" \
G_"v148,a60\n" \
G_"v149,a61\n" \
G_"v150,a62\n" \
G_"v151,a63\n" \
P_"v[144:145],v[144:145],v[200:201]\n" \
P_"v[146:147],v[146:147],v[200:201]\n" \
P_"v[148:149],v[148:149],v[200:201]\n" \
P_"v[150:151],v[150:151],v[200:201]\n" \
C_"v28,v144,v145\n" \
C_"v29,v146,v147\n" \
C_"v30,v148,v149\n" \
C_"v31,v150,v151\n" \
"ds_write_b128 v160,v[28:31] offset:22528\n" \
G_"v152,a88\n" \
G_"v153,a89\n" \
G_"v154,a90\n" \
G_"v155,a91\n" \
G_"v156,a92\n" \
G_"v157,a93\n" \
G_"v158,a94\n" \
G_"v159,a95\n" \
P_"v[152:153],v[152:153],v[204:205]\n" \
P_"v[154:155],v[154:155],v[204:205]\n" \
P_"v[156:157],v[156:157],v[204:205]\n" \
P_"v[158:159],v[158:159],v[204:205]\n" \
C_"v44,v152,v153\n" \
C_"v45,v154,v155\n" \
C_"v46,v156,v157\n" \
C_"v47,v158,v159\n" \
"ds_write_b128 v160,v[44:47] offset:23552\n" \
G_"v144,a120\n" \
G_"v145,a121\n" \
G_"v146,a122\n" \
G_"v147,a123\n" \
G_"v148,a124\n" \
G_"v149,a125\n" \
G_"v150,a126\n" \
G_"v151,a127\n" \
P_"v[144:145],v[144:145],v[208:209]\n" \
P_"v[146:147],v[146:147],v[208:209]\n" \
P_"v[148:149],v[148:149],v[208:209]\n" \
P_"v[150:151],v[150:151],v[208:209]\n" \
C_"v60,v144,v145\n" \
C_"v61,v146,v147\n" \
C_"v62,v148,v149\n" \
C_"v63,v150,v151\n" \
"ds_write_b128 v160,v[60:63] offset:24576\n" \
G_"v152,a152\n" \
G_"v153,a153\n" \
G_"v154,a154\n" \
G_"v155,a155\n" \
G_"v156,a156\n" \
G_"v157,a157\n" \
G_"v158,a158\n" \
G_"v159,a159\n" \
P_"v[152:153],v[152:153],v[212:213]\n" \
P_"v[154:155],v[154:155],v[212:213]\n" \
P_"v[156:157],v[156:157],v[212:213]\n" \
P_"v[158:159],v[158:159],v[212:213]\n" \
C_"v76,v152,v153\n" \
C_"v77,v154,v155\n" \
C_"v78,v156,v157\n" \
C_"v79,v158,v159\n" \
"ds_write_b128 v160,v[76:79] offset:25600\n" \
G_"v144,a184\n" \
G_"v145,a185\n" \
G_"v146,a186\n" \
G_"v147,a187\n" \
G_"v148,a188\n" \
G_"v149,a189\n" \
G_"v150,a190\n" \
G_"v151,a191\n" \
P_"v[144:145],v[144:145],v[216:217]\n" \
P_"v[146:147],v[146:147],v[216:217]\n" \
P_"v[148:149],v[148:149],v[216:217]\n" \
P_"v[150:151],v[150:151],v[216:217]\n" \
C_"v92,v144,v145\n" \
C_"v93,v146,v147\n" \
C_"v94,v148,v149\n" \
C_"v95,v150,v151\n" \
"ds_write_b128 v160,v[92:95] offset:26624\n" \
G_"v152,a216\n" \
G_"v153,a217\n" \
G_"v154,a218\n" \
G_"v155,a219\n" \
G_"v156,a220\n" \
G_"v157,a221\n" \
G_"v158,a222\n" \
G_"v159,a223\n" \
P_"v[152:153],v[152:153],v[220:221]\n" \
P_"v[154:155],v[154:155],v[220:221]\n" \
P_"v[156:157],v[156:157],v[220:221]\n" \
P_"v[158:159],v[158:159],v[220:221]\n" \
C_"v108,v152,v153\n" \
C_"v109,v154,v155\n" \
C_"v110,v156,v157\n" \
C_"v111,v158,v159\n" \
"ds_write_b128 v160,v[108:111] offset:27648\n" \
S_"lgkmcnt(0)\n" \
"s_barrier\n" \
A_"m0,s74,8192\n" \
A_"s60,s65,s69\n" \
A_"s60,s60,0\n" \
L_"v242,s[48:51],s60 offen lds\n" \
L_"v249,s[48:51],s60 offen offset:1024 lds\n" \
L_"v243,s[48:51],s60 offen offset:2048 lds\n" \
L_"v244,s[48:51],s60 offen offset:3072 lds\n" \
S_"vmcnt(8)\n" \
T_"a[112:113],v241 offset:0\n" \
T_"a[114:115],v241 offset:1024\n" \
T_"a[116:117],v241 offset:8\n" \
T_"a[118:119],v241 offset:1032\n" \
T_"a[120:121],v241 offset:2048\n" \
T_"a[122:123],v241 offset:3072\n" \
T_"a[124:125],v241 offset:2056\n" \
T_"a[126:127],v241 offset:3080\n" \
R_"a[144:147],v224 offset:0\n" \
R_"a[148:151],v224 offset:1024\n" \
R_"a[152:155],v224 offset:2048\n" \
R_"a[156:159],v224 offset:3072\n" \
R_"a[160:163],v224 offset:4096\n" \
R_"a[164:167],v224 offset:5120\n" \
R_"a[168:171],v224 offset:6144\n" \
S_"lgkmcnt(6)\n" \
M_"v[112:115],a[112:115],a[144:147],0\n" \
A_"m0,s74,0\n" \
A_"s60,s65,s69\n" \
A_"s60,s60,73728\n" \
M_"v[116:119],a[116:119],a[144:147],0\n" \
R_"a[144:147],v224 offset:7168\n" \
L_"v242,s[48:51],s60 offen lds\n" \
S_"lgkmcnt(6)\n" \
M_"v[128:131],a[112:115],a[148:151],0\n" \
L_"v249,s[48:51],s60 offen offset:1024 lds\n" \
M_"v[132:135],a[116:119],a[148:151],0\n" \
R_"a[148:151],v224 offset:8192\n" \
S_"lgkmcnt(6)\n" \
M_"v[144:147],a[112:115],a[152:155],0\n" \
L_"v243,s[48:51],s60 offen offset:2048 lds\n" \
S_"vmcnt(7)\n" \
T_"a[128:129],v241 offset:4096\n" \
M_"v[148:151],a[116:119],a[152:155],0\n" \
R_"a[152:155],v224 offset:9216\n" \
L_"v244,s[48:51],s60 offen offset:3072 lds\n" \
S_"lgkmcnt(4)\n" \
M_"v[160:163],a[112:115],a[156:159],0\n" \
T_"a[130:131],v241 offset:5120\n" \
M_"v[164:167],a[116:119],a[156:159],0\n" \
R_"a[156:159],v224 offset:10240\n" \
M_"v[176:179],a[112:115],a[160:163],0\n" \
T_"a[132:133],v241 offset:4104\n" \
M_"v[180:183],a[116:119],a[160:163],0\n" \
R_"a[160:163],v224 offset:11264\n" \
M_"v[192:195],a[112:115],a[164:167],0\n" \
T_"a[134:135],v241 offset:5128\n" \
M_"v[196:199],a[116:119],a[164:167],0\n" \
R_"a[164:167],v224 offset:12288\n" \
M_"v[208:211],a[112:115],a[168:171],0\n" \
T_"a[136:137],v241 offset:6144\n" \
M_"v[212:215],a[116:119],a[168:171],0\n" \
R_"a[168:171],v224 offset:13312\n" \
S_"lgkmcnt(6)\n" \
M_"v[112:115],a[120:123],a[144:147],v[112:115]\n" \
T_"a[138:139],v241 offset:7168\n" \
"v_lshrrev_b32 v236,3,v255\n" \
M_"v[116:119],a[124:127],a[144:147],v[116:119]\n" \
R_"a[144:147],v224 offset:14336\n" \
M_"v[128:131],a[120:123],a[148:151],v[128:131]\n" \
T_"a[140:141],v241 offset:6152\n" \
"v_lshrrev_b32 v237,2,v236\n" \
M_"v[132:135],a[124:127],a[148:151],v[132:135]\n" \
R_"a[148:151],v224 offset:15360\n" \
"v_lshlrev_b32 v237,4,v237\n" \
M_"v[144:147],a[120:123],a[152:155],v[144:147]\n" \
T_"a[142:143],v241 offset:7176\n" \
"v_and_b32 v238,3,v236\n" \
M_"v[148:151],a[124:127],a[152:155],v[148:151]\n" \
R_"a[152:155],v224 offset:16384\n" \
M_"v[160:163],a[120:123],a[156:159],v[160:163]\n" \
"v_add_u32 v237,v237,v238\n" \
M_"v[164:167],a[124:127],a[156:159],v[164:167]\n" \
R_"a[156:159],v224 offset:17408\n" \
"v_mul_u32_u24 v228,1152,v237\n" \
S_"lgkmcnt(5)\n" \
M_"v[176:179],a[120:123],a[160:163],v[176:179]\n" \
"v_bfe_u32 v237,v255,4,1\n" \
M_"v[180:183],a[124:127],a[160:163],v[180:183]\n" \
R_"a[160:163],v224 offset:18432\n" \
M_"v[192:195],a[120:123],a[164:167],v[192:195]\n" \
"v_lshlrev_b32 v237,2,v237\n" \
M_"v[196:199],a[124:127],a[164:167],v[196:199]\n" \
R_"a[164:167],v224 offset:19456\n" \
"v_and_b32 v238,7,v255\n" \
M_"v[208:211],a[120:123],a[168:171],v[208:211]\n" \
"v_xor_b32 v238,v238,v237\n" \
M_"v[212:215],a[124:127],a[168:171],v[212:215]\n" \
R_"a[168:171],v224 offset:20480\n" \
M_"v[112:115],a[128:131],a[144:147],v[112:115]\n" \
A_"m0,s74,4096\n" \
A_"s60,s66,s69\n" \
A_"s60,s60,0\n" \
M_"v[116:119],a[132:135],a[144:147],v[116:119]\n" \
R_"a[144:147],v224 offset:21504\n" \
S_"lgkmcnt(4)\n" \
L_"v242,s[48:51],s60 offen lds\n" \
M_"v[128:131],a[128:131],a[148:151],v[128:131]\n" \
L_"v249,s[48:51],s60 offen offset:1024 lds\n" \
M_"v[132:135],a[132:135],a[148:151],v[132:135]\n" \
R_"a[148:151],v224 offset:22528\n" \
M_"v[144:147],a[128:131],a[152:155],v[144:147]\n" \
L_"v243,s[48:51],s60 offen offset:2048 lds\n" \
S_"vmcnt(7)\n" \
T_"a[0:1],v241 offset:8192\n" \
M_"v[148:151],a[132:135],a[152:155],v[148:151]\n" \
R_"a[152:155],v224 offset:23552\n" \
L_"v244,s[48:51],s60 offen offset:3072 lds\n" \
T_"a[2:3],v241 offset:9216\n" \
M_"v[160:163],a[128:131],a[156:159],v[160:163]\n" \
T_"a[4:5],v241 offset:8200\n" \
T_"a[6:7],v241 offset:9224\n" \
M_"v[164:167],a[132:135],a[156:159],v[164:167]\n" \
R_"a[156:159],v224 offset:24576\n" \
S_"lgkmcnt(7)\n" \
M_"v[176:179],a[128:131],a[160:163],v[176:179]\n" \
T_"a[8:9],v241 offset:10240\n" \
T_"a[10:11],v241 offset:11264\n" \
M_"v[180:183],a[132:135],a[160:163],v[180:183]\n" \
R_"a[160:163],v224 offset:25600\n" \
T_"a[12:13],v241 offset:10248\n" \
M_"v[192:195],a[128:131],a[164:167],v[192:195]\n" \
T_"a[14:15],v241 offset:11272\n" \
M_"v[196:199],a[132:135],a[164:167],v[196:199]\n" \
R_"a[164:167],v224 offset:26624\n" \
M_"v[208:211],a[128:131],a[168:171],v[208:211]\n" \
M_"v[212:215],a[132:135],a[168:171],v[212:215]\n" \
R_"a[168:171],v224 offset:27648\n" \
M_"v[112:115],a[136:139],a[144:147],v[112:115]\n" \
A_"m0,s74,8192\n" \
A_"s60,s66,s69\n" \
A_"s60,s60,73728\n" \
M_"v[116:119],a[140:143],a[144:147],v[116:119]\n" \
R_"a[144:147],v225 offset:0\n" \
S_"lgkmcnt(3)\n" \
L_"v242,s[48:51],s60 offen lds\n" \
M_"v[128:131],a[136:139],a[148:151],v[128:131]\n" \
L_"v249,s[48:51],s60 offen offset:1024 lds\n" \
M_"v[132:135],a[140:143],a[148:151],v[132:135]\n" \
R_"a[148:151],v225 offset:1024\n" \
M_"v[144:147],a[136:139],a[152:155],v[144:147]\n" \
L_"v243,s[48:51],s60 offen offset:2048 lds\n" \
S_"vmcnt(7)\n" \
T_"a[16:17],v241 offset:0\n" \
M_"v[148:151],a[140:143],a[152:155],v[148:151]\n" \
R_"a[152:155],v225 offset:2048\n" \
L_"v244,s[48:51],s60 offen offset:3072 lds\n" \
T_"a[18:19],v241 offset:1024\n" \
M_"v[160:163],a[136:139],a[156:159],v[160:163]\n" \
T_"a[20:21],v241 offset:8\n" \
T_"a[22:23],v241 offset:1032\n" \
M_"v[164:167],a[140:143],a[156:159],v[164:167]\n" \
R_"a[156:159],v225 offset:3072\n" \
M_"v[176:179],a[136:139],a[160:163],v[176:179]\n" \
T_"a[24:25],v241 offset:2048\n" \
T_"a[26:27],v241 offset:3072\n" \
M_"v[180:183],a[140:143],a[160:163],v[180:183]\n" \
R_"a[160:163],v225 offset:4096\n" \
T_"a[28:29],v241 offset:2056\n" \
S_"lgkmcnt(10)\n" \
M_"v[192:195],a[136:139],a[164:167],v[192:195]\n" \
T_"a[30:31],v241 offset:3080\n" \
M_"v[196:199],a[140:143],a[164:167],v[196:199]\n" \
R_"a[164:167],v225 offset:5120\n" \
M_"v[208:211],a[136:139],a[168:171],v[208:211]\n" \
M_"v[212:215],a[140:143],a[168:171],v[212:215]\n" \
R_"a[168:171],v225 offset:6144\n" \
M_"v[112:115],a[0:3],a[144:147],v[112:115]\n" \
A_"m0,s74,0\n" \
A_"s60,s67,s69\n" \
A_"s60,s60,0\n" \
M_"v[116:119],a[4:7],a[144:147],v[116:119]\n" \
R_"a[144:147],v225 offset:7168\n" \
S_"lgkmcnt(3)\n" \
L_"v242,s[48:51],s60 offen lds\n" \
M_"v[128:131],a[0:3],a[148:151],v[128:131]\n" \
L_"v249,s[48:51],s60 offen offset:1024 lds\n" \
M_"v[132:135],a[4:7],a[148:151],v[132:135]\n" \
R_"a[148:151],v225 offset:8192\n" \
M_"v[144:147],a[0:3],a[152:155],v[144:147]\n" \
L_"v243,s[48:51],s60 offen offset:2048 lds\n" \
S_"vmcnt(7)\n" \
T_"a[32:33],v241 offset:4096\n" \
M_"v[148:151],a[4:7],a[152:155],v[148:151]\n" \
R_"a[152:155],v225 offset:9216\n" \
L_"v244,s[48:51],s60 offen offset:3072 lds\n" \
M_"v[160:163],a[0:3],a[156:159],v[160:163]\n" \
T_"a[34:35],v241 offset:5120\n" \
M_"v[164:167],a[4:7],a[156:159],v[164:167]\n" \
R_"a[156:159],v225 offset:10240\n" \
M_"v[176:179],a[0:3],a[160:163],v[176:179]\n" \
T_"a[36:37],v241 offset:4104\n" \
M_"v[180:183],a[4:7],a[160:163],v[180:183]\n" \
R_"a[160:163],v225 offset:11264\n" \
S_"lgkmcnt(6)\n" \
M_"v[192:195],a[0:3],a[164:167],v[192:195]\n" \
T_"a[38:39],v241 offset:5128\n" \
M_"v[196:199],a[4:7],a[164:167],v[196:199]\n" \
R_"a[164:167],v225 offset:12288\n" \
M_"v[208:211],a[0:3],a[168:171],v[208:211]\n" \
T_"a[40:41],v241 offset:6144\n" \
M_"v[212:215],a[4:7],a[168:171],v[212:215]\n" \
R_"a[168:171],v225 offset:13312\n" \
M_"v[112:115],a[8:11],a[144:147],v[112:115]\n" \
T_"a[42:43],v241 offset:7168\n" \
"v_lshl_add_u32 v228,v238,4,v228\n" \
M_"v[116:119],a[12:15],a[144:147],v[116:119]\n" \
R_"a[144:147],v225 offset:14336\n" \
M_"v[128:131],a[8:11],a[148:151],v[128:131]\n" \
T_"a[44:45],v241 offset:6152\n" \
"v_add_u32 v229,3584,v228\n" \
M_"v[132:135],a[12:15],a[148:151],v[132:135]\n" \
R_"a[148:151],v225 offset:15360\n" \
S_"lgkmcnt(6)\n" \
M_"v[144:147],a[8:11],a[152:155],v[144:147]\n" \
T_"a[46:47],v241 offset:7176\n" \
"v_add_u32 v230,7168,v228\n" \
M_"v[148:151],a[12:15],a[152:155],v[148:151]\n" \
R_"a[152:155],v225 offset:16384\n" \
M_"v[160:163],a[8:11],a[156:159],v[160:163]\n" \
M_"v[164:167],a[12:15],a[156:159],v[164:167]\n" \
R_"a[156:159],v225 offset:17408\n" \
"v_add_u32 v231,10752,v228\n" \
M_"v[176:179],a[8:11],a[160:163],v[176:179]\n" \
"s_mul_i32 s97,s89,36864\n" \
M_"v[180:183],a[12:15],a[160:163],v[180:183]\n" \
R_"a[160:163],v225 offset:18432\n" \
M_"v[192:195],a[8:11],a[164:167],v[192:195]\n" \
M_"v[196:199],a[12:15],a[164:167],v[196:199]\n" \
R_"a[164:167],v225 offset:19456\n" \
A_"s90,s97,s69\n" \
S_"lgkmcnt(3)\n" \
M_"v[208:211],a[8:11],a[168:171],v[208:211]\n" \
M_"v[212:215],a[12:15],a[168:171],v[212:215]\n" \
R_"a[168:171],v225 offset:20480\n" \
M_"v[112:115],a[16:19],a[144:147],v[112:115]\n" \
A_"m0,s74,4096\n" \
A_"s60,s67,s69\n" \
A_"s60,s60,73728\n" \
M_"v[116:119],a[20:23],a[144:147],v[116:119]\n" \
R_"a[144:147],v225 offset:21504\n" \
L_"v242,s[48:51],s60 offen lds\n" \
M_"v[128:131],a[16:19],a[148:151],v[128:131]\n" \
L_"v249,s[48:51],s60 offen offset:1024 lds\n" \
M_"v[132:135],a[20:23],a[148:151],v[132:135]\n" \
R_"a[148:151],v225 offset:22528\n" \
M_"v[144:147],a[16:19],a[152:155],v[144:147]\n" \
L_"v243,s[48:51],s60 offen offset:2048 lds\n" \
S_"vmcnt(7)\n" \
T_"a[48:49],v241 offset:8192\n" \
M_"v[148:151],a[20:23],a[152:155],v[148:151]\n" \
R_"a[152:155],v225 offset:23552\n" \
L_"v244,s[48:51],s60 offen offset:3072 lds\n" \
T_"a[50:51],v241 offset:9216\n" \
S_"lgkmcnt(5)\n" \
M_"v[160:163],a[16:19],a[156:159],v[160:163]\n" \
T_"a[52:53],v241 offset:8200\n" \
T_"a[54:55],v241 offset:9224\n" \
M_"v[164:167],a[20:23],a[156:159],v[164:167]\n" \
R_"a[156:159],v225 offset:24576\n" \
M_"v[176:179],a[16:19],a[160:163],v[176:179]\n" \
T_"a[56:57],v241 offset:10240\n" \
T_"a[58:59],v241 offset:11264\n" \
M_"v[180:183],a[20:23],a[160:163],v[180:183]\n" \
R_"a[160:163],v225 offset:25600\n" \
T_"a[60:61],v241 offset:10248\n" \
M_"v[192:195],a[16:19],a[164:167],v[192:195]\n" \
T_"a[62:63],v241 offset:11272\n" \
M_"v[196:199],a[20:23],a[164:167],v[196:199]\n" \
R_"a[164:167],v225 offset:26624\n" \
M_"v[208:211],a[16:19],a[168:171],v[208:211]\n" \
M_"v[212:215],a[20:23],a[168:171],v[212:215]\n" \
R_"a[168:171],v225 offset:27648\n" \
S_"lgkmcnt(7)\n" \
M_"v[112:115],a[24:27],a[144:147],v[112:115]\n" \
A_"m0,s74,8192\n" \
A_"s60,s64,s68\n" \
A_"s60,s60,0\n" \
M_"v[116:119],a[28:31],a[144:147],v[116:119]\n" \
R_"a[144:147],v226 offset:0\n" \
S_"lgkmcnt(3)\n" \
L_"v228,s[48:51],s60 offen lds\n" \
M_"v[128:131],a[24:27],a[148:151],v[128:131]\n" \
L_"v229,s[48:51],s60 offen offset:1024 lds\n" \
M_"v[132:135],a[28:31],a[148:151],v[132:135]\n" \
R_"a[148:151],v226 offset:1024\n" \
M_"v[144:147],a[24:27],a[152:155],v[144:147]\n" \
L_"v230,s[48:51],s60 offen offset:2048 lds\n" \
S_"vmcnt(7)\n" \
T_"a[64:65],v241 offset:0\n" \
M_"v[148:151],a[28:31],a[152:155],v[148:151]\n" \
R_"a[152:155],v226 offset:2048\n" \
L_"v231,s[48:51],s60 offen offset:3072 lds\n" \
T_"a[66:67],v241 offset:1024\n" \
M_"v[160:163],a[24:27],a[156:159],v[160:163]\n" \
T_"a[68:69],v241 offset:8\n" \
T_"a[70:71],v241 offset:1032\n" \
M_"v[164:167],a[28:31],a[156:159],v[164:167]\n" \
R_"a[156:159],v226 offset:3072\n" \
M_"v[176:179],a[24:27],a[160:163],v[176:179]\n" \
T_"a[72:73],v241 offset:2048\n" \
T_"a[74:75],v241 offset:3072\n" \
M_"v[180:183],a[28:31],a[160:163],v[180:183]\n" \
R_"a[160:163],v226 offset:4096\n" \
T_"a[76:77],v241 offset:2056\n" \
S_"lgkmcnt(10)\n" \
M_"v[192:195],a[24:27],a[164:167],v[192:195]\n" \
T_"a[78:79],v241 offset:3080\n" \
M_"v[196:199],a[28:31],a[164:167],v[196:199]\n" \
R_"a[164:167],v226 offset:5120\n" \
M_"v[208:211],a[24:27],a[168:171],v[208:211]\n" \
M_"v[212:215],a[28:31],a[168:171],v[212:215]\n" \
R_"a[168:171],v226 offset:6144\n" \
M_"v[112:115],a[32:35],a[144:147],v[112:115]\n" \
A_"m0,s74,0\n" \
A_"s60,s64,s68\n" \
A_"s60,s60,36864\n" \
M_"v[116:119],a[36:39],a[144:147],v[116:119]\n" \
R_"a[144:147],v226 offset:7168\n" \
S_"lgkmcnt(3)\n" \
L_"v228,s[48:51],s60 offen lds\n" \
M_"v[128:131],a[32:35],a[148:151],v[128:131]\n" \
L_"v229,s[48:51],s60 offen offset:1024 lds\n" \
M_"v[132:135],a[36:39],a[148:151],v[132:135]\n" \
R_"a[148:151],v226 offset:8192\n" \
M_"v[144:147],a[32:35],a[152:155],v[144:147]\n" \
L_"v230,s[48:51],s60 offen offset:2048 lds\n" \
S_"vmcnt(7)\n" \
T_"a[80:81],v241 offset:4096\n" \
M_"v[148:151],a[36:39],a[152:155],v[148:151]\n" \
R_"a[152:155],v226 offset:9216\n" \
L_"v231,s[48:51],s60 offen offset:3072 lds\n" \
M_"v[160:163],a[32:35],a[156:159],v[160:163]\n" \
T_"a[82:83],v241 offset:5120\n" \
M_"v[164:167],a[36:39],a[156:159],v[164:167]\n" \
R_"a[156:159],v226 offset:10240\n" \
M_"v[176:179],a[32:35],a[160:163],v[176:179]\n" \
T_"a[84:85],v241 offset:4104\n" \
M_"v[180:183],a[36:39],a[160:163],v[180:183]\n" \
R_"a[160:163],v226 offset:11264\n" \
S_"lgkmcnt(6)\n" \
M_"v[192:195],a[32:35],a[164:167],v[192:195]\n" \
T_"a[86:87],v241 offset:5128\n" \
M_"v[196:199],a[36:39],a[164:167],v[196:199]\n" \
R_"a[164:167],v226 offset:12288\n" \
M_"v[208:211],a[32:35],a[168:171],v[208:211]\n" \
T_"a[88:89],v241 offset:6144\n" \
M_"v[212:215],a[36:39],a[168:171],v[212:215]\n" \
R_"a[168:171],v226 offset:13312\n" \
M_"v[112:115],a[40:43],a[144:147],v[112:115]\n" \
T_"a[90:91],v241 offset:7168\n" \
M_"v[116:119],a[44:47],a[144:147],v[116:119]\n" \
R_"a[144:147],v226 offset:14336\n" \
M_"v[128:131],a[40:43],a[148:151],v[128:131]\n" \
T_"a[92:93],v241 offset:6152\n" \
M_"v[132:135],a[44:47],a[148:151],v[132:135]\n" \
R_"a[148:151],v226 offset:15360\n" \
S_"lgkmcnt(6)\n" \
M_"v[144:147],a[40:43],a[152:155],v[144:147]\n" \
T_"a[94:95],v241 offset:7176\n" \
M_"v[148:151],a[44:47],a[152:155],v[148:151]\n" \
R_"a[152:155],v226 offset:16384\n" \
M_"v[160:163],a[40:43],a[156:159],v[160:163]\n" \
M_"v[164:167],a[44:47],a[156:159],v[164:167]\n" \
R_"a[156:159],v226 offset:17408\n" \
M_"v[176:179],a[40:43],a[160:163],v[176:179]\n" \
M_"v[180:183],a[44:47],a[160:163],v[180:183]\n" \
R_"a[160:163],v226 offset:18432\n" \
M_"v[192:195],a[40:43],a[164:167],v[192:195]\n" \
M_"v[196:199],a[44:47],a[164:167],v[196:199]\n" \
R_"a[164:167],v226 offset:19456\n" \
S_"lgkmcnt(3)\n" \
M_"v[208:211],a[40:43],a[168:171],v[208:211]\n" \
M_"v[212:215],a[44:47],a[168:171],v[212:215]\n" \
R_"a[168:171],v226 offset:20480\n" \
M_"v[112:115],a[48:51],a[144:147],v[112:115]\n" \
A_"m0,s74,4096\n" \
A_"s60,s64,s68\n" \
A_"s60,s60,73728\n" \
M_"v[116:119],a[52:55],a[144:147],v[116:119]\n" \
R_"a[144:147],v226 offset:21504\n" \
L_"v228,s[48:51],s60 offen lds\n" \
M_"v[128:131],a[48:51],a[148:151],v[128:131]\n" \
L_"v229,s[48:51],s60 offen offset:1024 lds\n" \
M_"v[132:135],a[52:55],a[148:151],v[132:135]\n" \
R_"a[148:151],v226 offset:22528\n" \
M_"v[144:147],a[48:51],a[152:155],v[144:147]\n" \
L_"v230,s[48:51],s60 offen offset:2048 lds\n" \
S_"vmcnt(7)\n" \
T_"a[96:97],v250 offset:8192\n" \
M_"v[148:151],a[52:55],a[152:155],v[148:151]\n" \
R_"a[152:155],v226 offset:23552\n" \
L_"v231,s[48:51],s60 offen offset:3072 lds\n" \
T_"a[98:99],v250 offset:8704\n" \
S_"lgkmcnt(5)\n" \
M_"v[160:163],a[48:51],a[156:159],v[160:163]\n" \
T_"a[100:101],v250 offset:8200\n" \
T_"a[102:103],v250 offset:8712\n" \
M_"v[164:167],a[52:55],a[156:159],v[164:167]\n" \
R_"a[156:159],v226 offset:24576\n" \
M_"v[176:179],a[48:51],a[160:163],v[176:179]\n" \
T_"a[104:105],v251 offset:8192\n" \
T_"a[106:107],v251 offset:8704\n" \
M_"v[180:183],a[52:55],a[160:163],v[180:183]\n" \
R_"a[160:163],v226 offset:25600\n" \
T_"a[108:109],v251 offset:8200\n" \
M_"v[192:195],a[48:51],a[164:167],v[192:195]\n" \
T_"a[110:111],v251 offset:8712\n" \
M_"v[196:199],a[52:55],a[164:167],v[196:199]\n" \
R_"a[164:167],v226 offset:26624\n" \
M_"v[208:211],a[48:51],a[168:171],v[208:211]\n" \
M_"v[212:215],a[52:55],a[168:171],v[212:215]\n" \
R_"a[168:171],v226 offset:27648\n" \
S_"lgkmcnt(7)\n" \
M_"v[112:115],a[56:59],a[144:147],v[112:115]\n" \
A_"m0,s74,8192\n" \
A_"s60,s64,s68\n" \
A_"s60,s60,110592\n" \
M_"v[116:119],a[60:63],a[144:147],v[116:119]\n" \
R_"a[144:147],v227 offset:0\n" \
S_"lgkmcnt(3)\n" \
L_"v228,s[48:51],s60 offen lds\n" \
M_"v[128:131],a[56:59],a[148:151],v[128:131]\n" \
L_"v229,s[48:51],s60 offen offset:1024 lds\n" \
M_"v[132:135],a[60:63],a[148:151],v[132:135]\n" \
R_"a[148:151],v227 offset:1024\n" \
M_"v[144:147],a[56:59],a[152:155],v[144:147]\n" \
L_"v230,s[48:51],s60 offen offset:2048 lds\n" \
S_"vmcnt(7)\n" \
T_"a[172:173],v250 offset:0\n" \
M_"v[148:151],a[60:63],a[152:155],v[148:151]\n" \
R_"a[152:155],v227 offset:2048\n" \
L_"v231,s[48:51],s60 offen offset:3072 lds\n" \
T_"a[174:175],v250 offset:512\n" \
M_"v[160:163],a[56:59],a[156:159],v[160:163]\n" \
T_"a[176:177],v250 offset:8\n" \
T_"a[178:179],v250 offset:520\n" \
M_"v[164:167],a[60:63],a[156:159],v[164:167]\n" \
R_"a[156:159],v227 offset:3072\n" \
M_"v[176:179],a[56:59],a[160:163],v[176:179]\n" \
T_"a[180:181],v251 offset:0\n" \
T_"a[182:183],v251 offset:512\n" \
M_"v[180:183],a[60:63],a[160:163],v[180:183]\n" \
R_"a[160:163],v227 offset:4096\n" \
T_"a[184:185],v251 offset:8\n" \
S_"lgkmcnt(10)\n" \
M_"v[192:195],a[56:59],a[164:167],v[192:195]\n" \
T_"a[186:187],v251 offset:520\n" \
M_"v[196:199],a[60:63],a[164:167],v[196:199]\n" \
R_"a[164:167],v227 offset:5120\n" \
M_"v[208:211],a[56:59],a[168:171],v[208:211]\n" \
M_"v[212:215],a[60:63],a[168:171],v[212:215]\n" \
R_"a[168:171],v227 offset:6144\n" \
M_"v[112:115],a[64:67],a[144:147],v[112:115]\n" \
A_"m0,s74,0\n" \
A_"s60,s65,s68\n" \
A_"s60,s60,0\n" \
M_"v[116:119],a[68:71],a[144:147],v[116:119]\n" \
R_"a[144:147],v227 offset:7168\n" \
S_"lgkmcnt(3)\n" \
L_"v228,s[48:51],s60 offen lds\n" \
M_"v[128:131],a[64:67],a[148:151],v[128:131]\n" \
L_"v229,s[48:51],s60 offen offset:1024 lds\n" \
M_"v[132:135],a[68:71],a[148:151],v[132:135]\n" \
R_"a[148:151],v227 offset:8192\n" \
M_"v[144:147],a[64:67],a[152:155],v[144:147]\n" \
L_"v230,s[48:51],s60 offen offset:2048 lds\n" \
S_"vmcnt(7)\n" \
T_"a[188:189],v250 offset:4096\n" \
M_"v[148:151],a[68:71],a[152:155],v[148:151]\n" \
R_"a[152:155],v227 offset:9216\n" \
L_"v231,s[48:51],s60 offen offset:3072 lds\n" \
M_"v[160:163],a[64:67],a[156:159],v[160:163]\n" \
T_"a[190:191],v250 offset:4608\n" \
M_"v[164:167],a[68:71],a[156:159],v[164:167]\n" \
R_"a[156:159],v227 offset:10240\n" \
M_"v[176:179],a[64:67],a[160:163],v[176:179]\n" \
T_"a[192:193],v250 offset:4104\n" \
M_"v[180:183],a[68:71],a[160:163],v[180:183]\n" \
R_"a[160:163],v227 offset:11264\n" \
S_"lgkmcnt(6)\n" \
M_"v[192:195],a[64:67],a[164:167],v[192:195]\n" \
T_"a[194:195],v250 offset:4616\n" \
M_"v[196:199],a[68:71],a[164:167],v[196:199]\n" \
R_"a[164:167],v227 offset:12288\n" \
M_"v[208:211],a[64:67],a[168:171],v[208:211]\n" \
T_"a[196:197],v251 offset:4096\n" \
M_"v[212:215],a[68:71],a[168:171],v[212:215]\n" \
R_"a[168:171],v227 offset:13312\n" \
M_"v[112:115],a[72:75],a[144:147],v[112:115]\n" \
T_"a[198:199],v251 offset:4608\n" \
M_"v[116:119],a[76:79],a[144:147],v[116:119]\n" \
R_"a[144:147],v227 offset:14336\n" \
M_"v[128:131],a[72:75],a[148:151],v[128:131]\n" \
T_"a[200:201],v251 offset:4104\n" \
M_"v[132:135],a[76:79],a[148:151],v[132:135]\n" \
R_"a[148:151],v227 offset:15360\n" \
S_"lgkmcnt(6)\n" \
M_"v[144:147],a[72:75],a[152:155],v[144:147]\n" \
T_"a[202:203],v251 offset:4616\n" \
M_"v[148:151],a[76:79],a[152:155],v[148:151]\n" \
R_"a[152:155],v227 offset:16384\n" \
M_"v[160:163],a[72:75],a[156:159],v[160:163]\n" \
M_"v[164:167],a[76:79],a[156:159],v[164:167]\n" \
R_"a[156:159],v227 offset:17408\n" \
M_"v[176:179],a[72:75],a[160:163],v[176:179]\n" \
M_"v[180:183],a[76:79],a[160:163],v[180:183]\n" \
R_"a[160:163],v227 offset:18432\n" \
M_"v[192:195],a[72:75],a[164:167],v[192:195]\n" \
M_"v[196:199],a[76:79],a[164:167],v[196:199]\n" \
R_"a[164:167],v227 offset:19456\n" \
S_"lgkmcnt(3)\n" \
M_"v[208:211],a[72:75],a[168:171],v[208:211]\n" \
M_"v[212:215],a[76:79],a[168:171],v[212:215]\n" \
R_"a[168:171],v227 offset:20480\n" \
M_"v[112:115],a[80:83],a[144:147],v[112:115]\n" \
A_"m0,s74,4096\n" \
A_"s60,s65,s68\n" \
A_"s60,s60,36864\n" \
M_"v[116:119],a[84:87],a[144:147],v[116:119]\n" \
R_"a[144:147],v227 offset:21504\n" \
L_"v228,s[48:51],s60 offen lds\n" \
M_"v[128:131],a[80:83],a[148:151],v[128:131]\n" \
L_"v229,s[48:51],s60 offen offset:1024 lds\n" \
M_"v[132:135],a[84:87],a[148:151],v[132:135]\n" \
R_"a[148:151],v227 offset:22528\n" \
M_"v[144:147],a[80:83],a[152:155],v[144:147]\n" \
L_"v230,s[48:51],s60 offen offset:2048 lds\n" \
S_"vmcnt(7)\n" \
T_"a[204:205],v250 offset:8192\n" \
M_"v[148:151],a[84:87],a[152:155],v[148:151]\n" \
R_"a[152:155],v227 offset:23552\n" \
L_"v231,s[48:51],s60 offen offset:3072 lds\n" \
T_"a[206:207],v250 offset:8704\n" \
S_"lgkmcnt(5)\n" \
M_"v[160:163],a[80:83],a[156:159],v[160:163]\n" \
T_"a[208:209],v250 offset:8200\n" \
T_"a[210:211],v250 offset:8712\n" \
M_"v[164:167],a[84:87],a[156:159],v[164:167]\n" \
R_"a[156:159],v227 offset:24576\n" \
M_"v[176:179],a[80:83],a[160:163],v[176:179]\n" \
T_"a[212:213],v251 offset:8192\n" \
T_"a[214:215],v251 offset:8704\n" \
M_"v[180:183],a[84:87],a[160:163],v[180:183]\n" \
R_"a[160:163],v227 offset:25600\n" \
T_"a[216:217],v251 offset:8200\n" \
M_"v[192:195],a[80:83],a[164:167],v[192:195]\n" \
T_"a[218:219],v251 offset:8712\n" \
M_"v[196:199],a[84:87],a[164:167],v[196:199]\n" \
R_"a[164:167],v227 offset:26624\n" \
M_"v[208:211],a[80:83],a[168:171],v[208:211]\n" \
M_"v[212:215],a[84:87],a[168:171],v[212:215]\n" \
R_"a[168:171],v227 offset:27648\n" \
S_"lgkmcnt(7)\n" \
M_"v[112:115],a[88:91],a[144:147],v[112:115]\n" \
A_"m0,s74,8192\n" \
A_"s60,s65,s68\n" \
A_"s60,s60,73728\n" \
M_"v[116:119],a[92:95],a[144:147],v[116:119]\n" \
R_"a[144:147],v224 offset:0\n" \
S_"lgkmcnt(3)\n" \
L_"v228,s[48:51],s60 offen lds\n" \
M_"v[128:131],a[88:91],a[148:151],v[128:131]\n" \
L_"v229,s[48:51],s60 offen offset:1024 lds\n" \
M_"v[132:135],a[92:95],a[148:151],v[132:135]\n" \
R_"a[148:151],v224 offset:1024\n" \
M_"v[144:147],a[88:91],a[152:155],v[144:147]\n" \
L_"v230,s[48:51],s60 offen offset:2048 lds\n" \
S_"vmcnt(7)\n" \
T_"a[220:221],v250 offset:0\n" \
M_"v[148:151],a[92:95],a[152:155],v[148:151]\n" \
R_"a[152:155],v224 offset:2048\n" \
L_"v231,s[48:51],s60 offen offset:3072 lds\n" \
T_"a[222:223],v250 offset:512\n" \
M_"v[160:163],a[88:91],a[156:159],v[160:163]\n" \
T_"a[224:225],v250 offset:8\n" \
T_"a[226:227],v250 offset:520\n" \
M_"v[164:167],a[92:95],a[156:159],v[164:167]\n" \
R_"a[156:159],v224 offset:3072\n" \
M_"v[176:179],a[88:91],a[160:163],v[176:179]\n" \
T_"a[228:229],v251 offset:0\n" \
T_"a[230:231],v251 offset:512\n" \
M_"v[180:183],a[92:95],a[160:163],v[180:183]\n" \
R_"a[160:163],v224 offset:4096\n" \
T_"a[232:233],v251 offset:8\n" \
S_"lgkmcnt(10)\n" \
M_"v[192:195],a[88:91],a[164:167],v[192:195]\n" \
T_"a[234:235],v251 offset:520\n" \
M_"v[196:199],a[92:95],a[164:167],v[196:199]\n" \
R_"a[164:167],v224 offset:5120\n" \
M_"v[208:211],a[88:91],a[168:171],v[208:211]\n" \
M_"v[212:215],a[92:95],a[168:171],v[212:215]\n" \
R_"a[168:171],v224 offset:6144\n" \
M_"v[0:3],a[96:99],a[144:147],0\n" \
A_"m0,s74,0\n" \
A_"s60,s65,s68\n" \
A_"s60,s60,110592\n" \
M_"v[4:7],a[100:103],a[144:147],0\n" \
S_"lgkmcnt(2)\n" \
L_"v228,s[48:51],s60 offen lds\n" \
M_"v[8:11],a[104:107],a[144:147],0\n" \
L_"v229,s[48:51],s60 offen offset:1024 lds\n" \
M_"v[12:15],a[108:111],a[144:147],0\n" \
R_"a[144:147],v224 offset:7168\n" \
M_"v[16:19],a[96:99],a[148:151],0\n" \
L_"v230,s[48:51],s60 offen offset:2048 lds\n" \
S_"vmcnt(7)\n" \
T_"a[236:237],v250 offset:4096\n" \
M_"v[20:23],a[100:103],a[148:151],0\n" \
L_"v231,s[48:51],s60 offen offset:3072 lds\n" \
T_"a[238:239],v250 offset:4608\n" \
M_"v[24:27],a[104:107],a[148:151],0\n" \
T_"a[240:241],v250 offset:4104\n" \
C_"v128,v128,v129\n" \
C_"v129,v130,v131\n" \
M_"v[28:31],a[108:111],a[148:151],0\n" \
R_"a[148:151],v224 offset:8192\n" \
C_"v130,v132,v133\n" \
C_"v131,v134,v135\n" \
M_"v[32:35],a[96:99],a[152:155],0\n" \
T_"a[242:243],v250 offset:4616\n" \
A_"s61,s72,0\n" \
"buffer_store_dwordx4 v[128:131],v240,s[52:55],s61 offen offset:2048 sc1\n" \
M_"v[36:39],a[100:103],a[152:155],0\n" \
T_"a[244:245],v251 offset:4096\n" \
C_"v144,v144,v145\n" \
C_"v145,v146,v147\n" \
M_"v[40:43],a[104:107],a[152:155],0\n" \
T_"a[246:247],v251 offset:4608\n" \
C_"v146,v148,v149\n" \
C_"v147,v150,v151\n" \
M_"v[44:47],a[108:111],a[152:155],0\n" \
R_"a[152:155],v224 offset:9216\n" \
A_"s61,s72,4096\n" \
M_"v[48:51],a[96:99],a[156:159],0\n" \
T_"a[248:249],v251 offset:4104\n" \
"buffer_store_dwordx4 v[144:147],v240,s[52:55],s61 offen sc1\n" \
C_"v160,v160,v161\n" \
M_"v[52:55],a[100:103],a[156:159],0\n" \
T_"a[250:251],v251 offset:4616\n" \
C_"v161,v162,v163\n" \
C_"v162,v164,v165\n" \
M_"v[56:59],a[104:107],a[156:159],0\n" \
C_"v163,v166,v167\n" \
M_"v[60:63],a[108:111],a[156:159],0\n" \
R_"a[156:159],v224 offset:10240\n" \
M_"v[64:67],a[96:99],a[160:163],0\n" \
"buffer_store_dwordx4 v[160:163],v240,s[52:55],s61 offen offset:2048 sc1\n" \
C_"v176,v176,v177\n" \
C_"v177,v178,v179\n" \
M_"v[68:71],a[100:103],a[160:163],0\n" \
C_"v178,v180,v181\n" \
C_"v179,v182,v183\n" \
A_"s61,s72,8192\n" \
M_"v[72:75],a[104:107],a[160:163],0\n" \
M_"v[76:79],a[108:111],a[160:163],0\n" \
R_"a[160:163],v224 offset:11264\n" \
S_"lgkmcnt(2)\n" \
M_"v[80:83],a[96:99],a[164:167],0\n" \
"buffer_store_dwordx4 v[176:179],v240,s[52:55],s61 offen sc1\n" \
C_"v192,v192,v193\n" \
C_"v193,v194,v195\n" \
M_"v[84:87],a[100:103],a[164:167],0\n" \
C_"v194,v196,v197\n" \
C_"v195,v198,v199\n" \
M_"v[88:91],a[104:107],a[164:167],0\n" \
M_"v[92:95],a[108:111],a[164:167],0\n" \
R_"a[164:167],v224 offset:12288\n" \
M_"v[96:99],a[96:99],a[168:171],0\n" \
"buffer_store_dwordx4 v[192:195],v240,s[52:55],s61 offen offset:2048 sc1\n" \
C_"v208,v208,v209\n" \
C_"v209,v210,v211\n" \
M_"v[100:103],a[100:103],a[168:171],0\n" \
C_"v210,v212,v213\n" \
C_"v211,v214,v215\n" \
A_"s61,s72,12288\n" \
M_"v[104:107],a[104:107],a[168:171],0\n" \
M_"v[108:111],a[108:111],a[168:171],0\n" \
R_"a[168:171],v224 offset:13312\n" \
M_"v[0:3],a[172:175],a[144:147],v[0:3]\n" \
A_"m0,s74,4096\n" \
A_"s60,s66,s68\n" \
A_"s60,s60,0\n" \
M_"v[4:7],a[176:179],a[144:147],v[4:7]\n" \
L_"v228,s[48:51],s60 offen lds\n" \
"buffer_store_dwordx4 v[208:211],v240,s[52:55],s61 offen sc1\n" \
C_"v232,v112,v113\n" \
M_"v[8:11],a[180:183],a[144:147],v[8:11]\n" \
L_"v229,s[48:51],s60 offen offset:1024 lds\n" \
C_"v233,v114,v115\n" \
C_"v234,v116,v117\n" \
M_"v[12:15],a[184:187],a[144:147],v[12:15]\n" \
R_"a[144:147],v224 offset:14336\n" \
C_"v235,v118,v119\n" \
A_"s61,s72,0\n" \
M_"v[16:19],a[172:175],a[148:151],v[16:19]\n" \
L_"v230,s[48:51],s60 offen offset:2048 lds\n" \
S_"vmcnt(13)\n" \
T_"a[112:113],v250 offset:8192\n" \
M_"v[20:23],a[176:179],a[148:151],v[20:23]\n" \
L_"v231,s[48:51],s60 offen offset:3072 lds\n" \
T_"a[114:115],v250 offset:8704\n" \
"buffer_store_dwordx4 v[232:235],v247,s[52:55],s61 offen sc1\n" \
M_"v[24:27],a[180:183],a[148:151],v[24:27]\n" \
T_"a[116:117],v250 offset:8200\n" \
T_"a[118:119],v250 offset:8712\n" \
M_"v[28:31],a[184:187],a[148:151],v[28:31]\n" \
R_"a[148:151],v224 offset:15360\n" \
M_"v[32:35],a[172:175],a[152:155],v[32:35]\n" \
T_"a[120:121],v251 offset:8192\n" \
T_"a[122:123],v251 offset:8704\n" \
M_"v[36:39],a[176:179],a[152:155],v[36:39]\n" \
T_"a[124:125],v251 offset:8200\n" \
M_"v[40:43],a[180:183],a[152:155],v[40:43]\n" \
T_"a[126:127],v251 offset:8712\n" \
M_"v[44:47],a[184:187],a[152:155],v[44:47]\n" \
R_"a[152:155],v224 offset:16384\n" \
S_"lgkmcnt(8)\n" \
M_"v[48:51],a[172:175],a[156:159],v[48:51]\n" \
M_"v[52:55],a[176:179],a[156:159],v[52:55]\n" \
M_"v[56:59],a[180:183],a[156:159],v[56:59]\n" \
A_"m0,s74,8192\n" \
A_"s60,s66,s68\n" \
A_"s60,s60,36864\n" \
M_"v[60:63],a[184:187],a[156:159],v[60:63]\n" \
R_"a[156:159],v224 offset:17408\n" \
S_"lgkmcnt(2)\n" \
L_"v228,s[48:51],s60 offen lds\n" \
M_"v[64:67],a[172:175],a[160:163],v[64:67]\n" \
L_"v229,s[48:51],s60 offen offset:1024 lds\n" \
M_"v[68:71],a[176:179],a[160:163],v[68:71]\n" \
M_"v[72:75],a[180:183],a[160:163],v[72:75]\n" \
L_"v230,s[48:51],s60 offen offset:2048 lds\n" \
S_"vmcnt(14)\n" \
T_"a[128:129],v250 offset:0\n" \
M_"v[76:79],a[184:187],a[160:163],v[76:79]\n" \
R_"a[160:163],v224 offset:18432\n" \
L_"v231,s[48:51],s60 offen offset:3072 lds\n" \
T_"a[130:131],v250 offset:512\n" \
M_"v[80:83],a[172:175],a[164:167],v[80:83]\n" \
T_"a[132:133],v250 offset:8\n" \
T_"a[134:135],v250 offset:520\n" \
M_"v[84:87],a[176:179],a[164:167],v[84:87]\n" \
M_"v[88:91],a[180:183],a[164:167],v[88:91]\n" \
T_"a[136:137],v251 offset:0\n" \
T_"a[138:139],v251 offset:512\n" \
M_"v[92:95],a[184:187],a[164:167],v[92:95]\n" \
R_"a[164:167],v224 offset:19456\n" \
T_"a[140:141],v251 offset:8\n" \
M_"v[96:99],a[172:175],a[168:171],v[96:99]\n" \
T_"a[142:143],v251 offset:520\n" \
M_"v[100:103],a[176:179],a[168:171],v[100:103]\n" \
M_"v[104:107],a[180:183],a[168:171],v[104:107]\n" \
M_"v[108:111],a[184:187],a[168:171],v[108:111]\n" \
R_"a[168:171],v224 offset:20480\n" \
M_"v[0:3],a[188:191],a[144:147],v[0:3]\n" \
A_"m0,s74,0\n" \
A_"s60,s66,s68\n" \
A_"s60,s60,73728\n" \
M_"v[4:7],a[192:195],a[144:147],v[4:7]\n" \
S_"lgkmcnt(1)\n" \
L_"v228,s[48:51],s60 offen lds\n" \
M_"v[8:11],a[196:199],a[144:147],v[8:11]\n" \
L_"v229,s[48:51],s60 offen offset:1024 lds\n" \
M_"v[12:15],a[200:203],a[144:147],v[12:15]\n" \
R_"a[144:147],v224 offset:21504\n" \
M_"v[16:19],a[188:191],a[148:151],v[16:19]\n" \
L_"v230,s[48:51],s60 offen offset:2048 lds\n" \
S_"vmcnt(8)\n" \
T_"a[0:1],v250 offset:4096\n" \
M_"v[20:23],a[192:195],a[148:151],v[20:23]\n" \
L_"v231,s[48:51],s60 offen offset:3072 lds\n" \
T_"a[2:3],v250 offset:4608\n" \
M_"v[24:27],a[196:199],a[148:151],v[24:27]\n" \
T_"a[4:5],v250 offset:4104\n" \
M_"v[28:31],a[200:203],a[148:151],v[28:31]\n" \
R_"a[148:151],v224 offset:22528\n" \
M_"v[32:35],a[188:191],a[152:155],v[32:35]\n" \
T_"a[6:7],v250 offset:4616\n" \
M_"v[36:39],a[192:195],a[152:155],v[36:39]\n" \
T_"a[8:9],v251 offset:4096\n" \
M_"v[40:43],a[196:199],a[152:155],v[40:43]\n" \
T_"a[10:11],v251 offset:4608\n" \
M_"v[44:47],a[200:203],a[152:155],v[44:47]\n" \
R_"a[152:155],v224 offset:23552\n" \
M_"v[48:51],a[188:191],a[156:159],v[48:51]\n" \
T_"a[12:13],v251 offset:4104\n" \
M_"v[52:55],a[192:195],a[156:159],v[52:55]\n" \
T_"a[14:15],v251 offset:4616\n" \
M_"v[56:59],a[196:199],a[156:159],v[56:59]\n" \
M_"v[60:63],a[200:203],a[156:159],v[60:63]\n" \
R_"a[156:159],v224 offset:24576\n" \
M_"v[64:67],a[188:191],a[160:163],v[64:67]\n" \
M_"v[68:71],a[192:195],a[160:163],v[68:71]\n" \
M_"v[72:75],a[196:199],a[160:163],v[72:75]\n" \
M_"v[76:79],a[200:203],a[160:163],v[76:79]\n" \
R_"a[160:163],v224 offset:25600\n" \
M_"v[80:83],a[188:191],a[164:167],v[80:83]\n" \
M_"v[84:87],a[192:195],a[164:167],v[84:87]\n" \
M_"v[88:91],a[196:199],a[164:167],v[88:91]\n" \
M_"v[92:95],a[200:203],a[164:167],v[92:95]\n" \
R_"a[164:167],v224 offset:26624\n" \
S_"lgkmcnt(2)\n" \
M_"v[96:99],a[188:191],a[168:171],v[96:99]\n" \
M_"v[100:103],a[192:195],a[168:171],v[100:103]\n" \
M_"v[104:107],a[196:199],a[168:171],v[104:107]\n" \
M_"v[108:111],a[200:203],a[168:171],v[108:111]\n" \
R_"a[168:171],v224 offset:27648\n" \
M_"v[0:3],a[204:207],a[144:147],v[0:3]\n" \
A_"m0,s74,4096\n" \
A_"s60,s66,s68\n" \
A_"s60,s60,110592\n" \
M_"v[4:7],a[208:211],a[144:147],v[4:7]\n" \
L_"v228,s[48:51],s60 offen lds\n" \
M_"v[8:11],a[212:215],a[144:147],v[8:11]\n" \
L_"v229,s[48:51],s60 offen offset:1024 lds\n" \
M_"v[12:15],a[216:219],a[144:147],v[12:15]\n" \
R_"a[144:147],v225 offset:0\n" \
M_"v[16:19],a[204:207],a[148:151],v[16:19]\n" \
L_"v230,s[48:51],s60 offen offset:2048 lds\n" \
S_"vmcnt(7)\n" \
T_"a[16:17],v250 offset:8192\n" \
M_"v[20:23],a[208:211],a[148:151],v[20:23]\n" \
L_"v231,s[48:51],s60 offen offset:3072 lds\n" \
T_"a[18:19],v250 offset:8704\n" \
M_"v[24:27],a[212:215],a[148:151],v[24:27]\n" \
T_"a[20:21],v250 offset:8200\n" \
T_"a[22:23],v250 offset:8712\n" \
M_"v[28:31],a[216:219],a[148:151],v[28:31]\n" \
R_"a[148:151],v225 offset:1024\n" \
M_"v[32:35],a[204:207],a[152:155],v[32:35]\n" \
T_"a[24:25],v251 offset:8192\n" \
T_"a[26:27],v251 offset:8704\n" \
M_"v[36:39],a[208:211],a[152:155],v[36:39]\n" \
T_"a[28:29],v251 offset:8200\n" \
M_"v[40:43],a[212:215],a[152:155],v[40:43]\n" \
T_"a[30:31],v251 offset:8712\n" \
M_"v[44:47],a[216:219],a[152:155],v[44:47]\n" \
R_"a[152:155],v225 offset:2048\n" \
M_"v[48:51],a[204:207],a[156:159],v[48:51]\n" \
M_"v[52:55],a[208:211],a[156:159],v[52:55]\n" \
M_"v[56:59],a[212:215],a[156:159],v[56:59]\n" \
A_"m0,s74,8192\n" \
A_"s60,s67,s68\n" \
A_"s60,s60,0\n" \
M_"v[60:63],a[216:219],a[156:159],v[60:63]\n" \
R_"a[156:159],v225 offset:3072\n" \
S_"lgkmcnt(2)\n" \
L_"v228,s[48:51],s60 offen lds\n" \
M_"v[64:67],a[204:207],a[160:163],v[64:67]\n" \
L_"v229,s[48:51],s60 offen offset:1024 lds\n" \
M_"v[68:71],a[208:211],a[160:163],v[68:71]\n" \
M_"v[72:75],a[212:215],a[160:163],v[72:75]\n" \
L_"v230,s[48:51],s60 offen offset:2048 lds\n" \
S_"vmcnt(7)\n" \
T_"a[32:33],v250 offset:0\n" \
M_"v[76:79],a[216:219],a[160:163],v[76:79]\n" \
R_"a[160:163],v225 offset:4096\n" \
L_"v231,s[48:51],s60 offen offset:3072 lds\n" \
T_"a[34:35],v250 offset:512\n" \
M_"v[80:83],a[204:207],a[164:167],v[80:83]\n" \
T_"a[36:37],v250 offset:8\n" \
T_"a[38:39],v250 offset:520\n" \
M_"v[84:87],a[208:211],a[164:167],v[84:87]\n" \
M_"v[88:91],a[212:215],a[164:167],v[88:91]\n" \
T_"a[40:41],v251 offset:0\n" \
T_"a[42:43],v251 offset:512\n" \
M_"v[92:95],a[216:219],a[164:167],v[92:95]\n" \
R_"a[164:167],v225 offset:5120\n" \
T_"a[44:45],v251 offset:8\n" \
M_"v[96:99],a[204:207],a[168:171],v[96:99]\n" \
T_"a[46:47],v251 offset:520\n" \
M_"v[100:103],a[208:211],a[168:171],v[100:103]\n" \
M_"v[104:107],a[212:215],a[168:171],v[104:107]\n" \
M_"v[108:111],a[216:219],a[168:171],v[108:111]\n" \
R_"a[168:171],v225 offset:6144\n" \
M_"v[0:3],a[220:223],a[144:147],v[0:3]\n" \
A_"m0,s74,0\n" \
A_"s60,s67,s68\n" \
A_"s60,s60,36864\n" \
M_"v[4:7],a[224:227],a[144:147],v[4:7]\n" \
S_"lgkmcnt(1)\n" \
L_"v228,s[48:51],s60 offen lds\n" \
M_"v[8:11],a[228:231],a[144:147],v[8:11]\n" \
L_"v229,s[48:51],s60 offen offset:1024 lds\n" \
M_"v[12:15],a[232:235],a[144:147],v[12:15]\n" \
R_"a[144:147],v225 offset:7168\n" \
M_"v[16:19],a[220:223],a[148:151],v[16:19]\n" \
L_"v230,s[48:51],s60 offen offset:2048 lds\n" \
S_"vmcnt(7)\n" \
T_"a[48:49],v250 offset:4096\n" \
M_"v[20:23],a[224:227],a[148:151],v[20:23]\n" \
L_"v231,s[48:51],s60 offen offset:3072 lds\n" \
T_"a[50:51],v250 offset:4608\n" \
M_"v[24:27],a[228:231],a[148:151],v[24:27]\n" \
T_"a[52:53],v250 offset:4104\n" \
M_"v[28:31],a[232:235],a[148:151],v[28:31]\n" \
R_"a[148:151],v225 offset:8192\n" \
M_"v[32:35],a[220:223],a[152:155],v[32:35]\n" \
T_"a[54:55],v250 offset:4616\n" \
M_"v[36:39],a[224:227],a[152:155],v[36:39]\n" \
T_"a[56:57],v251 offset:4096\n" \
M_"v[40:43],a[228:231],a[152:155],v[40:43]\n" \
T_"a[58:59],v251 offset:4608\n" \
M_"v[44:47],a[232:235],a[152:155],v[44:47]\n" \
R_"a[152:155],v225 offset:9216\n" \
M_"v[48:51],a[220:223],a[156:159],v[48:51]\n" \
T_"a[60:61],v251 offset:4104\n" \
M_"v[52:55],a[224:227],a[156:159],v[52:55]\n" \
T_"a[62:63],v251 offset:4616\n" \
M_"v[56:59],a[228:231],a[156:159],v[56:59]\n" \
M_"v[60:63],a[232:235],a[156:159],v[60:63]\n" \
R_"a[156:159],v225 offset:10240\n" \
M_"v[64:67],a[220:223],a[160:163],v[64:67]\n" \
M_"v[68:71],a[224:227],a[160:163],v[68:71]\n" \
M_"v[72:75],a[228:231],a[160:163],v[72:75]\n" \
M_"v[76:79],a[232:235],a[160:163],v[76:79]\n" \
R_"a[160:163],v225 offset:11264\n" \
M_"v[80:83],a[220:223],a[164:167],v[80:83]\n" \
M_"v[84:87],a[224:227],a[164:167],v[84:87]\n" \
M_"v[88:91],a[228:231],a[164:167],v[88:91]\n" \
M_"v[92:95],a[232:235],a[164:167],v[92:95]\n" \
R_"a[164:167],v225 offset:12288\n" \
S_"lgkmcnt(2)\n" \
M_"v[96:99],a[220:223],a[168:171],v[96:99]\n" \
M_"v[100:103],a[224:227],a[168:171],v[100:103]\n" \
M_"v[104:107],a[228:231],a[168:171],v[104:107]\n" \
M_"v[108:111],a[232:235],a[168:171],v[108:111]\n" \
R_"a[168:171],v225 offset:13312\n" \
M_"v[0:3],a[236:239],a[144:147],v[0:3]\n" \
A_"m0,s74,4096\n" \
A_"s60,s67,s68\n" \
A_"s60,s60,73728\n" \
M_"v[4:7],a[240:243],a[144:147],v[4:7]\n" \
L_"v228,s[48:51],s60 offen lds\n" \
M_"v[8:11],a[244:247],a[144:147],v[8:11]\n" \
L_"v229,s[48:51],s60 offen offset:1024 lds\n" \
M_"v[12:15],a[248:251],a[144:147],v[12:15]\n" \
R_"a[144:147],v225 offset:14336\n" \
M_"v[16:19],a[236:239],a[148:151],v[16:19]\n" \
L_"v230,s[48:51],s60 offen offset:2048 lds\n" \
S_"vmcnt(7)\n" \
T_"a[64:65],v250 offset:8192\n" \
M_"v[20:23],a[240:243],a[148:151],v[20:23]\n" \
L_"v231,s[48:51],s60 offen offset:3072 lds\n" \
T_"a[66:67],v250 offset:8704\n" \
M_"v[24:27],a[244:247],a[148:151],v[24:27]\n" \
T_"a[68:69],v250 offset:8200\n" \
T_"a[70:71],v250 offset:8712\n" \
M_"v[28:31],a[248:251],a[148:151],v[28:31]\n" \
R_"a[148:151],v225 offset:15360\n" \
M_"v[32:35],a[236:239],a[152:155],v[32:35]\n" \
T_"a[72:73],v251 offset:8192\n" \
T_"a[74:75],v251 offset:8704\n" \
M_"v[36:39],a[240:243],a[152:155],v[36:39]\n" \
T_"a[76:77],v251 offset:8200\n" \
M_"v[40:43],a[244:247],a[152:155],v[40:43]\n" \
T_"a[78:79],v251 offset:8712\n" \
M_"v[44:47],a[248:251],a[152:155],v[44:47]\n" \
R_"a[152:155],v225 offset:16384\n" \
M_"v[48:51],a[236:239],a[156:159],v[48:51]\n" \
M_"v[52:55],a[240:243],a[156:159],v[52:55]\n" \
M_"v[56:59],a[244:247],a[156:159],v[56:59]\n" \
A_"m0,s74,8192\n" \
A_"s60,s67,s68\n" \
A_"s60,s60,110592\n" \
M_"v[60:63],a[248:251],a[156:159],v[60:63]\n" \
R_"a[156:159],v225 offset:17408\n" \
S_"lgkmcnt(2)\n" \
L_"v228,s[48:51],s60 offen lds\n" \
M_"v[64:67],a[236:239],a[160:163],v[64:67]\n" \
L_"v229,s[48:51],s60 offen offset:1024 lds\n" \
M_"v[68:71],a[240:243],a[160:163],v[68:71]\n" \
M_"v[72:75],a[244:247],a[160:163],v[72:75]\n" \
L_"v230,s[48:51],s60 offen offset:2048 lds\n" \
S_"vmcnt(7)\n" \
T_"a[80:81],v250 offset:0\n" \
M_"v[76:79],a[248:251],a[160:163],v[76:79]\n" \
R_"a[160:163],v225 offset:18432\n" \
L_"v231,s[48:51],s60 offen offset:3072 lds\n" \
T_"a[82:83],v250 offset:512\n" \
M_"v[80:83],a[236:239],a[164:167],v[80:83]\n" \
T_"a[84:85],v250 offset:8\n" \
T_"a[86:87],v250 offset:520\n" \
M_"v[84:87],a[240:243],a[164:167],v[84:87]\n" \
M_"v[88:91],a[244:247],a[164:167],v[88:91]\n" \
T_"a[88:89],v251 offset:0\n" \
T_"a[90:91],v251 offset:512\n" \
M_"v[92:95],a[248:251],a[164:167],v[92:95]\n" \
R_"a[164:167],v225 offset:19456\n" \
T_"a[92:93],v251 offset:8\n" \
M_"v[96:99],a[236:239],a[168:171],v[96:99]\n" \
T_"a[94:95],v251 offset:520\n" \
M_"v[100:103],a[240:243],a[168:171],v[100:103]\n" \
M_"v[104:107],a[244:247],a[168:171],v[104:107]\n" \
M_"v[108:111],a[248:251],a[168:171],v[108:111]\n" \
R_"a[168:171],v225 offset:20480\n" \
M_"v[0:3],a[112:115],a[144:147],v[0:3]\n" \
M_"v[4:7],a[116:119],a[144:147],v[4:7]\n" \
M_"v[8:11],a[120:123],a[144:147],v[8:11]\n" \
M_"v[12:15],a[124:127],a[144:147],v[12:15]\n" \
R_"a[144:147],v225 offset:21504\n" \
M_"v[16:19],a[112:115],a[148:151],v[16:19]\n" \
S_"vmcnt(4)\n" \
T_"a[96:97],v250 offset:4096\n" \
M_"v[20:23],a[116:119],a[148:151],v[20:23]\n" \
T_"a[98:99],v250 offset:4608\n" \
M_"v[24:27],a[120:123],a[148:151],v[24:27]\n" \
T_"a[100:101],v250 offset:4104\n" \
M_"v[28:31],a[124:127],a[148:151],v[28:31]\n" \
R_"a[148:151],v225 offset:22528\n" \
S_"lgkmcnt(5)\n" \
M_"v[32:35],a[112:115],a[152:155],v[32:35]\n" \
T_"a[102:103],v250 offset:4616\n" \
M_"v[36:39],a[116:119],a[152:155],v[36:39]\n" \
T_"a[104:105],v251 offset:4096\n" \
M_"v[40:43],a[120:123],a[152:155],v[40:43]\n" \
T_"a[106:107],v251 offset:4608\n" \
M_"v[44:47],a[124:127],a[152:155],v[44:47]\n" \
R_"a[152:155],v225 offset:23552\n" \
M_"v[48:51],a[112:115],a[156:159],v[48:51]\n" \
T_"a[108:109],v251 offset:4104\n" \
M_"v[52:55],a[116:119],a[156:159],v[52:55]\n" \
T_"a[110:111],v251 offset:4616\n" \
M_"v[56:59],a[120:123],a[156:159],v[56:59]\n" \
M_"v[60:63],a[124:127],a[156:159],v[60:63]\n" \
R_"a[156:159],v225 offset:24576\n" \
M_"v[64:67],a[112:115],a[160:163],v[64:67]\n" \
M_"v[68:71],a[116:119],a[160:163],v[68:71]\n" \
M_"v[72:75],a[120:123],a[160:163],v[72:75]\n" \
M_"v[76:79],a[124:127],a[160:163],v[76:79]\n" \
R_"a[160:163],v225 offset:25600\n" \
M_"v[80:83],a[112:115],a[164:167],v[80:83]\n" \
M_"v[84:87],a[116:119],a[164:167],v[84:87]\n" \
M_"v[88:91],a[120:123],a[164:167],v[88:91]\n" \
M_"v[92:95],a[124:127],a[164:167],v[92:95]\n" \
R_"a[164:167],v225 offset:26624\n" \
M_"v[96:99],a[112:115],a[168:171],v[96:99]\n" \
M_"v[100:103],a[116:119],a[168:171],v[100:103]\n" \
M_"v[104:107],a[120:123],a[168:171],v[104:107]\n" \
M_"v[108:111],a[124:127],a[168:171],v[108:111]\n" \
R_"a[168:171],v225 offset:27648\n" \
S_"lgkmcnt(2)\n" \
M_"v[0:3],a[128:131],a[144:147],v[0:3]\n" \
M_"v[4:7],a[132:135],a[144:147],v[4:7]\n" \
M_"v[8:11],a[136:139],a[144:147],v[8:11]\n" \
M_"v[12:15],a[140:143],a[144:147],v[12:15]\n" \
R_"a[144:147],v226 offset:0\n" \
M_"v[16:19],a[128:131],a[148:151],v[16:19]\n" \
S_"vmcnt(0)\n" \
T_"a[172:173],v250 offset:8192\n" \
M_"v[20:23],a[132:135],a[148:151],v[20:23]\n" \
T_"a[174:175],v250 offset:8704\n" \
M_"v[24:27],a[136:139],a[148:151],v[24:27]\n" \
T_"a[176:177],v250 offset:8200\n" \
T_"a[178:179],v250 offset:8712\n" \
M_"v[28:31],a[140:143],a[148:151],v[28:31]\n" \
R_"a[148:151],v226 offset:1024\n" \
M_"v[32:35],a[128:131],a[152:155],v[32:35]\n" \
T_"a[180:181],v251 offset:8192\n" \
T_"a[182:183],v251 offset:8704\n" \
M_"v[36:39],a[132:135],a[152:155],v[36:39]\n" \
T_"a[184:185],v251 offset:8200\n" \
M_"v[40:43],a[136:139],a[152:155],v[40:43]\n" \
T_"a[186:187],v251 offset:8712\n" \
M_"v[44:47],a[140:143],a[152:155],v[44:47]\n" \
R_"a[152:155],v226 offset:2048\n" \
M_"v[48:51],a[128:131],a[156:159],v[48:51]\n" \
M_"v[52:55],a[132:135],a[156:159],v[52:55]\n" \
M_"v[56:59],a[136:139],a[156:159],v[56:59]\n" \
M_"v[60:63],a[140:143],a[156:159],v[60:63]\n" \
R_"a[156:159],v226 offset:3072\n" \
M_"v[64:67],a[128:131],a[160:163],v[64:67]\n" \
M_"v[68:71],a[132:135],a[160:163],v[68:71]\n" \
M_"v[72:75],a[136:139],a[160:163],v[72:75]\n" \
M_"v[76:79],a[140:143],a[160:163],v[76:79]\n" \
R_"a[160:163],v226 offset:4096\n" \
S_"lgkmcnt(2)\n" \
M_"v[80:83],a[128:131],a[164:167],v[80:83]\n" \
S_"vmcnt(0)\n" \
"v_mov_b32 v253,1\n" \
"buffer_store_dword v253,v246,s[84:87],0 offen sc1\n" \
M_"v[84:87],a[132:135],a[164:167],v[84:87]\n" \
M_"v[88:91],a[136:139],a[164:167],v[88:91]\n" \
M_"v[92:95],a[140:143],a[164:167],v[92:95]\n" \
R_"a[164:167],v226 offset:5120\n" \
M_"v[96:99],a[128:131],a[168:171],v[96:99]\n" \
M_"v[100:103],a[132:135],a[168:171],v[100:103]\n" \
M_"v[104:107],a[136:139],a[168:171],v[104:107]\n" \
M_"v[108:111],a[140:143],a[168:171],v[108:111]\n" \
R_"a[168:171],v226 offset:6144\n" \
M_"v[0:3],a[0:3],a[144:147],v[0:3]\n" \
M_"v[4:7],a[4:7],a[144:147],v[4:7]\n" \
M_"v[8:11],a[8:11],a[144:147],v[8:11]\n" \
M_"v[12:15],a[12:15],a[144:147],v[12:15]\n" \
R_"a[144:147],v226 offset:7168\n" \
M_"v[16:19],a[0:3],a[148:151],v[16:19]\n" \
M_"v[20:23],a[4:7],a[148:151],v[20:23]\n" \
M_"v[24:27],a[8:11],a[148:151],v[24:27]\n" \
M_"v[28:31],a[12:15],a[148:151],v[28:31]\n" \
R_"a[148:151],v226 offset:8192\n" \
M_"v[32:35],a[0:3],a[152:155],v[32:35]\n" \
M_"v[36:39],a[4:7],a[152:155],v[36:39]\n" \
M_"v[40:43],a[8:11],a[152:155],v[40:43]\n" \
M_"v[44:47],a[12:15],a[152:155],v[44:47]\n" \
R_"a[152:155],v226 offset:9216\n" \
S_"lgkmcnt(2)\n" \
M_"v[48:51],a[0:3],a[156:159],v[48:51]\n" \
M_"v[52:55],a[4:7],a[156:159],v[52:55]\n" \
M_"v[56:59],a[8:11],a[156:159],v[56:59]\n" \
M_"v[60:63],a[12:15],a[156:159],v[60:63]\n" \
R_"a[156:159],v226 offset:10240\n" \
M_"v[64:67],a[0:3],a[160:163],v[64:67]\n" \
M_"v[68:71],a[4:7],a[160:163],v[68:71]\n" \
M_"v[72:75],a[8:11],a[160:163],v[72:75]\n" \
M_"v[76:79],a[12:15],a[160:163],v[76:79]\n" \
R_"a[160:163],v226 offset:11264\n" \
M_"v[80:83],a[0:3],a[164:167],v[80:83]\n" \
M_"v[84:87],a[4:7],a[164:167],v[84:87]\n" \
M_"v[88:91],a[8:11],a[164:167],v[88:91]\n" \
M_"v[92:95],a[12:15],a[164:167],v[92:95]\n" \
R_"a[164:167],v226 offset:12288\n" \
M_"v[96:99],a[0:3],a[168:171],v[96:99]\n" \
M_"v[100:103],a[4:7],a[168:171],v[100:103]\n" \
M_"v[104:107],a[8:11],a[168:171],v[104:107]\n" \
M_"v[108:111],a[12:15],a[168:171],v[108:111]\n" \
R_"a[168:171],v226 offset:13312\n" \
M_"v[0:3],a[16:19],a[144:147],v[0:3]\n" \
M_"v[4:7],a[20:23],a[144:147],v[4:7]\n" \
M_"v[8:11],a[24:27],a[144:147],v[8:11]\n" \
M_"v[12:15],a[28:31],a[144:147],v[12:15]\n" \
R_"a[144:147],v226 offset:14336\n" \
S_"lgkmcnt(2)\n" \
M_"v[16:19],a[16:19],a[148:151],v[16:19]\n" \
M_"v[20:23],a[20:23],a[148:151],v[20:23]\n" \
M_"v[24:27],a[24:27],a[148:151],v[24:27]\n" \
M_"v[28:31],a[28:31],a[148:151],v[28:31]\n" \
R_"a[148:151],v226 offset:15360\n" \
M_"v[32:35],a[16:19],a[152:155],v[32:35]\n" \
M_"v[36:39],a[20:23],a[152:155],v[36:39]\n" \
M_"v[40:43],a[24:27],a[152:155],v[40:43]\n" \
M_"v[44:47],a[28:31],a[152:155],v[44:47]\n" \
R_"a[152:155],v226 offset:16384\n" \
M_"v[48:51],a[16:19],a[156:159],v[48:51]\n" \
M_"v[52:55],a[20:23],a[156:159],v[52:55]\n" \
M_"v[56:59],a[24:27],a[156:159],v[56:59]\n" \
M_"v[60:63],a[28:31],a[156:159],v[60:63]\n" \
R_"a[156:159],v226 offset:17408\n" \
M_"v[64:67],a[16:19],a[160:163],v[64:67]\n" \
M_"v[68:71],a[20:23],a[160:163],v[68:71]\n" \
M_"v[72:75],a[24:27],a[160:163],v[72:75]\n" \
M_"v[76:79],a[28:31],a[160:163],v[76:79]\n" \
R_"a[160:163],v226 offset:18432\n" \
M_"v[80:83],a[16:19],a[164:167],v[80:83]\n" \
"buffer_load_dword v252,v248,s[84:87],0 offen sc1\n" \
M_"v[84:87],a[20:23],a[164:167],v[84:87]\n" \
M_"v[88:91],a[24:27],a[164:167],v[88:91]\n" \
M_"v[92:95],a[28:31],a[164:167],v[92:95]\n" \
R_"a[164:167],v226 offset:19456\n" \
S_"lgkmcnt(2)\n" \
M_"v[96:99],a[16:19],a[168:171],v[96:99]\n" \
M_"v[100:103],a[20:23],a[168:171],v[100:103]\n" \
M_"v[104:107],a[24:27],a[168:171],v[104:107]\n" \
M_"v[108:111],a[28:31],a[168:171],v[108:111]\n" \
R_"a[168:171],v226 offset:20480\n" \
M_"v[0:3],a[32:35],a[144:147],v[0:3]\n" \
M_"v[4:7],a[36:39],a[144:147],v[4:7]\n" \
M_"v[8:11],a[40:43],a[144:147],v[8:11]\n" \
M_"v[12:15],a[44:47],a[144:147],v[12:15]\n" \
R_"a[144:147],v226 offset:21504\n" \
M_"v[16:19],a[32:35],a[148:151],v[16:19]\n" \
M_"v[20:23],a[36:39],a[148:151],v[20:23]\n" \
M_"v[24:27],a[40:43],a[148:151],v[24:27]\n" \
M_"v[28:31],a[44:47],a[148:151],v[28:31]\n" \
R_"a[148:151],v226 offset:22528\n" \
M_"v[32:35],a[32:35],a[152:155],v[32:35]\n" \
M_"v[36:39],a[36:39],a[152:155],v[36:39]\n" \
M_"v[40:43],a[40:43],a[152:155],v[40:43]\n" \
M_"v[44:47],a[44:47],a[152:155],v[44:47]\n" \
R_"a[152:155],v226 offset:23552\n" \
M_"v[48:51],a[32:35],a[156:159],v[48:51]\n" \
M_"v[52:55],a[36:39],a[156:159],v[52:55]\n" \
M_"v[56:59],a[40:43],a[156:159],v[56:59]\n" \
M_"v[60:63],a[44:47],a[156:159],v[60:63]\n" \
R_"a[156:159],v226 offset:24576\n" \
S_"lgkmcnt(2)\n" \
M_"v[64:67],a[32:35],a[160:163],v[64:67]\n" \
M_"v[68:71],a[36:39],a[160:163],v[68:71]\n" \
M_"v[72:75],a[40:43],a[160:163],v[72:75]\n" \
M_"v[76:79],a[44:47],a[160:163],v[76:79]\n" \
R_"a[160:163],v226 offset:25600\n" \
M_"v[80:83],a[32:35],a[164:167],v[80:83]\n" \
M_"v[84:87],a[36:39],a[164:167],v[84:87]\n" \
M_"v[88:91],a[40:43],a[164:167],v[88:91]\n" \
M_"v[92:95],a[44:47],a[164:167],v[92:95]\n" \
R_"a[164:167],v226 offset:26624\n" \
M_"v[96:99],a[32:35],a[168:171],v[96:99]\n" \
M_"v[100:103],a[36:39],a[168:171],v[100:103]\n" \
M_"v[104:107],a[40:43],a[168:171],v[104:107]\n" \
M_"v[108:111],a[44:47],a[168:171],v[108:111]\n" \
R_"a[168:171],v226 offset:27648\n" \
M_"v[0:3],a[48:51],a[144:147],v[0:3]\n" \
M_"v[4:7],a[52:55],a[144:147],v[4:7]\n" \
M_"v[8:11],a[56:59],a[144:147],v[8:11]\n" \
M_"v[12:15],a[60:63],a[144:147],v[12:15]\n" \
R_"a[144:147],v227 offset:0\n" \
M_"v[16:19],a[48:51],a[148:151],v[16:19]\n" \
M_"v[20:23],a[52:55],a[148:151],v[20:23]\n" \
M_"v[24:27],a[56:59],a[148:151],v[24:27]\n" \
M_"v[28:31],a[60:63],a[148:151],v[28:31]\n" \
R_"a[148:151],v227 offset:1024\n" \
S_"lgkmcnt(2)\n" \
M_"v[32:35],a[48:51],a[152:155],v[32:35]\n" \
M_"v[36:39],a[52:55],a[152:155],v[36:39]\n" \
M_"v[40:43],a[56:59],a[152:155],v[40:43]\n" \
M_"v[44:47],a[60:63],a[152:155],v[44:47]\n" \
R_"a[152:155],v227 offset:2048\n" \
M_"v[48:51],a[48:51],a[156:159],v[48:51]\n" \
M_"v[52:55],a[52:55],a[156:159],v[52:55]\n" \
M_"v[56:59],a[56:59],a[156:159],v[56:59]\n" \
M_"v[60:63],a[60:63],a[156:159],v[60:63]\n" \
R_"a[156:159],v227 offset:3072\n" \
M_"v[64:67],a[48:51],a[160:163],v[64:67]\n" \
M_"v[68:71],a[52:55],a[160:163],v[68:71]\n" \
M_"v[72:75],a[56:59],a[160:163],v[72:75]\n" \
M_"v[76:79],a[60:63],a[160:163],v[76:79]\n" \
R_"a[160:163],v227 offset:4096\n" \
M_"v[80:83],a[48:51],a[164:167],v[80:83]\n" \
S_"vmcnt(0)\n" \
"v_cmp_gt_u32 vcc,1,v252\n" \
"s_cbranch_vccnz LWB0_%=\n" \
"LWB0d_%=:\n" \
A_"s62,s77,s8\n" \
L_"v[128:131],v240,s[52:55],s62 offen sc1\n" \
M_"v[84:87],a[52:55],a[164:167],v[84:87]\n" \
A_"s62,s77,s9\n" \
L_"v[132:135],v240,s[52:55],s62 offen sc1\n" \
M_"v[88:91],a[56:59],a[164:167],v[88:91]\n" \
A_"s62,s77,s10\n" \
L_"v[136:139],v240,s[52:55],s62 offen sc1\n" \
M_"v[92:95],a[60:63],a[164:167],v[92:95]\n" \
R_"a[164:167],v227 offset:5120\n" \
M_"v[96:99],a[48:51],a[168:171],v[96:99]\n" \
A_"s62,s77,s11\n" \
L_"v[140:143],v240,s[52:55],s62 offen sc1\n" \
A_"s62,s77,s12\n" \
L_"v[144:147],v240,s[52:55],s62 offen sc1\n" \
M_"v[100:103],a[52:55],a[168:171],v[100:103]\n" \
A_"s62,s77,s13\n" \
L_"v[148:151],v240,s[52:55],s62 offen sc1\n" \
M_"v[104:107],a[56:59],a[168:171],v[104:107]\n" \
A_"s62,s77,s14\n" \
L_"v[152:155],v240,s[52:55],s62 offen sc1\n" \
M_"v[108:111],a[60:63],a[168:171],v[108:111]\n" \
R_"a[168:171],v227 offset:6144\n" \
S_"lgkmcnt(2)\n" \
M_"v[0:3],a[64:67],a[144:147],v[0:3]\n" \
M_"v[4:7],a[68:71],a[144:147],v[4:7]\n" \
M_"v[8:11],a[72:75],a[144:147],v[8:11]\n" \
M_"v[12:15],a[76:79],a[144:147],v[12:15]\n" \
R_"a[144:147],v227 offset:7168\n" \
M_"v[16:19],a[64:67],a[148:151],v[16:19]\n" \
M_"v[20:23],a[68:71],a[148:151],v[20:23]\n" \
M_"v[24:27],a[72:75],a[148:151],v[24:27]\n" \
M_"v[28:31],a[76:79],a[148:151],v[28:31]\n" \
R_"a[148:151],v227 offset:8192\n" \
M_"v[32:35],a[64:67],a[152:155],v[32:35]\n" \
M_"v[36:39],a[68:71],a[152:155],v[36:39]\n" \
M_"v[40:43],a[72:75],a[152:155],v[40:43]\n" \
M_"v[44:47],a[76:79],a[152:155],v[44:47]\n" \
R_"a[152:155],v227 offset:9216\n" \
M_"v[48:51],a[64:67],a[156:159],v[48:51]\n" \
M_"v[52:55],a[68:71],a[156:159],v[52:55]\n" \
M_"v[56:59],a[72:75],a[156:159],v[56:59]\n" \
M_"v[60:63],a[76:79],a[156:159],v[60:63]\n" \
R_"a[156:159],v227 offset:10240\n" \
M_"v[64:67],a[64:67],a[160:163],v[64:67]\n" \
M_"v[68:71],a[68:71],a[160:163],v[68:71]\n" \
M_"v[72:75],a[72:75],a[160:163],v[72:75]\n" \
M_"v[76:79],a[76:79],a[160:163],v[76:79]\n" \
R_"a[160:163],v227 offset:11264\n" \
S_"lgkmcnt(2)\n" \
M_"v[80:83],a[64:67],a[164:167],v[80:83]\n" \
M_"v[84:87],a[68:71],a[164:167],v[84:87]\n" \
M_"v[88:91],a[72:75],a[164:167],v[88:91]\n" \
M_"v[92:95],a[76:79],a[164:167],v[92:95]\n" \
R_"a[164:167],v227 offset:12288\n" \
M_"v[96:99],a[64:67],a[168:171],v[96:99]\n" \
M_"v[100:103],a[68:71],a[168:171],v[100:103]\n" \
M_"v[104:107],a[72:75],a[168:171],v[104:107]\n" \
M_"v[108:111],a[76:79],a[168:171],v[108:111]\n" \
R_"a[168:171],v227 offset:13312\n" \
M_"v[0:3],a[80:83],a[144:147],v[0:3]\n" \
M_"v[4:7],a[84:87],a[144:147],v[4:7]\n" \
M_"v[8:11],a[88:91],a[144:147],v[8:11]\n" \
M_"v[12:15],a[92:95],a[144:147],v[12:15]\n" \
R_"a[144:147],v227 offset:14336\n" \
M_"v[16:19],a[80:83],a[148:151],v[16:19]\n" \
M_"v[20:23],a[84:87],a[148:151],v[20:23]\n" \
M_"v[24:27],a[88:91],a[148:151],v[24:27]\n" \
M_"v[28:31],a[92:95],a[148:151],v[28:31]\n" \
R_"a[148:151],v227 offset:15360\n" \
M_"v[32:35],a[80:83],a[152:155],v[32:35]\n" \
M_"v[36:39],a[84:87],a[152:155],v[36:39]\n" \
M_"v[40:43],a[88:91],a[152:155],v[40:43]\n" \
M_"v[44:47],a[92:95],a[152:155],v[44:47]\n" \
R_"a[152:155],v227 offset:16384\n" \
S_"lgkmcnt(2)\n" \
M_"v[48:51],a[80:83],a[156:159],v[48:51]\n" \
M_"v[52:55],a[84:87],a[156:159],v[52:55]\n" \
M_"v[56:59],a[88:91],a[156:159],v[56:59]\n" \
M_"v[60:63],a[92:95],a[156:159],v[60:63]\n" \
R_"a[156:159],v227 offset:17408\n" \
M_"v[64:67],a[80:83],a[160:163],v[64:67]\n" \
M_"v[68:71],a[84:87],a[160:163],v[68:71]\n" \
M_"v[72:75],a[88:91],a[160:163],v[72:75]\n" \
M_"v[76:79],a[92:95],a[160:163],v[76:79]\n" \
R_"a[160:163],v227 offset:18432\n" \
M_"v[80:83],a[80:83],a[164:167],v[80:83]\n" \
M_"v[84:87],a[84:87],a[164:167],v[84:87]\n" \
M_"v[88:91],a[88:91],a[164:167],v[88:91]\n" \
M_"v[92:95],a[92:95],a[164:167],v[92:95]\n" \
R_"a[164:167],v227 offset:19456\n" \
M_"v[96:99],a[80:83],a[168:171],v[96:99]\n" \
M_"v[100:103],a[84:87],a[168:171],v[100:103]\n" \
M_"v[104:107],a[88:91],a[168:171],v[104:107]\n" \
M_"v[108:111],a[92:95],a[168:171],v[108:111]\n" \
R_"a[168:171],v227 offset:20480\n" \
M_"v[0:3],a[96:99],a[144:147],v[0:3]\n" \
M_"v[4:7],a[100:103],a[144:147],v[4:7]\n" \
M_"v[8:11],a[104:107],a[144:147],v[8:11]\n" \
M_"v[12:15],a[108:111],a[144:147],v[12:15]\n" \
R_"a[144:147],v227 offset:21504\n" \
S_"lgkmcnt(2)\n" \
M_"v[16:19],a[96:99],a[148:151],v[16:19]\n" \
M_"v[20:23],a[100:103],a[148:151],v[20:23]\n" \
M_"v[24:27],a[104:107],a[148:151],v[24:27]\n" \
M_"v[28:31],a[108:111],a[148:151],v[28:31]\n" \
R_"a[148:151],v227 offset:22528\n" \
M_"v[32:35],a[96:99],a[152:155],v[32:35]\n" \
M_"v[36:39],a[100:103],a[152:155],v[36:39]\n" \
M_"v[40:43],a[104:107],a[152:155],v[40:43]\n" \
M_"v[44:47],a[108:111],a[152:155],v[44:47]\n" \
R_"a[152:155],v227 offset:23552\n" \
M_"v[48:51],a[96:99],a[156:159],v[48:51]\n" \
M_"v[52:55],a[100:103],a[156:159],v[52:55]\n" \
M_"v[56:59],a[104:107],a[156:159],v[56:59]\n" \
M_"v[60:63],a[108:111],a[156:159],v[60:63]\n" \
R_"a[156:159],v227 offset:24576\n" \
M_"v[64:67],a[96:99],a[160:163],v[64:67]\n" \
M_"v[68:71],a[100:103],a[160:163],v[68:71]\n" \
M_"v[72:75],a[104:107],a[160:163],v[72:75]\n" \
M_"v[76:79],a[108:111],a[160:163],v[76:79]\n" \
R_"a[160:163],v227 offset:25600\n" \
M_"v[80:83],a[96:99],a[164:167],v[80:83]\n" \
M_"v[84:87],a[100:103],a[164:167],v[84:87]\n" \
M_"v[88:91],a[104:107],a[164:167],v[88:91]\n" \
M_"v[92:95],a[108:111],a[164:167],v[92:95]\n" \
R_"a[164:167],v227 offset:26624\n" \
S_"lgkmcnt(2)\n" \
M_"v[96:99],a[96:99],a[168:171],v[96:99]\n" \
M_"v[100:103],a[100:103],a[168:171],v[100:103]\n" \
M_"v[104:107],a[104:107],a[168:171],v[104:107]\n" \
M_"v[108:111],a[108:111],a[168:171],v[108:111]\n" \
R_"a[168:171],v227 offset:27648\n" \
M_"v[0:3],a[172:175],a[144:147],v[0:3]\n" \
M_"v[4:7],a[176:179],a[144:147],v[4:7]\n" \
M_"v[8:11],a[180:183],a[144:147],v[8:11]\n" \
M_"v[12:15],a[184:187],a[144:147],v[12:15]\n" \
M_"v[16:19],a[172:175],a[148:151],v[16:19]\n" \
M_"v[20:23],a[176:179],a[148:151],v[20:23]\n" \
M_"v[24:27],a[180:183],a[148:151],v[24:27]\n" \
M_"v[28:31],a[184:187],a[148:151],v[28:31]\n" \
M_"v[32:35],a[172:175],a[152:155],v[32:35]\n" \
M_"v[36:39],a[176:179],a[152:155],v[36:39]\n" \
M_"v[40:43],a[180:183],a[152:155],v[40:43]\n" \
M_"v[44:47],a[184:187],a[152:155],v[44:47]\n" \
M_"v[48:51],a[172:175],a[156:159],v[48:51]\n" \
M_"v[52:55],a[176:179],a[156:159],v[52:55]\n" \
M_"v[56:59],a[180:183],a[156:159],v[56:59]\n" \
M_"v[60:63],a[184:187],a[156:159],v[60:63]\n" \
S_"lgkmcnt(0)\n" \
M_"v[64:67],a[172:175],a[160:163],v[64:67]\n" \
M_"v[68:71],a[176:179],a[160:163],v[68:71]\n" \
M_"v[72:75],a[180:183],a[160:163],v[72:75]\n" \
M_"v[76:79],a[184:187],a[160:163],v[76:79]\n" \
M_"v[80:83],a[172:175],a[164:167],v[80:83]\n" \
M_"v[84:87],a[176:179],a[164:167],v[84:87]\n" \
M_"v[88:91],a[180:183],a[164:167],v[88:91]\n" \
M_"v[92:95],a[184:187],a[164:167],v[92:95]\n" \
M_"v[96:99],a[172:175],a[168:171],v[96:99]\n" \
M_"v[100:103],a[176:179],a[168:171],v[100:103]\n" \
M_"v[104:107],a[180:183],a[168:171],v[104:107]\n" \
M_"v[108:111],a[184:187],a[168:171],v[108:111]\n" \
"s_nop 7\n" \
C_"v16,v16,v17\n" \
C_"v17,v18,v19\n" \
C_"v18,v20,v21\n" \
C_"v19,v22,v23\n" \
A_"s61,s71,0\n" \
"buffer_store_dwordx4 v[16:19],v240,s[52:55],s61 offen offset:2048 sc1\n" \
C_"v24,v24,v25\n" \
C_"v25,v26,v27\n" \
C_"v26,v28,v29\n" \
C_"v27,v30,v31\n" \
"buffer_store_dwordx4 v[24:27],v240,s[52:55],s61 offen offset:3072 sc1\n" \
C_"v32,v32,v33\n" \
C_"v33,v34,v35\n" \
C_"v34,v36,v37\n" \
C_"v35,v38,v39\n" \
A_"s61,s71,4096\n" \
"buffer_store_dwordx4 v[32:35],v240,s[52:55],s61 offen sc1\n" \
C_"v40,v40,v41\n" \
C_"v41,v42,v43\n" \
C_"v42,v44,v45\n" \
C_"v43,v46,v47\n" \
"buffer_store_dwordx4 v[40:43],v240,s[52:55],s61 offen offset:1024 sc1\n" \
C_"v48,v48,v49\n" \
C_"v49,v50,v51\n" \
C_"v50,v52,v53\n" \
C_"v51,v54,v55\n" \
"buffer_store_dwordx4 v[48:51],v240,s[52:55],s61 offen offset:2048 sc1\n" \
C_"v56,v56,v57\n" \
C_"v57,v58,v59\n" \
C_"v58,v60,v61\n" \
C_"v59,v62,v63\n" \
"buffer_store_dwordx4 v[56:59],v240,s[52:55],s61 offen offset:3072 sc1\n" \
C_"v64,v64,v65\n" \
C_"v65,v66,v67\n" \
C_"v66,v68,v69\n" \
C_"v67,v70,v71\n" \
A_"s61,s71,8192\n" \
"buffer_store_dwordx4 v[64:67],v240,s[52:55],s61 offen sc1\n" \
C_"v72,v72,v73\n" \
C_"v73,v74,v75\n" \
C_"v74,v76,v77\n" \
C_"v75,v78,v79\n" \
"buffer_store_dwordx4 v[72:75],v240,s[52:55],s61 offen offset:1024 sc1\n" \
C_"v80,v80,v81\n" \
C_"v81,v82,v83\n" \
C_"v82,v84,v85\n" \
C_"v83,v86,v87\n" \
"buffer_store_dwordx4 v[80:83],v240,s[52:55],s61 offen offset:2048 sc1\n" \
C_"v88,v88,v89\n" \
C_"v89,v90,v91\n" \
C_"v90,v92,v93\n" \
C_"v91,v94,v95\n" \
"buffer_store_dwordx4 v[88:91],v240,s[52:55],s61 offen offset:3072 sc1\n" \
C_"v96,v96,v97\n" \
C_"v97,v98,v99\n" \
C_"v98,v100,v101\n" \
C_"v99,v102,v103\n" \
A_"s61,s71,12288\n" \
"buffer_store_dwordx4 v[96:99],v240,s[52:55],s61 offen sc1\n" \
C_"v104,v104,v105\n" \
C_"v105,v106,v107\n" \
C_"v106,v108,v109\n" \
C_"v107,v110,v111\n" \
"buffer_store_dwordx4 v[104:107],v240,s[52:55],s61 offen offset:1024 sc1\n" \
C_"v232,v0,v1\n" \
C_"v233,v2,v3\n" \
C_"v234,v4,v5\n" \
C_"v235,v6,v7\n" \
A_"s61,s71,0\n" \
"buffer_store_dwordx4 v[232:235],v247,s[52:55],s61 offen sc1\n" \
C_"v236,v8,v9\n" \
C_"v237,v10,v11\n" \
C_"v238,v12,v13\n" \
C_"v239,v14,v15\n" \
"buffer_store_dwordx4 v[236:239],v247,s[52:55],s61 offen offset:1024 sc1\n" \
S_"vmcnt(20)\n" \
"v_lshlrev_b32 v216,16,v128\n" \
"v_and_b32 v217,0xffff0000,v128\n" \
"v_pk_add_f32 v[112:113],v[112:113],v[216:217]\n" \
"v_lshlrev_b32 v218,16,v129\n" \
"v_and_b32 v219,0xffff0000,v129\n" \
"v_pk_add_f32 v[114:115],v[114:115],v[218:219]\n" \
"v_lshlrev_b32 v220,16,v130\n" \
"v_and_b32 v221,0xffff0000,v130\n" \
"v_pk_add_f32 v[116:117],v[116:117],v[220:221]\n" \
"v_lshlrev_b32 v222,16,v131\n" \
"v_and_b32 v223,0xffff0000,v131\n" \
"v_pk_add_f32 v[118:119],v[118:119],v[222:223]\n" \
S_"vmcnt(19)\n" \
"v_lshlrev_b32 v216,16,v132\n" \
"v_and_b32 v217,0xffff0000,v132\n" \
"v_pk_add_f32 v[112:113],v[112:113],v[216:217]\n" \
"v_lshlrev_b32 v218,16,v133\n" \
"v_and_b32 v219,0xffff0000,v133\n" \
"v_pk_add_f32 v[114:115],v[114:115],v[218:219]\n" \
"v_lshlrev_b32 v220,16,v134\n" \
"v_and_b32 v221,0xffff0000,v134\n" \
"v_pk_add_f32 v[116:117],v[116:117],v[220:221]\n" \
"v_lshlrev_b32 v222,16,v135\n" \
"v_and_b32 v223,0xffff0000,v135\n" \
"v_pk_add_f32 v[118:119],v[118:119],v[222:223]\n" \
S_"vmcnt(18)\n" \
"v_lshlrev_b32 v216,16,v136\n" \
"v_and_b32 v217,0xffff0000,v136\n" \
"v_pk_add_f32 v[112:113],v[112:113],v[216:217]\n" \
"v_lshlrev_b32 v218,16,v137\n" \
"v_and_b32 v219,0xffff0000,v137\n" \
"v_pk_add_f32 v[114:115],v[114:115],v[218:219]\n" \
"v_lshlrev_b32 v220,16,v138\n" \
"v_and_b32 v221,0xffff0000,v138\n" \
"v_pk_add_f32 v[116:117],v[116:117],v[220:221]\n" \
"v_lshlrev_b32 v222,16,v139\n" \
"v_and_b32 v223,0xffff0000,v139\n" \
"v_pk_add_f32 v[118:119],v[118:119],v[222:223]\n" \
S_"vmcnt(17)\n" \
"v_lshlrev_b32 v216,16,v140\n" \
"v_and_b32 v217,0xffff0000,v140\n" \
"v_pk_add_f32 v[112:113],v[112:113],v[216:217]\n" \
"v_lshlrev_b32 v218,16,v141\n" \
"v_and_b32 v219,0xffff0000,v141\n" \
"v_pk_add_f32 v[114:115],v[114:115],v[218:219]\n" \
"v_lshlrev_b32 v220,16,v142\n" \
"v_and_b32 v221,0xffff0000,v142\n" \
"v_pk_add_f32 v[116:117],v[116:117],v[220:221]\n" \
"v_lshlrev_b32 v222,16,v143\n" \
"v_and_b32 v223,0xffff0000,v143\n" \
"v_pk_add_f32 v[118:119],v[118:119],v[222:223]\n" \
S_"vmcnt(16)\n" \
"v_lshlrev_b32 v216,16,v144\n" \
"v_and_b32 v217,0xffff0000,v144\n" \
"v_pk_add_f32 v[112:113],v[112:113],v[216:217]\n" \
"v_lshlrev_b32 v218,16,v145\n" \
"v_and_b32 v219,0xffff0000,v145\n" \
"v_pk_add_f32 v[114:115],v[114:115],v[218:219]\n" \
"v_lshlrev_b32 v220,16,v146\n" \
"v_and_b32 v221,0xffff0000,v146\n" \
"v_pk_add_f32 v[116:117],v[116:117],v[220:221]\n" \
"v_lshlrev_b32 v222,16,v147\n" \
"v_and_b32 v223,0xffff0000,v147\n" \
"v_pk_add_f32 v[118:119],v[118:119],v[222:223]\n" \
S_"vmcnt(15)\n" \
"v_lshlrev_b32 v216,16,v148\n" \
"v_and_b32 v217,0xffff0000,v148\n" \
"v_pk_add_f32 v[112:113],v[112:113],v[216:217]\n" \
"v_lshlrev_b32 v218,16,v149\n" \
"v_and_b32 v219,0xffff0000,v149\n" \
"v_pk_add_f32 v[114:115],v[114:115],v[218:219]\n" \
"v_lshlrev_b32 v220,16,v150\n" \
"v_and_b32 v221,0xffff0000,v150\n" \
"v_pk_add_f32 v[116:117],v[116:117],v[220:221]\n" \
"v_lshlrev_b32 v222,16,v151\n" \
"v_and_b32 v223,0xffff0000,v151\n" \
"v_pk_add_f32 v[118:119],v[118:119],v[222:223]\n" \
S_"vmcnt(14)\n" \
"v_lshlrev_b32 v216,16,v152\n" \
"v_and_b32 v217,0xffff0000,v152\n" \
"v_pk_add_f32 v[112:113],v[112:113],v[216:217]\n" \
"v_lshlrev_b32 v218,16,v153\n" \
"v_and_b32 v219,0xffff0000,v153\n" \
"v_pk_add_f32 v[114:115],v[114:115],v[218:219]\n" \
"v_lshlrev_b32 v220,16,v154\n" \
"v_and_b32 v221,0xffff0000,v154\n" \
"v_pk_add_f32 v[116:117],v[116:117],v[220:221]\n" \
"v_lshlrev_b32 v222,16,v155\n" \
"v_and_b32 v223,0xffff0000,v155\n" \
"v_pk_add_f32 v[118:119],v[118:119],v[222:223]\n" \
C_"v112,v112,v113\n" \
C_"v113,v114,v115\n" \
C_"v114,v116,v117\n" \
C_"v115,v118,v119\n" \
S_"vmcnt(0)\n" \
S_"vmcnt(0)\n" \
"v_mov_b32 v253,2\n" \
"buffer_store_dword v253,v246,s[84:87],0 offen sc1\n" \
"buffer_load_dword v252,v248,s[84:87],0 offen sc1\n" \
S_"vmcnt(0)\n" \
"v_cmp_gt_u32 vcc,2,v252\n" \
"s_cbranch_vccnz LWB2_%=\n" \
"LWB2d_%=:\n" \
A_"s62,s76,s8\n" \
L_"v[156:159],v240,s[52:55],s62 offen sc1\n" \
L_"v[160:163],v240,s[52:55],s62 offen offset:1024 sc1\n" \
A_"s62,s76,s9\n" \
L_"v[164:167],v240,s[52:55],s62 offen sc1\n" \
L_"v[168:171],v240,s[52:55],s62 offen offset:1024 sc1\n" \
A_"s62,s76,s10\n" \
L_"v[172:175],v240,s[52:55],s62 offen sc1\n" \
L_"v[176:179],v240,s[52:55],s62 offen offset:1024 sc1\n" \
A_"s62,s76,s11\n" \
L_"v[180:183],v240,s[52:55],s62 offen sc1\n" \
L_"v[184:187],v240,s[52:55],s62 offen offset:1024 sc1\n" \
A_"s62,s76,s12\n" \
L_"v[188:191],v240,s[52:55],s62 offen sc1\n" \
L_"v[192:195],v240,s[52:55],s62 offen offset:1024 sc1\n" \
A_"s62,s76,s13\n" \
L_"v[196:199],v240,s[52:55],s62 offen sc1\n" \
L_"v[200:203],v240,s[52:55],s62 offen offset:1024 sc1\n" \
A_"s62,s76,s14\n" \
L_"v[204:207],v240,s[52:55],s62 offen sc1\n" \
L_"v[208:211],v240,s[52:55],s62 offen offset:1024 sc1\n" \
"buffer_store_dwordx4 v[112:115],v245,s[92:95],s90 offen nt\n" \
S_"vmcnt(14)\n" \
"v_lshlrev_b32 v216,16,v156\n" \
"v_and_b32 v217,0xffff0000,v156\n" \
"v_pk_add_f32 v[0:1],v[0:1],v[216:217]\n" \
"v_lshlrev_b32 v218,16,v157\n" \
"v_and_b32 v219,0xffff0000,v157\n" \
"v_pk_add_f32 v[2:3],v[2:3],v[218:219]\n" \
"v_lshlrev_b32 v220,16,v158\n" \
"v_and_b32 v221,0xffff0000,v158\n" \
"v_pk_add_f32 v[4:5],v[4:5],v[220:221]\n" \
"v_lshlrev_b32 v222,16,v159\n" \
"v_and_b32 v223,0xffff0000,v159\n" \
"v_pk_add_f32 v[6:7],v[6:7],v[222:223]\n" \
S_"vmcnt(13)\n" \
"v_lshlrev_b32 v216,16,v160\n" \
"v_and_b32 v217,0xffff0000,v160\n" \
"v_pk_add_f32 v[8:9],v[8:9],v[216:217]\n" \
"v_lshlrev_b32 v218,16,v161\n" \
"v_and_b32 v219,0xffff0000,v161\n" \
"v_pk_add_f32 v[10:11],v[10:11],v[218:219]\n" \
"v_lshlrev_b32 v220,16,v162\n" \
"v_and_b32 v221,0xffff0000,v162\n" \
"v_pk_add_f32 v[12:13],v[12:13],v[220:221]\n" \
"v_lshlrev_b32 v222,16,v163\n" \
"v_and_b32 v223,0xffff0000,v163\n" \
"v_pk_add_f32 v[14:15],v[14:15],v[222:223]\n" \
S_"vmcnt(12)\n" \
"v_lshlrev_b32 v216,16,v164\n" \
"v_and_b32 v217,0xffff0000,v164\n" \
"v_pk_add_f32 v[0:1],v[0:1],v[216:217]\n" \
"v_lshlrev_b32 v218,16,v165\n" \
"v_and_b32 v219,0xffff0000,v165\n" \
"v_pk_add_f32 v[2:3],v[2:3],v[218:219]\n" \
"v_lshlrev_b32 v220,16,v166\n" \
"v_and_b32 v221,0xffff0000,v166\n" \
"v_pk_add_f32 v[4:5],v[4:5],v[220:221]\n" \
"v_lshlrev_b32 v222,16,v167\n" \
"v_and_b32 v223,0xffff0000,v167\n" \
"v_pk_add_f32 v[6:7],v[6:7],v[222:223]\n" \
S_"vmcnt(11)\n" \
"v_lshlrev_b32 v216,16,v168\n" \
"v_and_b32 v217,0xffff0000,v168\n" \
"v_pk_add_f32 v[8:9],v[8:9],v[216:217]\n" \
"v_lshlrev_b32 v218,16,v169\n" \
"v_and_b32 v219,0xffff0000,v169\n" \
"v_pk_add_f32 v[10:11],v[10:11],v[218:219]\n" \
"v_lshlrev_b32 v220,16,v170\n" \
"v_and_b32 v221,0xffff0000,v170\n" \
"v_pk_add_f32 v[12:13],v[12:13],v[220:221]\n" \
"v_lshlrev_b32 v222,16,v171\n" \
"v_and_b32 v223,0xffff0000,v171\n" \
"v_pk_add_f32 v[14:15],v[14:15],v[222:223]\n" \
S_"vmcnt(10)\n" \
"v_lshlrev_b32 v216,16,v172\n" \
"v_and_b32 v217,0xffff0000,v172\n" \
"v_pk_add_f32 v[0:1],v[0:1],v[216:217]\n" \
"v_lshlrev_b32 v218,16,v173\n" \
"v_and_b32 v219,0xffff0000,v173\n" \
"v_pk_add_f32 v[2:3],v[2:3],v[218:219]\n" \
"v_lshlrev_b32 v220,16,v174\n" \
"v_and_b32 v221,0xffff0000,v174\n" \
"v_pk_add_f32 v[4:5],v[4:5],v[220:221]\n" \
"v_lshlrev_b32 v222,16,v175\n" \
"v_and_b32 v223,0xffff0000,v175\n" \
"v_pk_add_f32 v[6:7],v[6:7],v[222:223]\n" \
S_"vmcnt(9)\n" \
"v_lshlrev_b32 v216,16,v176\n" \
"v_and_b32 v217,0xffff0000,v176\n" \
"v_pk_add_f32 v[8:9],v[8:9],v[216:217]\n" \
"v_lshlrev_b32 v218,16,v177\n" \
"v_and_b32 v219,0xffff0000,v177\n" \
"v_pk_add_f32 v[10:11],v[10:11],v[218:219]\n" \
"v_lshlrev_b32 v220,16,v178\n" \
"v_and_b32 v221,0xffff0000,v178\n" \
"v_pk_add_f32 v[12:13],v[12:13],v[220:221]\n" \
"v_lshlrev_b32 v222,16,v179\n" \
"v_and_b32 v223,0xffff0000,v179\n" \
"v_pk_add_f32 v[14:15],v[14:15],v[222:223]\n" \
S_"vmcnt(8)\n" \
"v_lshlrev_b32 v216,16,v180\n" \
"v_and_b32 v217,0xffff0000,v180\n" \
"v_pk_add_f32 v[0:1],v[0:1],v[216:217]\n" \
"v_lshlrev_b32 v218,16,v181\n" \
"v_and_b32 v219,0xffff0000,v181\n" \
"v_pk_add_f32 v[2:3],v[2:3],v[218:219]\n" \
"v_lshlrev_b32 v220,16,v182\n" \
"v_and_b32 v221,0xffff0000,v182\n" \
"v_pk_add_f32 v[4:5],v[4:5],v[220:221]\n" \
"v_lshlrev_b32 v222,16,v183\n" \
"v_and_b32 v223,0xffff0000,v183\n" \
"v_pk_add_f32 v[6:7],v[6:7],v[222:223]\n" \
S_"vmcnt(7)\n" \
"v_lshlrev_b32 v216,16,v184\n" \
"v_and_b32 v217,0xffff0000,v184\n" \
"v_pk_add_f32 v[8:9],v[8:9],v[216:217]\n" \
"v_lshlrev_b32 v218,16,v185\n" \
"v_and_b32 v219,0xffff0000,v185\n" \
"v_pk_add_f32 v[10:11],v[10:11],v[218:219]\n" \
"v_lshlrev_b32 v220,16,v186\n" \
"v_and_b32 v221,0xffff0000,v186\n" \
"v_pk_add_f32 v[12:13],v[12:13],v[220:221]\n" \
"v_lshlrev_b32 v222,16,v187\n" \
"v_and_b32 v223,0xffff0000,v187\n" \
"v_pk_add_f32 v[14:15],v[14:15],v[222:223]\n" \
S_"vmcnt(6)\n" \
"v_lshlrev_b32 v216,16,v188\n" \
"v_and_b32 v217,0xffff0000,v188\n" \
"v_pk_add_f32 v[0:1],v[0:1],v[216:217]\n" \
"v_lshlrev_b32 v218,16,v189\n" \
"v_and_b32 v219,0xffff0000,v189\n" \
"v_pk_add_f32 v[2:3],v[2:3],v[218:219]\n" \
"v_lshlrev_b32 v220,16,v190\n" \
"v_and_b32 v221,0xffff0000,v190\n" \
"v_pk_add_f32 v[4:5],v[4:5],v[220:221]\n" \
"v_lshlrev_b32 v222,16,v191\n" \
"v_and_b32 v223,0xffff0000,v191\n" \
"v_pk_add_f32 v[6:7],v[6:7],v[222:223]\n" \
S_"vmcnt(5)\n" \
"v_lshlrev_b32 v216,16,v192\n" \
"v_and_b32 v217,0xffff0000,v192\n" \
"v_pk_add_f32 v[8:9],v[8:9],v[216:217]\n" \
"v_lshlrev_b32 v218,16,v193\n" \
"v_and_b32 v219,0xffff0000,v193\n" \
"v_pk_add_f32 v[10:11],v[10:11],v[218:219]\n" \
"v_lshlrev_b32 v220,16,v194\n" \
"v_and_b32 v221,0xffff0000,v194\n" \
"v_pk_add_f32 v[12:13],v[12:13],v[220:221]\n" \
"v_lshlrev_b32 v222,16,v195\n" \
"v_and_b32 v223,0xffff0000,v195\n" \
"v_pk_add_f32 v[14:15],v[14:15],v[222:223]\n" \
S_"vmcnt(4)\n" \
"v_lshlrev_b32 v216,16,v196\n" \
"v_and_b32 v217,0xffff0000,v196\n" \
"v_pk_add_f32 v[0:1],v[0:1],v[216:217]\n" \
"v_lshlrev_b32 v218,16,v197\n" \
"v_and_b32 v219,0xffff0000,v197\n" \
"v_pk_add_f32 v[2:3],v[2:3],v[218:219]\n" \
"v_lshlrev_b32 v220,16,v198\n" \
"v_and_b32 v221,0xffff0000,v198\n" \
"v_pk_add_f32 v[4:5],v[4:5],v[220:221]\n" \
"v_lshlrev_b32 v222,16,v199\n" \
"v_and_b32 v223,0xffff0000,v199\n" \
"v_pk_add_f32 v[6:7],v[6:7],v[222:223]\n" \
S_"vmcnt(3)\n" \
"v_lshlrev_b32 v216,16,v200\n" \
"v_and_b32 v217,0xffff0000,v200\n" \
"v_pk_add_f32 v[8:9],v[8:9],v[216:217]\n" \
"v_lshlrev_b32 v218,16,v201\n" \
"v_and_b32 v219,0xffff0000,v201\n" \
"v_pk_add_f32 v[10:11],v[10:11],v[218:219]\n" \
"v_lshlrev_b32 v220,16,v202\n" \
"v_and_b32 v221,0xffff0000,v202\n" \
"v_pk_add_f32 v[12:13],v[12:13],v[220:221]\n" \
"v_lshlrev_b32 v222,16,v203\n" \
"v_and_b32 v223,0xffff0000,v203\n" \
"v_pk_add_f32 v[14:15],v[14:15],v[222:223]\n" \
S_"vmcnt(2)\n" \
"v_lshlrev_b32 v216,16,v204\n" \
"v_and_b32 v217,0xffff0000,v204\n" \
"v_pk_add_f32 v[0:1],v[0:1],v[216:217]\n" \
"v_lshlrev_b32 v218,16,v205\n" \
"v_and_b32 v219,0xffff0000,v205\n" \
"v_pk_add_f32 v[2:3],v[2:3],v[218:219]\n" \
"v_lshlrev_b32 v220,16,v206\n" \
"v_and_b32 v221,0xffff0000,v206\n" \
"v_pk_add_f32 v[4:5],v[4:5],v[220:221]\n" \
"v_lshlrev_b32 v222,16,v207\n" \
"v_and_b32 v223,0xffff0000,v207\n" \
"v_pk_add_f32 v[6:7],v[6:7],v[222:223]\n" \
S_"vmcnt(1)\n" \
"v_lshlrev_b32 v216,16,v208\n" \
"v_and_b32 v217,0xffff0000,v208\n" \
"v_pk_add_f32 v[8:9],v[8:9],v[216:217]\n" \
"v_lshlrev_b32 v218,16,v209\n" \
"v_and_b32 v219,0xffff0000,v209\n" \
"v_pk_add_f32 v[10:11],v[10:11],v[218:219]\n" \
"v_lshlrev_b32 v220,16,v210\n" \
"v_and_b32 v221,0xffff0000,v210\n" \
"v_pk_add_f32 v[12:13],v[12:13],v[220:221]\n" \
"v_lshlrev_b32 v222,16,v211\n" \
"v_and_b32 v223,0xffff0000,v211\n" \
"v_pk_add_f32 v[14:15],v[14:15],v[222:223]\n" \
C_"v0,v0,v1\n" \
C_"v1,v2,v3\n" \
C_"v2,v4,v5\n" \
C_"v3,v6,v7\n" \
C_"v8,v8,v9\n" \
C_"v9,v10,v11\n" \
C_"v10,v12,v13\n" \
C_"v11,v14,v15\n" \
"buffer_store_dwordx4 v[0:3],v245,s[92:95],s79 offen nt\n" \
"buffer_store_dwordx4 v[8:11],v245,s[92:95],s79 offen offset:64 nt\n" \
"; ---- fin\n" \
"LFIN_%=:\n" \
S_"vmcnt(0) lgkmcnt(0)\n" \
"s_branch LEND_%=\n" \
"; ---- fta\n" \
"LFTA_%=:\n" \
A_"s62,s77,s8\n" \
L_"v[88:91],v240,s[52:55],s62 offen sc1\n" \
L_"v[92:95],v240,s[52:55],s62 offen offset:1024 sc1\n" \
A_"s62,s77,s9\n" \
L_"v[96:99],v240,s[52:55],s62 offen sc1\n" \
L_"v[100:103],v240,s[52:55],s62 offen offset:1024 sc1\n" \
A_"s62,s77,s10\n" \
L_"v[104:107],v240,s[52:55],s62 offen sc1\n" \
L_"v[108:111],v240,s[52:55],s62 offen offset:1024 sc1\n" \
A_"s62,s77,s11\n" \
L_"v[132:135],v240,s[52:55],s62 offen sc1\n" \
L_"v[140:143],v240,s[52:55],s62 offen offset:1024 sc1\n" \
A_"s62,s77,s12\n" \
L_"v[148:151],v240,s[52:55],s62 offen sc1\n" \
L_"v[156:159],v240,s[52:55],s62 offen offset:1024 sc1\n" \
A_"s62,s77,s13\n" \
L_"v[180:183],v240,s[52:55],s62 offen sc1\n" \
L_"v[188:191],v240,s[52:55],s62 offen offset:1024 sc1\n" \
A_"s62,s77,s14\n" \
L_"v[196:199],v240,s[52:55],s62 offen sc1\n" \
L_"v[204:207],v240,s[52:55],s62 offen offset:1024 sc1\n" \
A_"s62,s78,s8\n" \
L_"v[128:131],v240,s[52:55],s62 offen sc1\n" \
L_"v[136:139],v240,s[52:55],s62 offen offset:1024 sc1\n" \
A_"s62,s78,s9\n" \
L_"v[144:147],v240,s[52:55],s62 offen sc1\n" \
L_"v[152:155],v240,s[52:55],s62 offen offset:1024 sc1\n" \
A_"s62,s78,s10\n" \
L_"v[160:163],v240,s[52:55],s62 offen sc1\n" \
L_"v[168:171],v240,s[52:55],s62 offen offset:1024 sc1\n" \
A_"s62,s78,s11\n" \
L_"v[176:179],v240,s[52:55],s62 offen sc1\n" \
L_"v[184:187],v240,s[52:55],s62 offen offset:1024 sc1\n" \
A_"s62,s78,s12\n" \
L_"v[192:195],v240,s[52:55],s62 offen sc1\n" \
L_"v[200:203],v240,s[52:55],s62 offen offset:1024 sc1\n" \
A_"s62,s78,s13\n" \
L_"v[208:211],v240,s[52:55],s62 offen sc1\n" \
L_"v[216:219],v240,s[52:55],s62 offen offset:1024 sc1\n" \
A_"s62,s78,s14\n" \
L_"v[212:215],v240,s[52:55],s62 offen sc1\n" \
L_"v[220:223],v240,s[52:55],s62 offen offset:1024 sc1\n" \
"buffer_store_dwordx4 v[72:75],v245,s[92:95],s79 offen nt\n" \
"buffer_store_dwordx4 v[80:83],v245,s[92:95],s79 offen offset:64 nt\n" \
S_"vmcnt(29)\n" \
"v_lshlrev_b32 v164,16,v88\n" \
"v_and_b32 v165,0xffff0000,v88\n" \
"v_pk_add_f32 v[0:1],v[0:1],v[164:165]\n" \
"v_lshlrev_b32 v166,16,v89\n" \
"v_and_b32 v167,0xffff0000,v89\n" \
"v_pk_add_f32 v[2:3],v[2:3],v[166:167]\n" \
"v_lshlrev_b32 v172,16,v90\n" \
"v_and_b32 v173,0xffff0000,v90\n" \
"v_pk_add_f32 v[4:5],v[4:5],v[172:173]\n" \
"v_lshlrev_b32 v174,16,v91\n" \
"v_and_b32 v175,0xffff0000,v91\n" \
"v_pk_add_f32 v[6:7],v[6:7],v[174:175]\n" \
S_"vmcnt(28)\n" \
"v_lshlrev_b32 v164,16,v92\n" \
"v_and_b32 v165,0xffff0000,v92\n" \
"v_pk_add_f32 v[8:9],v[8:9],v[164:165]\n" \
"v_lshlrev_b32 v166,16,v93\n" \
"v_and_b32 v167,0xffff0000,v93\n" \
"v_pk_add_f32 v[10:11],v[10:11],v[166:167]\n" \
"v_lshlrev_b32 v172,16,v94\n" \
"v_and_b32 v173,0xffff0000,v94\n" \
"v_pk_add_f32 v[12:13],v[12:13],v[172:173]\n" \
"v_lshlrev_b32 v174,16,v95\n" \
"v_and_b32 v175,0xffff0000,v95\n" \
"v_pk_add_f32 v[14:15],v[14:15],v[174:175]\n" \
S_"vmcnt(27)\n" \
"v_lshlrev_b32 v164,16,v96\n" \
"v_and_b32 v165,0xffff0000,v96\n" \
"v_pk_add_f32 v[0:1],v[0:1],v[164:165]\n" \
"v_lshlrev_b32 v166,16,v97\n" \
"v_and_b32 v167,0xffff0000,v97\n" \
"v_pk_add_f32 v[2:3],v[2:3],v[166:167]\n" \
"v_lshlrev_b32 v172,16,v98\n" \
"v_and_b32 v173,0xffff0000,v98\n" \
"v_pk_add_f32 v[4:5],v[4:5],v[172:173]\n" \
"v_lshlrev_b32 v174,16,v99\n" \
"v_and_b32 v175,0xffff0000,v99\n" \
"v_pk_add_f32 v[6:7],v[6:7],v[174:175]\n" \
S_"vmcnt(26)\n" \
"v_lshlrev_b32 v164,16,v100\n" \
"v_and_b32 v165,0xffff0000,v100\n" \
"v_pk_add_f32 v[8:9],v[8:9],v[164:165]\n" \
"v_lshlrev_b32 v166,16,v101\n" \
"v_and_b32 v167,0xffff0000,v101\n" \
"v_pk_add_f32 v[10:11],v[10:11],v[166:167]\n" \
"v_lshlrev_b32 v172,16,v102\n" \
"v_and_b32 v173,0xffff0000,v102\n" \
"v_pk_add_f32 v[12:13],v[12:13],v[172:173]\n" \
"v_lshlrev_b32 v174,16,v103\n" \
"v_and_b32 v175,0xffff0000,v103\n" \
"v_pk_add_f32 v[14:15],v[14:15],v[174:175]\n" \
S_"vmcnt(25)\n" \
"v_lshlrev_b32 v164,16,v104\n" \
"v_and_b32 v165,0xffff0000,v104\n" \
"v_pk_add_f32 v[0:1],v[0:1],v[164:165]\n" \
"v_lshlrev_b32 v166,16,v105\n" \
"v_and_b32 v167,0xffff0000,v105\n" \
"v_pk_add_f32 v[2:3],v[2:3],v[166:167]\n" \
"v_lshlrev_b32 v172,16,v106\n" \
"v_and_b32 v173,0xffff0000,v106\n" \
"v_pk_add_f32 v[4:5],v[4:5],v[172:173]\n" \
"v_lshlrev_b32 v174,16,v107\n" \
"v_and_b32 v175,0xffff0000,v107\n" \
"v_pk_add_f32 v[6:7],v[6:7],v[174:175]\n" \
S_"vmcnt(24)\n" \
"v_lshlrev_b32 v164,16,v108\n" \
"v_and_b32 v165,0xffff0000,v108\n" \
"v_pk_add_f32 v[8:9],v[8:9],v[164:165]\n" \
"v_lshlrev_b32 v166,16,v109\n" \
"v_and_b32 v167,0xffff0000,v109\n" \
"v_pk_add_f32 v[10:11],v[10:11],v[166:167]\n" \
"v_lshlrev_b32 v172,16,v110\n" \
"v_and_b32 v173,0xffff0000,v110\n" \
"v_pk_add_f32 v[12:13],v[12:13],v[172:173]\n" \
"v_lshlrev_b32 v174,16,v111\n" \
"v_and_b32 v175,0xffff0000,v111\n" \
"v_pk_add_f32 v[14:15],v[14:15],v[174:175]\n" \
S_"vmcnt(23)\n" \
"v_lshlrev_b32 v164,16,v132\n" \
"v_and_b32 v165,0xffff0000,v132\n" \
"v_pk_add_f32 v[0:1],v[0:1],v[164:165]\n" \
"v_lshlrev_b32 v166,16,v133\n" \
"v_and_b32 v167,0xffff0000,v133\n" \
"v_pk_add_f32 v[2:3],v[2:3],v[166:167]\n" \
"v_lshlrev_b32 v172,16,v134\n" \
"v_and_b32 v173,0xffff0000,v134\n" \
"v_pk_add_f32 v[4:5],v[4:5],v[172:173]\n" \
"v_lshlrev_b32 v174,16,v135\n" \
"v_and_b32 v175,0xffff0000,v135\n" \
"v_pk_add_f32 v[6:7],v[6:7],v[174:175]\n" \
S_"vmcnt(22)\n" \
"v_lshlrev_b32 v164,16,v140\n" \
"v_and_b32 v165,0xffff0000,v140\n" \
"v_pk_add_f32 v[8:9],v[8:9],v[164:165]\n" \
"v_lshlrev_b32 v166,16,v141\n" \
"v_and_b32 v167,0xffff0000,v141\n" \
"v_pk_add_f32 v[10:11],v[10:11],v[166:167]\n" \
"v_lshlrev_b32 v172,16,v142\n" \
"v_and_b32 v173,0xffff0000,v142\n" \
"v_pk_add_f32 v[12:13],v[12:13],v[172:173]\n" \
"v_lshlrev_b32 v174,16,v143\n" \
"v_and_b32 v175,0xffff0000,v143\n" \
"v_pk_add_f32 v[14:15],v[14:15],v[174:175]\n" \
S_"vmcnt(21)\n" \
"v_lshlrev_b32 v164,16,v148\n" \
"v_and_b32 v165,0xffff0000,v148\n" \
"v_pk_add_f32 v[0:1],v[0:1],v[164:165]\n" \
"v_lshlrev_b32 v166,16,v149\n" \
"v_and_b32 v167,0xffff0000,v149\n" \
"v_pk_add_f32 v[2:3],v[2:3],v[166:167]\n" \
"v_lshlrev_b32 v172,16,v150\n" \
"v_and_b32 v173,0xffff0000,v150\n" \
"v_pk_add_f32 v[4:5],v[4:5],v[172:173]\n" \
"v_lshlrev_b32 v174,16,v151\n" \
"v_and_b32 v175,0xffff0000,v151\n" \
"v_pk_add_f32 v[6:7],v[6:7],v[174:175]\n" \
S_"vmcnt(20)\n" \
"v_lshlrev_b32 v164,16,v156\n" \
"v_and_b32 v165,0xffff0000,v156\n" \
"v_pk_add_f32 v[8:9],v[8:9],v[164:165]\n" \
"v_lshlrev_b32 v166,16,v157\n" \
"v_and_b32 v167,0xffff0000,v157\n" \
"v_pk_add_f32 v[10:11],v[10:11],v[166:167]\n" \
"v_lshlrev_b32 v172,16,v158\n" \
"v_and_b32 v173,0xffff0000,v158\n" \
"v_pk_add_f32 v[12:13],v[12:13],v[172:173]\n" \
"v_lshlrev_b32 v174,16,v159\n" \
"v_and_b32 v175,0xffff0000,v159\n" \
"v_pk_add_f32 v[14:15],v[14:15],v[174:175]\n" \
S_"vmcnt(19)\n" \
"v_lshlrev_b32 v164,16,v180\n" \
"v_and_b32 v165,0xffff0000,v180\n" \
"v_pk_add_f32 v[0:1],v[0:1],v[164:165]\n" \
"v_lshlrev_b32 v166,16,v181\n" \
"v_and_b32 v167,0xffff0000,v181\n" \
"v_pk_add_f32 v[2:3],v[2:3],v[166:167]\n" \
"v_lshlrev_b32 v172,16,v182\n" \
"v_and_b32 v173,0xffff0000,v182\n" \
"v_pk_add_f32 v[4:5],v[4:5],v[172:173]\n" \
"v_lshlrev_b32 v174,16,v183\n" \
"v_and_b32 v175,0xffff0000,v183\n" \
"v_pk_add_f32 v[6:7],v[6:7],v[174:175]\n" \
S_"vmcnt(18)\n" \
"v_lshlrev_b32 v164,16,v188\n" \
"v_and_b32 v165,0xffff0000,v188\n" \
"v_pk_add_f32 v[8:9],v[8:9],v[164:165]\n" \
"v_lshlrev_b32 v166,16,v189\n" \
"v_and_b32 v167,0xffff0000,v189\n" \
"v_pk_add_f32 v[10:11],v[10:11],v[166:167]\n" \
"v_lshlrev_b32 v172,16,v190\n" \
"v_and_b32 v173,0xffff0000,v190\n" \
"v_pk_add_f32 v[12:13],v[12:13],v[172:173]\n" \
"v_lshlrev_b32 v174,16,v191\n" \
"v_and_b32 v175,0xffff0000,v191\n" \
"v_pk_add_f32 v[14:15],v[14:15],v[174:175]\n" \
S_"vmcnt(17)\n" \
"v_lshlrev_b32 v164,16,v196\n" \
"v_and_b32 v165,0xffff0000,v196\n" \
"v_pk_add_f32 v[0:1],v[0:1],v[164:165]\n" \
"v_lshlrev_b32 v166,16,v197\n" \
"v_and_b32 v167,0xffff0000,v197\n" \
"v_pk_add_f32 v[2:3],v[2:3],v[166:167]\n" \
"v_lshlrev_b32 v172,16,v198\n" \
"v_and_b32 v173,0xffff0000,v198\n" \
"v_pk_add_f32 v[4:5],v[4:5],v[172:173]\n" \
"v_lshlrev_b32 v174,16,v199\n" \
"v_and_b32 v175,0xffff0000,v199\n" \
"v_pk_add_f32 v[6:7],v[6:7],v[174:175]\n" \
S_"vmcnt(16)\n" \
"v_lshlrev_b32 v164,16,v204\n" \
"v_and_b32 v165,0xffff0000,v204\n" \
"v_pk_add_f32 v[8:9],v[8:9],v[164:165]\n" \
"v_lshlrev_b32 v166,16,v205\n" \
"v_and_b32 v167,0xffff0000,v205\n" \
"v_pk_add_f32 v[10:11],v[10:11],v[166:167]\n" \
"v_lshlrev_b32 v172,16,v206\n" \
"v_and_b32 v173,0xffff0000,v206\n" \
"v_pk_add_f32 v[12:13],v[12:13],v[172:173]\n" \
"v_lshlrev_b32 v174,16,v207\n" \
"v_and_b32 v175,0xffff0000,v207\n" \
"v_pk_add_f32 v[14:15],v[14:15],v[174:175]\n" \
C_"v0,v0,v1\n" \
C_"v1,v2,v3\n" \
C_"v2,v4,v5\n" \
C_"v3,v6,v7\n" \
C_"v8,v8,v9\n" \
C_"v9,v10,v11\n" \
C_"v10,v12,v13\n" \
C_"v11,v14,v15\n" \
"buffer_store_dwordx4 v[0:3],v245,s[92:95],s90 offen nt\n" \
"buffer_store_dwordx4 v[8:11],v245,s[92:95],s90 offen offset:64 nt\n" \
S_"vmcnt(17)\n" \
"v_lshlrev_b32 v164,16,v128\n" \
"v_and_b32 v165,0xffff0000,v128\n" \
"v_pk_add_f32 v[112:113],v[112:113],v[164:165]\n" \
"v_lshlrev_b32 v166,16,v129\n" \
"v_and_b32 v167,0xffff0000,v129\n" \
"v_pk_add_f32 v[114:115],v[114:115],v[166:167]\n" \
"v_lshlrev_b32 v172,16,v130\n" \
"v_and_b32 v173,0xffff0000,v130\n" \
"v_pk_add_f32 v[116:117],v[116:117],v[172:173]\n" \
"v_lshlrev_b32 v174,16,v131\n" \
"v_and_b32 v175,0xffff0000,v131\n" \
"v_pk_add_f32 v[118:119],v[118:119],v[174:175]\n" \
S_"vmcnt(16)\n" \
"v_lshlrev_b32 v164,16,v136\n" \
"v_and_b32 v165,0xffff0000,v136\n" \
"v_pk_add_f32 v[120:121],v[120:121],v[164:165]\n" \
"v_lshlrev_b32 v166,16,v137\n" \
"v_and_b32 v167,0xffff0000,v137\n" \
"v_pk_add_f32 v[122:123],v[122:123],v[166:167]\n" \
"v_lshlrev_b32 v172,16,v138\n" \
"v_and_b32 v173,0xffff0000,v138\n" \
"v_pk_add_f32 v[124:125],v[124:125],v[172:173]\n" \
"v_lshlrev_b32 v174,16,v139\n" \
"v_and_b32 v175,0xffff0000,v139\n" \
"v_pk_add_f32 v[126:127],v[126:127],v[174:175]\n" \
S_"vmcnt(15)\n" \
"v_lshlrev_b32 v164,16,v144\n" \
"v_and_b32 v165,0xffff0000,v144\n" \
"v_pk_add_f32 v[112:113],v[112:113],v[164:165]\n" \
"v_lshlrev_b32 v166,16,v145\n" \
"v_and_b32 v167,0xffff0000,v145\n" \
"v_pk_add_f32 v[114:115],v[114:115],v[166:167]\n" \
"v_lshlrev_b32 v172,16,v146\n" \
"v_and_b32 v173,0xffff0000,v146\n" \
"v_pk_add_f32 v[116:117],v[116:117],v[172:173]\n" \
"v_lshlrev_b32 v174,16,v147\n" \
"v_and_b32 v175,0xffff0000,v147\n" \
"v_pk_add_f32 v[118:119],v[118:119],v[174:175]\n" \
S_"vmcnt(14)\n" \
"v_lshlrev_b32 v164,16,v152\n" \
"v_and_b32 v165,0xffff0000,v152\n" \
"v_pk_add_f32 v[120:121],v[120:121],v[164:165]\n" \
"v_lshlrev_b32 v166,16,v153\n" \
"v_and_b32 v167,0xffff0000,v153\n" \
"v_pk_add_f32 v[122:123],v[122:123],v[166:167]\n" \
"v_lshlrev_b32 v172,16,v154\n" \
"v_and_b32 v173,0xffff0000,v154\n" \
"v_pk_add_f32 v[124:125],v[124:125],v[172:173]\n" \
"v_lshlrev_b32 v174,16,v155\n" \
"v_and_b32 v175,0xffff0000,v155\n" \
"v_pk_add_f32 v[126:127],v[126:127],v[174:175]\n" \
S_"vmcnt(13)\n" \
"v_lshlrev_b32 v164,16,v160\n" \
"v_and_b32 v165,0xffff0000,v160\n" \
"v_pk_add_f32 v[112:113],v[112:113],v[164:165]\n" \
"v_lshlrev_b32 v166,16,v161\n" \
"v_and_b32 v167,0xffff0000,v161\n" \
"v_pk_add_f32 v[114:115],v[114:115],v[166:167]\n" \
"v_lshlrev_b32 v172,16,v162\n" \
"v_and_b32 v173,0xffff0000,v162\n" \
"v_pk_add_f32 v[116:117],v[116:117],v[172:173]\n" \
"v_lshlrev_b32 v174,16,v163\n" \
"v_and_b32 v175,0xffff0000,v163\n" \
"v_pk_add_f32 v[118:119],v[118:119],v[174:175]\n" \
S_"vmcnt(12)\n" \
"v_lshlrev_b32 v164,16,v168\n" \
"v_and_b32 v165,0xffff0000,v168\n" \
"v_pk_add_f32 v[120:121],v[120:121],v[164:165]\n" \
"v_lshlrev_b32 v166,16,v169\n" \
"v_and_b32 v167,0xffff0000,v169\n" \
"v_pk_add_f32 v[122:123],v[122:123],v[166:167]\n" \
"v_lshlrev_b32 v172,16,v170\n" \
"v_and_b32 v173,0xffff0000,v170\n" \
"v_pk_add_f32 v[124:125],v[124:125],v[172:173]\n" \
"v_lshlrev_b32 v174,16,v171\n" \
"v_and_b32 v175,0xffff0000,v171\n" \
"v_pk_add_f32 v[126:127],v[126:127],v[174:175]\n" \
S_"vmcnt(11)\n" \
"v_lshlrev_b32 v164,16,v176\n" \
"v_and_b32 v165,0xffff0000,v176\n" \
"v_pk_add_f32 v[112:113],v[112:113],v[164:165]\n" \
"v_lshlrev_b32 v166,16,v177\n" \
"v_and_b32 v167,0xffff0000,v177\n" \
"v_pk_add_f32 v[114:115],v[114:115],v[166:167]\n" \
"v_lshlrev_b32 v172,16,v178\n" \
"v_and_b32 v173,0xffff0000,v178\n" \
"v_pk_add_f32 v[116:117],v[116:117],v[172:173]\n" \
"v_lshlrev_b32 v174,16,v179\n" \
"v_and_b32 v175,0xffff0000,v179\n" \
"v_pk_add_f32 v[118:119],v[118:119],v[174:175]\n" \
S_"vmcnt(10)\n" \
"v_lshlrev_b32 v164,16,v184\n" \
"v_and_b32 v165,0xffff0000,v184\n" \
"v_pk_add_f32 v[120:121],v[120:121],v[164:165]\n" \
"v_lshlrev_b32 v166,16,v185\n" \
"v_and_b32 v167,0xffff0000,v185\n" \
"v_pk_add_f32 v[122:123],v[122:123],v[166:167]\n" \
"v_lshlrev_b32 v172,16,v186\n" \
"v_and_b32 v173,0xffff0000,v186\n" \
"v_pk_add_f32 v[124:125],v[124:125],v[172:173]\n" \
"v_lshlrev_b32 v174,16,v187\n" \
"v_and_b32 v175,0xffff0000,v187\n" \
"v_pk_add_f32 v[126:127],v[126:127],v[174:175]\n" \
S_"vmcnt(9)\n" \
"v_lshlrev_b32 v164,16,v192\n" \
"v_and_b32 v165,0xffff0000,v192\n" \
"v_pk_add_f32 v[112:113],v[112:113],v[164:165]\n" \
"v_lshlrev_b32 v166,16,v193\n" \
"v_and_b32 v167,0xffff0000,v193\n" \
"v_pk_add_f32 v[114:115],v[114:115],v[166:167]\n" \
"v_lshlrev_b32 v172,16,v194\n" \
"v_and_b32 v173,0xffff0000,v194\n" \
"v_pk_add_f32 v[116:117],v[116:117],v[172:173]\n" \
"v_lshlrev_b32 v174,16,v195\n" \
"v_and_b32 v175,0xffff0000,v195\n" \
"v_pk_add_f32 v[118:119],v[118:119],v[174:175]\n" \
S_"vmcnt(8)\n" \
"v_lshlrev_b32 v164,16,v200\n" \
"v_and_b32 v165,0xffff0000,v200\n" \
"v_pk_add_f32 v[120:121],v[120:121],v[164:165]\n" \
"v_lshlrev_b32 v166,16,v201\n" \
"v_and_b32 v167,0xffff0000,v201\n" \
"v_pk_add_f32 v[122:123],v[122:123],v[166:167]\n" \
"v_lshlrev_b32 v172,16,v202\n" \
"v_and_b32 v173,0xffff0000,v202\n" \
"v_pk_add_f32 v[124:125],v[124:125],v[172:173]\n" \
"v_lshlrev_b32 v174,16,v203\n" \
"v_and_b32 v175,0xffff0000,v203\n" \
"v_pk_add_f32 v[126:127],v[126:127],v[174:175]\n" \
S_"vmcnt(7)\n" \
"v_lshlrev_b32 v164,16,v208\n" \
"v_and_b32 v165,0xffff0000,v208\n" \
"v_pk_add_f32 v[112:113],v[112:113],v[164:165]\n" \
"v_lshlrev_b32 v166,16,v209\n" \
"v_and_b32 v167,0xffff0000,v209\n" \
"v_pk_add_f32 v[114:115],v[114:115],v[166:167]\n" \
"v_lshlrev_b32 v172,16,v210\n" \
"v_and_b32 v173,0xffff0000,v210\n" \
"v_pk_add_f32 v[116:117],v[116:117],v[172:173]\n" \
"v_lshlrev_b32 v174,16,v211\n" \
"v_and_b32 v175,0xffff0000,v211\n" \
"v_pk_add_f32 v[118:119],v[118:119],v[174:175]\n" \
S_"vmcnt(6)\n" \
"v_lshlrev_b32 v164,16,v216\n" \
"v_and_b32 v165,0xffff0000,v216\n" \
"v_pk_add_f32 v[120:121],v[120:121],v[164:165]\n" \
"v_lshlrev_b32 v166,16,v217\n" \
"v_and_b32 v167,0xffff0000,v217\n" \
"v_pk_add_f32 v[122:123],v[122:123],v[166:167]\n" \
"v_lshlrev_b32 v172,16,v218\n" \
"v_and_b32 v173,0xffff0000,v218\n" \
"v_pk_add_f32 v[124:125],v[124:125],v[172:173]\n" \
"v_lshlrev_b32 v174,16,v219\n" \
"v_and_b32 v175,0xffff0000,v219\n" \
"v_pk_add_f32 v[126:127],v[126:127],v[174:175]\n" \
S_"vmcnt(5)\n" \
"v_lshlrev_b32 v164,16,v212\n" \
"v_and_b32 v165,0xffff0000,v212\n" \
"v_pk_add_f32 v[112:113],v[112:113],v[164:165]\n" \
"v_lshlrev_b32 v166,16,v213\n" \
"v_and_b32 v167,0xffff0000,v213\n" \
"v_pk_add_f32 v[114:115],v[114:115],v[166:167]\n" \
"v_lshlrev_b32 v172,16,v214\n" \
"v_and_b32 v173,0xffff0000,v214\n" \
"v_pk_add_f32 v[116:117],v[116:117],v[172:173]\n" \
"v_lshlrev_b32 v174,16,v215\n" \
"v_and_b32 v175,0xffff0000,v215\n" \
"v_pk_add_f32 v[118:119],v[118:119],v[174:175]\n" \
S_"vmcnt(4)\n" \
"v_lshlrev_b32 v164,16,v220\n" \
"v_and_b32 v165,0xffff0000,v220\n" \
"v_pk_add_f32 v[120:121],v[120:121],v[164:165]\n" \
"v_lshlrev_b32 v166,16,v221\n" \
"v_and_b32 v167,0xffff0000,v221\n" \
"v_pk_add_f32 v[122:123],v[122:123],v[166:167]\n" \
"v_lshlrev_b32 v172,16,v222\n" \
"v_and_b32 v173,0xffff0000,v222\n" \
"v_pk_add_f32 v[124:125],v[124:125],v[172:173]\n" \
"v_lshlrev_b32 v174,16,v223\n" \
"v_and_b32 v175,0xffff0000,v223\n" \
"v_pk_add_f32 v[126:127],v[126:127],v[174:175]\n" \
C_"v112,v112,v113\n" \
C_"v113,v114,v115\n" \
C_"v114,v116,v117\n" \
C_"v115,v118,v119\n" \
C_"v120,v120,v121\n" \
C_"v121,v122,v123\n" \
C_"v122,v124,v125\n" \
C_"v123,v126,v127\n" \
"buffer_store_dwordx4 v[112:115],v245,s[92:95],s91 offen nt\n" \
"buffer_store_dwordx4 v[120:123],v245,s[92:95],s91 offen offset:64 nt\n" \
"s_branch LFIN_%=\n" \
"LWA0_%=:\n" \
"s_cmp_eq_u32 s96,0\n" \
"s_cbranch_scc1 LWA0d_%=\n" \
"s_sub_u32 s96,s96,1\n" \
"buffer_load_dword v252,v248,s[84:87],0 offen sc1\n" \
S_"vmcnt(0)\n" \
"v_cmp_gt_u32 vcc,1,v252\n" \
"s_cbranch_vccnz LWA0_%=\n" \
"s_branch LWA0d_%=\n" \
"LWA1_%=:\n" \
"s_cmp_eq_u32 s96,0\n" \
"s_cbranch_scc1 LWA1d_%=\n" \
"s_sub_u32 s96,s96,1\n" \
"buffer_load_dword v252,v248,s[84:87],0 offen sc1\n" \
S_"vmcnt(0)\n" \
"v_cmp_gt_u32 vcc,2,v252\n" \
"s_cbranch_vccnz LWA1_%=\n" \
"s_branch LWA1d_%=\n" \
"LWA2_%=:\n" \
"s_cmp_eq_u32 s96,0\n" \
"s_cbranch_scc1 LWA2d_%=\n" \
"s_sub_u32 s96,s96,1\n" \
"buffer_load_dword v252,v248,s[84:87],0 offen sc1\n" \
S_"vmcnt(0)\n" \
"v_cmp_gt_u32 vcc,3,v252\n" \
"s_cbranch_vccnz LWA2_%=\n" \
"s_branch LWA2d_%=\n" \
"LWB0_%=:\n" \
"s_cmp_eq_u32 s96,0\n" \
"s_cbranch_scc1 LWB0d_%=\n" \
"s_sub_u32 s96,s96,1\n" \
"buffer_load_dword v252,v248,s[84:87],0 offen sc1\n" \
S_"vmcnt(0)\n" \
"v_cmp_gt_u32 vcc,1,v252\n" \
"s_cbranch_vccnz LWB0_%=\n" \
"s_branch LWB0d_%=\n" \
"LWB2_%=:\n" \
"s_cmp_eq_u32 s96,0\n" \
"s_cbranch_scc1 LWB2d_%=\n" \
"s_sub_u32 s96,s96,1\n" \
"buffer_load_dword v252,v248,s[84:87],0 offen sc1\n" \
S_"vmcnt(0)\n" \
"v_cmp_gt_u32 vcc,2,v252\n" \
"s_cbranch_vccnz LWB2_%=\n" \
"s_branch LWB2d_%=\n" \
"LCa_%=:\n" \
"s_memrealtime s[18:19]\n" \
S_"lgkmcnt(0)\n" \
"s_sub_u32 s60,s18,s31\n" \
"s_cmp_lt_i32 s60,0\n" \
"s_cbranch_scc1 LCa_%=\n" \
"s_branch LCad_%=\n" \
"LCb_%=:\n" \
"s_memrealtime s[18:19]\n" \
S_"lgkmcnt(0)\n" \
"s_sub_u32 s60,s18,s31\n" \
"s_cmp_lt_i32 s60,0\n" \
"s_cbranch_scc1 LCb_%=\n" \
"s_branch LCbd_%=\n" \
"LCc_%=:\n" \
"s_memrealtime s[18:19]\n" \
S_"lgkmcnt(0)\n" \
"s_sub_u32 s60,s18,s31\n" \
"s_cmp_lt_i32 s60,0\n" \
"s_cbranch_scc1 LCc_%=\n" \
"s_branch LCcd_%=\n" \
"LPS0_%=:\n" \
"s_sleep 1\n" \
"ds_read_b32 v248,v235 offset:0\n" \
S_"lgkmcnt(0)\n" \
"v_readfirstlane_b32 s63,v248\n" \
"s_nop 0\n" \
"s_cmp_ge_u32 s63,1\n" \
"s_cbranch_scc0 LPS0_%=\n" \
"s_branch LPS0d_%=\n" \
"LPS1_%=:\n" \
"s_sleep 1\n" \
"ds_read_b32 v248,v235 offset:0\n" \
S_"lgkmcnt(0)\n" \
"v_readfirstlane_b32 s63,v248\n" \
"s_nop 0\n" \
"s_cmp_ge_u32 s63,2\n" \
"s_cbranch_scc0 LPS1_%=\n" \
"s_branch LPS1d_%=\n" \
"LPS2_%=:\n" \
"s_sleep 1\n" \
"ds_read_b32 v248,v235 offset:0\n" \
S_"lgkmcnt(0)\n" \
"v_readfirstlane_b32 s63,v248\n" \
"s_nop 0\n" \
"s_cmp_ge_u32 s63,3\n" \
"s_cbranch_scc0 LPS2_%=\n" \
"s_branch LPS2d_%=\n" \
"LPS3_%=:\n" \
"s_sleep 1\n" \
"ds_read_b32 v248,v235 offset:0\n" \
S_"lgkmcnt(0)\n" \
"v_readfirstlane_b32 s63,v248\n" \
"s_nop 0\n" \
"s_cmp_ge_u32 s63,4\n" \
"s_cbranch_scc0 LPS3_%=\n" \
"s_branch LPS3d_%=\n" \
"LPS4_%=:\n" \
"s_sleep 1\n" \
"ds_read_b32 v248,v235 offset:0\n" \
S_"lgkmcnt(0)\n" \
"v_readfirstlane_b32 s63,v248\n" \
"s_nop 0\n" \
"s_cmp_ge_u32 s63,5\n" \
"s_cbranch_scc0 LPS4_%=\n" \
"s_branch LPS4d_%=\n" \
"LPS5_%=:\n" \
"s_sleep 1\n" \
"ds_read_b32 v248,v235 offset:0\n" \
S_"lgkmcnt(0)\n" \
"v_readfirstlane_b32 s63,v248\n" \
"s_nop 0\n" \
"s_cmp_ge_u32 s63,6\n" \
"s_cbranch_scc0 LPS5_%=\n" \
"s_branch LPS5d_%=\n" \
"LPS6_%=:\n" \
"s_sleep 1\n" \
"ds_read_b32 v248,v235 offset:0\n" \
S_"lgkmcnt(0)\n" \
"v_readfirstlane_b32 s63,v248\n" \
"s_nop 0\n" \
"s_cmp_ge_u32 s63,7\n" \
"s_cbranch_scc0 LPS6_%=\n" \
"s_branch LPS6d_%=\n" \
"LPS7_%=:\n" \
"s_sleep 1\n" \
"ds_read_b32 v248,v235 offset:0\n" \
S_"lgkmcnt(0)\n" \
"v_readfirstlane_b32 s63,v248\n" \
"s_nop 0\n" \
"s_cmp_ge_u32 s63,8\n" \
"s_cbranch_scc0 LPS7_%=\n" \
"s_branch LPS7d_%=\n" \
"LPS8_%=:\n" \
"s_sleep 1\n" \
"ds_read_b32 v248,v235 offset:0\n" \
S_"lgkmcnt(0)\n" \
"v_readfirstlane_b32 s63,v248\n" \
"s_nop 0\n" \
"s_cmp_ge_u32 s63,9\n" \
"s_cbranch_scc0 LPS8_%=\n" \
"s_branch LPS8d_%=\n" \
"; ---- redo\n" \
"LREDO_%=:\n" \
S_"vmcnt(0) lgkmcnt(0)\n" \
"s_mov_b32 s42,131200\n" \
"s_mov_b32 s46,131200\n" \
"s_mov_b32 s97,1\n" \
"s_lshl_b32 s66,s58,13\n" \
A_"s67,s66,4096\n" \
A_"m0,s83,0\n" \
"s_nop 0\n" \
L_"v228,s[40:43],s66 offen " KVP " lds\n" \
L_"v229,s[40:43],s66 offen offset:1056 " KVP " lds\n" \
L_"v230,s[40:43],s66 offen offset:2112 " KVP " lds\n" \
L_"v231,s[40:43],s66 offen offset:3168 " KVP " lds\n" \
A_"m0,s83,4224\n" \
"s_nop 0\n" \
L_"v228,s[40:43],s67 offen " KVP " lds\n" \
L_"v229,s[40:43],s67 offen offset:1056 " KVP " lds\n" \
L_"v230,s[40:43],s67 offen offset:2112 " KVP " lds\n" \
L_"v231,s[40:43],s67 offen offset:3168 " KVP " lds\n" \
A_"m0,s83,8704\n" \
"s_nop 0\n" \
L_"v228,s[44:47],s66 offen " KVP " lds\n" \
L_"v229,s[44:47],s66 offen offset:1056 " KVP " lds\n" \
L_"v230,s[44:47],s66 offen offset:2112 " KVP " lds\n" \
L_"v231,s[44:47],s66 offen offset:3168 " KVP " lds\n" \
A_"m0,s83,12928\n" \
"s_nop 0\n" \
L_"v228,s[44:47],s67 offen " KVP " lds\n" \
L_"v229,s[44:47],s67 offen offset:1056 " KVP " lds\n" \
L_"v230,s[44:47],s67 offen offset:2112 " KVP " lds\n" \
L_"v231,s[44:47],s67 offen offset:3168 " KVP " lds\n" \
A_"s66,s66,8192\n" \
"s_and_b32 s66,s66,131071\n" \
A_"s67,s66,4096\n" \
A_"m0,s83,17408\n" \
"s_nop 0\n" \
L_"v228,s[40:43],s66 offen " KVP " lds\n" \
L_"v229,s[40:43],s66 offen offset:1056 " KVP " lds\n" \
L_"v230,s[40:43],s66 offen offset:2112 " KVP " lds\n" \
L_"v231,s[40:43],s66 offen offset:3168 " KVP " lds\n" \
A_"m0,s83,21632\n" \
"s_nop 0\n" \
L_"v228,s[40:43],s67 offen " KVP " lds\n" \
L_"v229,s[40:43],s67 offen offset:1056 " KVP " lds\n" \
L_"v230,s[40:43],s67 offen offset:2112 " KVP " lds\n" \
L_"v231,s[40:43],s67 offen offset:3168 " KVP " lds\n" \
A_"m0,s83,26112\n" \
"s_nop 0\n" \
L_"v228,s[44:47],s66 offen " KVP " lds\n" \
L_"v229,s[44:47],s66 offen offset:1056 " KVP " lds\n" \
L_"v230,s[44:47],s66 offen offset:2112 " KVP " lds\n" \
L_"v231,s[44:47],s66 offen offset:3168 " KVP " lds\n" \
A_"m0,s83,30336\n" \
"s_nop 0\n" \
L_"v228,s[44:47],s67 offen " KVP " lds\n" \
L_"v229,s[44:47],s67 offen offset:1056 " KVP " lds\n" \
L_"v230,s[44:47],s67 offen offset:2112 " KVP " lds\n" \
L_"v231,s[44:47],s67 offen offset:3168 " KVP " lds\n" \
W_"a160,0\n" \
W_"a161,0\n" \
W_"a162,0\n" \
W_"a163,0\n" \
W_"a164,0\n" \
W_"a165,0\n" \
W_"a166,0\n" \
W_"a167,0\n" \
W_"a168,0\n" \
W_"a169,0\n" \
W_"a170,0\n" \
W_"a171,0\n" \
W_"a172,0\n" \
W_"a173,0\n" \
W_"a174,0\n" \
W_"a175,0\n" \
W_"a176,0\n" \
W_"a177,0\n" \
W_"a178,0\n" \
W_"a179,0\n" \
W_"a180,0\n" \
W_"a181,0\n" \
W_"a182,0\n" \
W_"a183,0\n" \
W_"a184,0\n" \
W_"a185,0\n" \
W_"a186,0\n" \
W_"a187,0\n" \
W_"a188,0\n" \
W_"a189,0\n" \
W_"a190,0\n" \
W_"a191,0\n" \
W_"a192,0\n" \
W_"a193,0\n" \
W_"a194,0\n" \
W_"a195,0\n" \
W_"a196,0\n" \
W_"a197,0\n" \
W_"a198,0\n" \
W_"a199,0\n" \
W_"a200,0\n" \
W_"a201,0\n" \
W_"a202,0\n" \
W_"a203,0\n" \
W_"a204,0\n" \
W_"a205,0\n" \
W_"a206,0\n" \
W_"a207,0\n" \
W_"a208,0\n" \
W_"a209,0\n" \
W_"a210,0\n" \
W_"a211,0\n" \
W_"a212,0\n" \
W_"a213,0\n" \
W_"a214,0\n" \
W_"a215,0\n" \
W_"a216,0\n" \
W_"a217,0\n" \
W_"a218,0\n" \
W_"a219,0\n" \
W_"a220,0\n" \
W_"a221,0\n" \
W_"a222,0\n" \
W_"a223,0\n" \
"s_branch LATT2_%=\n" \
"; ---- att\n" \
"LA_%=:\n" \
S_"lgkmcnt(0)\n" \
M_"v[152:155],a[224:227],v[0:3],v[168:171]\n" \
"v_max3_f32 v234,v144,v145,v146\n" \
"v_max3_f32 v235,v147,v148,v149\n" \
M_"v[152:155],a[228:231],v[4:7],v[152:155]\n" \
"v_max3_f32 v234,v234,v150,v151\n" \
"v_max_f32 v234,v234,v235\n" \
M_"v[152:155],a[232:235],v[8:11],v[152:155]\n" \
"v_cmp_lt_f32 vcc,s82,v234\n" \
A_"s66,s66,8192\n" \
M_"v[152:155],a[236:239],v[12:15],v[152:155]\n" \
"s_cbranch_vccz LK6a_%=\n" \
"s_call_b64 s[90:91],LR6p0_%=\n" \
"LK6a_%=:\n" \
"s_and_b32 s66,s66,131071\n" \
M_"v[156:159],a[240:243],v[0:3],v[168:171]\n" \
"v_exp_f32 v144,v144\n" \
M_"v[156:159],a[244:247],v[4:7],v[156:159]\n" \
"v_exp_f32 v145,v145\n" \
M_"v[156:159],a[248:251],v[8:11],v[156:159]\n" \
A_"m0,s83,17408\n" \
A_"s67,s66,4096\n" \
M_"v[156:159],a[252:255],v[12:15],v[156:159]\n" \
"v_exp_f32 v146,v146\n" \
M_"a[160:163],v[112:115],v[164:167],a[160:163]\n" \
"v_exp_f32 v147,v147\n" \
M_"a[164:167],v[116:119],v[164:167],a[164:167]\n" \
C_"v160,v144,v145\n" \
C_"v161,v146,v147\n" \
M_"a[168:171],v[120:123],v[164:167],a[168:171]\n" \
"v_exp_f32 v148,v148\n" \
M_"a[172:175],v[124:127],v[164:167],a[172:175]\n" \
"v_exp_f32 v149,v149\n" \
M_"a[176:179],v[128:131],v[164:167],a[176:179]\n" \
L_"v228,s[40:43],s66 offen " KVP " lds\n" \
L_"v229,s[40:43],s66 offen offset:1056 " KVP " lds\n" \
M_"a[180:183],v[132:135],v[164:167],a[180:183]\n" \
"v_exp_f32 v150,v150\n" \
M_"a[184:187],v[136:139],v[164:167],a[184:187]\n" \
"v_exp_f32 v151,v151\n" \
M_"a[188:191],v[140:143],v[164:167],a[188:191]\n" \
L_"v230,s[40:43],s66 offen offset:2112 " KVP " lds\n" \
L_"v231,s[40:43],s66 offen offset:3168 " KVP " lds\n" \
M_"v[216:219],v[224:227],v[164:167],v[216:219]\n" \
C_"v162,v148,v149\n" \
C_"v163,v150,v151\n" \
M_"v[144:147],a[224:227],v[16:19],v[172:175]\n" \
"v_max3_f32 v234,v152,v153,v154\n" \
"v_max3_f32 v235,v155,v156,v157\n" \
M_"v[144:147],a[228:231],v[20:23],v[144:147]\n" \
"v_max3_f32 v234,v234,v158,v159\n" \
"v_max_f32 v234,v234,v235\n" \
M_"v[144:147],a[232:235],v[24:27],v[144:147]\n" \
"v_cmp_lt_f32 vcc,s82,v234\n" \
M_"v[144:147],a[236:239],v[28:31],v[144:147]\n" \
"s_cbranch_vccz LK0a_%=\n" \
"s_call_b64 s[90:91],LR0p1_%=\n" \
"LK0a_%=:\n" \
M_"v[148:151],a[240:243],v[16:19],v[172:175]\n" \
"v_exp_f32 v152,v152\n" \
M_"v[148:151],a[244:247],v[20:23],v[148:151]\n" \
"v_exp_f32 v153,v153\n" \
M_"v[148:151],a[248:251],v[24:27],v[148:151]\n" \
"v_exp_f32 v154,v154\n" \
M_"v[148:151],a[252:255],v[28:31],v[148:151]\n" \
"v_exp_f32 v155,v155\n" \
M_"a[192:195],v[112:115],v[160:163],a[192:195]\n" \
C_"v164,v152,v153\n" \
C_"v165,v154,v155\n" \
M_"a[196:199],v[116:119],v[160:163],a[196:199]\n" \
S_"vmcnt(20)\n" \
T_"v[112:113],v233 offset:26112\n" \
M_"a[200:203],v[120:123],v[160:163],a[200:203]\n" \
"v_exp_f32 v156,v156\n" \
M_"a[204:207],v[124:127],v[160:163],a[204:207]\n" \
"v_exp_f32 v157,v157\n" \
T_"v[114:115],v233 offset:26624\n" \
M_"a[208:211],v[128:131],v[160:163],a[208:211]\n" \
T_"v[116:117],v233 offset:26144\n" \
T_"v[118:119],v233 offset:26656\n" \
M_"a[212:215],v[132:135],v[160:163],a[212:215]\n" \
"v_exp_f32 v158,v158\n" \
M_"a[216:219],v[136:139],v[160:163],a[216:219]\n" \
"v_exp_f32 v159,v159\n" \
T_"v[120:121],v233 offset:26176\n" \
M_"a[220:223],v[140:143],v[160:163],a[220:223]\n" \
T_"v[122:123],v233 offset:26688\n" \
T_"v[124:125],v233 offset:26208\n" \
M_"v[220:223],v[224:227],v[160:163],v[220:223]\n" \
C_"v166,v156,v157\n" \
C_"v167,v158,v159\n" \
T_"v[126:127],v233 offset:26720\n" \
M_"v[152:155],a[224:227],v[32:35],v[176:179]\n" \
"v_max3_f32 v234,v144,v145,v146\n" \
"v_max3_f32 v235,v147,v148,v149\n" \
M_"v[152:155],a[228:231],v[36:39],v[152:155]\n" \
"v_max3_f32 v234,v234,v150,v151\n" \
"v_max_f32 v234,v234,v235\n" \
M_"v[152:155],a[232:235],v[40:43],v[152:155]\n" \
"v_cmp_lt_f32 vcc,s82,v234\n" \
T_"v[128:129],v233 offset:26240\n" \
M_"v[152:155],a[236:239],v[44:47],v[152:155]\n" \
"s_cbranch_vccz LK1a_%=\n" \
"s_call_b64 s[90:91],LR1p0_%=\n" \
"LK1a_%=:\n" \
T_"v[130:131],v233 offset:26752\n" \
M_"v[156:159],a[240:243],v[32:35],v[176:179]\n" \
T_"v[132:133],v233 offset:26272\n" \
T_"v[134:135],v233 offset:26784\n" \
M_"v[156:159],a[244:247],v[36:39],v[156:159]\n" \
T_"v[136:137],v233 offset:26304\n" \
T_"v[138:139],v233 offset:26816\n" \
M_"v[156:159],a[248:251],v[40:43],v[156:159]\n" \
T_"v[140:141],v233 offset:26336\n" \
"v_exp_f32 v144,v144\n" \
T_"v[142:143],v233 offset:26848\n" \
M_"v[156:159],a[252:255],v[44:47],v[156:159]\n" \
"v_exp_f32 v145,v145\n" \
S_"lgkmcnt(8)\n" \
M_"a[0:3],v[112:115],v[164:167],a[0:3]\n" \
"v_exp_f32 v146,v146\n" \
M_"a[4:7],v[116:119],v[164:167],a[4:7]\n" \
"v_exp_f32 v147,v147\n" \
M_"a[8:11],v[120:123],v[164:167],a[8:11]\n" \
C_"v160,v144,v145\n" \
C_"v161,v146,v147\n" \
M_"a[12:15],v[124:127],v[164:167],a[12:15]\n" \
"v_exp_f32 v148,v148\n" \
S_"lgkmcnt(0)\n" \
M_"a[16:19],v[128:131],v[164:167],a[16:19]\n" \
"v_exp_f32 v149,v149\n" \
M_"a[20:23],v[132:135],v[164:167],a[20:23]\n" \
"v_exp_f32 v150,v150\n" \
M_"a[24:27],v[136:139],v[164:167],a[24:27]\n" \
"v_exp_f32 v151,v151\n" \
M_"a[28:31],v[140:143],v[164:167],a[28:31]\n" \
M_"v[196:199],v[224:227],v[164:167],v[196:199]\n" \
C_"v162,v148,v149\n" \
C_"v163,v150,v151\n" \
M_"v[144:147],a[224:227],v[48:51],v[180:183]\n" \
"v_max3_f32 v234,v152,v153,v154\n" \
"v_max3_f32 v235,v155,v156,v157\n" \
M_"v[144:147],a[228:231],v[52:55],v[144:147]\n" \
"v_max3_f32 v234,v234,v158,v159\n" \
"v_max_f32 v234,v234,v235\n" \
M_"v[144:147],a[232:235],v[56:59],v[144:147]\n" \
"v_cmp_lt_f32 vcc,s82,v234\n" \
A_"m0,s83,21632\n" \
M_"v[144:147],a[236:239],v[60:63],v[144:147]\n" \
"s_cbranch_vccz LK2a_%=\n" \
"s_call_b64 s[90:91],LR2p1_%=\n" \
"LK2a_%=:\n" \
L_"v228,s[40:43],s67 offen " KVP " lds\n" \
M_"v[148:151],a[240:243],v[48:51],v[180:183]\n" \
"v_exp_f32 v152,v152\n" \
M_"v[148:151],a[244:247],v[52:55],v[148:151]\n" \
"v_exp_f32 v153,v153\n" \
M_"v[148:151],a[248:251],v[56:59],v[148:151]\n" \
L_"v229,s[40:43],s67 offen offset:1056 " KVP " lds\n" \
L_"v230,s[40:43],s67 offen offset:2112 " KVP " lds\n" \
M_"v[148:151],a[252:255],v[60:63],v[148:151]\n" \
"v_exp_f32 v154,v154\n" \
M_"a[32:35],v[112:115],v[160:163],a[32:35]\n" \
"v_exp_f32 v155,v155\n" \
M_"a[36:39],v[116:119],v[160:163],a[36:39]\n" \
C_"v164,v152,v153\n" \
C_"v165,v154,v155\n" \
M_"a[40:43],v[120:123],v[160:163],a[40:43]\n" \
"v_exp_f32 v156,v156\n" \
M_"a[44:47],v[124:127],v[160:163],a[44:47]\n" \
"v_exp_f32 v157,v157\n" \
M_"a[48:51],v[128:131],v[160:163],a[48:51]\n" \
L_"v231,s[40:43],s67 offen offset:3168 " KVP " lds\n" \
M_"a[52:55],v[132:135],v[160:163],a[52:55]\n" \
"v_exp_f32 v158,v158\n" \
M_"a[56:59],v[136:139],v[160:163],a[56:59]\n" \
"v_exp_f32 v159,v159\n" \
M_"a[60:63],v[140:143],v[160:163],a[60:63]\n" \
M_"v[200:203],v[224:227],v[160:163],v[200:203]\n" \
C_"v166,v156,v157\n" \
C_"v167,v158,v159\n" \
M_"v[152:155],a[224:227],v[64:67],v[184:187]\n" \
"v_max3_f32 v234,v144,v145,v146\n" \
"v_max3_f32 v235,v147,v148,v149\n" \
M_"v[152:155],a[228:231],v[68:71],v[152:155]\n" \
"v_max3_f32 v234,v234,v150,v151\n" \
"v_max_f32 v234,v234,v235\n" \
M_"v[152:155],a[232:235],v[72:75],v[152:155]\n" \
"v_cmp_lt_f32 vcc,s82,v234\n" \
A_"m0,s83,26112\n" \
M_"v[152:155],a[236:239],v[76:79],v[152:155]\n" \
"s_cbranch_vccz LK3a_%=\n" \
"s_call_b64 s[90:91],LR3p0_%=\n" \
"LK3a_%=:\n" \
L_"v228,s[44:47],s66 offen " KVP " lds\n" \
M_"v[156:159],a[240:243],v[64:67],v[184:187]\n" \
"v_exp_f32 v144,v144\n" \
M_"v[156:159],a[244:247],v[68:71],v[156:159]\n" \
"v_exp_f32 v145,v145\n" \
M_"v[156:159],a[248:251],v[72:75],v[156:159]\n" \
L_"v229,s[44:47],s66 offen offset:1056 " KVP " lds\n" \
L_"v230,s[44:47],s66 offen offset:2112 " KVP " lds\n" \
M_"v[156:159],a[252:255],v[76:79],v[156:159]\n" \
"v_exp_f32 v146,v146\n" \
M_"a[64:67],v[112:115],v[164:167],a[64:67]\n" \
"v_exp_f32 v147,v147\n" \
M_"a[68:71],v[116:119],v[164:167],a[68:71]\n" \
C_"v160,v144,v145\n" \
C_"v161,v146,v147\n" \
M_"a[72:75],v[120:123],v[164:167],a[72:75]\n" \
"v_exp_f32 v148,v148\n" \
M_"a[76:79],v[124:127],v[164:167],a[76:79]\n" \
"v_exp_f32 v149,v149\n" \
M_"a[80:83],v[128:131],v[164:167],a[80:83]\n" \
L_"v231,s[44:47],s66 offen offset:3168 " KVP " lds\n" \
M_"a[84:87],v[132:135],v[164:167],a[84:87]\n" \
"v_exp_f32 v150,v150\n" \
M_"a[88:91],v[136:139],v[164:167],a[88:91]\n" \
"v_exp_f32 v151,v151\n" \
M_"a[92:95],v[140:143],v[164:167],a[92:95]\n" \
M_"v[204:207],v[224:227],v[164:167],v[204:207]\n" \
C_"v162,v148,v149\n" \
C_"v163,v150,v151\n" \
M_"v[144:147],a[224:227],v[80:83],v[188:191]\n" \
"v_max3_f32 v234,v152,v153,v154\n" \
"v_max3_f32 v235,v155,v156,v157\n" \
M_"v[144:147],a[228:231],v[84:87],v[144:147]\n" \
"v_max3_f32 v234,v234,v158,v159\n" \
"v_max_f32 v234,v234,v235\n" \
M_"v[144:147],a[232:235],v[88:91],v[144:147]\n" \
"v_cmp_lt_f32 vcc,s82,v234\n" \
A_"m0,s83,30336\n" \
M_"v[144:147],a[236:239],v[92:95],v[144:147]\n" \
"s_cbranch_vccz LK4a_%=\n" \
"s_call_b64 s[90:91],LR4p1_%=\n" \
"LK4a_%=:\n" \
L_"v228,s[44:47],s67 offen " KVP " lds\n" \
M_"v[148:151],a[240:243],v[80:83],v[188:191]\n" \
"v_exp_f32 v152,v152\n" \
M_"v[148:151],a[244:247],v[84:87],v[148:151]\n" \
"v_exp_f32 v153,v153\n" \
M_"v[148:151],a[248:251],v[88:91],v[148:151]\n" \
L_"v229,s[44:47],s67 offen offset:1056 " KVP " lds\n" \
L_"v230,s[44:47],s67 offen offset:2112 " KVP " lds\n" \
M_"v[148:151],a[252:255],v[92:95],v[148:151]\n" \
"v_exp_f32 v154,v154\n" \
M_"a[96:99],v[112:115],v[160:163],a[96:99]\n" \
"v_exp_f32 v155,v155\n" \
M_"a[100:103],v[116:119],v[160:163],a[100:103]\n" \
C_"v164,v152,v153\n" \
C_"v165,v154,v155\n" \
M_"a[104:107],v[120:123],v[160:163],a[104:107]\n" \
"v_exp_f32 v156,v156\n" \
M_"a[108:111],v[124:127],v[160:163],a[108:111]\n" \
"v_exp_f32 v157,v157\n" \
M_"a[112:115],v[128:131],v[160:163],a[112:115]\n" \
L_"v231,s[44:47],s67 offen offset:3168 " KVP " lds\n" \
M_"a[116:119],v[132:135],v[160:163],a[116:119]\n" \
"v_exp_f32 v158,v158\n" \
M_"a[120:123],v[136:139],v[160:163],a[120:123]\n" \
"v_exp_f32 v159,v159\n" \
M_"a[124:127],v[140:143],v[160:163],a[124:127]\n" \
M_"v[208:211],v[224:227],v[160:163],v[208:211]\n" \
C_"v166,v156,v157\n" \
C_"v167,v158,v159\n" \
M_"v[152:155],a[224:227],v[96:99],v[192:195]\n" \
"v_max3_f32 v234,v144,v145,v146\n" \
"v_max3_f32 v235,v147,v148,v149\n" \
M_"v[152:155],a[228:231],v[100:103],v[152:155]\n" \
"v_max3_f32 v234,v234,v150,v151\n" \
"v_max_f32 v234,v234,v235\n" \
M_"v[152:155],a[232:235],v[104:107],v[152:155]\n" \
"v_cmp_lt_f32 vcc,s82,v234\n" \
S_"vmcnt(24)\n" \
M_"v[152:155],a[236:239],v[108:111],v[152:155]\n" \
"s_cbranch_vccz LK5a_%=\n" \
"s_call_b64 s[90:91],LR5p0_%=\n" \
"LK5a_%=:\n" \
R_"a[224:227],v232 offset:0\n" \
M_"v[156:159],a[240:243],v[96:99],v[192:195]\n" \
R_"a[228:231],v232 offset:64\n" \
R_"a[232:235],v232 offset:128\n" \
M_"v[156:159],a[244:247],v[100:103],v[156:159]\n" \
"v_exp_f32 v144,v144\n" \
M_"v[156:159],a[248:251],v[104:107],v[156:159]\n" \
R_"a[236:239],v232 offset:192\n" \
R_"a[240:243],v232 offset:512\n" \
M_"v[156:159],a[252:255],v[108:111],v[156:159]\n" \
"v_exp_f32 v145,v145\n" \
M_"a[128:131],v[112:115],v[164:167],a[128:131]\n" \
"v_exp_f32 v146,v146\n" \
M_"a[132:135],v[116:119],v[164:167],a[132:135]\n" \
R_"a[244:247],v232 offset:576\n" \
R_"a[248:251],v232 offset:640\n" \
M_"a[136:139],v[120:123],v[164:167],a[136:139]\n" \
"v_exp_f32 v147,v147\n" \
M_"a[140:143],v[124:127],v[164:167],a[140:143]\n" \
C_"v160,v144,v145\n" \
C_"v161,v146,v147\n" \
R_"a[252:255],v232 offset:704\n" \
M_"a[144:147],v[128:131],v[164:167],a[144:147]\n" \
"v_exp_f32 v148,v148\n" \
M_"a[148:151],v[132:135],v[164:167],a[148:151]\n" \
"v_exp_f32 v149,v149\n" \
M_"a[152:155],v[136:139],v[164:167],a[152:155]\n" \
"v_exp_f32 v150,v150\n" \
M_"a[156:159],v[140:143],v[164:167],a[156:159]\n" \
"v_exp_f32 v151,v151\n" \
M_"v[212:215],v[224:227],v[164:167],v[212:215]\n" \
C_"v162,v148,v149\n" \
C_"v163,v150,v151\n" \
"s_cmp_eq_u32 s59,0\n" \
"s_cselect_b32 s42,0,s42\n" \
"s_cselect_b32 s46,0,s46\n" \
S_"lgkmcnt(0)\n" \
M_"v[144:147],a[224:227],v[0:3],v[168:171]\n" \
"v_max3_f32 v234,v152,v153,v154\n" \
"v_max3_f32 v235,v155,v156,v157\n" \
M_"v[144:147],a[228:231],v[4:7],v[144:147]\n" \
"v_max3_f32 v234,v234,v158,v159\n" \
"v_max_f32 v234,v234,v235\n" \
M_"v[144:147],a[232:235],v[8:11],v[144:147]\n" \
"v_cmp_lt_f32 vcc,s82,v234\n" \
A_"s66,s66,8192\n" \
M_"v[144:147],a[236:239],v[12:15],v[144:147]\n" \
"s_cbranch_vccz LK6b_%=\n" \
"s_call_b64 s[90:91],LR6p1_%=\n" \
"LK6b_%=:\n" \
"s_and_b32 s66,s66,131071\n" \
M_"v[148:151],a[240:243],v[0:3],v[168:171]\n" \
"v_exp_f32 v152,v152\n" \
M_"v[148:151],a[244:247],v[4:7],v[148:151]\n" \
"v_exp_f32 v153,v153\n" \
M_"v[148:151],a[248:251],v[8:11],v[148:151]\n" \
A_"m0,s83,0\n" \
A_"s67,s66,4096\n" \
M_"v[148:151],a[252:255],v[12:15],v[148:151]\n" \
"v_exp_f32 v154,v154\n" \
M_"a[160:163],v[112:115],v[160:163],a[160:163]\n" \
"v_exp_f32 v155,v155\n" \
M_"a[164:167],v[116:119],v[160:163],a[164:167]\n" \
C_"v164,v152,v153\n" \
C_"v165,v154,v155\n" \
M_"a[168:171],v[120:123],v[160:163],a[168:171]\n" \
"v_exp_f32 v156,v156\n" \
M_"a[172:175],v[124:127],v[160:163],a[172:175]\n" \
"v_exp_f32 v157,v157\n" \
M_"a[176:179],v[128:131],v[160:163],a[176:179]\n" \
L_"v228,s[40:43],s66 offen " KVP " lds\n" \
L_"v229,s[40:43],s66 offen offset:1056 " KVP " lds\n" \
M_"a[180:183],v[132:135],v[160:163],a[180:183]\n" \
"v_exp_f32 v158,v158\n" \
M_"a[184:187],v[136:139],v[160:163],a[184:187]\n" \
"v_exp_f32 v159,v159\n" \
M_"a[188:191],v[140:143],v[160:163],a[188:191]\n" \
L_"v230,s[40:43],s66 offen offset:2112 " KVP " lds\n" \
L_"v231,s[40:43],s66 offen offset:3168 " KVP " lds\n" \
M_"v[216:219],v[224:227],v[160:163],v[216:219]\n" \
C_"v166,v156,v157\n" \
C_"v167,v158,v159\n" \
M_"v[152:155],a[224:227],v[16:19],v[172:175]\n" \
"v_max3_f32 v234,v144,v145,v146\n" \
"v_max3_f32 v235,v147,v148,v149\n" \
M_"v[152:155],a[228:231],v[20:23],v[152:155]\n" \
"v_max3_f32 v234,v234,v150,v151\n" \
"v_max_f32 v234,v234,v235\n" \
M_"v[152:155],a[232:235],v[24:27],v[152:155]\n" \
"v_cmp_lt_f32 vcc,s82,v234\n" \
M_"v[152:155],a[236:239],v[28:31],v[152:155]\n" \
"s_cbranch_vccz LK0b_%=\n" \
"s_call_b64 s[90:91],LR0p0_%=\n" \
"LK0b_%=:\n" \
M_"v[156:159],a[240:243],v[16:19],v[172:175]\n" \
"v_exp_f32 v144,v144\n" \
M_"v[156:159],a[244:247],v[20:23],v[156:159]\n" \
"v_exp_f32 v145,v145\n" \
M_"v[156:159],a[248:251],v[24:27],v[156:159]\n" \
"v_exp_f32 v146,v146\n" \
M_"v[156:159],a[252:255],v[28:31],v[156:159]\n" \
"v_exp_f32 v147,v147\n" \
M_"a[192:195],v[112:115],v[164:167],a[192:195]\n" \
C_"v160,v144,v145\n" \
C_"v161,v146,v147\n" \
M_"a[196:199],v[116:119],v[164:167],a[196:199]\n" \
S_"vmcnt(20)\n" \
T_"v[112:113],v233 offset:8704\n" \
M_"a[200:203],v[120:123],v[164:167],a[200:203]\n" \
"v_exp_f32 v148,v148\n" \
M_"a[204:207],v[124:127],v[164:167],a[204:207]\n" \
"v_exp_f32 v149,v149\n" \
T_"v[114:115],v233 offset:9216\n" \
M_"a[208:211],v[128:131],v[164:167],a[208:211]\n" \
T_"v[116:117],v233 offset:8736\n" \
T_"v[118:119],v233 offset:9248\n" \
M_"a[212:215],v[132:135],v[164:167],a[212:215]\n" \
"v_exp_f32 v150,v150\n" \
M_"a[216:219],v[136:139],v[164:167],a[216:219]\n" \
"v_exp_f32 v151,v151\n" \
T_"v[120:121],v233 offset:8768\n" \
M_"a[220:223],v[140:143],v[164:167],a[220:223]\n" \
T_"v[122:123],v233 offset:9280\n" \
T_"v[124:125],v233 offset:8800\n" \
M_"v[220:223],v[224:227],v[164:167],v[220:223]\n" \
C_"v162,v148,v149\n" \
C_"v163,v150,v151\n" \
T_"v[126:127],v233 offset:9312\n" \
M_"v[144:147],a[224:227],v[32:35],v[176:179]\n" \
"v_max3_f32 v234,v152,v153,v154\n" \
"v_max3_f32 v235,v155,v156,v157\n" \
M_"v[144:147],a[228:231],v[36:39],v[144:147]\n" \
"v_max3_f32 v234,v234,v158,v159\n" \
"v_max_f32 v234,v234,v235\n" \
M_"v[144:147],a[232:235],v[40:43],v[144:147]\n" \
"v_cmp_lt_f32 vcc,s82,v234\n" \
T_"v[128:129],v233 offset:8832\n" \
M_"v[144:147],a[236:239],v[44:47],v[144:147]\n" \
"s_cbranch_vccz LK1b_%=\n" \
"s_call_b64 s[90:91],LR1p1_%=\n" \
"LK1b_%=:\n" \
T_"v[130:131],v233 offset:9344\n" \
M_"v[148:151],a[240:243],v[32:35],v[176:179]\n" \
T_"v[132:133],v233 offset:8864\n" \
T_"v[134:135],v233 offset:9376\n" \
M_"v[148:151],a[244:247],v[36:39],v[148:151]\n" \
T_"v[136:137],v233 offset:8896\n" \
T_"v[138:139],v233 offset:9408\n" \
M_"v[148:151],a[248:251],v[40:43],v[148:151]\n" \
T_"v[140:141],v233 offset:8928\n" \
"v_exp_f32 v152,v152\n" \
T_"v[142:143],v233 offset:9440\n" \
M_"v[148:151],a[252:255],v[44:47],v[148:151]\n" \
"v_exp_f32 v153,v153\n" \
S_"lgkmcnt(8)\n" \
M_"a[0:3],v[112:115],v[160:163],a[0:3]\n" \
"v_exp_f32 v154,v154\n" \
M_"a[4:7],v[116:119],v[160:163],a[4:7]\n" \
"v_exp_f32 v155,v155\n" \
M_"a[8:11],v[120:123],v[160:163],a[8:11]\n" \
C_"v164,v152,v153\n" \
C_"v165,v154,v155\n" \
M_"a[12:15],v[124:127],v[160:163],a[12:15]\n" \
"v_exp_f32 v156,v156\n" \
S_"lgkmcnt(0)\n" \
M_"a[16:19],v[128:131],v[160:163],a[16:19]\n" \
"v_exp_f32 v157,v157\n" \
M_"a[20:23],v[132:135],v[160:163],a[20:23]\n" \
"v_exp_f32 v158,v158\n" \
M_"a[24:27],v[136:139],v[160:163],a[24:27]\n" \
"v_exp_f32 v159,v159\n" \
M_"a[28:31],v[140:143],v[160:163],a[28:31]\n" \
M_"v[196:199],v[224:227],v[160:163],v[196:199]\n" \
C_"v166,v156,v157\n" \
C_"v167,v158,v159\n" \
M_"v[152:155],a[224:227],v[48:51],v[180:183]\n" \
"v_max3_f32 v234,v144,v145,v146\n" \
"v_max3_f32 v235,v147,v148,v149\n" \
M_"v[152:155],a[228:231],v[52:55],v[152:155]\n" \
"v_max3_f32 v234,v234,v150,v151\n" \
"v_max_f32 v234,v234,v235\n" \
M_"v[152:155],a[232:235],v[56:59],v[152:155]\n" \
"v_cmp_lt_f32 vcc,s82,v234\n" \
A_"m0,s83,4224\n" \
M_"v[152:155],a[236:239],v[60:63],v[152:155]\n" \
"s_cbranch_vccz LK2b_%=\n" \
"s_call_b64 s[90:91],LR2p0_%=\n" \
"LK2b_%=:\n" \
L_"v228,s[40:43],s67 offen " KVP " lds\n" \
M_"v[156:159],a[240:243],v[48:51],v[180:183]\n" \
"v_exp_f32 v144,v144\n" \
M_"v[156:159],a[244:247],v[52:55],v[156:159]\n" \
"v_exp_f32 v145,v145\n" \
M_"v[156:159],a[248:251],v[56:59],v[156:159]\n" \
L_"v229,s[40:43],s67 offen offset:1056 " KVP " lds\n" \
L_"v230,s[40:43],s67 offen offset:2112 " KVP " lds\n" \
M_"v[156:159],a[252:255],v[60:63],v[156:159]\n" \
"v_exp_f32 v146,v146\n" \
M_"a[32:35],v[112:115],v[164:167],a[32:35]\n" \
"v_exp_f32 v147,v147\n" \
M_"a[36:39],v[116:119],v[164:167],a[36:39]\n" \
C_"v160,v144,v145\n" \
C_"v161,v146,v147\n" \
M_"a[40:43],v[120:123],v[164:167],a[40:43]\n" \
"v_exp_f32 v148,v148\n" \
M_"a[44:47],v[124:127],v[164:167],a[44:47]\n" \
"v_exp_f32 v149,v149\n" \
M_"a[48:51],v[128:131],v[164:167],a[48:51]\n" \
L_"v231,s[40:43],s67 offen offset:3168 " KVP " lds\n" \
M_"a[52:55],v[132:135],v[164:167],a[52:55]\n" \
"v_exp_f32 v150,v150\n" \
M_"a[56:59],v[136:139],v[164:167],a[56:59]\n" \
"v_exp_f32 v151,v151\n" \
M_"a[60:63],v[140:143],v[164:167],a[60:63]\n" \
M_"v[200:203],v[224:227],v[164:167],v[200:203]\n" \
C_"v162,v148,v149\n" \
C_"v163,v150,v151\n" \
M_"v[144:147],a[224:227],v[64:67],v[184:187]\n" \
"v_max3_f32 v234,v152,v153,v154\n" \
"v_max3_f32 v235,v155,v156,v157\n" \
M_"v[144:147],a[228:231],v[68:71],v[144:147]\n" \
"v_max3_f32 v234,v234,v158,v159\n" \
"v_max_f32 v234,v234,v235\n" \
M_"v[144:147],a[232:235],v[72:75],v[144:147]\n" \
"v_cmp_lt_f32 vcc,s82,v234\n" \
A_"m0,s83,8704\n" \
M_"v[144:147],a[236:239],v[76:79],v[144:147]\n" \
"s_cbranch_vccz LK3b_%=\n" \
"s_call_b64 s[90:91],LR3p1_%=\n" \
"LK3b_%=:\n" \
L_"v228,s[44:47],s66 offen " KVP " lds\n" \
M_"v[148:151],a[240:243],v[64:67],v[184:187]\n" \
"v_exp_f32 v152,v152\n" \
M_"v[148:151],a[244:247],v[68:71],v[148:151]\n" \
"v_exp_f32 v153,v153\n" \
M_"v[148:151],a[248:251],v[72:75],v[148:151]\n" \
L_"v229,s[44:47],s66 offen offset:1056 " KVP " lds\n" \
L_"v230,s[44:47],s66 offen offset:2112 " KVP " lds\n" \
M_"v[148:151],a[252:255],v[76:79],v[148:151]\n" \
"v_exp_f32 v154,v154\n" \
M_"a[64:67],v[112:115],v[160:163],a[64:67]\n" \
"v_exp_f32 v155,v155\n" \
M_"a[68:71],v[116:119],v[160:163],a[68:71]\n" \
C_"v164,v152,v153\n" \
C_"v165,v154,v155\n" \
M_"a[72:75],v[120:123],v[160:163],a[72:75]\n" \
"v_exp_f32 v156,v156\n" \
M_"a[76:79],v[124:127],v[160:163],a[76:79]\n" \
"v_exp_f32 v157,v157\n" \
M_"a[80:83],v[128:131],v[160:163],a[80:83]\n" \
L_"v231,s[44:47],s66 offen offset:3168 " KVP " lds\n" \
M_"a[84:87],v[132:135],v[160:163],a[84:87]\n" \
"v_exp_f32 v158,v158\n" \
M_"a[88:91],v[136:139],v[160:163],a[88:91]\n" \
"v_exp_f32 v159,v159\n" \
M_"a[92:95],v[140:143],v[160:163],a[92:95]\n" \
M_"v[204:207],v[224:227],v[160:163],v[204:207]\n" \
C_"v166,v156,v157\n" \
C_"v167,v158,v159\n" \
M_"v[152:155],a[224:227],v[80:83],v[188:191]\n" \
"v_max3_f32 v234,v144,v145,v146\n" \
"v_max3_f32 v235,v147,v148,v149\n" \
M_"v[152:155],a[228:231],v[84:87],v[152:155]\n" \
"v_max3_f32 v234,v234,v150,v151\n" \
"v_max_f32 v234,v234,v235\n" \
M_"v[152:155],a[232:235],v[88:91],v[152:155]\n" \
"v_cmp_lt_f32 vcc,s82,v234\n" \
A_"m0,s83,12928\n" \
M_"v[152:155],a[236:239],v[92:95],v[152:155]\n" \
"s_cbranch_vccz LK4b_%=\n" \
"s_call_b64 s[90:91],LR4p0_%=\n" \
"LK4b_%=:\n" \
L_"v228,s[44:47],s67 offen " KVP " lds\n" \
M_"v[156:159],a[240:243],v[80:83],v[188:191]\n" \
"v_exp_f32 v144,v144\n" \
M_"v[156:159],a[244:247],v[84:87],v[156:159]\n" \
"v_exp_f32 v145,v145\n" \
M_"v[156:159],a[248:251],v[88:91],v[156:159]\n" \
L_"v229,s[44:47],s67 offen offset:1056 " KVP " lds\n" \
L_"v230,s[44:47],s67 offen offset:2112 " KVP " lds\n" \
M_"v[156:159],a[252:255],v[92:95],v[156:159]\n" \
"v_exp_f32 v146,v146\n" \
M_"a[96:99],v[112:115],v[164:167],a[96:99]\n" \
"v_exp_f32 v147,v147\n" \
M_"a[100:103],v[116:119],v[164:167],a[100:103]\n" \
C_"v160,v144,v145\n" \
C_"v161,v146,v147\n" \
M_"a[104:107],v[120:123],v[164:167],a[104:107]\n" \
"v_exp_f32 v148,v148\n" \
M_"a[108:111],v[124:127],v[164:167],a[108:111]\n" \
"v_exp_f32 v149,v149\n" \
M_"a[112:115],v[128:131],v[164:167],a[112:115]\n" \
L_"v231,s[44:47],s67 offen offset:3168 " KVP " lds\n" \
M_"a[116:119],v[132:135],v[164:167],a[116:119]\n" \
"v_exp_f32 v150,v150\n" \
M_"a[120:123],v[136:139],v[164:167],a[120:123]\n" \
"v_exp_f32 v151,v151\n" \
M_"a[124:127],v[140:143],v[164:167],a[124:127]\n" \
M_"v[208:211],v[224:227],v[164:167],v[208:211]\n" \
C_"v162,v148,v149\n" \
C_"v163,v150,v151\n" \
M_"v[144:147],a[224:227],v[96:99],v[192:195]\n" \
"v_max3_f32 v234,v152,v153,v154\n" \
"v_max3_f32 v235,v155,v156,v157\n" \
M_"v[144:147],a[228:231],v[100:103],v[144:147]\n" \
"v_max3_f32 v234,v234,v158,v159\n" \
"v_max_f32 v234,v234,v235\n" \
M_"v[144:147],a[232:235],v[104:107],v[144:147]\n" \
"v_cmp_lt_f32 vcc,s82,v234\n" \
S_"vmcnt(24)\n" \
M_"v[144:147],a[236:239],v[108:111],v[144:147]\n" \
"s_cbranch_vccz LK5b_%=\n" \
"s_call_b64 s[90:91],LR5p1_%=\n" \
"LK5b_%=:\n" \
R_"a[224:227],v232 offset:17408\n" \
M_"v[148:151],a[240:243],v[96:99],v[192:195]\n" \
R_"a[228:231],v232 offset:17472\n" \
R_"a[232:235],v232 offset:17536\n" \
M_"v[148:151],a[244:247],v[100:103],v[148:151]\n" \
"v_exp_f32 v152,v152\n" \
M_"v[148:151],a[248:251],v[104:107],v[148:151]\n" \
R_"a[236:239],v232 offset:17600\n" \
R_"a[240:243],v232 offset:17920\n" \
M_"v[148:151],a[252:255],v[108:111],v[148:151]\n" \
"v_exp_f32 v153,v153\n" \
M_"a[128:131],v[112:115],v[160:163],a[128:131]\n" \
"v_exp_f32 v154,v154\n" \
M_"a[132:135],v[116:119],v[160:163],a[132:135]\n" \
R_"a[244:247],v232 offset:17984\n" \
R_"a[248:251],v232 offset:18048\n" \
M_"a[136:139],v[120:123],v[160:163],a[136:139]\n" \
"v_exp_f32 v155,v155\n" \
M_"a[140:143],v[124:127],v[160:163],a[140:143]\n" \
C_"v164,v152,v153\n" \
C_"v165,v154,v155\n" \
R_"a[252:255],v232 offset:18112\n" \
M_"a[144:147],v[128:131],v[160:163],a[144:147]\n" \
"v_exp_f32 v156,v156\n" \
M_"a[148:151],v[132:135],v[160:163],a[148:151]\n" \
"v_exp_f32 v157,v157\n" \
M_"a[152:155],v[136:139],v[160:163],a[152:155]\n" \
"v_exp_f32 v158,v158\n" \
M_"a[156:159],v[140:143],v[160:163],a[156:159]\n" \
"v_exp_f32 v159,v159\n" \
M_"v[212:215],v[224:227],v[160:163],v[212:215]\n" \
C_"v166,v156,v157\n" \
C_"v167,v158,v159\n" \
"s_sub_u32 s59,s59,1\n" \
"s_cbranch_scc0 LA_%=\n" \
"; ---- att15\n" \
S_"lgkmcnt(0)\n" \
M_"v[152:155],a[224:227],v[0:3],v[168:171]\n" \
"v_max3_f32 v234,v144,v145,v146\n" \
"v_max3_f32 v235,v147,v148,v149\n" \
M_"v[152:155],a[228:231],v[4:7],v[152:155]\n" \
"v_max3_f32 v234,v234,v150,v151\n" \
"v_max_f32 v234,v234,v235\n" \
M_"v[152:155],a[232:235],v[8:11],v[152:155]\n" \
"v_cmp_lt_f32 vcc,s82,v234\n" \
M_"v[152:155],a[236:239],v[12:15],v[152:155]\n" \
"s_cbranch_vccz LK6c_%=\n" \
"s_call_b64 s[90:91],LR6p0_%=\n" \
"LK6c_%=:\n" \
M_"v[156:159],a[240:243],v[0:3],v[168:171]\n" \
"v_exp_f32 v144,v144\n" \
M_"v[156:159],a[244:247],v[4:7],v[156:159]\n" \
"v_exp_f32 v145,v145\n" \
M_"v[156:159],a[248:251],v[8:11],v[156:159]\n" \
M_"v[156:159],a[252:255],v[12:15],v[156:159]\n" \
"v_exp_f32 v146,v146\n" \
M_"a[160:163],v[112:115],v[164:167],a[160:163]\n" \
"v_exp_f32 v147,v147\n" \
M_"a[164:167],v[116:119],v[164:167],a[164:167]\n" \
C_"v160,v144,v145\n" \
C_"v161,v146,v147\n" \
M_"a[168:171],v[120:123],v[164:167],a[168:171]\n" \
"v_exp_f32 v148,v148\n" \
M_"a[172:175],v[124:127],v[164:167],a[172:175]\n" \
"v_exp_f32 v149,v149\n" \
M_"a[176:179],v[128:131],v[164:167],a[176:179]\n" \
M_"a[180:183],v[132:135],v[164:167],a[180:183]\n" \
"v_exp_f32 v150,v150\n" \
M_"a[184:187],v[136:139],v[164:167],a[184:187]\n" \
"v_exp_f32 v151,v151\n" \
M_"a[188:191],v[140:143],v[164:167],a[188:191]\n" \
M_"v[216:219],v[224:227],v[164:167],v[216:219]\n" \
C_"v162,v148,v149\n" \
C_"v163,v150,v151\n" \
M_"v[144:147],a[224:227],v[16:19],v[172:175]\n" \
"v_max3_f32 v234,v152,v153,v154\n" \
"v_max3_f32 v235,v155,v156,v157\n" \
M_"v[144:147],a[228:231],v[20:23],v[144:147]\n" \
"v_max3_f32 v234,v234,v158,v159\n" \
"v_max_f32 v234,v234,v235\n" \
M_"v[144:147],a[232:235],v[24:27],v[144:147]\n" \
"v_cmp_lt_f32 vcc,s82,v234\n" \
M_"v[144:147],a[236:239],v[28:31],v[144:147]\n" \
"s_cbranch_vccz LK0c_%=\n" \
"s_call_b64 s[90:91],LR0p1_%=\n" \
"LK0c_%=:\n" \
M_"v[148:151],a[240:243],v[16:19],v[172:175]\n" \
"v_exp_f32 v152,v152\n" \
M_"v[148:151],a[244:247],v[20:23],v[148:151]\n" \
"v_exp_f32 v153,v153\n" \
M_"v[148:151],a[248:251],v[24:27],v[148:151]\n" \
"v_exp_f32 v154,v154\n" \
M_"v[148:151],a[252:255],v[28:31],v[148:151]\n" \
"v_exp_f32 v155,v155\n" \
M_"a[192:195],v[112:115],v[160:163],a[192:195]\n" \
C_"v164,v152,v153\n" \
C_"v165,v154,v155\n" \
M_"a[196:199],v[116:119],v[160:163],a[196:199]\n" \
S_"vmcnt(16)\n" \
T_"v[112:113],v233 offset:26112\n" \
M_"a[200:203],v[120:123],v[160:163],a[200:203]\n" \
"v_exp_f32 v156,v156\n" \
M_"a[204:207],v[124:127],v[160:163],a[204:207]\n" \
"v_exp_f32 v157,v157\n" \
T_"v[114:115],v233 offset:26624\n" \
M_"a[208:211],v[128:131],v[160:163],a[208:211]\n" \
T_"v[116:117],v233 offset:26144\n" \
T_"v[118:119],v233 offset:26656\n" \
M_"a[212:215],v[132:135],v[160:163],a[212:215]\n" \
"v_exp_f32 v158,v158\n" \
M_"a[216:219],v[136:139],v[160:163],a[216:219]\n" \
"v_exp_f32 v159,v159\n" \
T_"v[120:121],v233 offset:26176\n" \
M_"a[220:223],v[140:143],v[160:163],a[220:223]\n" \
T_"v[122:123],v233 offset:26688\n" \
T_"v[124:125],v233 offset:26208\n" \
M_"v[220:223],v[224:227],v[160:163],v[220:223]\n" \
C_"v166,v156,v157\n" \
C_"v167,v158,v159\n" \
T_"v[126:127],v233 offset:26720\n" \
M_"v[152:155],a[224:227],v[32:35],v[176:179]\n" \
"v_max3_f32 v234,v144,v145,v146\n" \
"v_max3_f32 v235,v147,v148,v149\n" \
M_"v[152:155],a[228:231],v[36:39],v[152:155]\n" \
"v_max3_f32 v234,v234,v150,v151\n" \
"v_max_f32 v234,v234,v235\n" \
M_"v[152:155],a[232:235],v[40:43],v[152:155]\n" \
"v_cmp_lt_f32 vcc,s82,v234\n" \
T_"v[128:129],v233 offset:26240\n" \
M_"v[152:155],a[236:239],v[44:47],v[152:155]\n" \
"s_cbranch_vccz LK1c_%=\n" \
"s_call_b64 s[90:91],LR1p0_%=\n" \
"LK1c_%=:\n" \
T_"v[130:131],v233 offset:26752\n" \
M_"v[156:159],a[240:243],v[32:35],v[176:179]\n" \
T_"v[132:133],v233 offset:26272\n" \
T_"v[134:135],v233 offset:26784\n" \
M_"v[156:159],a[244:247],v[36:39],v[156:159]\n" \
T_"v[136:137],v233 offset:26304\n" \
T_"v[138:139],v233 offset:26816\n" \
M_"v[156:159],a[248:251],v[40:43],v[156:159]\n" \
T_"v[140:141],v233 offset:26336\n" \
"v_exp_f32 v144,v144\n" \
T_"v[142:143],v233 offset:26848\n" \
M_"v[156:159],a[252:255],v[44:47],v[156:159]\n" \
"v_exp_f32 v145,v145\n" \
S_"lgkmcnt(8)\n" \
M_"a[0:3],v[112:115],v[164:167],a[0:3]\n" \
"v_exp_f32 v146,v146\n" \
M_"a[4:7],v[116:119],v[164:167],a[4:7]\n" \
"v_exp_f32 v147,v147\n" \
M_"a[8:11],v[120:123],v[164:167],a[8:11]\n" \
C_"v160,v144,v145\n" \
C_"v161,v146,v147\n" \
M_"a[12:15],v[124:127],v[164:167],a[12:15]\n" \
"v_exp_f32 v148,v148\n" \
S_"lgkmcnt(0)\n" \
M_"a[16:19],v[128:131],v[164:167],a[16:19]\n" \
"v_exp_f32 v149,v149\n" \
M_"a[20:23],v[132:135],v[164:167],a[20:23]\n" \
"v_exp_f32 v150,v150\n" \
M_"a[24:27],v[136:139],v[164:167],a[24:27]\n" \
"v_exp_f32 v151,v151\n" \
M_"a[28:31],v[140:143],v[164:167],a[28:31]\n" \
M_"v[196:199],v[224:227],v[164:167],v[196:199]\n" \
C_"v162,v148,v149\n" \
C_"v163,v150,v151\n" \
M_"v[144:147],a[224:227],v[48:51],v[180:183]\n" \
"v_max3_f32 v234,v152,v153,v154\n" \
"v_max3_f32 v235,v155,v156,v157\n" \
M_"v[144:147],a[228:231],v[52:55],v[144:147]\n" \
"v_max3_f32 v234,v234,v158,v159\n" \
"v_max_f32 v234,v234,v235\n" \
M_"v[144:147],a[232:235],v[56:59],v[144:147]\n" \
"v_cmp_lt_f32 vcc,s82,v234\n" \
M_"v[144:147],a[236:239],v[60:63],v[144:147]\n" \
"s_cbranch_vccz LK2c_%=\n" \
"s_call_b64 s[90:91],LR2p1_%=\n" \
"LK2c_%=:\n" \
M_"v[148:151],a[240:243],v[48:51],v[180:183]\n" \
"v_exp_f32 v152,v152\n" \
M_"v[148:151],a[244:247],v[52:55],v[148:151]\n" \
"v_exp_f32 v153,v153\n" \
M_"v[148:151],a[248:251],v[56:59],v[148:151]\n" \
M_"v[148:151],a[252:255],v[60:63],v[148:151]\n" \
"v_exp_f32 v154,v154\n" \
M_"a[32:35],v[112:115],v[160:163],a[32:35]\n" \
"v_exp_f32 v155,v155\n" \
M_"a[36:39],v[116:119],v[160:163],a[36:39]\n" \
C_"v164,v152,v153\n" \
C_"v165,v154,v155\n" \
M_"a[40:43],v[120:123],v[160:163],a[40:43]\n" \
"v_exp_f32 v156,v156\n" \
M_"a[44:47],v[124:127],v[160:163],a[44:47]\n" \
"v_exp_f32 v157,v157\n" \
M_"a[48:51],v[128:131],v[160:163],a[48:51]\n" \
M_"a[52:55],v[132:135],v[160:163],a[52:55]\n" \
"v_exp_f32 v158,v158\n" \
M_"a[56:59],v[136:139],v[160:163],a[56:59]\n" \
"v_exp_f32 v159,v159\n" \
M_"a[60:63],v[140:143],v[160:163],a[60:63]\n" \
M_"v[200:203],v[224:227],v[160:163],v[200:203]\n" \
C_"v166,v156,v157\n" \
C_"v167,v158,v159\n" \
M_"v[152:155],a[224:227],v[64:67],v[184:187]\n" \
"v_max3_f32 v234,v144,v145,v146\n" \
"v_max3_f32 v235,v147,v148,v149\n" \
M_"v[152:155],a[228:231],v[68:71],v[152:155]\n" \
"v_max3_f32 v234,v234,v150,v151\n" \
"v_max_f32 v234,v234,v235\n" \
M_"v[152:155],a[232:235],v[72:75],v[152:155]\n" \
"v_cmp_lt_f32 vcc,s82,v234\n" \
M_"v[152:155],a[236:239],v[76:79],v[152:155]\n" \
"s_cbranch_vccz LK3c_%=\n" \
"s_call_b64 s[90:91],LR3p0_%=\n" \
"LK3c_%=:\n" \
M_"v[156:159],a[240:243],v[64:67],v[184:187]\n" \
"v_exp_f32 v144,v144\n" \
M_"v[156:159],a[244:247],v[68:71],v[156:159]\n" \
"v_exp_f32 v145,v145\n" \
M_"v[156:159],a[248:251],v[72:75],v[156:159]\n" \
M_"v[156:159],a[252:255],v[76:79],v[156:159]\n" \
"v_exp_f32 v146,v146\n" \
M_"a[64:67],v[112:115],v[164:167],a[64:67]\n" \
"v_exp_f32 v147,v147\n" \
M_"a[68:71],v[116:119],v[164:167],a[68:71]\n" \
C_"v160,v144,v145\n" \
C_"v161,v146,v147\n" \
M_"a[72:75],v[120:123],v[164:167],a[72:75]\n" \
"v_exp_f32 v148,v148\n" \
M_"a[76:79],v[124:127],v[164:167],a[76:79]\n" \
"v_exp_f32 v149,v149\n" \
M_"a[80:83],v[128:131],v[164:167],a[80:83]\n" \
M_"a[84:87],v[132:135],v[164:167],a[84:87]\n" \
"v_exp_f32 v150,v150\n" \
M_"a[88:91],v[136:139],v[164:167],a[88:91]\n" \
"v_exp_f32 v151,v151\n" \
M_"a[92:95],v[140:143],v[164:167],a[92:95]\n" \
M_"v[204:207],v[224:227],v[164:167],v[204:207]\n" \
C_"v162,v148,v149\n" \
C_"v163,v150,v151\n" \
M_"v[144:147],a[224:227],v[80:83],v[188:191]\n" \
"v_max3_f32 v234,v152,v153,v154\n" \
"v_max3_f32 v235,v155,v156,v157\n" \
M_"v[144:147],a[228:231],v[84:87],v[144:147]\n" \
"v_max3_f32 v234,v234,v158,v159\n" \
"v_max_f32 v234,v234,v235\n" \
M_"v[144:147],a[232:235],v[88:91],v[144:147]\n" \
"v_cmp_lt_f32 vcc,s82,v234\n" \
M_"v[144:147],a[236:239],v[92:95],v[144:147]\n" \
"s_cbranch_vccz LK4c_%=\n" \
"s_call_b64 s[90:91],LR4p1_%=\n" \
"LK4c_%=:\n" \
M_"v[148:151],a[240:243],v[80:83],v[188:191]\n" \
"v_exp_f32 v152,v152\n" \
M_"v[148:151],a[244:247],v[84:87],v[148:151]\n" \
"v_exp_f32 v153,v153\n" \
M_"v[148:151],a[248:251],v[88:91],v[148:151]\n" \
M_"v[148:151],a[252:255],v[92:95],v[148:151]\n" \
"v_exp_f32 v154,v154\n" \
M_"a[96:99],v[112:115],v[160:163],a[96:99]\n" \
"v_exp_f32 v155,v155\n" \
M_"a[100:103],v[116:119],v[160:163],a[100:103]\n" \
C_"v164,v152,v153\n" \
C_"v165,v154,v155\n" \
M_"a[104:107],v[120:123],v[160:163],a[104:107]\n" \
"v_exp_f32 v156,v156\n" \
M_"a[108:111],v[124:127],v[160:163],a[108:111]\n" \
"v_exp_f32 v157,v157\n" \
M_"a[112:115],v[128:131],v[160:163],a[112:115]\n" \
M_"a[116:119],v[132:135],v[160:163],a[116:119]\n" \
"v_exp_f32 v158,v158\n" \
M_"a[120:123],v[136:139],v[160:163],a[120:123]\n" \
"v_exp_f32 v159,v159\n" \
M_"a[124:127],v[140:143],v[160:163],a[124:127]\n" \
M_"v[208:211],v[224:227],v[160:163],v[208:211]\n" \
C_"v166,v156,v157\n" \
C_"v167,v158,v159\n" \
M_"v[152:155],a[224:227],v[96:99],v[192:195]\n" \
"v_max3_f32 v234,v144,v145,v146\n" \
"v_max3_f32 v235,v147,v148,v149\n" \
M_"v[152:155],a[228:231],v[100:103],v[152:155]\n" \
"v_max3_f32 v234,v234,v150,v151\n" \
"v_max_f32 v234,v234,v235\n" \
M_"v[152:155],a[232:235],v[104:107],v[152:155]\n" \
"v_cmp_lt_f32 vcc,s82,v234\n" \
M_"v[152:155],a[236:239],v[108:111],v[152:155]\n" \
"s_cbranch_vccz LK5c_%=\n" \
"s_call_b64 s[90:91],LR5p0_%=\n" \
"LK5c_%=:\n" \
M_"v[156:159],a[240:243],v[96:99],v[192:195]\n" \
M_"v[156:159],a[244:247],v[100:103],v[156:159]\n" \
"v_exp_f32 v144,v144\n" \
M_"v[156:159],a[248:251],v[104:107],v[156:159]\n" \
M_"v[156:159],a[252:255],v[108:111],v[156:159]\n" \
"v_exp_f32 v145,v145\n" \
M_"a[128:131],v[112:115],v[164:167],a[128:131]\n" \
"v_exp_f32 v146,v146\n" \
M_"a[132:135],v[116:119],v[164:167],a[132:135]\n" \
M_"a[136:139],v[120:123],v[164:167],a[136:139]\n" \
"v_exp_f32 v147,v147\n" \
M_"a[140:143],v[124:127],v[164:167],a[140:143]\n" \
C_"v160,v144,v145\n" \
C_"v161,v146,v147\n" \
M_"a[144:147],v[128:131],v[164:167],a[144:147]\n" \
"v_exp_f32 v148,v148\n" \
M_"a[148:151],v[132:135],v[164:167],a[148:151]\n" \
"v_exp_f32 v149,v149\n" \
M_"a[152:155],v[136:139],v[164:167],a[152:155]\n" \
"v_exp_f32 v150,v150\n" \
M_"a[156:159],v[140:143],v[164:167],a[156:159]\n" \
"v_exp_f32 v151,v151\n" \
M_"v[212:215],v[224:227],v[164:167],v[212:215]\n" \
C_"v162,v148,v149\n" \
C_"v163,v150,v151\n" \
"; ---- drain\n" \
"v_max3_f32 v234,v152,v153,v154\n" \
"v_max3_f32 v235,v155,v156,v157\n" \
"v_max3_f32 v234,v234,v158,v159\n" \
"v_max_f32 v234,v234,v235\n" \
"v_cmp_lt_f32 vcc,s82,v234\n" \
"s_cbranch_vccz LK6d_%=\n" \
"s_call_b64 s[90:91],LR6p1_%=\n" \
"LK6d_%=:\n" \
M_"a[160:163],v[112:115],v[160:163],a[160:163]\n" \
"v_exp_f32 v152,v152\n" \
M_"a[164:167],v[116:119],v[160:163],a[164:167]\n" \
"v_exp_f32 v153,v153\n" \
M_"a[168:171],v[120:123],v[160:163],a[168:171]\n" \
"v_exp_f32 v154,v154\n" \
M_"a[172:175],v[124:127],v[160:163],a[172:175]\n" \
"v_exp_f32 v155,v155\n" \
M_"a[176:179],v[128:131],v[160:163],a[176:179]\n" \
"v_exp_f32 v156,v156\n" \
M_"a[180:183],v[132:135],v[160:163],a[180:183]\n" \
"v_exp_f32 v157,v157\n" \
M_"a[184:187],v[136:139],v[160:163],a[184:187]\n" \
"v_exp_f32 v158,v158\n" \
M_"a[188:191],v[140:143],v[160:163],a[188:191]\n" \
"v_exp_f32 v159,v159\n" \
M_"v[216:219],v[224:227],v[160:163],v[216:219]\n" \
C_"v164,v152,v153\n" \
C_"v165,v154,v155\n" \
C_"v166,v156,v157\n" \
C_"v167,v158,v159\n" \
"s_nop 1\n" \
M_"a[192:195],v[112:115],v[164:167],a[192:195]\n" \
M_"a[196:199],v[116:119],v[164:167],a[196:199]\n" \
M_"a[200:203],v[120:123],v[164:167],a[200:203]\n" \
M_"a[204:207],v[124:127],v[164:167],a[204:207]\n" \
M_"a[208:211],v[128:131],v[164:167],a[208:211]\n" \
M_"a[212:215],v[132:135],v[164:167],a[212:215]\n" \
M_"a[216:219],v[136:139],v[164:167],a[216:219]\n" \
M_"a[220:223],v[140:143],v[164:167],a[220:223]\n" \
M_"v[220:223],v[224:227],v[164:167],v[220:223]\n" \
"s_nop 7\n" \
"s_branch LATTD_%=\n" \
"LR6p0_%=:\n" \
"v_mov_b32 v235,v234\n" \
"s_nop 1\n" \
"v_permlane32_swap_b32 v234,v235\n" \
"v_max_f32 v234,v234,v235\n" \
"v_mov_b32 v235,v234\n" \
"s_nop 1\n" \
"v_permlane16_swap_b32 v234,v235\n" \
"v_max3_f32 v234,v234,v235,0\n" \
"v_sub_f32 v144,v144,v234\n" \
"v_sub_f32 v145,v145,v234\n" \
"v_sub_f32 v146,v146,v234\n" \
"v_sub_f32 v147,v147,v234\n" \
"v_sub_f32 v148,v148,v234\n" \
"v_sub_f32 v149,v149,v234\n" \
"v_sub_f32 v150,v150,v234\n" \
"v_sub_f32 v151,v151,v234\n" \
"v_sub_f32 v192,v192,v234\n" \
"v_sub_f32 v193,v193,v234\n" \
"v_sub_f32 v194,v194,v234\n" \
"v_sub_f32 v195,v195,v234\n" \
"v_exp_f32 v235,-v234\n" \
"s_nop 0\n" \
"v_mul_f32 v220,v220,v235\n" \
"v_mul_f32 v221,v221,v235\n" \
"v_mul_f32 v222,v222,v235\n" \
"v_mul_f32 v223,v223,v235\n" \
G_"v236,a192\n" \
"v_mul_f32 v236,v236,v235\n" \
W_"a192,v236\n" \
G_"v237,a193\n" \
"v_mul_f32 v237,v237,v235\n" \
W_"a193,v237\n" \
G_"v238,a194\n" \
"v_mul_f32 v238,v238,v235\n" \
W_"a194,v238\n" \
G_"v239,a195\n" \
"v_mul_f32 v239,v239,v235\n" \
W_"a195,v239\n" \
G_"v236,a196\n" \
"v_mul_f32 v236,v236,v235\n" \
W_"a196,v236\n" \
G_"v237,a197\n" \
"v_mul_f32 v237,v237,v235\n" \
W_"a197,v237\n" \
G_"v238,a198\n" \
"v_mul_f32 v238,v238,v235\n" \
W_"a198,v238\n" \
G_"v239,a199\n" \
"v_mul_f32 v239,v239,v235\n" \
W_"a199,v239\n" \
G_"v236,a200\n" \
"v_mul_f32 v236,v236,v235\n" \
W_"a200,v236\n" \
G_"v237,a201\n" \
"v_mul_f32 v237,v237,v235\n" \
W_"a201,v237\n" \
G_"v238,a202\n" \
"v_mul_f32 v238,v238,v235\n" \
W_"a202,v238\n" \
G_"v239,a203\n" \
"v_mul_f32 v239,v239,v235\n" \
W_"a203,v239\n" \
G_"v236,a204\n" \
"v_mul_f32 v236,v236,v235\n" \
W_"a204,v236\n" \
G_"v237,a205\n" \
"v_mul_f32 v237,v237,v235\n" \
W_"a205,v237\n" \
G_"v238,a206\n" \
"v_mul_f32 v238,v238,v235\n" \
W_"a206,v238\n" \
G_"v239,a207\n" \
"v_mul_f32 v239,v239,v235\n" \
W_"a207,v239\n" \
G_"v236,a208\n" \
"v_mul_f32 v236,v236,v235\n" \
W_"a208,v236\n" \
G_"v237,a209\n" \
"v_mul_f32 v237,v237,v235\n" \
W_"a209,v237\n" \
G_"v238,a210\n" \
"v_mul_f32 v238,v238,v235\n" \
W_"a210,v238\n" \
G_"v239,a211\n" \
"v_mul_f32 v239,v239,v235\n" \
W_"a211,v239\n" \
G_"v236,a212\n" \
"v_mul_f32 v236,v236,v235\n" \
W_"a212,v236\n" \
G_"v237,a213\n" \
"v_mul_f32 v237,v237,v235\n" \
W_"a213,v237\n" \
G_"v238,a214\n" \
"v_mul_f32 v238,v238,v235\n" \
W_"a214,v238\n" \
G_"v239,a215\n" \
"v_mul_f32 v239,v239,v235\n" \
W_"a215,v239\n" \
G_"v236,a216\n" \
"v_mul_f32 v236,v236,v235\n" \
W_"a216,v236\n" \
G_"v237,a217\n" \
"v_mul_f32 v237,v237,v235\n" \
W_"a217,v237\n" \
G_"v238,a218\n" \
"v_mul_f32 v238,v238,v235\n" \
W_"a218,v238\n" \
G_"v239,a219\n" \
"v_mul_f32 v239,v239,v235\n" \
W_"a219,v239\n" \
G_"v236,a220\n" \
"v_mul_f32 v236,v236,v235\n" \
W_"a220,v236\n" \
G_"v237,a221\n" \
"v_mul_f32 v237,v237,v235\n" \
W_"a221,v237\n" \
G_"v238,a222\n" \
"v_mul_f32 v238,v238,v235\n" \
W_"a222,v238\n" \
G_"v239,a223\n" \
"v_mul_f32 v239,v239,v235\n" \
W_"a223,v239\n" \
"s_nop 1\n" \
"s_setpc_b64 s[90:91]\n" \
"LR0p1_%=:\n" \
"v_mov_b32 v235,v234\n" \
"s_nop 1\n" \
"v_permlane32_swap_b32 v234,v235\n" \
"v_max_f32 v234,v234,v235\n" \
"v_mov_b32 v235,v234\n" \
"s_nop 1\n" \
"v_permlane16_swap_b32 v234,v235\n" \
"v_max3_f32 v234,v234,v235,0\n" \
"v_sub_f32 v152,v152,v234\n" \
"v_sub_f32 v153,v153,v234\n" \
"v_sub_f32 v154,v154,v234\n" \
"v_sub_f32 v155,v155,v234\n" \
"v_sub_f32 v156,v156,v234\n" \
"v_sub_f32 v157,v157,v234\n" \
"v_sub_f32 v158,v158,v234\n" \
"v_sub_f32 v159,v159,v234\n" \
"v_sub_f32 v168,v168,v234\n" \
"v_sub_f32 v169,v169,v234\n" \
"v_sub_f32 v170,v170,v234\n" \
"v_sub_f32 v171,v171,v234\n" \
"v_exp_f32 v235,-v234\n" \
"s_nop 0\n" \
"v_mul_f32 v196,v196,v235\n" \
"v_mul_f32 v197,v197,v235\n" \
"v_mul_f32 v198,v198,v235\n" \
"v_mul_f32 v199,v199,v235\n" \
G_"v236,a0\n" \
"v_mul_f32 v236,v236,v235\n" \
W_"a0,v236\n" \
G_"v237,a1\n" \
"v_mul_f32 v237,v237,v235\n" \
W_"a1,v237\n" \
G_"v238,a2\n" \
"v_mul_f32 v238,v238,v235\n" \
W_"a2,v238\n" \
G_"v239,a3\n" \
"v_mul_f32 v239,v239,v235\n" \
W_"a3,v239\n" \
G_"v236,a4\n" \
"v_mul_f32 v236,v236,v235\n" \
W_"a4,v236\n" \
G_"v237,a5\n" \
"v_mul_f32 v237,v237,v235\n" \
W_"a5,v237\n" \
G_"v238,a6\n" \
"v_mul_f32 v238,v238,v235\n" \
W_"a6,v238\n" \
G_"v239,a7\n" \
"v_mul_f32 v239,v239,v235\n" \
W_"a7,v239\n" \
G_"v236,a8\n" \
"v_mul_f32 v236,v236,v235\n" \
W_"a8,v236\n" \
G_"v237,a9\n" \
"v_mul_f32 v237,v237,v235\n" \
W_"a9,v237\n" \
G_"v238,a10\n" \
"v_mul_f32 v238,v238,v235\n" \
W_"a10,v238\n" \
G_"v239,a11\n" \
"v_mul_f32 v239,v239,v235\n" \
W_"a11,v239\n" \
G_"v236,a12\n" \
"v_mul_f32 v236,v236,v235\n" \
W_"a12,v236\n" \
G_"v237,a13\n" \
"v_mul_f32 v237,v237,v235\n" \
W_"a13,v237\n" \
G_"v238,a14\n" \
"v_mul_f32 v238,v238,v235\n" \
W_"a14,v238\n" \
G_"v239,a15\n" \
"v_mul_f32 v239,v239,v235\n" \
W_"a15,v239\n" \
G_"v236,a16\n" \
"v_mul_f32 v236,v236,v235\n" \
W_"a16,v236\n" \
G_"v237,a17\n" \
"v_mul_f32 v237,v237,v235\n" \
W_"a17,v237\n" \
G_"v238,a18\n" \
"v_mul_f32 v238,v238,v235\n" \
W_"a18,v238\n" \
G_"v239,a19\n" \
"v_mul_f32 v239,v239,v235\n" \
W_"a19,v239\n" \
G_"v236,a20\n" \
"v_mul_f32 v236,v236,v235\n" \
W_"a20,v236\n" \
G_"v237,a21\n" \
"v_mul_f32 v237,v237,v235\n" \
W_"a21,v237\n" \
G_"v238,a22\n" \
"v_mul_f32 v238,v238,v235\n" \
W_"a22,v238\n" \
G_"v239,a23\n" \
"v_mul_f32 v239,v239,v235\n" \
W_"a23,v239\n" \
G_"v236,a24\n" \
"v_mul_f32 v236,v236,v235\n" \
W_"a24,v236\n" \
G_"v237,a25\n" \
"v_mul_f32 v237,v237,v235\n" \
W_"a25,v237\n" \
G_"v238,a26\n" \
"v_mul_f32 v238,v238,v235\n" \
W_"a26,v238\n" \
G_"v239,a27\n" \
"v_mul_f32 v239,v239,v235\n" \
W_"a27,v239\n" \
G_"v236,a28\n" \
"v_mul_f32 v236,v236,v235\n" \
W_"a28,v236\n" \
G_"v237,a29\n" \
"v_mul_f32 v237,v237,v235\n" \
W_"a29,v237\n" \
G_"v238,a30\n" \
"v_mul_f32 v238,v238,v235\n" \
W_"a30,v238\n" \
G_"v239,a31\n" \
"v_mul_f32 v239,v239,v235\n" \
W_"a31,v239\n" \
"s_nop 1\n" \
"s_setpc_b64 s[90:91]\n" \
"LR1p0_%=:\n" \
"v_mov_b32 v235,v234\n" \
"s_nop 1\n" \
"v_permlane32_swap_b32 v234,v235\n" \
"v_max_f32 v234,v234,v235\n" \
"v_mov_b32 v235,v234\n" \
"s_nop 1\n" \
"v_permlane16_swap_b32 v234,v235\n" \
"v_max3_f32 v234,v234,v235,0\n" \
"v_sub_f32 v144,v144,v234\n" \
"v_sub_f32 v145,v145,v234\n" \
"v_sub_f32 v146,v146,v234\n" \
"v_sub_f32 v147,v147,v234\n" \
"v_sub_f32 v148,v148,v234\n" \
"v_sub_f32 v149,v149,v234\n" \
"v_sub_f32 v150,v150,v234\n" \
"v_sub_f32 v151,v151,v234\n" \
"v_sub_f32 v172,v172,v234\n" \
"v_sub_f32 v173,v173,v234\n" \
"v_sub_f32 v174,v174,v234\n" \
"v_sub_f32 v175,v175,v234\n" \
"v_exp_f32 v235,-v234\n" \
"s_nop 0\n" \
"v_mul_f32 v200,v200,v235\n" \
"v_mul_f32 v201,v201,v235\n" \
"v_mul_f32 v202,v202,v235\n" \
"v_mul_f32 v203,v203,v235\n" \
G_"v236,a32\n" \
"v_mul_f32 v236,v236,v235\n" \
W_"a32,v236\n" \
G_"v237,a33\n" \
"v_mul_f32 v237,v237,v235\n" \
W_"a33,v237\n" \
G_"v238,a34\n" \
"v_mul_f32 v238,v238,v235\n" \
W_"a34,v238\n" \
G_"v239,a35\n" \
"v_mul_f32 v239,v239,v235\n" \
W_"a35,v239\n" \
G_"v236,a36\n" \
"v_mul_f32 v236,v236,v235\n" \
W_"a36,v236\n" \
G_"v237,a37\n" \
"v_mul_f32 v237,v237,v235\n" \
W_"a37,v237\n" \
G_"v238,a38\n" \
"v_mul_f32 v238,v238,v235\n" \
W_"a38,v238\n" \
G_"v239,a39\n" \
"v_mul_f32 v239,v239,v235\n" \
W_"a39,v239\n" \
G_"v236,a40\n" \
"v_mul_f32 v236,v236,v235\n" \
W_"a40,v236\n" \
G_"v237,a41\n" \
"v_mul_f32 v237,v237,v235\n" \
W_"a41,v237\n" \
G_"v238,a42\n" \
"v_mul_f32 v238,v238,v235\n" \
W_"a42,v238\n" \
G_"v239,a43\n" \
"v_mul_f32 v239,v239,v235\n" \
W_"a43,v239\n" \
G_"v236,a44\n" \
"v_mul_f32 v236,v236,v235\n" \
W_"a44,v236\n" \
G_"v237,a45\n" \
"v_mul_f32 v237,v237,v235\n" \
W_"a45,v237\n" \
G_"v238,a46\n" \
"v_mul_f32 v238,v238,v235\n" \
W_"a46,v238\n" \
G_"v239,a47\n" \
"v_mul_f32 v239,v239,v235\n" \
W_"a47,v239\n" \
G_"v236,a48\n" \
"v_mul_f32 v236,v236,v235\n" \
W_"a48,v236\n" \
G_"v237,a49\n" \
"v_mul_f32 v237,v237,v235\n" \
W_"a49,v237\n" \
G_"v238,a50\n" \
"v_mul_f32 v238,v238,v235\n" \
W_"a50,v238\n" \
G_"v239,a51\n" \
"v_mul_f32 v239,v239,v235\n" \
W_"a51,v239\n" \
G_"v236,a52\n" \
"v_mul_f32 v236,v236,v235\n" \
W_"a52,v236\n" \
G_"v237,a53\n" \
"v_mul_f32 v237,v237,v235\n" \
W_"a53,v237\n" \
G_"v238,a54\n" \
"v_mul_f32 v238,v238,v235\n" \
W_"a54,v238\n" \
G_"v239,a55\n" \
"v_mul_f32 v239,v239,v235\n" \
W_"a55,v239\n" \
G_"v236,a56\n" \
"v_mul_f32 v236,v236,v235\n" \
W_"a56,v236\n" \
G_"v237,a57\n" \
"v_mul_f32 v237,v237,v235\n" \
W_"a57,v237\n" \
G_"v238,a58\n" \
"v_mul_f32 v238,v238,v235\n" \
W_"a58,v238\n" \
G_"v239,a59\n" \
"v_mul_f32 v239,v239,v235\n" \
W_"a59,v239\n" \
G_"v236,a60\n" \
"v_mul_f32 v236,v236,v235\n" \
W_"a60,v236\n" \
G_"v237,a61\n" \
"v_mul_f32 v237,v237,v235\n" \
W_"a61,v237\n" \
G_"v238,a62\n" \
"v_mul_f32 v238,v238,v235\n" \
W_"a62,v238\n" \
G_"v239,a63\n" \
"v_mul_f32 v239,v239,v235\n" \
W_"a63,v239\n" \
"s_nop 1\n" \
"s_setpc_b64 s[90:91]\n" \
"LR2p1_%=:\n" \
"v_mov_b32 v235,v234\n" \
"s_nop 1\n" \
"v_permlane32_swap_b32 v234,v235\n" \
"v_max_f32 v234,v234,v235\n" \
"v_mov_b32 v235,v234\n" \
"s_nop 1\n" \
"v_permlane16_swap_b32 v234,v235\n" \
"v_max3_f32 v234,v234,v235,0\n" \
"v_sub_f32 v152,v152,v234\n" \
"v_sub_f32 v153,v153,v234\n" \
"v_sub_f32 v154,v154,v234\n" \
"v_sub_f32 v155,v155,v234\n" \
"v_sub_f32 v156,v156,v234\n" \
"v_sub_f32 v157,v157,v234\n" \
"v_sub_f32 v158,v158,v234\n" \
"v_sub_f32 v159,v159,v234\n" \
"v_sub_f32 v176,v176,v234\n" \
"v_sub_f32 v177,v177,v234\n" \
"v_sub_f32 v178,v178,v234\n" \
"v_sub_f32 v179,v179,v234\n" \
"v_exp_f32 v235,-v234\n" \
"s_nop 0\n" \
"v_mul_f32 v204,v204,v235\n" \
"v_mul_f32 v205,v205,v235\n" \
"v_mul_f32 v206,v206,v235\n" \
"v_mul_f32 v207,v207,v235\n" \
G_"v236,a64\n" \
"v_mul_f32 v236,v236,v235\n" \
W_"a64,v236\n" \
G_"v237,a65\n" \
"v_mul_f32 v237,v237,v235\n" \
W_"a65,v237\n" \
G_"v238,a66\n" \
"v_mul_f32 v238,v238,v235\n" \
W_"a66,v238\n" \
G_"v239,a67\n" \
"v_mul_f32 v239,v239,v235\n" \
W_"a67,v239\n" \
G_"v236,a68\n" \
"v_mul_f32 v236,v236,v235\n" \
W_"a68,v236\n" \
G_"v237,a69\n" \
"v_mul_f32 v237,v237,v235\n" \
W_"a69,v237\n" \
G_"v238,a70\n" \
"v_mul_f32 v238,v238,v235\n" \
W_"a70,v238\n" \
G_"v239,a71\n" \
"v_mul_f32 v239,v239,v235\n" \
W_"a71,v239\n" \
G_"v236,a72\n" \
"v_mul_f32 v236,v236,v235\n" \
W_"a72,v236\n" \
G_"v237,a73\n" \
"v_mul_f32 v237,v237,v235\n" \
W_"a73,v237\n" \
G_"v238,a74\n" \
"v_mul_f32 v238,v238,v235\n" \
W_"a74,v238\n" \
G_"v239,a75\n" \
"v_mul_f32 v239,v239,v235\n" \
W_"a75,v239\n" \
G_"v236,a76\n" \
"v_mul_f32 v236,v236,v235\n" \
W_"a76,v236\n" \
G_"v237,a77\n" \
"v_mul_f32 v237,v237,v235\n" \
W_"a77,v237\n" \
G_"v238,a78\n" \
"v_mul_f32 v238,v238,v235\n" \
W_"a78,v238\n" \
G_"v239,a79\n" \
"v_mul_f32 v239,v239,v235\n" \
W_"a79,v239\n" \
G_"v236,a80\n" \
"v_mul_f32 v236,v236,v235\n" \
W_"a80,v236\n" \
G_"v237,a81\n" \
"v_mul_f32 v237,v237,v235\n" \
W_"a81,v237\n" \
G_"v238,a82\n" \
"v_mul_f32 v238,v238,v235\n" \
W_"a82,v238\n" \
G_"v239,a83\n" \
"v_mul_f32 v239,v239,v235\n" \
W_"a83,v239\n" \
G_"v236,a84\n" \
"v_mul_f32 v236,v236,v235\n" \
W_"a84,v236\n" \
G_"v237,a85\n" \
"v_mul_f32 v237,v237,v235\n" \
W_"a85,v237\n" \
G_"v238,a86\n" \
"v_mul_f32 v238,v238,v235\n" \
W_"a86,v238\n" \
G_"v239,a87\n" \
"v_mul_f32 v239,v239,v235\n" \
W_"a87,v239\n" \
G_"v236,a88\n" \
"v_mul_f32 v236,v236,v235\n" \
W_"a88,v236\n" \
G_"v237,a89\n" \
"v_mul_f32 v237,v237,v235\n" \
W_"a89,v237\n" \
G_"v238,a90\n" \
"v_mul_f32 v238,v238,v235\n" \
W_"a90,v238\n" \
G_"v239,a91\n" \
"v_mul_f32 v239,v239,v235\n" \
W_"a91,v239\n" \
G_"v236,a92\n" \
"v_mul_f32 v236,v236,v235\n" \
W_"a92,v236\n" \
G_"v237,a93\n" \
"v_mul_f32 v237,v237,v235\n" \
W_"a93,v237\n" \
G_"v238,a94\n" \
"v_mul_f32 v238,v238,v235\n" \
W_"a94,v238\n" \
G_"v239,a95\n" \
"v_mul_f32 v239,v239,v235\n" \
W_"a95,v239\n" \
"s_nop 1\n" \
"s_setpc_b64 s[90:91]\n" \
"LR3p0_%=:\n" \
"v_mov_b32 v235,v234\n" \
"s_nop 1\n" \
"v_permlane32_swap_b32 v234,v235\n" \
"v_max_f32 v234,v234,v235\n" \
"v_mov_b32 v235,v234\n" \
"s_nop 1\n" \
"v_permlane16_swap_b32 v234,v235\n" \
"v_max3_f32 v234,v234,v235,0\n" \
"v_sub_f32 v144,v144,v234\n" \
"v_sub_f32 v145,v145,v234\n" \
"v_sub_f32 v146,v146,v234\n" \
"v_sub_f32 v147,v147,v234\n" \
"v_sub_f32 v148,v148,v234\n" \
"v_sub_f32 v149,v149,v234\n" \
"v_sub_f32 v150,v150,v234\n" \
"v_sub_f32 v151,v151,v234\n" \
"v_sub_f32 v180,v180,v234\n" \
"v_sub_f32 v181,v181,v234\n" \
"v_sub_f32 v182,v182,v234\n" \
"v_sub_f32 v183,v183,v234\n" \
"v_exp_f32 v235,-v234\n" \
"s_nop 0\n" \
"v_mul_f32 v208,v208,v235\n" \
"v_mul_f32 v209,v209,v235\n" \
"v_mul_f32 v210,v210,v235\n" \
"v_mul_f32 v211,v211,v235\n" \
G_"v236,a96\n" \
"v_mul_f32 v236,v236,v235\n" \
W_"a96,v236\n" \
G_"v237,a97\n" \
"v_mul_f32 v237,v237,v235\n" \
W_"a97,v237\n" \
G_"v238,a98\n" \
"v_mul_f32 v238,v238,v235\n" \
W_"a98,v238\n" \
G_"v239,a99\n" \
"v_mul_f32 v239,v239,v235\n" \
W_"a99,v239\n" \
G_"v236,a100\n" \
"v_mul_f32 v236,v236,v235\n" \
W_"a100,v236\n" \
G_"v237,a101\n" \
"v_mul_f32 v237,v237,v235\n" \
W_"a101,v237\n" \
G_"v238,a102\n" \
"v_mul_f32 v238,v238,v235\n" \
W_"a102,v238\n" \
G_"v239,a103\n" \
"v_mul_f32 v239,v239,v235\n" \
W_"a103,v239\n" \
G_"v236,a104\n" \
"v_mul_f32 v236,v236,v235\n" \
W_"a104,v236\n" \
G_"v237,a105\n" \
"v_mul_f32 v237,v237,v235\n" \
W_"a105,v237\n" \
G_"v238,a106\n" \
"v_mul_f32 v238,v238,v235\n" \
W_"a106,v238\n" \
G_"v239,a107\n" \
"v_mul_f32 v239,v239,v235\n" \
W_"a107,v239\n" \
G_"v236,a108\n" \
"v_mul_f32 v236,v236,v235\n" \
W_"a108,v236\n" \
G_"v237,a109\n" \
"v_mul_f32 v237,v237,v235\n" \
W_"a109,v237\n" \
G_"v238,a110\n" \
"v_mul_f32 v238,v238,v235\n" \
W_"a110,v238\n" \
G_"v239,a111\n" \
"v_mul_f32 v239,v239,v235\n" \
W_"a111,v239\n" \
G_"v236,a112\n" \
"v_mul_f32 v236,v236,v235\n" \
W_"a112,v236\n" \
G_"v237,a113\n" \
"v_mul_f32 v237,v237,v235\n" \
W_"a113,v237\n" \
G_"v238,a114\n" \
"v_mul_f32 v238,v238,v235\n" \
W_"a114,v238\n" \
G_"v239,a115\n" \
"v_mul_f32 v239,v239,v235\n" \
W_"a115,v239\n" \
G_"v236,a116\n" \
"v_mul_f32 v236,v236,v235\n" \
W_"a116,v236\n" \
G_"v237,a117\n" \
"v_mul_f32 v237,v237,v235\n" \
W_"a117,v237\n" \
G_"v238,a118\n" \
"v_mul_f32 v238,v238,v235\n" \
W_"a118,v238\n" \
G_"v239,a119\n" \
"v_mul_f32 v239,v239,v235\n" \
W_"a119,v239\n" \
G_"v236,a120\n" \
"v_mul_f32 v236,v236,v235\n" \
W_"a120,v236\n" \
G_"v237,a121\n" \
"v_mul_f32 v237,v237,v235\n" \
W_"a121,v237\n" \
G_"v238,a122\n" \
"v_mul_f32 v238,v238,v235\n" \
W_"a122,v238\n" \
G_"v239,a123\n" \
"v_mul_f32 v239,v239,v235\n" \
W_"a123,v239\n" \
G_"v236,a124\n" \
"v_mul_f32 v236,v236,v235\n" \
W_"a124,v236\n" \
G_"v237,a125\n" \
"v_mul_f32 v237,v237,v235\n" \
W_"a125,v237\n" \
G_"v238,a126\n" \
"v_mul_f32 v238,v238,v235\n" \
W_"a126,v238\n" \
G_"v239,a127\n" \
"v_mul_f32 v239,v239,v235\n" \
W_"a127,v239\n" \
"s_nop 1\n" \
"s_setpc_b64 s[90:91]\n" \
"LR4p1_%=:\n" \
"v_mov_b32 v235,v234\n" \
"s_nop 1\n" \
"v_permlane32_swap_b32 v234,v235\n" \
"v_max_f32 v234,v234,v235\n" \
"v_mov_b32 v235,v234\n" \
"s_nop 1\n" \
"v_permlane16_swap_b32 v234,v235\n" \
"v_max3_f32 v234,v234,v235,0\n" \
"v_sub_f32 v152,v152,v234\n" \
"v_sub_f32 v153,v153,v234\n" \
"v_sub_f32 v154,v154,v234\n" \
"v_sub_f32 v155,v155,v234\n" \
"v_sub_f32 v156,v156,v234\n" \
"v_sub_f32 v157,v157,v234\n" \
"v_sub_f32 v158,v158,v234\n" \
"v_sub_f32 v159,v159,v234\n" \
"v_sub_f32 v184,v184,v234\n" \
"v_sub_f32 v185,v185,v234\n" \
"v_sub_f32 v186,v186,v234\n" \
"v_sub_f32 v187,v187,v234\n" \
"v_exp_f32 v235,-v234\n" \
"s_nop 0\n" \
"v_mul_f32 v212,v212,v235\n" \
"v_mul_f32 v213,v213,v235\n" \
"v_mul_f32 v214,v214,v235\n" \
"v_mul_f32 v215,v215,v235\n" \
G_"v236,a128\n" \
"v_mul_f32 v236,v236,v235\n" \
W_"a128,v236\n" \
G_"v237,a129\n" \
"v_mul_f32 v237,v237,v235\n" \
W_"a129,v237\n" \
G_"v238,a130\n" \
"v_mul_f32 v238,v238,v235\n" \
W_"a130,v238\n" \
G_"v239,a131\n" \
"v_mul_f32 v239,v239,v235\n" \
W_"a131,v239\n" \
G_"v236,a132\n" \
"v_mul_f32 v236,v236,v235\n" \
W_"a132,v236\n" \
G_"v237,a133\n" \
"v_mul_f32 v237,v237,v235\n" \
W_"a133,v237\n" \
G_"v238,a134\n" \
"v_mul_f32 v238,v238,v235\n" \
W_"a134,v238\n" \
G_"v239,a135\n" \
"v_mul_f32 v239,v239,v235\n" \
W_"a135,v239\n" \
G_"v236,a136\n" \
"v_mul_f32 v236,v236,v235\n" \
W_"a136,v236\n" \
G_"v237,a137\n" \
"v_mul_f32 v237,v237,v235\n" \
W_"a137,v237\n" \
G_"v238,a138\n" \
"v_mul_f32 v238,v238,v235\n" \
W_"a138,v238\n" \
G_"v239,a139\n" \
"v_mul_f32 v239,v239,v235\n" \
W_"a139,v239\n" \
G_"v236,a140\n" \
"v_mul_f32 v236,v236,v235\n" \
W_"a140,v236\n" \
G_"v237,a141\n" \
"v_mul_f32 v237,v237,v235\n" \
W_"a141,v237\n" \
G_"v238,a142\n" \
"v_mul_f32 v238,v238,v235\n" \
W_"a142,v238\n" \
G_"v239,a143\n" \
"v_mul_f32 v239,v239,v235\n" \
W_"a143,v239\n" \
G_"v236,a144\n" \
"v_mul_f32 v236,v236,v235\n" \
W_"a144,v236\n" \
G_"v237,a145\n" \
"v_mul_f32 v237,v237,v235\n" \
W_"a145,v237\n" \
G_"v238,a146\n" \
"v_mul_f32 v238,v238,v235\n" \
W_"a146,v238\n" \
G_"v239,a147\n" \
"v_mul_f32 v239,v239,v235\n" \
W_"a147,v239\n" \
G_"v236,a148\n" \
"v_mul_f32 v236,v236,v235\n" \
W_"a148,v236\n" \
G_"v237,a149\n" \
"v_mul_f32 v237,v237,v235\n" \
W_"a149,v237\n" \
G_"v238,a150\n" \
"v_mul_f32 v238,v238,v235\n" \
W_"a150,v238\n" \
G_"v239,a151\n" \
"v_mul_f32 v239,v239,v235\n" \
W_"a151,v239\n" \
G_"v236,a152\n" \
"v_mul_f32 v236,v236,v235\n" \
W_"a152,v236\n" \
G_"v237,a153\n" \
"v_mul_f32 v237,v237,v235\n" \
W_"a153,v237\n" \
G_"v238,a154\n" \
"v_mul_f32 v238,v238,v235\n" \
W_"a154,v238\n" \
G_"v239,a155\n" \
"v_mul_f32 v239,v239,v235\n" \
W_"a155,v239\n" \
G_"v236,a156\n" \
"v_mul_f32 v236,v236,v235\n" \
W_"a156,v236\n" \
G_"v237,a157\n" \
"v_mul_f32 v237,v237,v235\n" \
W_"a157,v237\n" \
G_"v238,a158\n" \
"v_mul_f32 v238,v238,v235\n" \
W_"a158,v238\n" \
G_"v239,a159\n" \
"v_mul_f32 v239,v239,v235\n" \
W_"a159,v239\n" \
"s_nop 1\n" \
"s_setpc_b64 s[90:91]\n" \
"LR5p0_%=:\n" \
"v_mov_b32 v235,v234\n" \
"s_nop 1\n" \
"v_permlane32_swap_b32 v234,v235\n" \
"v_max_f32 v234,v234,v235\n" \
"v_mov_b32 v235,v234\n" \
"s_nop 1\n" \
"v_permlane16_swap_b32 v234,v235\n" \
"v_max3_f32 v234,v234,v235,0\n" \
"v_sub_f32 v144,v144,v234\n" \
"v_sub_f32 v145,v145,v234\n" \
"v_sub_f32 v146,v146,v234\n" \
"v_sub_f32 v147,v147,v234\n" \
"v_sub_f32 v148,v148,v234\n" \
"v_sub_f32 v149,v149,v234\n" \
"v_sub_f32 v150,v150,v234\n" \
"v_sub_f32 v151,v151,v234\n" \
"v_sub_f32 v188,v188,v234\n" \
"v_sub_f32 v189,v189,v234\n" \
"v_sub_f32 v190,v190,v234\n" \
"v_sub_f32 v191,v191,v234\n" \
"v_exp_f32 v235,-v234\n" \
"s_nop 0\n" \
"v_mul_f32 v216,v216,v235\n" \
"v_mul_f32 v217,v217,v235\n" \
"v_mul_f32 v218,v218,v235\n" \
"v_mul_f32 v219,v219,v235\n" \
G_"v236,a160\n" \
"v_mul_f32 v236,v236,v235\n" \
W_"a160,v236\n" \
G_"v237,a161\n" \
"v_mul_f32 v237,v237,v235\n" \
W_"a161,v237\n" \
G_"v238,a162\n" \
"v_mul_f32 v238,v238,v235\n" \
W_"a162,v238\n" \
G_"v239,a163\n" \
"v_mul_f32 v239,v239,v235\n" \
W_"a163,v239\n" \
G_"v236,a164\n" \
"v_mul_f32 v236,v236,v235\n" \
W_"a164,v236\n" \
G_"v237,a165\n" \
"v_mul_f32 v237,v237,v235\n" \
W_"a165,v237\n" \
G_"v238,a166\n" \
"v_mul_f32 v238,v238,v235\n" \
W_"a166,v238\n" \
G_"v239,a167\n" \
"v_mul_f32 v239,v239,v235\n" \
W_"a167,v239\n" \
G_"v236,a168\n" \
"v_mul_f32 v236,v236,v235\n" \
W_"a168,v236\n" \
G_"v237,a169\n" \
"v_mul_f32 v237,v237,v235\n" \
W_"a169,v237\n" \
G_"v238,a170\n" \
"v_mul_f32 v238,v238,v235\n" \
W_"a170,v238\n" \
G_"v239,a171\n" \
"v_mul_f32 v239,v239,v235\n" \
W_"a171,v239\n" \
G_"v236,a172\n" \
"v_mul_f32 v236,v236,v235\n" \
W_"a172,v236\n" \
G_"v237,a173\n" \
"v_mul_f32 v237,v237,v235\n" \
W_"a173,v237\n" \
G_"v238,a174\n" \
"v_mul_f32 v238,v238,v235\n" \
W_"a174,v238\n" \
G_"v239,a175\n" \
"v_mul_f32 v239,v239,v235\n" \
W_"a175,v239\n" \
G_"v236,a176\n" \
"v_mul_f32 v236,v236,v235\n" \
W_"a176,v236\n" \
G_"v237,a177\n" \
"v_mul_f32 v237,v237,v235\n" \
W_"a177,v237\n" \
G_"v238,a178\n" \
"v_mul_f32 v238,v238,v235\n" \
W_"a178,v238\n" \
G_"v239,a179\n" \
"v_mul_f32 v239,v239,v235\n" \
W_"a179,v239\n" \
G_"v236,a180\n" \
"v_mul_f32 v236,v236,v235\n" \
W_"a180,v236\n" \
G_"v237,a181\n" \
"v_mul_f32 v237,v237,v235\n" \
W_"a181,v237\n" \
G_"v238,a182\n" \
"v_mul_f32 v238,v238,v235\n" \
W_"a182,v238\n" \
G_"v239,a183\n" \
"v_mul_f32 v239,v239,v235\n" \
W_"a183,v239\n" \
G_"v236,a184\n" \
"v_mul_f32 v236,v236,v235\n" \
W_"a184,v236\n" \
G_"v237,a185\n" \
"v_mul_f32 v237,v237,v235\n" \
W_"a185,v237\n" \
G_"v238,a186\n" \
"v_mul_f32 v238,v238,v235\n" \
W_"a186,v238\n" \
G_"v239,a187\n" \
"v_mul_f32 v239,v239,v235\n" \
W_"a187,v239\n" \
G_"v236,a188\n" \
"v_mul_f32 v236,v236,v235\n" \
W_"a188,v236\n" \
G_"v237,a189\n" \
"v_mul_f32 v237,v237,v235\n" \
W_"a189,v237\n" \
G_"v238,a190\n" \
"v_mul_f32 v238,v238,v235\n" \
W_"a190,v238\n" \
G_"v239,a191\n" \
"v_mul_f32 v239,v239,v235\n" \
W_"a191,v239\n" \
"s_nop 1\n" \
"s_setpc_b64 s[90:91]\n" \
"LR6p1_%=:\n" \
"v_mov_b32 v235,v234\n" \
"s_nop 1\n" \
"v_permlane32_swap_b32 v234,v235\n" \
"v_max_f32 v234,v234,v235\n" \
"v_mov_b32 v235,v234\n" \
"s_nop 1\n" \
"v_permlane16_swap_b32 v234,v235\n" \
"v_max3_f32 v234,v234,v235,0\n" \
"v_sub_f32 v152,v152,v234\n" \
"v_sub_f32 v153,v153,v234\n" \
"v_sub_f32 v154,v154,v234\n" \
"v_sub_f32 v155,v155,v234\n" \
"v_sub_f32 v156,v156,v234\n" \
"v_sub_f32 v157,v157,v234\n" \
"v_sub_f32 v158,v158,v234\n" \
"v_sub_f32 v159,v159,v234\n" \
"v_sub_f32 v192,v192,v234\n" \
"v_sub_f32 v193,v193,v234\n" \
"v_sub_f32 v194,v194,v234\n" \
"v_sub_f32 v195,v195,v234\n" \
"v_exp_f32 v235,-v234\n" \
"s_nop 0\n" \
"v_mul_f32 v220,v220,v235\n" \
"v_mul_f32 v221,v221,v235\n" \
"v_mul_f32 v222,v222,v235\n" \
"v_mul_f32 v223,v223,v235\n" \
G_"v236,a192\n" \
"v_mul_f32 v236,v236,v235\n" \
W_"a192,v236\n" \
G_"v237,a193\n" \
"v_mul_f32 v237,v237,v235\n" \
W_"a193,v237\n" \
G_"v238,a194\n" \
"v_mul_f32 v238,v238,v235\n" \
W_"a194,v238\n" \
G_"v239,a195\n" \
"v_mul_f32 v239,v239,v235\n" \
W_"a195,v239\n" \
G_"v236,a196\n" \
"v_mul_f32 v236,v236,v235\n" \
W_"a196,v236\n" \
G_"v237,a197\n" \
"v_mul_f32 v237,v237,v235\n" \
W_"a197,v237\n" \
G_"v238,a198\n" \
"v_mul_f32 v238,v238,v235\n" \
W_"a198,v238\n" \
G_"v239,a199\n" \
"v_mul_f32 v239,v239,v235\n" \
W_"a199,v239\n" \
G_"v236,a200\n" \
"v_mul_f32 v236,v236,v235\n" \
W_"a200,v236\n" \
G_"v237,a201\n" \
"v_mul_f32 v237,v237,v235\n" \
W_"a201,v237\n" \
G_"v238,a202\n" \
"v_mul_f32 v238,v238,v235\n" \
W_"a202,v238\n" \
G_"v239,a203\n" \
"v_mul_f32 v239,v239,v235\n" \
W_"a203,v239\n" \
G_"v236,a204\n" \
"v_mul_f32 v236,v236,v235\n" \
W_"a204,v236\n" \
G_"v237,a205\n" \
"v_mul_f32 v237,v237,v235\n" \
W_"a205,v237\n" \
G_"v238,a206\n" \
"v_mul_f32 v238,v238,v235\n" \
W_"a206,v238\n" \
G_"v239,a207\n" \
"v_mul_f32 v239,v239,v235\n" \
W_"a207,v239\n" \
G_"v236,a208\n" \
"v_mul_f32 v236,v236,v235\n" \
W_"a208,v236\n" \
G_"v237,a209\n" \
"v_mul_f32 v237,v237,v235\n" \
W_"a209,v237\n" \
G_"v238,a210\n" \
"v_mul_f32 v238,v238,v235\n" \
W_"a210,v238\n" \
G_"v239,a211\n" \
"v_mul_f32 v239,v239,v235\n" \
W_"a211,v239\n" \
G_"v236,a212\n" \
"v_mul_f32 v236,v236,v235\n" \
W_"a212,v236\n" \
G_"v237,a213\n" \
"v_mul_f32 v237,v237,v235\n" \
W_"a213,v237\n" \
G_"v238,a214\n" \
"v_mul_f32 v238,v238,v235\n" \
W_"a214,v238\n" \
G_"v239,a215\n" \
"v_mul_f32 v239,v239,v235\n" \
W_"a215,v239\n" \
G_"v236,a216\n" \
"v_mul_f32 v236,v236,v235\n" \
W_"a216,v236\n" \
G_"v237,a217\n" \
"v_mul_f32 v237,v237,v235\n" \
W_"a217,v237\n" \
G_"v238,a218\n" \
"v_mul_f32 v238,v238,v235\n" \
W_"a218,v238\n" \
G_"v239,a219\n" \
"v_mul_f32 v239,v239,v235\n" \
W_"a219,v239\n" \
G_"v236,a220\n" \
"v_mul_f32 v236,v236,v235\n" \
W_"a220,v236\n" \
G_"v237,a221\n" \
"v_mul_f32 v237,v237,v235\n" \
W_"a221,v237\n" \
G_"v238,a222\n" \
"v_mul_f32 v238,v238,v235\n" \
W_"a222,v238\n" \
G_"v239,a223\n" \
"v_mul_f32 v239,v239,v235\n" \
W_"a223,v239\n" \
"s_nop 1\n" \
"s_setpc_b64 s[90:91]\n" \
"LR0p0_%=:\n" \
"v_mov_b32 v235,v234\n" \
"s_nop 1\n" \
"v_permlane32_swap_b32 v234,v235\n" \
"v_max_f32 v234,v234,v235\n" \
"v_mov_b32 v235,v234\n" \
"s_nop 1\n" \
"v_permlane16_swap_b32 v234,v235\n" \
"v_max3_f32 v234,v234,v235,0\n" \
"v_sub_f32 v144,v144,v234\n" \
"v_sub_f32 v145,v145,v234\n" \
"v_sub_f32 v146,v146,v234\n" \
"v_sub_f32 v147,v147,v234\n" \
"v_sub_f32 v148,v148,v234\n" \
"v_sub_f32 v149,v149,v234\n" \
"v_sub_f32 v150,v150,v234\n" \
"v_sub_f32 v151,v151,v234\n" \
"v_sub_f32 v168,v168,v234\n" \
"v_sub_f32 v169,v169,v234\n" \
"v_sub_f32 v170,v170,v234\n" \
"v_sub_f32 v171,v171,v234\n" \
"v_exp_f32 v235,-v234\n" \
"s_nop 0\n" \
"v_mul_f32 v196,v196,v235\n" \
"v_mul_f32 v197,v197,v235\n" \
"v_mul_f32 v198,v198,v235\n" \
"v_mul_f32 v199,v199,v235\n" \
G_"v236,a0\n" \
"v_mul_f32 v236,v236,v235\n" \
W_"a0,v236\n" \
G_"v237,a1\n" \
"v_mul_f32 v237,v237,v235\n" \
W_"a1,v237\n" \
G_"v238,a2\n" \
"v_mul_f32 v238,v238,v235\n" \
W_"a2,v238\n" \
G_"v239,a3\n" \
"v_mul_f32 v239,v239,v235\n" \
W_"a3,v239\n" \
G_"v236,a4\n" \
"v_mul_f32 v236,v236,v235\n" \
W_"a4,v236\n" \
G_"v237,a5\n" \
"v_mul_f32 v237,v237,v235\n" \
W_"a5,v237\n" \
G_"v238,a6\n" \
"v_mul_f32 v238,v238,v235\n" \
W_"a6,v238\n" \
G_"v239,a7\n" \
"v_mul_f32 v239,v239,v235\n" \
W_"a7,v239\n" \
G_"v236,a8\n" \
"v_mul_f32 v236,v236,v235\n" \
W_"a8,v236\n" \
G_"v237,a9\n" \
"v_mul_f32 v237,v237,v235\n" \
W_"a9,v237\n" \
G_"v238,a10\n" \
"v_mul_f32 v238,v238,v235\n" \
W_"a10,v238\n" \
G_"v239,a11\n" \
"v_mul_f32 v239,v239,v235\n" \
W_"a11,v239\n" \
G_"v236,a12\n" \
"v_mul_f32 v236,v236,v235\n" \
W_"a12,v236\n" \
G_"v237,a13\n" \
"v_mul_f32 v237,v237,v235\n" \
W_"a13,v237\n" \
G_"v238,a14\n" \
"v_mul_f32 v238,v238,v235\n" \
W_"a14,v238\n" \
G_"v239,a15\n" \
"v_mul_f32 v239,v239,v235\n" \
W_"a15,v239\n" \
G_"v236,a16\n" \
"v_mul_f32 v236,v236,v235\n" \
W_"a16,v236\n" \
G_"v237,a17\n" \
"v_mul_f32 v237,v237,v235\n" \
W_"a17,v237\n" \
G_"v238,a18\n" \
"v_mul_f32 v238,v238,v235\n" \
W_"a18,v238\n" \
G_"v239,a19\n" \
"v_mul_f32 v239,v239,v235\n" \
W_"a19,v239\n" \
G_"v236,a20\n" \
"v_mul_f32 v236,v236,v235\n" \
W_"a20,v236\n" \
G_"v237,a21\n" \
"v_mul_f32 v237,v237,v235\n" \
W_"a21,v237\n" \
G_"v238,a22\n" \
"v_mul_f32 v238,v238,v235\n" \
W_"a22,v238\n" \
G_"v239,a23\n" \
"v_mul_f32 v239,v239,v235\n" \
W_"a23,v239\n" \
G_"v236,a24\n" \
"v_mul_f32 v236,v236,v235\n" \
W_"a24,v236\n" \
G_"v237,a25\n" \
"v_mul_f32 v237,v237,v235\n" \
W_"a25,v237\n" \
G_"v238,a26\n" \
"v_mul_f32 v238,v238,v235\n" \
W_"a26,v238\n" \
G_"v239,a27\n" \
"v_mul_f32 v239,v239,v235\n" \
W_"a27,v239\n" \
G_"v236,a28\n" \
"v_mul_f32 v236,v236,v235\n" \
W_"a28,v236\n" \
G_"v237,a29\n" \
"v_mul_f32 v237,v237,v235\n" \
W_"a29,v237\n" \
G_"v238,a30\n" \
"v_mul_f32 v238,v238,v235\n" \
W_"a30,v238\n" \
G_"v239,a31\n" \
"v_mul_f32 v239,v239,v235\n" \
W_"a31,v239\n" \
"s_nop 1\n" \
"s_setpc_b64 s[90:91]\n" \
"LR1p1_%=:\n" \
"v_mov_b32 v235,v234\n" \
"s_nop 1\n" \
"v_permlane32_swap_b32 v234,v235\n" \
"v_max_f32 v234,v234,v235\n" \
"v_mov_b32 v235,v234\n" \
"s_nop 1\n" \
"v_permlane16_swap_b32 v234,v235\n" \
"v_max3_f32 v234,v234,v235,0\n" \
"v_sub_f32 v152,v152,v234\n" \
"v_sub_f32 v153,v153,v234\n" \
"v_sub_f32 v154,v154,v234\n" \
"v_sub_f32 v155,v155,v234\n" \
"v_sub_f32 v156,v156,v234\n" \
"v_sub_f32 v157,v157,v234\n" \
"v_sub_f32 v158,v158,v234\n" \
"v_sub_f32 v159,v159,v234\n" \
"v_sub_f32 v172,v172,v234\n" \
"v_sub_f32 v173,v173,v234\n" \
"v_sub_f32 v174,v174,v234\n" \
"v_sub_f32 v175,v175,v234\n" \
"v_exp_f32 v235,-v234\n" \
"s_nop 0\n" \
"v_mul_f32 v200,v200,v235\n" \
"v_mul_f32 v201,v201,v235\n" \
"v_mul_f32 v202,v202,v235\n" \
"v_mul_f32 v203,v203,v235\n" \
G_"v236,a32\n" \
"v_mul_f32 v236,v236,v235\n" \
W_"a32,v236\n" \
G_"v237,a33\n" \
"v_mul_f32 v237,v237,v235\n" \
W_"a33,v237\n" \
G_"v238,a34\n" \
"v_mul_f32 v238,v238,v235\n" \
W_"a34,v238\n" \
G_"v239,a35\n" \
"v_mul_f32 v239,v239,v235\n" \
W_"a35,v239\n" \
G_"v236,a36\n" \
"v_mul_f32 v236,v236,v235\n" \
W_"a36,v236\n" \
G_"v237,a37\n" \
"v_mul_f32 v237,v237,v235\n" \
W_"a37,v237\n" \
G_"v238,a38\n" \
"v_mul_f32 v238,v238,v235\n" \
W_"a38,v238\n" \
G_"v239,a39\n" \
"v_mul_f32 v239,v239,v235\n" \
W_"a39,v239\n" \
G_"v236,a40\n" \
"v_mul_f32 v236,v236,v235\n" \
W_"a40,v236\n" \
G_"v237,a41\n" \
"v_mul_f32 v237,v237,v235\n" \
W_"a41,v237\n" \
G_"v238,a42\n" \
"v_mul_f32 v238,v238,v235\n" \
W_"a42,v238\n" \
G_"v239,a43\n" \
"v_mul_f32 v239,v239,v235\n" \
W_"a43,v239\n" \
G_"v236,a44\n" \
"v_mul_f32 v236,v236,v235\n" \
W_"a44,v236\n" \
G_"v237,a45\n" \
"v_mul_f32 v237,v237,v235\n" \
W_"a45,v237\n" \
G_"v238,a46\n" \
"v_mul_f32 v238,v238,v235\n" \
W_"a46,v238\n" \
G_"v239,a47\n" \
"v_mul_f32 v239,v239,v235\n" \
W_"a47,v239\n" \
G_"v236,a48\n" \
"v_mul_f32 v236,v236,v235\n" \
W_"a48,v236\n" \
G_"v237,a49\n" \
"v_mul_f32 v237,v237,v235\n" \
W_"a49,v237\n" \
G_"v238,a50\n" \
"v_mul_f32 v238,v238,v235\n" \
W_"a50,v238\n" \
G_"v239,a51\n" \
"v_mul_f32 v239,v239,v235\n" \
W_"a51,v239\n" \
G_"v236,a52\n" \
"v_mul_f32 v236,v236,v235\n" \
W_"a52,v236\n" \
G_"v237,a53\n" \
"v_mul_f32 v237,v237,v235\n" \
W_"a53,v237\n" \
G_"v238,a54\n" \
"v_mul_f32 v238,v238,v235\n" \
W_"a54,v238\n" \
G_"v239,a55\n" \
"v_mul_f32 v239,v239,v235\n" \
W_"a55,v239\n" \
G_"v236,a56\n" \
"v_mul_f32 v236,v236,v235\n" \
W_"a56,v236\n" \
G_"v237,a57\n" \
"v_mul_f32 v237,v237,v235\n" \
W_"a57,v237\n" \
G_"v238,a58\n" \
"v_mul_f32 v238,v238,v235\n" \
W_"a58,v238\n" \
G_"v239,a59\n" \
"v_mul_f32 v239,v239,v235\n" \
W_"a59,v239\n" \
G_"v236,a60\n" \
"v_mul_f32 v236,v236,v235\n" \
W_"a60,v236\n" \
G_"v237,a61\n" \
"v_mul_f32 v237,v237,v235\n" \
W_"a61,v237\n" \
G_"v238,a62\n" \
"v_mul_f32 v238,v238,v235\n" \
W_"a62,v238\n" \
G_"v239,a63\n" \
"v_mul_f32 v239,v239,v235\n" \
W_"a63,v239\n" \
"s_nop 1\n" \
"s_setpc_b64 s[90:91]\n" \
"LR2p0_%=:\n" \
"v_mov_b32 v235,v234\n" \
"s_nop 1\n" \
"v_permlane32_swap_b32 v234,v235\n" \
"v_max_f32 v234,v234,v235\n" \
"v_mov_b32 v235,v234\n" \
"s_nop 1\n" \
"v_permlane16_swap_b32 v234,v235\n" \
"v_max3_f32 v234,v234,v235,0\n" \
"v_sub_f32 v144,v144,v234\n" \
"v_sub_f32 v145,v145,v234\n" \
"v_sub_f32 v146,v146,v234\n" \
"v_sub_f32 v147,v147,v234\n" \
"v_sub_f32 v148,v148,v234\n" \
"v_sub_f32 v149,v149,v234\n" \
"v_sub_f32 v150,v150,v234\n" \
"v_sub_f32 v151,v151,v234\n" \
"v_sub_f32 v176,v176,v234\n" \
"v_sub_f32 v177,v177,v234\n" \
"v_sub_f32 v178,v178,v234\n" \
"v_sub_f32 v179,v179,v234\n" \
"v_exp_f32 v235,-v234\n" \
"s_nop 0\n" \
"v_mul_f32 v204,v204,v235\n" \
"v_mul_f32 v205,v205,v235\n" \
"v_mul_f32 v206,v206,v235\n" \
"v_mul_f32 v207,v207,v235\n" \
G_"v236,a64\n" \
"v_mul_f32 v236,v236,v235\n" \
W_"a64,v236\n" \
G_"v237,a65\n" \
"v_mul_f32 v237,v237,v235\n" \
W_"a65,v237\n" \
G_"v238,a66\n" \
"v_mul_f32 v238,v238,v235\n" \
W_"a66,v238\n" \
G_"v239,a67\n" \
"v_mul_f32 v239,v239,v235\n" \
W_"a67,v239\n" \
G_"v236,a68\n" \
"v_mul_f32 v236,v236,v235\n" \
W_"a68,v236\n" \
G_"v237,a69\n" \
"v_mul_f32 v237,v237,v235\n" \
W_"a69,v237\n" \
G_"v238,a70\n" \
"v_mul_f32 v238,v238,v235\n" \
W_"a70,v238\n" \
G_"v239,a71\n" \
"v_mul_f32 v239,v239,v235\n" \
W_"a71,v239\n" \
G_"v236,a72\n" \
"v_mul_f32 v236,v236,v235\n" \
W_"a72,v236\n" \
G_"v237,a73\n" \
"v_mul_f32 v237,v237,v235\n" \
W_"a73,v237\n" \
G_"v238,a74\n" \
"v_mul_f32 v238,v238,v235\n" \
W_"a74,v238\n" \
G_"v239,a75\n" \
"v_mul_f32 v239,v239,v235\n" \
W_"a75,v239\n" \
G_"v236,a76\n" \
"v_mul_f32 v236,v236,v235\n" \
W_"a76,v236\n" \
G_"v237,a77\n" \
"v_mul_f32 v237,v237,v235\n" \
W_"a77,v237\n" \
G_"v238,a78\n" \
"v_mul_f32 v238,v238,v235\n" \
W_"a78,v238\n" \
G_"v239,a79\n" \
"v_mul_f32 v239,v239,v235\n" \
W_"a79,v239\n" \
G_"v236,a80\n" \
"v_mul_f32 v236,v236,v235\n" \
W_"a80,v236\n" \
G_"v237,a81\n" \
"v_mul_f32 v237,v237,v235\n" \
W_"a81,v237\n" \
G_"v238,a82\n" \
"v_mul_f32 v238,v238,v235\n" \
W_"a82,v238\n" \
G_"v239,a83\n" \
"v_mul_f32 v239,v239,v235\n" \
W_"a83,v239\n" \
G_"v236,a84\n" \
"v_mul_f32 v236,v236,v235\n" \
W_"a84,v236\n" \
G_"v237,a85\n" \
"v_mul_f32 v237,v237,v235\n" \
W_"a85,v237\n" \
G_"v238,a86\n" \
"v_mul_f32 v238,v238,v235\n" \
W_"a86,v238\n" \
G_"v239,a87\n" \
"v_mul_f32 v239,v239,v235\n" \
W_"a87,v239\n" \
G_"v236,a88\n" \
"v_mul_f32 v236,v236,v235\n" \
W_"a88,v236\n" \
G_"v237,a89\n" \
"v_mul_f32 v237,v237,v235\n" \
W_"a89,v237\n" \
G_"v238,a90\n" \
"v_mul_f32 v238,v238,v235\n" \
W_"a90,v238\n" \
G_"v239,a91\n" \
"v_mul_f32 v239,v239,v235\n" \
W_"a91,v239\n" \
G_"v236,a92\n" \
"v_mul_f32 v236,v236,v235\n" \
W_"a92,v236\n" \
G_"v237,a93\n" \
"v_mul_f32 v237,v237,v235\n" \
W_"a93,v237\n" \
G_"v238,a94\n" \
"v_mul_f32 v238,v238,v235\n" \
W_"a94,v238\n" \
G_"v239,a95\n" \
"v_mul_f32 v239,v239,v235\n" \
W_"a95,v239\n" \
"s_nop 1\n" \
"s_setpc_b64 s[90:91]\n" \
"LR3p1_%=:\n" \
"v_mov_b32 v235,v234\n" \
"s_nop 1\n" \
"v_permlane32_swap_b32 v234,v235\n" \
"v_max_f32 v234,v234,v235\n" \
"v_mov_b32 v235,v234\n" \
"s_nop 1\n" \
"v_permlane16_swap_b32 v234,v235\n" \
"v_max3_f32 v234,v234,v235,0\n" \
"v_sub_f32 v152,v152,v234\n" \
"v_sub_f32 v153,v153,v234\n" \
"v_sub_f32 v154,v154,v234\n" \
"v_sub_f32 v155,v155,v234\n" \
"v_sub_f32 v156,v156,v234\n" \
"v_sub_f32 v157,v157,v234\n" \
"v_sub_f32 v158,v158,v234\n" \
"v_sub_f32 v159,v159,v234\n" \
"v_sub_f32 v180,v180,v234\n" \
"v_sub_f32 v181,v181,v234\n" \
"v_sub_f32 v182,v182,v234\n" \
"v_sub_f32 v183,v183,v234\n" \
"v_exp_f32 v235,-v234\n" \
"s_nop 0\n" \
"v_mul_f32 v208,v208,v235\n" \
"v_mul_f32 v209,v209,v235\n" \
"v_mul_f32 v210,v210,v235\n" \
"v_mul_f32 v211,v211,v235\n" \
G_"v236,a96\n" \
"v_mul_f32 v236,v236,v235\n" \
W_"a96,v236\n" \
G_"v237,a97\n" \
"v_mul_f32 v237,v237,v235\n" \
W_"a97,v237\n" \
G_"v238,a98\n" \
"v_mul_f32 v238,v238,v235\n" \
W_"a98,v238\n" \
G_"v239,a99\n" \
"v_mul_f32 v239,v239,v235\n" \
W_"a99,v239\n" \
G_"v236,a100\n" \
"v_mul_f32 v236,v236,v235\n" \
W_"a100,v236\n" \
G_"v237,a101\n" \
"v_mul_f32 v237,v237,v235\n" \
W_"a101,v237\n" \
G_"v238,a102\n" \
"v_mul_f32 v238,v238,v235\n" \
W_"a102,v238\n" \
G_"v239,a103\n" \
"v_mul_f32 v239,v239,v235\n" \
W_"a103,v239\n" \
G_"v236,a104\n" \
"v_mul_f32 v236,v236,v235\n" \
W_"a104,v236\n" \
G_"v237,a105\n" \
"v_mul_f32 v237,v237,v235\n" \
W_"a105,v237\n" \
G_"v238,a106\n" \
"v_mul_f32 v238,v238,v235\n" \
W_"a106,v238\n" \
G_"v239,a107\n" \
"v_mul_f32 v239,v239,v235\n" \
W_"a107,v239\n" \
G_"v236,a108\n" \
"v_mul_f32 v236,v236,v235\n" \
W_"a108,v236\n" \
G_"v237,a109\n" \
"v_mul_f32 v237,v237,v235\n" \
W_"a109,v237\n" \
G_"v238,a110\n" \
"v_mul_f32 v238,v238,v235\n" \
W_"a110,v238\n" \
G_"v239,a111\n" \
"v_mul_f32 v239,v239,v235\n" \
W_"a111,v239\n" \
G_"v236,a112\n" \
"v_mul_f32 v236,v236,v235\n" \
W_"a112,v236\n" \
G_"v237,a113\n" \
"v_mul_f32 v237,v237,v235\n" \
W_"a113,v237\n" \
G_"v238,a114\n" \
"v_mul_f32 v238,v238,v235\n" \
W_"a114,v238\n" \
G_"v239,a115\n" \
"v_mul_f32 v239,v239,v235\n" \
W_"a115,v239\n" \
G_"v236,a116\n" \
"v_mul_f32 v236,v236,v235\n" \
W_"a116,v236\n" \
G_"v237,a117\n" \
"v_mul_f32 v237,v237,v235\n" \
W_"a117,v237\n" \
G_"v238,a118\n" \
"v_mul_f32 v238,v238,v235\n" \
W_"a118,v238\n" \
G_"v239,a119\n" \
"v_mul_f32 v239,v239,v235\n" \
W_"a119,v239\n" \
G_"v236,a120\n" \
"v_mul_f32 v236,v236,v235\n" \
W_"a120,v236\n" \
G_"v237,a121\n" \
"v_mul_f32 v237,v237,v235\n" \
W_"a121,v237\n" \
G_"v238,a122\n" \
"v_mul_f32 v238,v238,v235\n" \
W_"a122,v238\n" \
G_"v239,a123\n" \
"v_mul_f32 v239,v239,v235\n" \
W_"a123,v239\n" \
G_"v236,a124\n" \
"v_mul_f32 v236,v236,v235\n" \
W_"a124,v236\n" \
G_"v237,a125\n" \
"v_mul_f32 v237,v237,v235\n" \
W_"a125,v237\n" \
G_"v238,a126\n" \
"v_mul_f32 v238,v238,v235\n" \
W_"a126,v238\n" \
G_"v239,a127\n" \
"v_mul_f32 v239,v239,v235\n" \
W_"a127,v239\n" \
"s_nop 1\n" \
"s_setpc_b64 s[90:91]\n" \
"LR4p0_%=:\n" \
"v_mov_b32 v235,v234\n" \
"s_nop 1\n" \
"v_permlane32_swap_b32 v234,v235\n" \
"v_max_f32 v234,v234,v235\n" \
"v_mov_b32 v235,v234\n" \
"s_nop 1\n" \
"v_permlane16_swap_b32 v234,v235\n" \
"v_max3_f32 v234,v234,v235,0\n" \
"v_sub_f32 v144,v144,v234\n" \
"v_sub_f32 v145,v145,v234\n" \
"v_sub_f32 v146,v146,v234\n" \
"v_sub_f32 v147,v147,v234\n" \
"v_sub_f32 v148,v148,v234\n" \
"v_sub_f32 v149,v149,v234\n" \
"v_sub_f32 v150,v150,v234\n" \
"v_sub_f32 v151,v151,v234\n" \
"v_sub_f32 v184,v184,v234\n" \
"v_sub_f32 v185,v185,v234\n" \
"v_sub_f32 v186,v186,v234\n" \
"v_sub_f32 v187,v187,v234\n" \
"v_exp_f32 v235,-v234\n" \
"s_nop 0\n" \
"v_mul_f32 v212,v212,v235\n" \
"v_mul_f32 v213,v213,v235\n" \
"v_mul_f32 v214,v214,v235\n" \
"v_mul_f32 v215,v215,v235\n" \
G_"v236,a128\n" \
"v_mul_f32 v236,v236,v235\n" \
W_"a128,v236\n" \
G_"v237,a129\n" \
"v_mul_f32 v237,v237,v235\n" \
W_"a129,v237\n" \
G_"v238,a130\n" \
"v_mul_f32 v238,v238,v235\n" \
W_"a130,v238\n" \
G_"v239,a131\n" \
"v_mul_f32 v239,v239,v235\n" \
W_"a131,v239\n" \
G_"v236,a132\n" \
"v_mul_f32 v236,v236,v235\n" \
W_"a132,v236\n" \
G_"v237,a133\n" \
"v_mul_f32 v237,v237,v235\n" \
W_"a133,v237\n" \
G_"v238,a134\n" \
"v_mul_f32 v238,v238,v235\n" \
W_"a134,v238\n" \
G_"v239,a135\n" \
"v_mul_f32 v239,v239,v235\n" \
W_"a135,v239\n" \
G_"v236,a136\n" \
"v_mul_f32 v236,v236,v235\n" \
W_"a136,v236\n" \
G_"v237,a137\n" \
"v_mul_f32 v237,v237,v235\n" \
W_"a137,v237\n" \
G_"v238,a138\n" \
"v_mul_f32 v238,v238,v235\n" \
W_"a138,v238\n" \
G_"v239,a139\n" \
"v_mul_f32 v239,v239,v235\n" \
W_"a139,v239\n" \
G_"v236,a140\n" \
"v_mul_f32 v236,v236,v235\n" \
W_"a140,v236\n" \
G_"v237,a141\n" \
"v_mul_f32 v237,v237,v235\n" \
W_"a141,v237\n" \
G_"v238,a142\n" \
"v_mul_f32 v238,v238,v235\n" \
W_"a142,v238\n" \
G_"v239,a143\n" \
"v_mul_f32 v239,v239,v235\n" \
W_"a143,v239\n" \
G_"v236,a144\n" \
"v_mul_f32 v236,v236,v235\n" \
W_"a144,v236\n" \
G_"v237,a145\n" \
"v_mul_f32 v237,v237,v235\n" \
W_"a145,v237\n" \
G_"v238,a146\n" \
"v_mul_f32 v238,v238,v235\n" \
W_"a146,v238\n" \
G_"v239,a147\n" \
"v_mul_f32 v239,v239,v235\n" \
W_"a147,v239\n" \
G_"v236,a148\n" \
"v_mul_f32 v236,v236,v235\n" \
W_"a148,v236\n" \
G_"v237,a149\n" \
"v_mul_f32 v237,v237,v235\n" \
W_"a149,v237\n" \
G_"v238,a150\n" \
"v_mul_f32 v238,v238,v235\n" \
W_"a150,v238\n" \
G_"v239,a151\n" \
"v_mul_f32 v239,v239,v235\n" \
W_"a151,v239\n" \
G_"v236,a152\n" \
"v_mul_f32 v236,v236,v235\n" \
W_"a152,v236\n" \
G_"v237,a153\n" \
"v_mul_f32 v237,v237,v235\n" \
W_"a153,v237\n" \
G_"v238,a154\n" \
"v_mul_f32 v238,v238,v235\n" \
W_"a154,v238\n" \
G_"v239,a155\n" \
"v_mul_f32 v239,v239,v235\n" \
W_"a155,v239\n" \
G_"v236,a156\n" \
"v_mul_f32 v236,v236,v235\n" \
W_"a156,v236\n" \
G_"v237,a157\n" \
"v_mul_f32 v237,v237,v235\n" \
W_"a157,v237\n" \
G_"v238,a158\n" \
"v_mul_f32 v238,v238,v235\n" \
W_"a158,v238\n" \
G_"v239,a159\n" \
"v_mul_f32 v239,v239,v235\n" \
W_"a159,v239\n" \
"s_nop 1\n" \
"s_setpc_b64 s[90:91]\n" \
"LR5p1_%=:\n" \
"v_mov_b32 v235,v234\n" \
"s_nop 1\n" \
"v_permlane32_swap_b32 v234,v235\n" \
"v_max_f32 v234,v234,v235\n" \
"v_mov_b32 v235,v234\n" \
"s_nop 1\n" \
"v_permlane16_swap_b32 v234,v235\n" \
"v_max3_f32 v234,v234,v235,0\n" \
"v_sub_f32 v152,v152,v234\n" \
"v_sub_f32 v153,v153,v234\n" \
"v_sub_f32 v154,v154,v234\n" \
"v_sub_f32 v155,v155,v234\n" \
"v_sub_f32 v156,v156,v234\n" \
"v_sub_f32 v157,v157,v234\n" \
"v_sub_f32 v158,v158,v234\n" \
"v_sub_f32 v159,v159,v234\n" \
"v_sub_f32 v188,v188,v234\n" \
"v_sub_f32 v189,v189,v234\n" \
"v_sub_f32 v190,v190,v234\n" \
"v_sub_f32 v191,v191,v234\n" \
"v_exp_f32 v235,-v234\n" \
"s_nop 0\n" \
"v_mul_f32 v216,v216,v235\n" \
"v_mul_f32 v217,v217,v235\n" \
"v_mul_f32 v218,v218,v235\n" \
"v_mul_f32 v219,v219,v235\n" \
G_"v236,a160\n" \
"v_mul_f32 v236,v236,v235\n" \
W_"a160,v236\n" \
G_"v237,a161\n" \
"v_mul_f32 v237,v237,v235\n" \
W_"a161,v237\n" \
G_"v238,a162\n" \
"v_mul_f32 v238,v238,v235\n" \
W_"a162,v238\n" \
G_"v239,a163\n" \
"v_mul_f32 v239,v239,v235\n" \
W_"a163,v239\n" \
G_"v236,a164\n" \
"v_mul_f32 v236,v236,v235\n" \
W_"a164,v236\n" \
G_"v237,a165\n" \
"v_mul_f32 v237,v237,v235\n" \
W_"a165,v237\n" \
G_"v238,a166\n" \
"v_mul_f32 v238,v238,v235\n" \
W_"a166,v238\n" \
G_"v239,a167\n" \
"v_mul_f32 v239,v239,v235\n" \
W_"a167,v239\n" \
G_"v236,a168\n" \
"v_mul_f32 v236,v236,v235\n" \
W_"a168,v236\n" \
G_"v237,a169\n" \
"v_mul_f32 v237,v237,v235\n" \
W_"a169,v237\n" \
G_"v238,a170\n" \
"v_mul_f32 v238,v238,v235\n" \
W_"a170,v238\n" \
G_"v239,a171\n" \
"v_mul_f32 v239,v239,v235\n" \
W_"a171,v239\n" \
G_"v236,a172\n" \
"v_mul_f32 v236,v236,v235\n" \
W_"a172,v236\n" \
G_"v237,a173\n" \
"v_mul_f32 v237,v237,v235\n" \
W_"a173,v237\n" \
G_"v238,a174\n" \
"v_mul_f32 v238,v238,v235\n" \
W_"a174,v238\n" \
G_"v239,a175\n" \
"v_mul_f32 v239,v239,v235\n" \
W_"a175,v239\n" \
G_"v236,a176\n" \
"v_mul_f32 v236,v236,v235\n" \
W_"a176,v236\n" \
G_"v237,a177\n" \
"v_mul_f32 v237,v237,v235\n" \
W_"a177,v237\n" \
G_"v238,a178\n" \
"v_mul_f32 v238,v238,v235\n" \
W_"a178,v238\n" \
G_"v239,a179\n" \
"v_mul_f32 v239,v239,v235\n" \
W_"a179,v239\n" \
G_"v236,a180\n" \
"v_mul_f32 v236,v236,v235\n" \
W_"a180,v236\n" \
G_"v237,a181\n" \
"v_mul_f32 v237,v237,v235\n" \
W_"a181,v237\n" \
G_"v238,a182\n" \
"v_mul_f32 v238,v238,v235\n" \
W_"a182,v238\n" \
G_"v239,a183\n" \
"v_mul_f32 v239,v239,v235\n" \
W_"a183,v239\n" \
G_"v236,a184\n" \
"v_mul_f32 v236,v236,v235\n" \
W_"a184,v236\n" \
G_"v237,a185\n" \
"v_mul_f32 v237,v237,v235\n" \
W_"a185,v237\n" \
G_"v238,a186\n" \
"v_mul_f32 v238,v238,v235\n" \
W_"a186,v238\n" \
G_"v239,a187\n" \
"v_mul_f32 v239,v239,v235\n" \
W_"a187,v239\n" \
G_"v236,a188\n" \
"v_mul_f32 v236,v236,v235\n" \
W_"a188,v236\n" \
G_"v237,a189\n" \
"v_mul_f32 v237,v237,v235\n" \
W_"a189,v237\n" \
G_"v238,a190\n" \
"v_mul_f32 v238,v238,v235\n" \
W_"a190,v238\n" \
G_"v239,a191\n" \
"v_mul_f32 v239,v239,v235\n" \
W_"a191,v239\n" \
"s_nop 1\n" \
"s_setpc_b64 s[90:91]\n" \
"LEND_%=:\n" \
    : : [ka] "s"(ka), [bid] "s"(bid), [wid] "s"(wid), [lds] "s"(ldsb) : "v0","v1","v2","v3","v4","v5","v6","v7","v8","v9","v10","v11","v12","v13","v14","v15","v16","v17","v18","v19","v20","v21","v22","v23","v24","v25","v26","v27","v28","v29","v30","v31","v32","v33","v34","v35","v36","v37","v38","v39","v40","v41","v42","v43","v44","v45","v46","v47","v48","v49","v50","v51","v52","v53","v54","v55","v56","v57","v58","v59","v60","v61","v62","v63","v64","v65","v66","v67","v68","v69","v70","v71","v72","v73","v74","v75","v76","v77","v78","v79","v80","v81","v82","v83","v84","v85","v86","v87","v88","v89","v90","v91","v92","v93","v94","v95","v96","v97","v98","v99","v100","v101","v102","v103","v104","v105","v106","v107","v108","v109","v110","v111","v112","v113","v114","v115","v116","v117","v118","v119","v120","v121","v122","v123","v124","v125","v126","v127","v128","v129","v130","v131","v132","v133","v134","v135","v136","v137","v138","v139","v140","v141","v142","v143","v144","v145","v146","v147","v148","v149","v150","v151","v152","v153","v154","v155","v156","v157","v158","v159","v160","v161","v162","v163","v164","v165","v166","v167","v168","v169","v170","v171","v172","v173","v174","v175","v176","v177","v178","v179","v180","v181","v182","v183","v184","v185","v186","v187","v188","v189","v190","v191","v192","v193","v194","v195","v196","v197","v198","v199","v200","v201","v202","v203","v204","v205","v206","v207","v208","v209","v210","v211","v212","v213","v214","v215","v216","v217","v218","v219","v220","v221","v222","v223","v224","v225","v226","v227","v228","v229","v230","v231","v232","v233","v234","v235","v236","v237","v238","v239","v240","v241","v242","v243","v244","v245","v246","v247","v248","v249","v250","v251","v252","v253","v254","v255","a0","a1","a2","a3","a4","a5","a6","a7","a8","a9","a10","a11","a12","a13","a14","a15","a16","a17","a18","a19","a20","a21","a22","a23","a24","a25","a26","a27","a28","a29","a30","a31","a32","a33","a34","a35","a36","a37","a38","a39","a40","a41","a42","a43","a44","a45","a46","a47","a48","a49","a50","a51","a52","a53","a54","a55","a56","a57","a58","a59","a60","a61","a62","a63","a64","a65","a66","a67","a68","a69","a70","a71","a72","a73","a74","a75","a76","a77","a78","a79","a80","a81","a82","a83","a84","a85","a86","a87","a88","a89","a90","a91","a92","a93","a94","a95","a96","a97","a98","a99","a100","a101","a102","a103","a104","a105","a106","a107","a108","a109","a110","a111","a112","a113","a114","a115","a116","a117","a118","a119","a120","a121","a122","a123","a124","a125","a126","a127","a128","a129","a130","a131","a132","a133","a134","a135","a136","a137","a138","a139","a140","a141","a142","a143","a144","a145","a146","a147","a148","a149","a150","a151","a152","a153","a154","a155","a156","a157","a158","a159","a160","a161","a162","a163","a164","a165","a166","a167","a168","a169","a170","a171","a172","a173","a174","a175","a176","a177","a178","a179","a180","a181","a182","a183","a184","a185","a186","a187","a188","a189","a190","a191","a192","a193","a194","a195","a196","a197","a198","a199","a200","a201","a202","a203","a204","a205","a206","a207","a208","a209","a210","a211","a212","a213","a214","a215","a216","a217","a218","a219","a220","a221","a222","a223","a224","a225","a226","a227","a228","a229","a230","a231","a232","a233","a234","a235","a236","a237","a238","a239","a240","a241","a242","a243","a244","a245","a246","a247","a248","a249","a250","a251","a252","a253","a254","a255","s8","s9","s10","s11","s12","s13","s14","s15","s16","s17","s18","s19","s20","s21","s22","s23","s24","s25","s26","s27","s28","s29","s30","s31","s36","s37","s38","s39","s40","s41","s42","s43","s44","s45","s46","s47","s48","s49","s50","s51","s52","s53","s54","s55","s56","s57","s58","s59","s60","s61","s62","s63","s64","s65","s66","s67","s68","s69","s70","s71","s72","s73","s74","s75","s76","s77","s78","s79","s80","s81","s82","s83","s84","s85","s86","s87","s88","s89","s90","s91","s92","s93","s94","s95","s96","s97","s98","s99","vcc","scc","memory")

__global__ __launch_bounds__(256, 1) void attn_fused(Ptrs p) {
  __shared__ __attribute__((aligned(256))) char lds[163840];
  const unsigned long long ka = (unsigned long long)__builtin_amdgcn_kernarg_segment_ptr();
  const unsigned bid = blockIdx.x;
  const unsigned wid = __builtin_amdgcn_readfirstlane(threadIdx.x >> 6);
  const unsigned ldsb = (unsigned)(size_t)lds;
  KBODY("nt");
}

extern "C" void kernel_launch(void* const* d_in, const int* in_sizes, int n_in, void* d_out, int out_size, void* d_ws, size_t ws_size, hipStream_t stream) {
  Ptrs p;
  for (int i = 0; i < 10; ++i) p.in[i] = (const char*)d_in[i];
  p.part = (char*)d_ws;
  p.out = (char*)d_out;
  attn_fused<<<256, 256, 0, stream>>>(p);
}
```
